```python
import torch

def _softmax(x, axis=-1):
    return torch.softmax(x, dim=axis)

def _gelu(x):
    return torch.nn.functional.gelu(x, approximate='tanh')

def _silu(x):
    return torch.nn.functional.silu(x)

def _sigmoid(x):
    return torch.sigmoid(x)

def _layernorm(x, axis=-1, eps=1e-05):
    m = x.mean(dim=axis, keepdim=True)
    v = x.var(dim=axis, keepdim=True, unbiased=False)
    return (x - m) / torch.sqrt(v + eps)

def _scatter_add(tgt, idx, src, axis=0):
    return tgt.clone().index_add_(axis, idx, src)

def _segment_sum(src, seg, n):
    out = torch.zeros((n,) + src.shape[1:], dtype=src.dtype)
    return out.index_add_(0, seg, src)

def _pad(x, pw):
    flat = tuple((v for pair in reversed(pw) for v in pair))
    return torch.nn.functional.pad(x, flat)
kernel_name = 'adaLN_cond__compressed_kv_cla__mixer_token_mlp__prompt'

def setup_inputs():

    _w = lambda a: (lambda t: t.to(torch.bfloat16) if t.is_floating_point() else t)(torch.as_tensor(a))

    def _A_setup():
        B, H, Sq, Sk, D, M = (1, 32, 2048, 2048, 128, 576)
        Dc = 4 * D
        return {'x': _w((lambda *s: torch.randn(*s, dtype=torch.bfloat16) * 0.05)(B, Sq, M)), 'c_kv': _w((lambda *s: torch.randn(*s, dtype=torch.bfloat16) * 0.8)(B, Sk, Dc)), 'w_q': _w((lambda *s: torch.randn(*s, dtype=torch.bfloat16) * 1.6)(M, H, D) * M ** (-0.5)), 'w_uk': _w((lambda *s: torch.randn(*s, dtype=torch.bfloat16) * 1.6)(Dc, H, D) * Dc ** (-0.5)), 'w_uv': _w((lambda *s: torch.randn(*s, dtype=torch.bfloat16) * 0.8)(Dc, H, D) * Dc ** (-0.5)), 'w_o': _w((lambda *s: torch.randn(*s, dtype=torch.bfloat16) * 0.8)(H, D, M) * (H * D) ** (-0.5))}
    _A_in = _A_setup()

    def _B_setup():
        B, S, D, F = (1, 2048, 576, 4608)
        return {'x': _w((lambda *s: torch.randn(*s, dtype=torch.bfloat16) * 0.05)(B, S, D)), 'w1': _w((lambda *s: torch.randn(*s, dtype=torch.bfloat16) * 0.8)(S, F) * S ** (-0.5)), 'b1': _w((lambda *s: torch.randn(*s, dtype=torch.bfloat16) * 12.8)(F) * 0.02), 'w2': _w((lambda *s: torch.randn(*s, dtype=torch.bfloat16) * 0.4)(F, S) * F ** (-0.5)), 'b2': _w((lambda *s: torch.randn(*s, dtype=torch.bfloat16) * 3.2)(S) * 0.02)}
    _B_in = _B_setup()
    return {'A_x': _A_in['x'], 'A_c_kv': _A_in['c_kv'], 'A_w_q': _A_in['w_q'], 'A_w_uk': _A_in['w_uk'], 'A_w_uv': _A_in['w_uv'], 'A_w_o': _A_in['w_o'], 'B_x': _B_in['x'], 'B_w1': _B_in['w1'], 'B_b1': _B_in['b1'], 'B_w2': _B_in['w2'], 'B_b2': _B_in['b2'], 'c': _w((lambda *s: torch.randn(*s, dtype=torch.bfloat16) * 0.1)(1, 576) * 8.0), 'w_mod': _w((lambda *s: torch.randn(*s, dtype=torch.bfloat16) * 0.1)(576, 6 * 576) * 576 ** (-0.5) * 8.0)}

def reference(A_x, A_c_kv, A_w_q, A_w_uk, A_w_uv, A_w_o, B_x, B_w1, B_b1, B_w2, B_b2, c, w_mod):

    def _A_ref(x, c_kv, w_q, w_uk, w_uv, w_o):
        B, Sq, M = (x.shape[0], x.shape[1], x.shape[2])
        Sk = c_kv.shape[1]
        H, D = (w_q.shape[1], w_q.shape[2])
        q = torch.einsum('bqm,mhd->bhqd', x, w_q)
        k = torch.einsum('bkc,chd->bhkd', c_kv, w_uk)
        v = torch.einsum('bkc,chd->bhkd', c_kv, w_uv)
        scores = torch.einsum('bhqd,bhkd->bhqk', q, k) * D ** (-0.5)
        off = Sk - Sq
        qi = torch.arange(Sq)[:, None]
        kj = torch.arange(Sk)[None, :]
        cmask = kj <= qi + off
        scores = torch.where(cmask[None, None], scores, -1000000000.0)
        p = _softmax(scores, axis=-1)
        ctx = torch.einsum('bhqk,bhkd->bhqd', p, v)
        return torch.einsum('bhqd,hdm->bqm', ctx, w_o)

    def _B_ref(x, w1, b1, w2, b2):
        B, S, D = x.shape
        h = _layernorm(x, axis=-1)
        u = torch.einsum('bsd,sf->bdf', h, w1) + b1
        u = _gelu(u)
        y = torch.einsum('bdf,fs->bds', u, w2) + b2
        return (lambda x, *d: x.permute(*d))(y, 0, 2, 1)
    x = A_x
    mod = (_silu(c) @ w_mod).reshape(c.shape[0], 6, 1, -1)
    sa, ca, ga, sb, cb, gb = (mod[:, 0].clone(), mod[:, 1].clone(), mod[:, 2].clone(), mod[:, 3].clone(), mod[:, 4].clone(), mod[:, 5].clone())
    h = _layernorm(x, axis=-1) * (1 + ca) + sa
    x = x + ga * _A_ref(h, A_c_kv, A_w_q, A_w_uk, A_w_uv, A_w_o)
    h = _layernorm(x, axis=-1) * (1 + cb) + sb
    x = x + gb * _B_ref(h, B_w1, B_b1, B_w2, B_b2)
    return x
```

```cpp
#include <hip/hip_runtime.h>
#include <hip/hip_bf16.h>
typedef unsigned short u16;
__device__ __forceinline__ float bf2f(u16 v){ return __uint_as_float(((unsigned)v)<<16); }
__device__ __forceinline__ u16 f2bf(float f){ unsigned u=__float_as_uint(f); u += 0x7fff + ((u>>16)&1); return (u16)(u>>16); }
constexpr int S=2048, M=576, H=32, D=128, DC=512, F=4608;

__global__ void k_mod(const u16* c, const u16* w, float* mod){
  int j = blockIdx.x*256+threadIdx.x; if(j>=6*M) return;
  float acc=0; for(int i=0;i<M;i++){ float x=bf2f(c[i]); float s=x/(1.f+__expf(-x)); s=bf2f(f2bf(s)); acc+=s*bf2f(w[i*6*M+j]); }
  mod[j]=bf2f(f2bf(acc));
}
__device__ float block_sum(float v, float* sh){
  for(int o=32;o;o>>=1) v+=__shfl_xor(v,o);
  __syncthreads(); if((threadIdx.x&63)==0) sh[threadIdx.x>>6]=v; __syncthreads();
  float r=0; for(int i=0;i<blockDim.x/64;i++) r+=sh[i]; return r;
}
__global__ void k_ln1(const u16* x, const float* mod, u16* h){
  __shared__ float sh[16]; int r=blockIdx.x, t=threadIdx.x;
  float v=bf2f(x[r*M+t]); float m=block_sum(v,sh)/M; float d=v-m; float var=block_sum(d*d,sh)/M;
  float y=d/sqrtf(var+1e-5f); h[r*M+t]=f2bf(y*(1.f+mod[M+t])+mod[t]);
}
template<int MODE> __global__ void k_gemm(const u16* A,int sam,int sak,const u16* B,int sbk,int sbn,int K, u16* Cb, float* Cf, int ldc, const u16* bias){
  __shared__ float As[16][17],Bs[16][17]; int tx=threadIdx.x,ty=threadIdx.y; int m=blockIdx.y*16+ty,n=blockIdx.x*16+tx; float acc=0;
  for(int k0=0;k0<K;k0+=16){ As[ty][tx]=bf2f(A[(long)m*sam+(long)(k0+tx)*sak]); Bs[ty][tx]=bf2f(B[(long)(k0+ty)*sbk+(long)n*sbn]); __syncthreads();
    for(int k=0;k<16;k++) acc+=As[ty][k]*Bs[k][tx]; __syncthreads(); }
  if(MODE==0) Cb[(long)m*ldc+n]=f2bf(acc);
  if(MODE==1) Cf[(long)m*ldc+n]=acc;
  if(MODE==2){ float u=acc+bf2f(bias[n]); float g=0.5f*u*(1.f+tanhf(0.7978845608f*(u+0.044715f*u*u*u))); Cb[(long)m*ldc+n]=f2bf(g);}
  if(MODE==3){ Cf[(long)m*ldc+n]=acc+bf2f(bias[n]); }
}
__global__ void k_attn(const u16* q,const u16* k,const u16* v,u16* ctx){
  __shared__ float sc[S]; __shared__ float qs[D]; __shared__ float sh[16]; int i=blockIdx.x,h=blockIdx.y,t=threadIdx.x;
  qs[t]=bf2f(q[(long)i*H*D+h*D+t]); __syncthreads(); float mx=-1e30f;
  for(int j=t;j<=i;j+=128){ float a=0; const u16* kr=k+(long)j*H*D+h*D; for(int d=0;d<D;d++) a+=qs[d]*bf2f(kr[d]); a*=0.08838834764f; sc[j]=a; mx=fmaxf(mx,a);}
  for(int o=32;o;o>>=1) mx=fmaxf(mx,__shfl_xor(mx,o)); __syncthreads(); if((t&63)==0) sh[t>>6]=mx; __syncthreads(); mx=fmaxf(sh[0],sh[1]); __syncthreads();
  float l=0; for(int j=t;j<=i;j+=128){ float p=__expf(sc[j]-mx); sc[j]=p; l+=p;} l=block_sum(l,sh); __syncthreads();
  float o=0; for(int j=0;j<=i;j++) o+=sc[j]*bf2f(v[(long)j*H*D+h*D+t]); ctx[(long)i*H*D+h*D+t]=f2bf(o/l);
}
__global__ void k_ln2(const u16* x,const float* ao,const float* mod,float* x1,u16* h2){
  __shared__ float sh[16]; int r=blockIdx.x,t=threadIdx.x;
  float v=bf2f(x[r*M+t])+mod[2*M+t]*ao[r*M+t]; x1[r*M+t]=v; float m=block_sum(v,sh)/M; float d=v-m; float var=block_sum(d*d,sh)/M;
  float y=d/sqrtf(var+1e-5f); y=y*(1.f+mod[4*M+t])+mod[3*M+t]; m=block_sum(y,sh)/M; d=y-m; var=block_sum(d*d,sh)/M; h2[r*M+t]=f2bf(d/sqrtf(var+1e-5f));
}
__global__ void k_fin(const float* x1,const float* y,const float* mod,u16* out){ int r=blockIdx.x,t=threadIdx.x; out[r*M+t]=f2bf(x1[r*M+t]+mod[5*M+t]*y[(long)t*S+r]); }

extern "C" void kernel_launch(void* const* d_in, const int* in_sizes, int n_in, void* d_out, int out_size, void* d_ws, size_t ws_size, hipStream_t stream) {
  const u16 *x=(const u16*)d_in[0],*ckv=(const u16*)d_in[1],*wq=(const u16*)d_in[2],*wuk=(const u16*)d_in[3],*wuv=(const u16*)d_in[4],*wo=(const u16*)d_in[5];
  const u16 *w1=(const u16*)d_in[7],*b1=(const u16*)d_in[8],*w2=(const u16*)d_in[9],*b2=(const u16*)d_in[10],*c=(const u16*)d_in[11],*wm=(const u16*)d_in[12];
  char* ws=(char*)d_ws; size_t o=0; auto al=[&](size_t n){ char* p=ws+o; o+=(n+255)&~255ull; return p; };
  float* mod=(float*)al(6*M*4); u16* h=(u16*)al(S*M*2); u16* q=(u16*)al((size_t)S*H*D*2); u16* k=(u16*)al((size_t)S*H*D*2); u16* v=(u16*)al((size_t)S*H*D*2); u16* ctx=(u16*)al((size_t)S*H*D*2);
  float* ao=(float*)al(S*M*4); float* x1=(float*)al(S*M*4); u16* h2=(u16*)al(S*M*2); u16* u=(u16*)al(M*F*2); float* y=(float*)al(M*S*4);
  k_mod<<<(6*M+255)/256,256,0,stream>>>(c,wm,mod); k_ln1<<<S,M,0,stream>>>(x,mod,h); dim3 b(16,16);
  k_gemm<0><<<dim3(H*D/16,S/16),b,0,stream>>>(h,M,1,wq,H*D,1,M,q,0,H*D,0);
  k_gemm<0><<<dim3(H*D/16,S/16),b,0,stream>>>(ckv,DC,1,wuk,H*D,1,DC,k,0,H*D,0);
  k_gemm<0><<<dim3(H*D/16,S/16),b,0,stream>>>(ckv,DC,1,wuv,H*D,1,DC,v,0,H*D,0);
  k_attn<<<dim3(S,H),128,0,stream>>>(q,k,v,ctx);
  k_gemm<1><<<dim3(M/16,S/16),b,0,stream>>>(ctx,H*D,1,wo,M,1,H*D,0,ao,M,0);
  k_ln2<<<S,M,0,stream>>>(x,ao,mod,x1,h2);
  k_gemm<2><<<dim3(F/16,M/16),b,0,stream>>>(h2,1,M,w1,F,1,S,u,0,F,b1);
  k_gemm<3><<<dim3(S/16,M/16),b,0,stream>>>(u,F,1,w2,S,1,F,0,y,S,b2);
  k_fin<<<S,M,0,stream>>>(x1,y,mod,(u16*)d_out);
}
```

```cpp
#include <hip/hip_runtime.h>
#include <hip/hip_bf16.h>
#include <stdint.h>
typedef unsigned short u16;
typedef unsigned u32;
typedef unsigned u32x4 __attribute__((ext_vector_type(4)));
typedef unsigned u32x2 __attribute__((ext_vector_type(2)));
typedef float f32x4 __attribute__((ext_vector_type(4)));
typedef float f32x2 __attribute__((ext_vector_type(2)));
typedef __bf16 bf16x2_t __attribute__((ext_vector_type(2)));
#define DI __device__ __forceinline__
DI float bf2f(u16 v) { return __uint_as_float(((u32)v) << 16); }
DI float bflo(u32 v) { return __uint_as_float(v << 16); }
DI float bfhi(u32 v) { return __uint_as_float(v & 0xffff0000u); }
DI u32 pk2(float lo, float hi) { f32x2 v = {lo, hi}; bf16x2_t b = __builtin_convertvector(v, bf16x2_t); return __builtin_bit_cast(u32, b); }
DI u16 f2bf(float f) { return (u16)(pk2(f, 0.f) & 0xffffu); }
DI float rbf(float f) { return bflo(pk2(f, 0.f)); }
DI void st16(void* p, u32x4 v) { asm volatile("global_store_dwordx4 %0, %1, off sc1\n\ts_nop 1" :: "v"(p), "v"(v) : "memory"); }
DI void st4(float* p, float v) { asm volatile("global_store_dword %0, %1, off sc1" :: "v"(p), "v"(v) : "memory"); }
constexpr int S = 2048, M = 576, H = 32, D = 128, DC = 512, F = 4608, HD = H * D;
constexpr size_t WS_MODP = 0;
constexpr size_t WS_FLAG = 65536;
constexpr size_t WS_XB   = 524288;
constexpr size_t WS_KB   = WS_XB + (size_t)128 * 18 * 1024;
constexpr size_t WS_VB   = WS_KB + (size_t)S * HD * 2;
constexpr size_t WS_CB   = WS_VB + (size_t)S * HD * 2;
constexpr size_t WS_P3   = WS_CB + (size_t)S * HD * 2;
constexpr size_t WS_X1   = WS_P3 + (size_t)5 * S * M * 2;
constexpr size_t WS_HB   = WS_X1 + (size_t)S * M * 4;
constexpr size_t WS_UB   = WS_HB + (size_t)S * M * 2;
constexpr size_t WS_P6   = WS_UB + (size_t)M * F * 2;
constexpr size_t WS_END  = WS_P6 + (size_t)5 * S * M * 2;
DI int pi_k(int g, int j) { return 16 * (j >> 2) + 4 * g + (j & 3); }
DI int dperm(int db, int c) { return 32 * (db >> 1) + 8 * (c >> 2) + 4 * (db & 1) + (c & 3); }
DI float modv(const float* modp, int j) { return rbf(modp[j] + modp[3456 + j] + modp[2 * 3456 + j]); }
#define DBGW(k)
#define DBGT(k)
#define STAMP_BEGIN()
#define STAMP_END(k)
template <int CTRL> DI float dpp_f(float v) { return __builtin_bit_cast(float, __builtin_amdgcn_update_dpp(0, __builtin_bit_cast(int, v), CTRL, 0xf, 0xf, true)); }
template <int CTRL> DI float xchg_add(float a, float b, bool up) {
  asm volatile("" : "+v"(a), "+v"(b));
  const float keep = up ? b : a, give = up ? a : b; return keep + dpp_f<CTRL>(give);
}
DI float swap16_add(float v) { auto r = __builtin_amdgcn_permlane16_swap(__float_as_uint(v), __float_as_uint(v), false, false); return __uint_as_float(r[0]) + __uint_as_float(r[1]); }
DI float swap32_add(float v) { auto r = __builtin_amdgcn_permlane32_swap(__float_as_uint(v), __float_as_uint(v), false, false); return __uint_as_float(r[0]) + __uint_as_float(r[1]); }
DI float rdlane(float v, int l) { return __uint_as_float(__builtin_amdgcn_readlane(__float_as_uint(v), l)); }

constexpr u32 FSTR = 128, FARR = 256 * FSTR;
typedef __attribute__((address_space(1))) u32 gu32;
typedef short bf16x8_t __attribute__((ext_vector_type(8)));
DI u32 ld_flag(const char* p) { return __hip_atomic_load((gu32*)p, __ATOMIC_RELAXED, __HIP_MEMORY_SCOPE_AGENT); }
DI void st_flag(char* p, u32 v) { __hip_atomic_store((gu32*)p, v, __ATOMIC_RELAXED, __HIP_MEMORY_SCOPE_AGENT); }
DI void wg_barrier() { asm volatile("s_waitcnt lgkmcnt(0)\n\ts_barrier" ::: "memory"); }
template <int NLEFT> DI void publish(char* flag, u32 tag, u32 tid) {
  asm volatile("s_waitcnt vmcnt(%0)\n\ts_barrier" :: "i"(NLEFT) : "memory");
  if (tid == 0) st_flag(flag, tag);
}
template <int N> DI void await(const char* flags, u32 idx, u32 tag, u32 wid) {
  if (wid == 0) {
    const unsigned long long t0 = __builtin_amdgcn_s_memrealtime();
    for (;;) { bool ok = true;
#pragma unroll
      for (int k = 0; k < N; k++) ok &= ld_flag(flags + (idx + 64 * k) * FSTR) == tag;
      if (__all(ok) || __builtin_amdgcn_s_memrealtime() - t0 > 2000000ull) break;
      __builtin_amdgcn_s_sleep(1);
    }
  }
  asm volatile("s_barrier" ::: "memory");
}
DI u32x4 ldb16(__amdgpu_buffer_rsrc_t r, u32 v, u32 s) { return __builtin_amdgcn_raw_buffer_load_b128(r, v, s, 16); }
DI void stb16(__amdgpu_buffer_rsrc_t r, u32 v, u32 s, u32x4 d) { asm volatile("buffer_store_dwordx4 %0, %1, %2, %3 offen sc1\n\ts_nop 1" :: "v"(d), "v"(v), "s"(r), "s"(s) : "memory"); }
DI f32x4 mfma16(u32x4 a, u32x4 b, f32x4 c) { return __builtin_amdgcn_mfma_f32_16x16x32_bf16(__builtin_bit_cast(bf16x8_t, a), __builtin_bit_cast(bf16x8_t, b), c, 0, 0, 0); }
DI u32x4 one_hot(bool on, u32 e) { const u32 w = on ? (e & 1 ? 0x3f800000u : 0x3f80u) : 0u; return u32x4{e >> 1 == 0 ? w : 0u, e >> 1 == 1 ? w : 0u, e >> 1 == 2 ? w : 0u, e >> 1 == 3 ? w : 0u}; }
#define CODE_TOUCH() do { u32 t0_, t1_; asm volatile("s_getpc_b64 s[52:53]\nCT%=:\n\ts_add_u32 s52, s52, TS9002-CT%=\n\ts_addc_u32 s53, s53, -1\n\tv_min_u32 %0, TE9002-TS9002-4, %2\n\t" \
  "global_load_dword %1, %0, s[52:53]\n\ts_waitcnt vmcnt(0)" : "=&v"(t0_), "=&v"(t1_) : "v"(tid * 128) : "s52", "s53", "scc"); } while (0)
__global__ __launch_bounds__(256, 1) void k_tail(char* __restrict__ ws, const u16* __restrict__ x, const char* __restrict__ wo, const char* __restrict__ w1, const char* __restrict__ b1,
                                                 const char* __restrict__ w2, const u16* __restrict__ b2, u16* __restrict__ out) {
  STAMP_BEGIN();
  asm volatile("TS9002:" :: "s"(ws), "s"(x), "s"(wo), "s"(w1), "s"(b1), "s"(w2), "s"(b2), "s"(out));
  __shared__ __attribute__((aligned(1024))) char lds[123904];
  const u32 lb = (u32)(uintptr_t)(__attribute__((address_space(3))) char*)lds;
  const u32 b = blockIdx.x, j = b >> 3, tid = threadIdx.x, lane = tid & 63, wid = __builtin_amdgcn_readfirstlane(tid >> 6), wx = wid >> 1, ww = wid & 1, vo = lane * 16;
  char* fl = ws + WS_FLAG; const float* modp = (const float*)(ws + WS_MODP);
  const u32 combo = (b & 7) * 10 + j / 3, t3 = j % 3, sl = combo >> 4, tt = combo & 15;
  const __amdgpu_buffer_rsrc_t srdws = __builtin_amdgcn_make_buffer_rsrc((void*)ws, 0, WS_END, 0x00020000);
  u32 tag;
  if (j < 30) {
    constexpr u32 XSTRIDE = 128 * 1024, LDW = M * 2;
    const u32 st0 = sl * 64 / 5, nst = (sl + 1) * 64 / 5 - st0;
    const __amdgpu_buffer_rsrc_t srdx = __builtin_amdgcn_make_buffer_rsrc((void*)(ws + WS_CB - 4096), 0, S * HD * 2 + 4096, 0x00020000), srdw = __builtin_amdgcn_make_buffer_rsrc((void*)(wo - 4096), 0, HD * M * 2 + 4096, 0x00020000),
                                  srdo = __builtin_amdgcn_make_buffer_rsrc((void*)(ws + WS_P3), 0, 5 * S * M * 2, 0x00020000);
    u32 sox = tt * 8 * XSTRIDE + st0 * 2048, sow = st0 * 64 * LDW + t3 * 384;
    u32 vw[6];
#pragma unroll
    for (int i = 0; i < 6; i++) { const u32 jj = wid * 6 + i; vw[i] = 4096 - (i & 3) * 1024 + (8 * (jj & 7) + (lane >> 3)) * LDW + (jj >> 3) * 128 + ((lane & 7) ^ (((lane >> 4) & 3) << 1)) * 16; }
    const u32 m0w = lb + 16384 + wid * 6 * 1024, dw = 64 * LDW;
    asm volatile(
    "s_nop 4\n"
    "s_mov_b32 s41, %[m0w]\n"
    "s_add_u32 m0, s41, 0\n"
    "s_nop 0\n"
    "buffer_load_dwordx4 %[vw0], %[srdw], %[sow] offen offset:0 lds\n"
    "buffer_load_dwordx4 %[vw1], %[srdw], %[sow] offen offset:1024 lds\n"
    "buffer_load_dwordx4 %[vw2], %[srdw], %[sow] offen offset:2048 lds\n"
    "buffer_load_dwordx4 %[vw3], %[srdw], %[sow] offen offset:3072 lds\n"
    "s_add_u32 m0, s41, 4096\n"
    "s_nop 0\n"
    "buffer_load_dwordx4 %[vw4], %[srdw], %[sow] offen offset:0 lds\n"
    "buffer_load_dwordx4 %[vw5], %[srdw], %[sow] offen offset:1024 lds\n"
    "s_add_u32 s41, s41, 40960\n"
    "s_add_u32 %[sow], %[sow], %[dw]\n"
    "s_add_u32 m0, s41, 0\n"
    "s_nop 0\n"
    "buffer_load_dwordx4 %[vw0], %[srdw], %[sow] offen offset:0 lds\n"
    "buffer_load_dwordx4 %[vw1], %[srdw], %[sow] offen offset:1024 lds\n"
    "buffer_load_dwordx4 %[vw2], %[srdw], %[sow] offen offset:2048 lds\n"
    "buffer_load_dwordx4 %[vw3], %[srdw], %[sow] offen offset:3072 lds\n"
    "s_add_u32 m0, s41, 4096\n"
    "s_nop 0\n"
    "buffer_load_dwordx4 %[vw4], %[srdw], %[sow] offen offset:0 lds\n"
    "buffer_load_dwordx4 %[vw5], %[srdw], %[sow] offen offset:1024 lds\n"
    : [sow]"+&s"(sow)
    : [vw0]"v"(vw[0]), [vw1]"v"(vw[1]), [vw2]"v"(vw[2]), [vw3]"v"(vw[3]), [vw4]"v"(vw[4]), [vw5]"v"(vw[5]), [srdw]"s"(srdw), [dw]"s"(dw), [m0w]"s"(m0w)
    : "memory", "scc", "s41");
    u32 vx[4], rdw[6];
#pragma unroll
    for (int i = 0; i < 4; i++) { const u32 jj = wid * 4 + i; vx[i] = 4096 - (i & 3) * 1024 + (jj >> 1) * XSTRIDE + (jj & 1) * 1024 + lane * 16; }
    u32 rdx = lb + wx * 4 * 2048 + lane * 16;
    { const u32 r16 = lane >> 2;
#pragma unroll
      for (int w = 0; w < 6; w++) { const u32 bb = ww * 6 + w; rdw[w] = lb + 16384 + (bb >> 2) * 8192 + (r16 >> 3) * 1024 + (r16 & 7) * 128 + (((bb & 3) ^ ((r16 >> 1) & 3)) * 32) + (lane & 3) * 8; } }
    const u32 m0x = lb + wid * 4 * 1024; u32 nmid = nst - 3;
    const u32 soo = (((sl * 64 + tt * 4 + wx * 2) * 36) + t3 * 12 + ww * 6) * 1024;
    asm volatile(
    "s_nop 4\n"
    "s_mov_b32 s40, %[m0x]\n"
    "s_mov_b32 s49, 2048\n"
    "s_add_u32 m0, s40, 0\n"
    "s_nop 0\n"
    "buffer_load_dwordx4 %[vx0], %[srdx], %[sox] offen offset:0 lds\n"
    "buffer_load_dwordx4 %[vx1], %[srdx], %[sox] offen offset:1024 lds\n"
    "buffer_load_dwordx4 %[vx2], %[srdx], %[sox] offen offset:2048 lds\n"
    "buffer_load_dwordx4 %[vx3], %[srdx], %[sox] offen offset:3072 lds\n"
    "s_add_u32 s40, s40, 40960\n"
    "s_add_u32 %[sox], %[sox], s49\n"
    "s_add_u32 m0, s40, 0\n"
    "s_nop 0\n"
    "buffer_load_dwordx4 %[vx0], %[srdx], %[sox] offen offset:0 lds\n"
    "buffer_load_dwordx4 %[vx1], %[srdx], %[sox] offen offset:1024 lds\n"
    "buffer_load_dwordx4 %[vx2], %[srdx], %[sox] offen offset:2048 lds\n"
    "buffer_load_dwordx4 %[vx3], %[srdx], %[sox] offen offset:3072 lds\n"
    "s_getpc_b64 s[52:53]\n"
    "CT_%=:\n"
    "s_add_u32 s52, s52, LM_%=-CT_%=\n"
    "s_addc_u32 s53, s53, 0\n"
    "s_mul_i32 s44, 0, 8192\n"
    "v_lshl_add_u32 v181, %[vo], 3, s44\n"
    "v_min_u32 v182, GE_%=-LM_%=-4, v181\n"
    "global_load_dword v183, v182, s[52:53] ; dummy\n"
    "s_mov_b32 s42, 40960\n"
    "s_mov_b32 s43, 0\n"
    "s_mov_b32 s47, 4294885376\n"
    "s_mov_b32 s48, 40960\n"
    "s_mov_b32 s50, 122880\n"
    "s_waitcnt vmcnt(5)\n"
    "s_barrier\n"
    "ds_read_b128 v[96:99], %[rdx] offset:0\n"
    "ds_read_b64_tr_b16 v[112:113], %[rdw0] offset:0\n"
    "ds_read_b64_tr_b16 v[114:115], %[rdw0] offset:2048\n"
    "ds_read_b64_tr_b16 v[116:117], %[rdw1] offset:0\n"
    "ds_read_b64_tr_b16 v[118:119], %[rdw1] offset:2048\n"
    "ds_read_b64_tr_b16 v[120:121], %[rdw2] offset:0\n"
    "ds_read_b64_tr_b16 v[122:123], %[rdw2] offset:2048\n"
    "ds_read_b64_tr_b16 v[124:125], %[rdw3] offset:0\n"
    "ds_read_b64_tr_b16 v[126:127], %[rdw3] offset:2048\n"
    "ds_read_b64_tr_b16 v[128:129], %[rdw4] offset:0\n"
    "ds_read_b64_tr_b16 v[130:131], %[rdw4] offset:2048\n"
    "ds_read_b64_tr_b16 v[132:133], %[rdw5] offset:0\n"
    "ds_read_b64_tr_b16 v[134:135], %[rdw5] offset:2048\n"
    "ds_read_b128 v[100:103], %[rdx] offset:2048\n"
    "ds_read_b128 v[104:107], %[rdx] offset:4096\n"
    "ds_read_b128 v[108:111], %[rdx] offset:6144\n"
    "s_waitcnt lgkmcnt(0)\n"
    "v_mfma_f32_16x16x32_bf16 v[0:3], v[96:99], v[112:115], 0\n"
    "s_add_u32 %[sox], %[sox], s49\n"
    "ds_read_b128 v[136:139], %[rdx] offset:1024\n"
    "s_add_u32 %[sow], %[sow], %[dw]\n"
    "v_mfma_f32_16x16x32_bf16 v[4:7], v[96:99], v[116:119], 0\n"
    "s_add_u32 s42, s42, s48\n"
    "ds_read_b64_tr_b16 v[152:153], %[rdw0] offset:4096\n"
    "s_cmp_eq_u32 s42, s50\n"
    "v_mfma_f32_16x16x32_bf16 v[8:11], v[96:99], v[120:123], 0\n"
    "s_cselect_b32 s42, 0, s42\n"
    "ds_read_b64_tr_b16 v[154:155], %[rdw0] offset:6144\n"
    "s_add_u32 s40, s42, %[m0x]\n"
    "v_mfma_f32_16x16x32_bf16 v[12:15], v[96:99], v[124:127], 0\n"
    "s_add_u32 s41, s42, %[m0w]\n"
    "ds_read_b64_tr_b16 v[156:157], %[rdw1] offset:4096\n"
    "v_mfma_f32_16x16x32_bf16 v[16:19], v[96:99], v[128:131], 0\n"
    "s_add_u32 m0, s40, 0\n"
    "ds_read_b64_tr_b16 v[158:159], %[rdw1] offset:6144\n"
    "v_mfma_f32_16x16x32_bf16 v[20:23], v[96:99], v[132:135], 0\n"
    "ds_read_b64_tr_b16 v[160:161], %[rdw2] offset:4096\n"
    "buffer_load_dwordx4 %[vx0], %[srdx], %[sox] offen offset:0 lds\n"
    "v_mfma_f32_16x16x32_bf16 v[44:47], v[100:103], v[132:135], 0\n"
    "ds_read_b64_tr_b16 v[162:163], %[rdw2] offset:6144\n"
    "buffer_load_dwordx4 %[vx1], %[srdx], %[sox] offen offset:1024 lds\n"
    "v_mfma_f32_16x16x32_bf16 v[40:43], v[100:103], v[128:131], 0\n"
    "ds_read_b64_tr_b16 v[164:165], %[rdw3] offset:4096\n"
    "v_mfma_f32_16x16x32_bf16 v[36:39], v[100:103], v[124:127], 0\n"
    "ds_read_b64_tr_b16 v[166:167], %[rdw3] offset:6144\n"
    "buffer_load_dwordx4 %[vx2], %[srdx], %[sox] offen offset:2048 lds\n"
    "v_mfma_f32_16x16x32_bf16 v[32:35], v[100:103], v[120:123], 0\n"
    "ds_read_b64_tr_b16 v[168:169], %[rdw4] offset:4096\n"
    "v_mfma_f32_16x16x32_bf16 v[28:31], v[100:103], v[116:119], 0\n"
    "ds_read_b64_tr_b16 v[170:171], %[rdw4] offset:6144\n"
    "buffer_load_dwordx4 %[vx3], %[srdx], %[sox] offen offset:3072 lds\n"
    "v_mfma_f32_16x16x32_bf16 v[24:27], v[100:103], v[112:115], 0\n"
    "s_add_u32 m0, s41, 0\n"
    "ds_read_b64_tr_b16 v[172:173], %[rdw5] offset:4096\n"
    "v_mfma_f32_16x16x32_bf16 v[48:51], v[104:107], v[112:115], 0\n"
    "ds_read_b64_tr_b16 v[174:175], %[rdw5] offset:6144\n"
    "buffer_load_dwordx4 %[vw0], %[srdw], %[sow] offen offset:0 lds\n"
    "v_mfma_f32_16x16x32_bf16 v[52:55], v[104:107], v[116:119], 0\n"
    "ds_read_b128 v[140:143], %[rdx] offset:3072\n"
    "v_mfma_f32_16x16x32_bf16 v[56:59], v[104:107], v[120:123], 0\n"
    "ds_read_b128 v[144:147], %[rdx] offset:5120\n"
    "buffer_load_dwordx4 %[vw1], %[srdw], %[sow] offen offset:1024 lds\n"
    "v_mfma_f32_16x16x32_bf16 v[60:63], v[104:107], v[124:127], 0\n"
    "ds_read_b128 v[148:151], %[rdx] offset:7168\n"
    "buffer_load_dwordx4 %[vw2], %[srdw], %[sow] offen offset:2048 lds\n"
    "v_mfma_f32_16x16x32_bf16 v[64:67], v[104:107], v[128:131], 0\n"
    "v_mfma_f32_16x16x32_bf16 v[68:71], v[104:107], v[132:135], 0\n"
    "buffer_load_dwordx4 %[vw3], %[srdw], %[sow] offen offset:3072 lds\n"
    "v_mfma_f32_16x16x32_bf16 v[92:95], v[108:111], v[132:135], 0\n"
    "s_add_u32 m0, s41, 4096\n"
    "v_mfma_f32_16x16x32_bf16 v[88:91], v[108:111], v[128:131], 0\n"
    "s_add_u32 s43, s43, 1\n"
    "buffer_load_dwordx4 %[vw4], %[srdw], %[sow] offen offset:0 lds\n"
    "s_cmp_eq_u32 s43, 3\n"
    "s_cselect_b32 s46, s47, s48\n"
    "v_mfma_f32_16x16x32_bf16 v[84:87], v[108:111], v[124:127], 0\n"
    "s_cselect_b32 s43, 0, s43\n"
    "v_add_u32 %[rdx], s46, %[rdx]\n"
    "v_add_u32 %[rdw0], s46, %[rdw0]\n"
    "v_add_u32 %[rdw1], s46, %[rdw1]\n"
    "v_mfma_f32_16x16x32_bf16 v[80:83], v[108:111], v[120:123], 0\n"
    "buffer_load_dwordx4 %[vw5], %[srdw], %[sow] offen offset:1024 lds\n"
    "v_add_u32 %[rdw2], s46, %[rdw2]\n"
    "v_mfma_f32_16x16x32_bf16 v[76:79], v[108:111], v[116:119], 0\n"
    "v_add_u32 %[rdw3], s46, %[rdw3]\n"
    "v_add_u32 %[rdw4], s46, %[rdw4]\n"
    "v_mfma_f32_16x16x32_bf16 v[72:75], v[108:111], v[112:115], 0\n"
    "v_add_u32 %[rdw5], s46, %[rdw5]\n"
    "s_waitcnt lgkmcnt(0) vmcnt(10)\n"
    "s_barrier\n"
    "LM_%=:\n"
    "v_mfma_f32_16x16x32_bf16 v[0:3], v[136:139], v[152:155], v[0:3]\n"
    "s_add_u32 %[sox], %[sox], s49\n"
    "ds_read_b128 v[96:99], %[rdx] offset:0\n"
    "v_mfma_f32_16x16x32_bf16 v[4:7], v[136:139], v[156:159], v[4:7]\n"
    "s_add_u32 %[sow], %[sow], %[dw]\n"
    "ds_read_b64_tr_b16 v[112:113], %[rdw0] offset:0\n"
    "v_mfma_f32_16x16x32_bf16 v[8:11], v[136:139], v[160:163], v[8:11]\n"
    "s_add_u32 s42, s42, s48\n"
    "ds_read_b64_tr_b16 v[114:115], %[rdw0] offset:2048\n"
    "v_mfma_f32_16x16x32_bf16 v[12:15], v[136:139], v[164:167], v[12:15]\n"
    "s_cmp_eq_u32 s42, s50\n"
    "ds_read_b64_tr_b16 v[116:117], %[rdw1] offset:0\n"
    "v_mfma_f32_16x16x32_bf16 v[16:19], v[136:139], v[168:171], v[16:19]\n"
    "s_cselect_b32 s42, 0, s42\n"
    "ds_read_b64_tr_b16 v[118:119], %[rdw1] offset:2048\n"
    "v_mfma_f32_16x16x32_bf16 v[20:23], v[136:139], v[172:175], v[20:23]\n"
    "s_add_u32 s40, s42, %[m0x]\n"
    "ds_read_b64_tr_b16 v[120:121], %[rdw2] offset:0\n"
    "v_mfma_f32_16x16x32_bf16 v[44:47], v[140:143], v[172:175], v[44:47]\n"
    "s_add_u32 s41, s42, %[m0w]\n"
    "ds_read_b64_tr_b16 v[122:123], %[rdw2] offset:2048\n"
    "v_mfma_f32_16x16x32_bf16 v[40:43], v[140:143], v[168:171], v[40:43]\n"
    "s_add_u32 m0, s40, 0\n"
    "ds_read_b64_tr_b16 v[124:125], %[rdw3] offset:0\n"
    "v_mfma_f32_16x16x32_bf16 v[36:39], v[140:143], v[164:167], v[36:39]\n"
    "ds_read_b64_tr_b16 v[126:127], %[rdw3] offset:2048\n"
    "buffer_load_dwordx4 %[vx0], %[srdx], %[sox] offen offset:0 lds\n"
    "v_mfma_f32_16x16x32_bf16 v[32:35], v[140:143], v[160:163], v[32:35]\n"
    "ds_read_b64_tr_b16 v[128:129], %[rdw4] offset:0\n"
    "v_mfma_f32_16x16x32_bf16 v[28:31], v[140:143], v[156:159], v[28:31]\n"
    "ds_read_b64_tr_b16 v[130:131], %[rdw4] offset:2048\n"
    "v_mfma_f32_16x16x32_bf16 v[24:27], v[140:143], v[152:155], v[24:27]\n"
    "ds_read_b64_tr_b16 v[132:133], %[rdw5] offset:0\n"
    "buffer_load_dwordx4 %[vx1], %[srdx], %[sox] offen offset:1024 lds\n"
    "v_mfma_f32_16x16x32_bf16 v[48:51], v[144:147], v[152:155], v[48:51]\n"
    "ds_read_b64_tr_b16 v[134:135], %[rdw5] offset:2048\n"
    "v_mfma_f32_16x16x32_bf16 v[52:55], v[144:147], v[156:159], v[52:55]\n"
    "ds_read_b128 v[100:103], %[rdx] offset:2048\n"
    "v_mfma_f32_16x16x32_bf16 v[56:59], v[144:147], v[160:163], v[56:59]\n"
    "ds_read_b128 v[104:107], %[rdx] offset:4096\n"
    "v_mfma_f32_16x16x32_bf16 v[60:63], v[144:147], v[164:167], v[60:63]\n"
    "ds_read_b128 v[108:111], %[rdx] offset:6144\n"
    "buffer_load_dwordx4 %[vx2], %[srdx], %[sox] offen offset:2048 lds\n"
    "v_mfma_f32_16x16x32_bf16 v[64:67], v[144:147], v[168:171], v[64:67]\n"
    "v_mfma_f32_16x16x32_bf16 v[68:71], v[144:147], v[172:175], v[68:71]\n"
    "v_mfma_f32_16x16x32_bf16 v[92:95], v[148:151], v[172:175], v[92:95]\n"
    "v_mfma_f32_16x16x32_bf16 v[88:91], v[148:151], v[168:171], v[88:91]\n"
    "buffer_load_dwordx4 %[vx3], %[srdx], %[sox] offen offset:3072 lds\n"
    "v_mfma_f32_16x16x32_bf16 v[84:87], v[148:151], v[164:167], v[84:87]\n"
    "v_mfma_f32_16x16x32_bf16 v[80:83], v[148:151], v[160:163], v[80:83]\n"
    "v_mfma_f32_16x16x32_bf16 v[76:79], v[148:151], v[156:159], v[76:79]\n"
    "s_add_u32 m0, s41, 0\n"
    "v_mfma_f32_16x16x32_bf16 v[72:75], v[148:151], v[152:155], v[72:75]\n"
    "s_waitcnt lgkmcnt(0)\n"
    "buffer_load_dwordx4 %[vw0], %[srdw], %[sow] offen offset:0 lds\n"
    "v_mfma_f32_16x16x32_bf16 v[0:3], v[96:99], v[112:115], v[0:3]\n"
    "ds_read_b128 v[136:139], %[rdx] offset:1024\n"
    "v_mfma_f32_16x16x32_bf16 v[4:7], v[96:99], v[116:119], v[4:7]\n"
    "ds_read_b64_tr_b16 v[152:153], %[rdw0] offset:4096\n"
    "v_mfma_f32_16x16x32_bf16 v[8:11], v[96:99], v[120:123], v[8:11]\n"
    "ds_read_b64_tr_b16 v[154:155], %[rdw0] offset:6144\n"
    "v_mfma_f32_16x16x32_bf16 v[12:15], v[96:99], v[124:127], v[12:15]\n"
    "ds_read_b64_tr_b16 v[156:157], %[rdw1] offset:4096\n"
    "buffer_load_dwordx4 %[vw1], %[srdw], %[sow] offen offset:1024 lds\n"
    "v_mfma_f32_16x16x32_bf16 v[16:19], v[96:99], v[128:131], v[16:19]\n"
    "ds_read_b64_tr_b16 v[158:159], %[rdw1] offset:6144\n"
    "v_mfma_f32_16x16x32_bf16 v[20:23], v[96:99], v[132:135], v[20:23]\n"
    "ds_read_b64_tr_b16 v[160:161], %[rdw2] offset:4096\n"
    "v_mfma_f32_16x16x32_bf16 v[44:47], v[100:103], v[132:135], v[44:47]\n"
    "ds_read_b64_tr_b16 v[162:163], %[rdw2] offset:6144\n"
    "v_mfma_f32_16x16x32_bf16 v[40:43], v[100:103], v[128:131], v[40:43]\n"
    "ds_read_b64_tr_b16 v[164:165], %[rdw3] offset:4096\n"
    "buffer_load_dwordx4 %[vw2], %[srdw], %[sow] offen offset:2048 lds\n"
    "v_mfma_f32_16x16x32_bf16 v[36:39], v[100:103], v[124:127], v[36:39]\n"
    "ds_read_b64_tr_b16 v[166:167], %[rdw3] offset:6144\n"
    "v_mfma_f32_16x16x32_bf16 v[32:35], v[100:103], v[120:123], v[32:35]\n"
    "ds_read_b64_tr_b16 v[168:169], %[rdw4] offset:4096\n"
    "v_mfma_f32_16x16x32_bf16 v[28:31], v[100:103], v[116:119], v[28:31]\n"
    "ds_read_b64_tr_b16 v[170:171], %[rdw4] offset:6144\n"
    "v_mfma_f32_16x16x32_bf16 v[24:27], v[100:103], v[112:115], v[24:27]\n"
    "ds_read_b64_tr_b16 v[172:173], %[rdw5] offset:4096\n"
    "buffer_load_dwordx4 %[vw3], %[srdw], %[sow] offen offset:3072 lds\n"
    "v_mfma_f32_16x16x32_bf16 v[48:51], v[104:107], v[112:115], v[48:51]\n"
    "ds_read_b64_tr_b16 v[174:175], %[rdw5] offset:6144\n"
    "v_mfma_f32_16x16x32_bf16 v[52:55], v[104:107], v[116:119], v[52:55]\n"
    "ds_read_b128 v[140:143], %[rdx] offset:3072\n"
    "v_mfma_f32_16x16x32_bf16 v[56:59], v[104:107], v[120:123], v[56:59]\n"
    "s_add_u32 m0, s41, 4096\n"
    "ds_read_b128 v[144:147], %[rdx] offset:5120\n"
    "v_mfma_f32_16x16x32_bf16 v[60:63], v[104:107], v[124:127], v[60:63]\n"
    "ds_read_b128 v[148:151], %[rdx] offset:7168\n"
    "buffer_load_dwordx4 %[vw4], %[srdw], %[sow] offen offset:0 lds\n"
    "v_mfma_f32_16x16x32_bf16 v[64:67], v[104:107], v[128:131], v[64:67]\n"
    "s_add_u32 s43, s43, 1\n"
    "s_cmp_eq_u32 s43, 3\n"
    "s_cselect_b32 s46, s47, s48\n"
    "v_mfma_f32_16x16x32_bf16 v[68:71], v[104:107], v[132:135], v[68:71]\n"
    "s_cselect_b32 s43, 0, s43\n"
    "v_add_u32 %[rdx], s46, %[rdx]\n"
    "v_add_u32 %[rdw0], s46, %[rdw0]\n"
    "v_mfma_f32_16x16x32_bf16 v[92:95], v[108:111], v[132:135], v[92:95]\n"
    "v_add_u32 %[rdw1], s46, %[rdw1]\n"
    "v_mfma_f32_16x16x32_bf16 v[88:91], v[108:111], v[128:131], v[88:91]\n"
    "buffer_load_dwordx4 %[vw5], %[srdw], %[sow] offen offset:1024 lds\n"
    "v_add_u32 %[rdw2], s46, %[rdw2]\n"
    "v_mfma_f32_16x16x32_bf16 v[84:87], v[108:111], v[124:127], v[84:87]\n"
    "v_add_u32 %[rdw3], s46, %[rdw3]\n"
    "v_mfma_f32_16x16x32_bf16 v[80:83], v[108:111], v[120:123], v[80:83]\n"
    "v_add_u32 %[rdw4], s46, %[rdw4]\n"
    "v_mfma_f32_16x16x32_bf16 v[76:79], v[108:111], v[116:119], v[76:79]\n"
    "v_add_u32 %[rdw5], s46, %[rdw5]\n"
    "s_sub_u32 %[nmid], %[nmid], 1\n"
    "v_mfma_f32_16x16x32_bf16 v[72:75], v[108:111], v[112:115], v[72:75]\n"
    "s_cmp_lg_u32 %[nmid], 0\n"
    "s_waitcnt lgkmcnt(0) vmcnt(10)\n"
    "s_barrier\n"
    "s_cbranch_scc1 LM_%=\n"
    "v_mfma_f32_16x16x32_bf16 v[0:3], v[136:139], v[152:155], v[0:3]\n"
    "ds_read_b128 v[96:99], %[rdx] offset:0\n"
    "v_mfma_f32_16x16x32_bf16 v[4:7], v[136:139], v[156:159], v[4:7]\n"
    "ds_read_b64_tr_b16 v[112:113], %[rdw0] offset:0\n"
    "v_mfma_f32_16x16x32_bf16 v[8:11], v[136:139], v[160:163], v[8:11]\n"
    "ds_read_b64_tr_b16 v[114:115], %[rdw0] offset:2048\n"
    "v_mfma_f32_16x16x32_bf16 v[12:15], v[136:139], v[164:167], v[12:15]\n"
    "ds_read_b64_tr_b16 v[116:117], %[rdw1] offset:0\n"
    "v_mfma_f32_16x16x32_bf16 v[16:19], v[136:139], v[168:171], v[16:19]\n"
    "ds_read_b64_tr_b16 v[118:119], %[rdw1] offset:2048\n"
    "v_mfma_f32_16x16x32_bf16 v[20:23], v[136:139], v[172:175], v[20:23]\n"
    "ds_read_b64_tr_b16 v[120:121], %[rdw2] offset:0\n"
    "v_mfma_f32_16x16x32_bf16 v[44:47], v[140:143], v[172:175], v[44:47]\n"
    "ds_read_b64_tr_b16 v[122:123], %[rdw2] offset:2048\n"
    "v_mfma_f32_16x16x32_bf16 v[40:43], v[140:143], v[168:171], v[40:43]\n"
    "ds_read_b64_tr_b16 v[124:125], %[rdw3] offset:0\n"
    "v_mfma_f32_16x16x32_bf16 v[36:39], v[140:143], v[164:167], v[36:39]\n"
    "ds_read_b64_tr_b16 v[126:127], %[rdw3] offset:2048\n"
    "v_mfma_f32_16x16x32_bf16 v[32:35], v[140:143], v[160:163], v[32:35]\n"
    "ds_read_b64_tr_b16 v[128:129], %[rdw4] offset:0\n"
    "v_mfma_f32_16x16x32_bf16 v[28:31], v[140:143], v[156:159], v[28:31]\n"
    "ds_read_b64_tr_b16 v[130:131], %[rdw4] offset:2048\n"
    "v_mfma_f32_16x16x32_bf16 v[24:27], v[140:143], v[152:155], v[24:27]\n"
    "ds_read_b64_tr_b16 v[132:133], %[rdw5] offset:0\n"
    "v_mfma_f32_16x16x32_bf16 v[48:51], v[144:147], v[152:155], v[48:51]\n"
    "ds_read_b64_tr_b16 v[134:135], %[rdw5] offset:2048\n"
    "v_mfma_f32_16x16x32_bf16 v[52:55], v[144:147], v[156:159], v[52:55]\n"
    "ds_read_b128 v[100:103], %[rdx] offset:2048\n"
    "v_mfma_f32_16x16x32_bf16 v[56:59], v[144:147], v[160:163], v[56:59]\n"
    "ds_read_b128 v[104:107], %[rdx] offset:4096\n"
    "v_mfma_f32_16x16x32_bf16 v[60:63], v[144:147], v[164:167], v[60:63]\n"
    "ds_read_b128 v[108:111], %[rdx] offset:6144\n"
    "v_mfma_f32_16x16x32_bf16 v[64:67], v[144:147], v[168:171], v[64:67]\n"
    "v_mfma_f32_16x16x32_bf16 v[68:71], v[144:147], v[172:175], v[68:71]\n"
    "v_mfma_f32_16x16x32_bf16 v[92:95], v[148:151], v[172:175], v[92:95]\n"
    "v_mfma_f32_16x16x32_bf16 v[88:91], v[148:151], v[168:171], v[88:91]\n"
    "v_mfma_f32_16x16x32_bf16 v[84:87], v[148:151], v[164:167], v[84:87]\n"
    "v_mfma_f32_16x16x32_bf16 v[80:83], v[148:151], v[160:163], v[80:83]\n"
    "v_mfma_f32_16x16x32_bf16 v[76:79], v[148:151], v[156:159], v[76:79]\n"
    "v_mfma_f32_16x16x32_bf16 v[72:75], v[148:151], v[152:155], v[72:75]\n"
    "s_waitcnt lgkmcnt(0)\n"
    "v_mfma_f32_16x16x32_bf16 v[0:3], v[96:99], v[112:115], v[0:3]\n"
    "ds_read_b128 v[136:139], %[rdx] offset:1024\n"
    "v_mfma_f32_16x16x32_bf16 v[4:7], v[96:99], v[116:119], v[4:7]\n"
    "ds_read_b64_tr_b16 v[152:153], %[rdw0] offset:4096\n"
    "v_mfma_f32_16x16x32_bf16 v[8:11], v[96:99], v[120:123], v[8:11]\n"
    "ds_read_b64_tr_b16 v[154:155], %[rdw0] offset:6144\n"
    "v_mfma_f32_16x16x32_bf16 v[12:15], v[96:99], v[124:127], v[12:15]\n"
    "ds_read_b64_tr_b16 v[156:157], %[rdw1] offset:4096\n"
    "v_mfma_f32_16x16x32_bf16 v[16:19], v[96:99], v[128:131], v[16:19]\n"
    "ds_read_b64_tr_b16 v[158:159], %[rdw1] offset:6144\n"
    "v_mfma_f32_16x16x32_bf16 v[20:23], v[96:99], v[132:135], v[20:23]\n"
    "ds_read_b64_tr_b16 v[160:161], %[rdw2] offset:4096\n"
    "v_mfma_f32_16x16x32_bf16 v[44:47], v[100:103], v[132:135], v[44:47]\n"
    "ds_read_b64_tr_b16 v[162:163], %[rdw2] offset:6144\n"
    "v_mfma_f32_16x16x32_bf16 v[40:43], v[100:103], v[128:131], v[40:43]\n"
    "ds_read_b64_tr_b16 v[164:165], %[rdw3] offset:4096\n"
    "v_mfma_f32_16x16x32_bf16 v[36:39], v[100:103], v[124:127], v[36:39]\n"
    "ds_read_b64_tr_b16 v[166:167], %[rdw3] offset:6144\n"
    "v_mfma_f32_16x16x32_bf16 v[32:35], v[100:103], v[120:123], v[32:35]\n"
    "ds_read_b64_tr_b16 v[168:169], %[rdw4] offset:4096\n"
    "v_mfma_f32_16x16x32_bf16 v[28:31], v[100:103], v[116:119], v[28:31]\n"
    "ds_read_b64_tr_b16 v[170:171], %[rdw4] offset:6144\n"
    "v_mfma_f32_16x16x32_bf16 v[24:27], v[100:103], v[112:115], v[24:27]\n"
    "ds_read_b64_tr_b16 v[172:173], %[rdw5] offset:4096\n"
    "v_mfma_f32_16x16x32_bf16 v[48:51], v[104:107], v[112:115], v[48:51]\n"
    "ds_read_b64_tr_b16 v[174:175], %[rdw5] offset:6144\n"
    "v_mfma_f32_16x16x32_bf16 v[52:55], v[104:107], v[116:119], v[52:55]\n"
    "ds_read_b128 v[140:143], %[rdx] offset:3072\n"
    "v_mfma_f32_16x16x32_bf16 v[56:59], v[104:107], v[120:123], v[56:59]\n"
    "ds_read_b128 v[144:147], %[rdx] offset:5120\n"
    "v_mfma_f32_16x16x32_bf16 v[60:63], v[104:107], v[124:127], v[60:63]\n"
    "ds_read_b128 v[148:151], %[rdx] offset:7168\n"
    "v_mfma_f32_16x16x32_bf16 v[64:67], v[104:107], v[128:131], v[64:67]\n"
    "s_add_u32 s43, s43, 1\n"
    "s_cmp_eq_u32 s43, 3\n"
    "s_cselect_b32 s46, s47, s48\n"
    "v_mfma_f32_16x16x32_bf16 v[68:71], v[104:107], v[132:135], v[68:71]\n"
    "s_cselect_b32 s43, 0, s43\n"
    "v_add_u32 %[rdx], s46, %[rdx]\n"
    "v_add_u32 %[rdw0], s46, %[rdw0]\n"
    "v_mfma_f32_16x16x32_bf16 v[92:95], v[108:111], v[132:135], v[92:95]\n"
    "v_add_u32 %[rdw1], s46, %[rdw1]\n"
    "v_mfma_f32_16x16x32_bf16 v[88:91], v[108:111], v[128:131], v[88:91]\n"
    "v_add_u32 %[rdw2], s46, %[rdw2]\n"
    "v_mfma_f32_16x16x32_bf16 v[84:87], v[108:111], v[124:127], v[84:87]\n"
    "v_add_u32 %[rdw3], s46, %[rdw3]\n"
    "v_mfma_f32_16x16x32_bf16 v[80:83], v[108:111], v[120:123], v[80:83]\n"
    "v_add_u32 %[rdw4], s46, %[rdw4]\n"
    "v_mfma_f32_16x16x32_bf16 v[76:79], v[108:111], v[116:119], v[76:79]\n"
    "v_add_u32 %[rdw5], s46, %[rdw5]\n"
    "v_mfma_f32_16x16x32_bf16 v[72:75], v[108:111], v[112:115], v[72:75]\n"
    "s_waitcnt lgkmcnt(0) vmcnt(0)\n"
    "s_barrier\n"
    "v_mfma_f32_16x16x32_bf16 v[0:3], v[136:139], v[152:155], v[0:3]\n"
    "ds_read_b128 v[96:99], %[rdx] offset:0\n"
    "v_mfma_f32_16x16x32_bf16 v[4:7], v[136:139], v[156:159], v[4:7]\n"
    "ds_read_b64_tr_b16 v[112:113], %[rdw0] offset:0\n"
    "v_mfma_f32_16x16x32_bf16 v[8:11], v[136:139], v[160:163], v[8:11]\n"
    "ds_read_b64_tr_b16 v[114:115], %[rdw0] offset:2048\n"
    "v_mfma_f32_16x16x32_bf16 v[12:15], v[136:139], v[164:167], v[12:15]\n"
    "ds_read_b64_tr_b16 v[116:117], %[rdw1] offset:0\n"
    "v_mfma_f32_16x16x32_bf16 v[16:19], v[136:139], v[168:171], v[16:19]\n"
    "ds_read_b64_tr_b16 v[118:119], %[rdw1] offset:2048\n"
    "v_mfma_f32_16x16x32_bf16 v[20:23], v[136:139], v[172:175], v[20:23]\n"
    "ds_read_b64_tr_b16 v[120:121], %[rdw2] offset:0\n"
    "v_mfma_f32_16x16x32_bf16 v[44:47], v[140:143], v[172:175], v[44:47]\n"
    "ds_read_b64_tr_b16 v[122:123], %[rdw2] offset:2048\n"
    "v_mfma_f32_16x16x32_bf16 v[40:43], v[140:143], v[168:171], v[40:43]\n"
    "ds_read_b64_tr_b16 v[124:125], %[rdw3] offset:0\n"
    "v_mfma_f32_16x16x32_bf16 v[36:39], v[140:143], v[164:167], v[36:39]\n"
    "ds_read_b64_tr_b16 v[126:127], %[rdw3] offset:2048\n"
    "v_mfma_f32_16x16x32_bf16 v[32:35], v[140:143], v[160:163], v[32:35]\n"
    "ds_read_b64_tr_b16 v[128:129], %[rdw4] offset:0\n"
    "v_mfma_f32_16x16x32_bf16 v[28:31], v[140:143], v[156:159], v[28:31]\n"
    "ds_read_b64_tr_b16 v[130:131], %[rdw4] offset:2048\n"
    "v_mfma_f32_16x16x32_bf16 v[24:27], v[140:143], v[152:155], v[24:27]\n"
    "ds_read_b64_tr_b16 v[132:133], %[rdw5] offset:0\n"
    "v_mfma_f32_16x16x32_bf16 v[48:51], v[144:147], v[152:155], v[48:51]\n"
    "ds_read_b64_tr_b16 v[134:135], %[rdw5] offset:2048\n"
    "v_mfma_f32_16x16x32_bf16 v[52:55], v[144:147], v[156:159], v[52:55]\n"
    "ds_read_b128 v[100:103], %[rdx] offset:2048\n"
    "v_mfma_f32_16x16x32_bf16 v[56:59], v[144:147], v[160:163], v[56:59]\n"
    "ds_read_b128 v[104:107], %[rdx] offset:4096\n"
    "v_mfma_f32_16x16x32_bf16 v[60:63], v[144:147], v[164:167], v[60:63]\n"
    "ds_read_b128 v[108:111], %[rdx] offset:6144\n"
    "v_mfma_f32_16x16x32_bf16 v[64:67], v[144:147], v[168:171], v[64:67]\n"
    "v_mfma_f32_16x16x32_bf16 v[68:71], v[144:147], v[172:175], v[68:71]\n"
    "v_mfma_f32_16x16x32_bf16 v[92:95], v[148:151], v[172:175], v[92:95]\n"
    "v_mfma_f32_16x16x32_bf16 v[88:91], v[148:151], v[168:171], v[88:91]\n"
    "v_mfma_f32_16x16x32_bf16 v[84:87], v[148:151], v[164:167], v[84:87]\n"
    "v_mfma_f32_16x16x32_bf16 v[80:83], v[148:151], v[160:163], v[80:83]\n"
    "v_mfma_f32_16x16x32_bf16 v[76:79], v[148:151], v[156:159], v[76:79]\n"
    "v_mfma_f32_16x16x32_bf16 v[72:75], v[148:151], v[152:155], v[72:75]\n"
    "s_waitcnt lgkmcnt(0)\n"
    "v_mfma_f32_16x16x32_bf16 v[0:3], v[96:99], v[112:115], v[0:3]\n"
    "ds_read_b128 v[136:139], %[rdx] offset:1024\n"
    "v_mfma_f32_16x16x32_bf16 v[4:7], v[96:99], v[116:119], v[4:7]\n"
    "ds_read_b64_tr_b16 v[152:153], %[rdw0] offset:4096\n"
    "v_mfma_f32_16x16x32_bf16 v[8:11], v[96:99], v[120:123], v[8:11]\n"
    "ds_read_b64_tr_b16 v[154:155], %[rdw0] offset:6144\n"
    "v_mfma_f32_16x16x32_bf16 v[12:15], v[96:99], v[124:127], v[12:15]\n"
    "ds_read_b64_tr_b16 v[156:157], %[rdw1] offset:4096\n"
    "v_mfma_f32_16x16x32_bf16 v[16:19], v[96:99], v[128:131], v[16:19]\n"
    "ds_read_b64_tr_b16 v[158:159], %[rdw1] offset:6144\n"
    "v_mfma_f32_16x16x32_bf16 v[20:23], v[96:99], v[132:135], v[20:23]\n"
    "ds_read_b64_tr_b16 v[160:161], %[rdw2] offset:4096\n"
    "v_mfma_f32_16x16x32_bf16 v[44:47], v[100:103], v[132:135], v[44:47]\n"
    "ds_read_b64_tr_b16 v[162:163], %[rdw2] offset:6144\n"
    "v_mfma_f32_16x16x32_bf16 v[40:43], v[100:103], v[128:131], v[40:43]\n"
    "ds_read_b64_tr_b16 v[164:165], %[rdw3] offset:4096\n"
    "v_mfma_f32_16x16x32_bf16 v[36:39], v[100:103], v[124:127], v[36:39]\n"
    "ds_read_b64_tr_b16 v[166:167], %[rdw3] offset:6144\n"
    "v_mfma_f32_16x16x32_bf16 v[32:35], v[100:103], v[120:123], v[32:35]\n"
    "ds_read_b64_tr_b16 v[168:169], %[rdw4] offset:4096\n"
    "v_mfma_f32_16x16x32_bf16 v[28:31], v[100:103], v[116:119], v[28:31]\n"
    "ds_read_b64_tr_b16 v[170:171], %[rdw4] offset:6144\n"
    "v_mfma_f32_16x16x32_bf16 v[24:27], v[100:103], v[112:115], v[24:27]\n"
    "ds_read_b64_tr_b16 v[172:173], %[rdw5] offset:4096\n"
    "v_mfma_f32_16x16x32_bf16 v[48:51], v[104:107], v[112:115], v[48:51]\n"
    "ds_read_b64_tr_b16 v[174:175], %[rdw5] offset:6144\n"
    "v_mfma_f32_16x16x32_bf16 v[52:55], v[104:107], v[116:119], v[52:55]\n"
    "ds_read_b128 v[140:143], %[rdx] offset:3072\n"
    "v_mfma_f32_16x16x32_bf16 v[56:59], v[104:107], v[120:123], v[56:59]\n"
    "ds_read_b128 v[144:147], %[rdx] offset:5120\n"
    "v_mfma_f32_16x16x32_bf16 v[60:63], v[104:107], v[124:127], v[60:63]\n"
    "ds_read_b128 v[148:151], %[rdx] offset:7168\n"
    "v_mfma_f32_16x16x32_bf16 v[64:67], v[104:107], v[128:131], v[64:67]\n"
    "s_add_u32 s43, s43, 1\n"
    "s_cmp_eq_u32 s43, 3\n"
    "s_cselect_b32 s46, s47, s48\n"
    "v_mfma_f32_16x16x32_bf16 v[68:71], v[104:107], v[132:135], v[68:71]\n"
    "s_cselect_b32 s43, 0, s43\n"
    "v_add_u32 %[rdx], s46, %[rdx]\n"
    "v_add_u32 %[rdw0], s46, %[rdw0]\n"
    "v_mfma_f32_16x16x32_bf16 v[92:95], v[108:111], v[132:135], v[92:95]\n"
    "v_add_u32 %[rdw1], s46, %[rdw1]\n"
    "v_mfma_f32_16x16x32_bf16 v[88:91], v[108:111], v[128:131], v[88:91]\n"
    "v_add_u32 %[rdw2], s46, %[rdw2]\n"
    "v_mfma_f32_16x16x32_bf16 v[84:87], v[108:111], v[124:127], v[84:87]\n"
    "v_add_u32 %[rdw3], s46, %[rdw3]\n"
    "v_mfma_f32_16x16x32_bf16 v[80:83], v[108:111], v[120:123], v[80:83]\n"
    "v_add_u32 %[rdw4], s46, %[rdw4]\n"
    "v_mfma_f32_16x16x32_bf16 v[76:79], v[108:111], v[116:119], v[76:79]\n"
    "v_add_u32 %[rdw5], s46, %[rdw5]\n"
    "v_mfma_f32_16x16x32_bf16 v[72:75], v[108:111], v[112:115], v[72:75]\n"
    "s_waitcnt lgkmcnt(0)\n"
    "s_barrier\n"
    "v_mfma_f32_16x16x32_bf16 v[0:3], v[136:139], v[152:155], v[0:3]\n"
    "v_mfma_f32_16x16x32_bf16 v[4:7], v[136:139], v[156:159], v[4:7]\n"
    "v_mfma_f32_16x16x32_bf16 v[8:11], v[136:139], v[160:163], v[8:11]\n"
    "v_mfma_f32_16x16x32_bf16 v[12:15], v[136:139], v[164:167], v[12:15]\n"
    "v_mfma_f32_16x16x32_bf16 v[16:19], v[136:139], v[168:171], v[16:19]\n"
    "v_mfma_f32_16x16x32_bf16 v[20:23], v[136:139], v[172:175], v[20:23]\n"
    "v_mfma_f32_16x16x32_bf16 v[44:47], v[140:143], v[172:175], v[44:47]\n"
    "v_mfma_f32_16x16x32_bf16 v[40:43], v[140:143], v[168:171], v[40:43]\n"
    "v_mfma_f32_16x16x32_bf16 v[36:39], v[140:143], v[164:167], v[36:39]\n"
    "v_mfma_f32_16x16x32_bf16 v[32:35], v[140:143], v[160:163], v[32:35]\n"
    "v_mfma_f32_16x16x32_bf16 v[28:31], v[140:143], v[156:159], v[28:31]\n"
    "v_mfma_f32_16x16x32_bf16 v[24:27], v[140:143], v[152:155], v[24:27]\n"
    "v_mfma_f32_16x16x32_bf16 v[48:51], v[144:147], v[152:155], v[48:51]\n"
    "v_mfma_f32_16x16x32_bf16 v[52:55], v[144:147], v[156:159], v[52:55]\n"
    "v_mfma_f32_16x16x32_bf16 v[56:59], v[144:147], v[160:163], v[56:59]\n"
    "v_mfma_f32_16x16x32_bf16 v[60:63], v[144:147], v[164:167], v[60:63]\n"
    "v_cvt_pk_bf16_f32 v0, v0, v1\n"
    "v_cvt_pk_bf16_f32 v1, v2, v3\n"
    "v_mfma_f32_16x16x32_bf16 v[64:67], v[144:147], v[168:171], v[64:67]\n"
    "s_nop 0\n"
    "v_cvt_pk_bf16_f32 v2, v24, v25\n"
    "v_cvt_pk_bf16_f32 v3, v26, v27\n"
    "v_mfma_f32_16x16x32_bf16 v[68:71], v[144:147], v[172:175], v[68:71]\n"
    "s_add_u32 s44, %[soo], 0\n"
    "buffer_store_dwordx4 v[0:3], %[vo], %[srdo], s44 offen sc1\n"
    "v_mfma_f32_16x16x32_bf16 v[92:95], v[148:151], v[172:175], v[92:95]\n"
    "v_cvt_pk_bf16_f32 v4, v4, v5\n"
    "v_cvt_pk_bf16_f32 v5, v6, v7\n"
    "v_mfma_f32_16x16x32_bf16 v[88:91], v[148:151], v[168:171], v[88:91]\n"
    "v_cvt_pk_bf16_f32 v6, v28, v29\n"
    "v_cvt_pk_bf16_f32 v7, v30, v31\n"
    "v_mfma_f32_16x16x32_bf16 v[84:87], v[148:151], v[164:167], v[84:87]\n"
    "s_add_u32 s44, %[soo], 1024\n"
    "buffer_store_dwordx4 v[4:7], %[vo], %[srdo], s44 offen sc1\n"
    "v_mfma_f32_16x16x32_bf16 v[80:83], v[148:151], v[160:163], v[80:83]\n"
    "v_cvt_pk_bf16_f32 v8, v8, v9\n"
    "v_cvt_pk_bf16_f32 v9, v10, v11\n"
    "v_mfma_f32_16x16x32_bf16 v[76:79], v[148:151], v[156:159], v[76:79]\n"
    "v_cvt_pk_bf16_f32 v10, v32, v33\n"
    "v_cvt_pk_bf16_f32 v11, v34, v35\n"
    "v_mfma_f32_16x16x32_bf16 v[72:75], v[148:151], v[152:155], v[72:75]\n"
    "s_add_u32 s44, %[soo], 2048\n"
    "buffer_store_dwordx4 v[8:11], %[vo], %[srdo], s44 offen sc1\n"
    "v_cvt_pk_bf16_f32 v12, v12, v13\n"
    "v_cvt_pk_bf16_f32 v13, v14, v15\n"
    "v_cvt_pk_bf16_f32 v14, v36, v37\n"
    "v_cvt_pk_bf16_f32 v15, v38, v39\n"
    "s_add_u32 s44, %[soo], 3072\n"
    "buffer_store_dwordx4 v[12:15], %[vo], %[srdo], s44 offen sc1\n"
    "v_cvt_pk_bf16_f32 v16, v16, v17\n"
    "v_cvt_pk_bf16_f32 v17, v18, v19\n"
    "v_cvt_pk_bf16_f32 v18, v40, v41\n"
    "v_cvt_pk_bf16_f32 v19, v42, v43\n"
    "s_add_u32 s44, %[soo], 4096\n"
    "buffer_store_dwordx4 v[16:19], %[vo], %[srdo], s44 offen sc1\n"
    "v_cvt_pk_bf16_f32 v20, v20, v21\n"
    "v_cvt_pk_bf16_f32 v21, v22, v23\n"
    "v_cvt_pk_bf16_f32 v22, v44, v45\n"
    "v_cvt_pk_bf16_f32 v23, v46, v47\n"
    "s_add_u32 s44, %[soo], 5120\n"
    "buffer_store_dwordx4 v[20:23], %[vo], %[srdo], s44 offen sc1\n"
    "v_cvt_pk_bf16_f32 v48, v48, v49\n"
    "v_cvt_pk_bf16_f32 v49, v50, v51\n"
    "v_cvt_pk_bf16_f32 v50, v72, v73\n"
    "v_cvt_pk_bf16_f32 v51, v74, v75\n"
    "s_add_u32 s44, %[soo], 36864\n"
    "buffer_store_dwordx4 v[48:51], %[vo], %[srdo], s44 offen sc1\n"
    "v_cvt_pk_bf16_f32 v52, v52, v53\n"
    "v_cvt_pk_bf16_f32 v53, v54, v55\n"
    "v_cvt_pk_bf16_f32 v54, v76, v77\n"
    "v_cvt_pk_bf16_f32 v55, v78, v79\n"
    "s_add_u32 s44, %[soo], 37888\n"
    "buffer_store_dwordx4 v[52:55], %[vo], %[srdo], s44 offen sc1\n"
    "v_cvt_pk_bf16_f32 v56, v56, v57\n"
    "v_cvt_pk_bf16_f32 v57, v58, v59\n"
    "v_cvt_pk_bf16_f32 v58, v80, v81\n"
    "v_cvt_pk_bf16_f32 v59, v82, v83\n"
    "s_add_u32 s44, %[soo], 38912\n"
    "buffer_store_dwordx4 v[56:59], %[vo], %[srdo], s44 offen sc1\n"
    "v_cvt_pk_bf16_f32 v60, v60, v61\n"
    "v_cvt_pk_bf16_f32 v61, v62, v63\n"
    "v_cvt_pk_bf16_f32 v62, v84, v85\n"
    "v_cvt_pk_bf16_f32 v63, v86, v87\n"
    "s_add_u32 s44, %[soo], 39936\n"
    "buffer_store_dwordx4 v[60:63], %[vo], %[srdo], s44 offen sc1\n"
    "v_cvt_pk_bf16_f32 v64, v64, v65\n"
    "v_cvt_pk_bf16_f32 v65, v66, v67\n"
    "v_cvt_pk_bf16_f32 v66, v88, v89\n"
    "v_cvt_pk_bf16_f32 v67, v90, v91\n"
    "s_add_u32 s44, %[soo], 40960\n"
    "buffer_store_dwordx4 v[64:67], %[vo], %[srdo], s44 offen sc1\n"
    "v_cvt_pk_bf16_f32 v68, v68, v69\n"
    "v_cvt_pk_bf16_f32 v69, v70, v71\n"
    "v_cvt_pk_bf16_f32 v70, v92, v93\n"
    "v_cvt_pk_bf16_f32 v71, v94, v95\n"
    "s_add_u32 s44, %[soo], 41984\n"
    "buffer_store_dwordx4 v[68:71], %[vo], %[srdo], s44 offen sc1\n"
    "GE_%=:\n"
    : [rdx]"+&v"(rdx), [rdw0]"+&v"(rdw[0]), [rdw1]"+&v"(rdw[1]), [rdw2]"+&v"(rdw[2]), [rdw3]"+&v"(rdw[3]), [rdw4]"+&v"(rdw[4]), [rdw5]"+&v"(rdw[5]), [sox]"+&s"(sox), [sow]"+&s"(sow), [nmid]"+&s"(nmid)
    : [vw0]"v"(vw[0]), [vw1]"v"(vw[1]), [vw2]"v"(vw[2]), [vw3]"v"(vw[3]), [vw4]"v"(vw[4]), [vw5]"v"(vw[5]), [srdw]"s"(srdw), [dw]"s"(dw), [m0w]"s"(m0w), [vx0]"v"(vx[0]), [vx1]"v"(vx[1]), [vx2]"v"(vx[2]), [vx3]"v"(vx[3]), [srdx]"s"(srdx), [srdo]"s"(srdo), [m0x]"s"(m0x), [vo]"v"(vo), [soo]"s"(soo)
    : "memory", "scc", "s40","s41","s42","s43","s44","s45","s46","s47","s48","s49","s50","s51","s52","s53", "v0","v1","v2","v3","v4","v5","v6","v7","v8","v9","v10","v11","v12","v13","v14","v15","v16","v17","v18","v19","v20","v21","v22","v23","v24","v25","v26","v27","v28","v29","v30","v31","v32","v33","v34","v35","v36","v37","v38","v39","v40","v41","v42","v43","v44","v45","v46","v47","v48","v49","v50","v51","v52","v53","v54","v55","v56","v57","v58","v59","v60","v61","v62","v63","v64","v65","v66","v67","v68","v69","v70","v71","v72","v73","v74","v75","v76","v77","v78","v79","v80","v81","v82","v83","v84","v85","v86","v87","v88","v89","v90","v91","v92","v93","v94","v95","v96","v97","v98","v99","v100","v101","v102","v103","v104","v105","v106","v107","v108","v109","v110","v111","v112","v113","v114","v115","v116","v117","v118","v119","v120","v121","v122","v123","v124","v125","v126","v127","v128","v129","v130","v131","v132","v133","v134","v135","v136","v137","v138","v139","v140","v141","v142","v143","v144","v145","v146","v147","v148","v149","v150","v151","v152","v153","v154","v155","v156","v157","v158","v159","v160","v161","v162","v163","v164","v165","v166","v167","v168","v169","v170","v171","v172","v173","v174","v175","v176","v177","v178","v179","v180","v181","v182","v183");
  }
  asm volatile("s_load_dword %0, %1, 0x0\n\ts_waitcnt lgkmcnt(0)\n\ts_add_u32 %0, %0, 1" : "=s"(tag) : "s"(fl) : "memory", "scc");
  const u32 q4 = (b & 7) * 27 + j, dt4 = q4 % 6, ft4 = q4 / 6;
  u32 sow4 = ft4 * 256;
  if (j < 27) {
    constexpr u32 LDW = F * 2; u32& sow = sow4;
    const __amdgpu_buffer_rsrc_t srdw = __builtin_amdgcn_make_buffer_rsrc((void*)(w1 - 4096), 0, S * F * 2 + 4096, 0x00020000);
    u32 vw[4];
#pragma unroll
    for (int i = 0; i < 4; i++) { const u32 jj = wid * 4 + i; vw[i] = 4096 - (i & 3) * 1024 + (8 * (jj & 7) + (lane >> 3)) * LDW + (jj >> 3) * 128 + ((lane & 7) ^ (((lane >> 4) & 3) << 1)) * 16; }
    const u32 m0w = lb + 12288 + wid * 4 * 1024, dw = 64 * LDW;
    asm volatile(
    "s_nop 4\n"
    "s_mov_b32 s41, %[m0w]\n"
    "s_add_u32 m0, s41, 0\n"
    "s_nop 0\n"
    "buffer_load_dwordx4 %[vw0], %[srdw], %[sow] offen offset:0 lds\n"
    "buffer_load_dwordx4 %[vw1], %[srdw], %[sow] offen offset:1024 lds\n"
    "buffer_load_dwordx4 %[vw2], %[srdw], %[sow] offen offset:2048 lds\n"
    "buffer_load_dwordx4 %[vw3], %[srdw], %[sow] offen offset:3072 lds\n"
    "s_add_u32 s41, s41, 28672\n"
    "s_add_u32 %[sow], %[sow], %[dw]\n"
    "s_add_u32 m0, s41, 0\n"
    "s_nop 0\n"
    "buffer_load_dwordx4 %[vw0], %[srdw], %[sow] offen offset:0 lds\n"
    "buffer_load_dwordx4 %[vw1], %[srdw], %[sow] offen offset:1024 lds\n"
    "buffer_load_dwordx4 %[vw2], %[srdw], %[sow] offen offset:2048 lds\n"
    "buffer_load_dwordx4 %[vw3], %[srdw], %[sow] offen offset:3072 lds\n"
    "s_add_u32 s41, s41, 28672\n"
    "s_add_u32 %[sow], %[sow], %[dw]\n"
    "s_add_u32 m0, s41, 0\n"
    "s_nop 0\n"
    "buffer_load_dwordx4 %[vw0], %[srdw], %[sow] offen offset:0 lds\n"
    "buffer_load_dwordx4 %[vw1], %[srdw], %[sow] offen offset:1024 lds\n"
    "buffer_load_dwordx4 %[vw2], %[srdw], %[sow] offen offset:2048 lds\n"
    "buffer_load_dwordx4 %[vw3], %[srdw], %[sow] offen offset:3072 lds\n"
    : [sow]"+&s"(sow)
    : [vw0]"v"(vw[0]), [vw1]"v"(vw[1]), [vw2]"v"(vw[2]), [vw3]"v"(vw[3]), [srdw]"s"(srdw), [dw]"s"(dw), [m0w]"s"(m0w)
    : "memory", "scc", "s41");
    publish<12>(fl + 5 * FARR + b * FSTR, tag, tid);
  } else publish<0>(fl + 5 * FARR + b * FSTR, tag, tid);
  DBGT(8);
  const u32x4 sel0 = one_hot((lane >> 4) == ((lane & 15) >> 2), lane & 3), sel1 = one_hot((lane >> 4) == ((lane & 15) >> 2), 4 + (lane & 3));
  {
    float (*red)[4][16] = (float (*)[4][16])(lds + 122880);
    const u32 tg = b >> 2, g = b & 3; float v[3][8], ga[3], sb[3], cb1[3]; u32 vp[3], vh[3];
    const bool own2 = lane < 16; const float w2f = own2 ? 1.f : 0.f;
#pragma unroll
    for (int r = 0; r < 3; r++) { const u32 t = r < 2 ? tid + 256 * r : 512 + wid * 16 + (lane & 15);
#pragma unroll
      for (int e = 0; e < 8; e++) v[r][e] = bf2f(x[(tg * 32 + pi_k(g, e)) * M + t]);
      ga[r] = modv(modp, 2 * M + t); sb[r] = modv(modp, 3 * M + t); cb1[r] = rbf(1.f + modv(modp, 4 * M + t));
      vp[r] = ((t >> 4) * 64 + (t & 15)) * 16;
      vh[r] = r < 2 || own2 ? ((2 * (t >> 5) + ((t >> 2) & 1)) * 64 * 64 + 4 * ((t >> 3) & 3) + (t & 3)) * 16 : 0x80000000u; }
    CODE_TOUCH();
    asm volatile("" : "+v"(v[0][0]), "+v"(v[0][1]), "+v"(v[0][2]), "+v"(v[0][3]), "+v"(v[0][4]), "+v"(v[0][5]), "+v"(v[0][6]), "+v"(v[0][7]), "+v"(v[1][0]), "+v"(v[1][1]), "+v"(v[1][2]), "+v"(v[1][3]),
                      "+v"(v[1][4]), "+v"(v[1][5]), "+v"(v[1][6]), "+v"(v[1][7]), "+v"(v[2][0]), "+v"(v[2][1]), "+v"(v[2][2]), "+v"(v[2][3]), "+v"(v[2][4]), "+v"(v[2][5]), "+v"(v[2][6]), "+v"(v[2][7]));
    asm volatile("" : "+v"(ga[0]), "+v"(ga[1]), "+v"(ga[2]), "+v"(sb[0]), "+v"(sb[1]), "+v"(sb[2]), "+v"(cb1[0]), "+v"(cb1[1]), "+v"(cb1[2]), "+v"(vp[0]), "+v"(vp[1]), "+v"(vp[2]), "+v"(vh[0]), "+v"(vh[1]), "+v"(vh[2]));
    {
      const u32 k = lane < 15 ? lane : 0, cmb = (k / 3) * 16 + (tg >> 2);
      await<1>(fl + 5 * FARR, (cmb / 10) + 8 * ((cmb % 10) * 3 + k % 3), tag, wid); }
    DBGT(9);
    u32x4 p[3][5];
#pragma unroll
    for (int r = 0; r < 3; r++)
#pragma unroll
      for (int s = 0; s < 5; s++) p[r][s] = ldb16(srdws, vp[r], (u32)WS_P3 + ((s * 64 + tg) * 36 * 64 + 16 * g) * 16);
    const u32 sh = (tg * 64 + 16 * g) * 16;
    DBGW(16);
#pragma unroll
    for (int r = 0; r < 3; r++) { f32x4 a0 = {}, a1 = {};
#pragma unroll
      for (int s = 0; s < 5; s++) { a0 = mfma16(sel0, p[r][s], a0); a1 = mfma16(sel1, p[r][s], a1); }
#pragma unroll
      for (int e = 0; e < 4; e++) { v[r][e] += ga[r] * a0[e]; v[r][4 + e] += ga[r] * a1[e]; }
      stb16(srdws, vh[r], (u32)WS_X1 + sh, u32x4{pk2(v[r][0], v[r][1]), pk2(v[r][2], v[r][3]), pk2(v[r][4], v[r][5]), pk2(v[r][6], v[r][7])}); }
    DBGT(17);
    const bool b0 = lane & 1, b1_ = lane & 2, b2_ = lane & 4, b3 = lane & 8;
#pragma unroll
    for (int pass = 0; pass < 2; pass++) {
      float r8[8];
#pragma unroll
      for (int e = 0; e < 8; e++) { const float u2 = v[2][e] * w2f; r8[e] = xchg_add<0xB1>(v[0][e] + v[1][e] + u2, v[0][e] * v[0][e] + v[1][e] * v[1][e] + u2 * u2, b0); }
#pragma unroll
      for (int e = 0; e < 4; e++) r8[e] = xchg_add<0x4E>(r8[e], r8[e + 4], b1_);
#pragma unroll
      for (int e = 0; e < 2; e++) r8[e] = xchg_add<0x124>(r8[e], r8[e + 2], b2_);
      const float q = swap32_add(swap16_add(xchg_add<0x128>(r8[0], r8[1], b3)));
      if (lane < 16) red[pass][wid][lane] = q;
      DBGT(18 + 2 * pass);
      wg_barrier();
      DBGT(19 + 2 * pass);
      const float tot = ((red[pass][0][lane & 15] + red[pass][1][lane & 15]) + (red[pass][2][lane & 15] + red[pass][3][lane & 15])) * (1.f / M);
      const float m = dpp_f<0xA0>(tot), rs = __builtin_amdgcn_rsqf(fmaxf(dpp_f<0xF5>(tot) - m * m, 0.f) + 1e-5f), mr = -m * rs;
#pragma unroll
      for (int e = 0; e < 8; e++) { const int l = 2 * (e >> 2) + 4 * ((e >> 1) & 1) + 8 * (e & 1); const float mm = rdlane(mr, l), rr = rdlane(rs, l);
#pragma unroll
        for (int r = 0; r < 3; r++) { v[r][e] = v[r][e] * rr + mm; if (pass == 0) v[r][e] = v[r][e] * cb1[r] + sb[r]; } }
    }
#pragma unroll
    for (int r = 0; r < 3; r++) stb16(srdws, vh[r], (u32)WS_HB + sh, u32x4{pk2(v[r][0], v[r][1]), pk2(v[r][2], v[r][3]), pk2(v[r][4], v[r][5]), pk2(v[r][6], v[r][7])});
    DBGT(22);
    publish<0>(fl + 6 * FARR + b * FSTR, tag, tid);
  }
  DBGT(10);
  await<4>(fl + 6 * FARR, lane, tag, wid);
  if (b == 0 && tid == 0) st_flag(fl, tag);
  DBGT(11);
  if (j < 27) {
    constexpr u32 XSTRIDE = 64 * 1024, LDW = F * 2, nst = 32; u32& sow = sow4;
    const __amdgpu_buffer_rsrc_t srdx = __builtin_amdgcn_make_buffer_rsrc((void*)(ws + WS_HB - 4096), 0, S * M * 2 + 4096, 0x00020000), srdw = __builtin_amdgcn_make_buffer_rsrc((void*)(w1 - 4096), 0, S * F * 2 + 4096, 0x00020000),
                                  srdo = __builtin_amdgcn_make_buffer_rsrc((void*)(ws + WS_UB), 0, M * F * 2, 0x00020000), srdb = __builtin_amdgcn_make_buffer_rsrc((void*)b1, 0, F * 2, 0x00020000);
    u32 sox = dt4 * 6 * XSTRIDE;
    u32 vw[4];
#pragma unroll
    for (int i = 0; i < 4; i++) { const u32 jj = wid * 4 + i; vw[i] = 4096 - (i & 3) * 1024 + (8 * (jj & 7) + (lane >> 3)) * LDW + (jj >> 3) * 128 + ((lane & 7) ^ (((lane >> 4) & 3) << 1)) * 16; }
    const u32 m0w = lb + 12288 + wid * 4 * 1024, dw = 64 * LDW;
    u32 vx[3], rdw[4];
#pragma unroll
    for (int i = 0; i < 3; i++) { const u32 jj = wid * 3 + i; vx[i] = 4096 - (i & 3) * 1024 + (jj >> 1) * XSTRIDE + (jj & 1) * 1024 + lane * 16; }
    u32 rdx = lb + wx * 3 * 2048 + lane * 16;
    { const u32 r16 = lane >> 2;
#pragma unroll
      for (int w = 0; w < 4; w++) { const u32 bb = ww * 4 + w; rdw[w] = lb + 12288 + (bb >> 2) * 8192 + (r16 >> 3) * 1024 + (r16 & 7) * 128 + (((bb & 3) ^ ((r16 >> 1) & 3)) * 32) + (lane & 3) * 8; } }
    const u32 m0x = lb + wid * 3 * 1024; u32 nmid = nst - 4;
    const u32 soo = ((dt4 * 6 + wx * 3) * 144 + ft4 * 4 + ww * 2) * 1024, vb = (ft4 * 128 + ww * 64 + (lane >> 4) * 4) * 2;
    asm volatile(
    "s_nop 4\n"
    "s_mov_b32 s40, %[m0x]\n"
    "s_mov_b32 s49, 2048\n"
    "s_add_u32 m0, s40, 0\n"
    "s_nop 0\n"
    "buffer_load_dwordx4 %[vx0], %[srdx], %[sox] offen offset:0 sc1 lds\n"
    "buffer_load_dwordx4 %[vx1], %[srdx], %[sox] offen offset:1024 sc1 lds\n"
    "buffer_load_dwordx4 %[vx2], %[srdx], %[sox] offen offset:2048 sc1 lds\n"
    "s_add_u32 s40, s40, 28672\n"
    "s_add_u32 %[sox], %[sox], s49\n"
    "s_add_u32 m0, s40, 0\n"
    "s_nop 0\n"
    "buffer_load_dwordx4 %[vx0], %[srdx], %[sox] offen offset:0 sc1 lds\n"
    "buffer_load_dwordx4 %[vx1], %[srdx], %[sox] offen offset:1024 sc1 lds\n"
    "buffer_load_dwordx4 %[vx2], %[srdx], %[sox] offen offset:2048 sc1 lds\n"
    "s_add_u32 s40, s40, 28672\n"
    "s_add_u32 %[sox], %[sox], s49\n"
    "s_add_u32 m0, s40, 0\n"
    "s_nop 0\n"
    "buffer_load_dwordx4 %[vx0], %[srdx], %[sox] offen offset:0 sc1 lds\n"
    "buffer_load_dwordx4 %[vx1], %[srdx], %[sox] offen offset:1024 sc1 lds\n"
    "buffer_load_dwordx4 %[vx2], %[srdx], %[sox] offen offset:2048 sc1 lds\n"
    "buffer_load_dwordx2 v[104:105], %[vb], %[srdb], 0 offen offset:0\n"
    "buffer_load_dwordx2 v[108:109], %[vb], %[srdb], 0 offen offset:32\n"
    "buffer_load_dwordx2 v[112:113], %[vb], %[srdb], 0 offen offset:64\n"
    "buffer_load_dwordx2 v[116:117], %[vb], %[srdb], 0 offen offset:96\n"
    "s_getpc_b64 s[52:53]\n"
    "CT_%=:\n"
    "s_add_u32 s52, s52, LM_%=-CT_%=\n"
    "s_addc_u32 s53, s53, 0\n"
    "s_mul_i32 s44, 0, 8192\n"
    "v_lshl_add_u32 v130, %[vo], 3, s44\n"
    "v_min_u32 v131, GE_%=-LM_%=-4, v130\n"
    "global_load_dword v132, v131, s[52:53] ; dummy\n"
    "s_mov_b32 s42, 57344\n"
    "s_mov_b32 s43, 0\n"
    "s_mov_b32 s47, 4294881280\n"
    "s_mov_b32 s48, 28672\n"
    "s_mov_b32 s50, 114688\n"
    "s_waitcnt vmcnt(11)\n"
    "s_barrier\n"
    "ds_read_b128 v[48:51], %[rdx] offset:0\n"
    "ds_read_b64_tr_b16 v[60:61], %[rdw0] offset:0\n"
    "ds_read_b64_tr_b16 v[62:63], %[rdw0] offset:2048\n"
    "ds_read_b64_tr_b16 v[64:65], %[rdw1] offset:0\n"
    "ds_read_b64_tr_b16 v[66:67], %[rdw1] offset:2048\n"
    "ds_read_b64_tr_b16 v[68:69], %[rdw2] offset:0\n"
    "ds_read_b64_tr_b16 v[70:71], %[rdw2] offset:2048\n"
    "ds_read_b64_tr_b16 v[72:73], %[rdw3] offset:0\n"
    "ds_read_b64_tr_b16 v[74:75], %[rdw3] offset:2048\n"
    "ds_read_b128 v[52:55], %[rdx] offset:2048\n"
    "ds_read_b128 v[56:59], %[rdx] offset:4096\n"
    "s_waitcnt lgkmcnt(0)\n"
    "v_mfma_f32_16x16x32_bf16 v[0:3], v[60:63], v[48:51], 0\n"
    "s_add_u32 %[sox], %[sox], s49\n"
    "ds_read_b128 v[76:79], %[rdx] offset:1024\n"
    "ds_read_b64_tr_b16 v[88:89], %[rdw0] offset:4096\n"
    "s_add_u32 %[sow], %[sow], %[dw]\n"
    "v_mfma_f32_16x16x32_bf16 v[4:7], v[64:67], v[48:51], 0\n"
    "s_add_u32 s42, s42, s48\n"
    "ds_read_b64_tr_b16 v[90:91], %[rdw0] offset:6144\n"
    "ds_read_b64_tr_b16 v[92:93], %[rdw1] offset:4096\n"
    "s_cmp_eq_u32 s42, s50\n"
    "v_mfma_f32_16x16x32_bf16 v[8:11], v[68:71], v[48:51], 0\n"
    "s_cselect_b32 s42, 0, s42\n"
    "ds_read_b64_tr_b16 v[94:95], %[rdw1] offset:6144\n"
    "ds_read_b64_tr_b16 v[96:97], %[rdw2] offset:4096\n"
    "s_add_u32 s40, s42, %[m0x]\n"
    "v_mfma_f32_16x16x32_bf16 v[12:15], v[72:75], v[48:51], 0\n"
    "s_add_u32 s41, s42, %[m0w]\n"
    "ds_read_b64_tr_b16 v[98:99], %[rdw2] offset:6144\n"
    "ds_read_b64_tr_b16 v[100:101], %[rdw3] offset:4096\n"
    "v_mfma_f32_16x16x32_bf16 v[28:31], v[72:75], v[52:55], 0\n"
    "s_add_u32 m0, s40, 0\n"
    "ds_read_b64_tr_b16 v[102:103], %[rdw3] offset:6144\n"
    "ds_read_b128 v[80:83], %[rdx] offset:3072\n"
    "v_mfma_f32_16x16x32_bf16 v[24:27], v[68:71], v[52:55], 0\n"
    "ds_read_b128 v[84:87], %[rdx] offset:5120\n"
    "buffer_load_dwordx4 %[vx0], %[srdx], %[sox] offen offset:0 sc1 lds\n"
    "buffer_load_dwordx4 %[vx1], %[srdx], %[sox] offen offset:1024 sc1 lds\n"
    "v_mfma_f32_16x16x32_bf16 v[20:23], v[64:67], v[52:55], 0\n"
    "buffer_load_dwordx4 %[vx2], %[srdx], %[sox] offen offset:2048 sc1 lds\n"
    "s_add_u32 m0, s41, 0\n"
    "v_mfma_f32_16x16x32_bf16 v[16:19], v[60:63], v[52:55], 0\n"
    "s_add_u32 s43, s43, 1\n"
    "buffer_load_dwordx4 %[vw0], %[srdw], %[sow] offen offset:0 lds\n"
    "s_cmp_eq_u32 s43, 4\n"
    "s_cselect_b32 s46, s47, s48\n"
    "v_mfma_f32_16x16x32_bf16 v[32:35], v[60:63], v[56:59], 0\n"
    "s_cselect_b32 s43, 0, s43\n"
    "buffer_load_dwordx4 %[vw1], %[srdw], %[sow] offen offset:1024 lds\n"
    "v_add_u32 %[rdx], s46, %[rdx]\n"
    "v_add_u32 %[rdw0], s46, %[rdw0]\n"
    "v_mfma_f32_16x16x32_bf16 v[36:39], v[64:67], v[56:59], 0\n"
    "buffer_load_dwordx4 %[vw2], %[srdw], %[sow] offen offset:2048 lds\n"
    "v_add_u32 %[rdw1], s46, %[rdw1]\n"
    "v_mfma_f32_16x16x32_bf16 v[40:43], v[68:71], v[56:59], 0\n"
    "buffer_load_dwordx4 %[vw3], %[srdw], %[sow] offen offset:3072 lds\n"
    "v_add_u32 %[rdw2], s46, %[rdw2]\n"
    "v_mfma_f32_16x16x32_bf16 v[44:47], v[72:75], v[56:59], 0\n"
    "v_add_u32 %[rdw3], s46, %[rdw3]\n"
    "s_waitcnt lgkmcnt(0) vmcnt(15)\n"
    "s_barrier\n"
    "LM_%=:\n"
    "v_mfma_f32_16x16x32_bf16 v[0:3], v[88:91], v[76:79], v[0:3]\n"
    "s_add_u32 %[sox], %[sox], s49\n"
    "ds_read_b128 v[48:51], %[rdx] offset:0\n"
    "ds_read_b64_tr_b16 v[60:61], %[rdw0] offset:0\n"
    "s_add_u32 %[sow], %[sow], %[dw]\n"
    "v_mfma_f32_16x16x32_bf16 v[4:7], v[92:95], v[76:79], v[4:7]\n"
    "s_add_u32 s42, s42, s48\n"
    "ds_read_b64_tr_b16 v[62:63], %[rdw0] offset:2048\n"
    "ds_read_b64_tr_b16 v[64:65], %[rdw1] offset:0\n"
    "s_cmp_eq_u32 s42, s50\n"
    "v_mfma_f32_16x16x32_bf16 v[8:11], v[96:99], v[76:79], v[8:11]\n"
    "s_cselect_b32 s42, 0, s42\n"
    "ds_read_b64_tr_b16 v[66:67], %[rdw1] offset:2048\n"
    "ds_read_b64_tr_b16 v[68:69], %[rdw2] offset:0\n"
    "s_add_u32 s40, s42, %[m0x]\n"
    "v_mfma_f32_16x16x32_bf16 v[12:15], v[100:103], v[76:79], v[12:15]\n"
    "s_add_u32 s41, s42, %[m0w]\n"
    "ds_read_b64_tr_b16 v[70:71], %[rdw2] offset:2048\n"
    "ds_read_b64_tr_b16 v[72:73], %[rdw3] offset:0\n"
    "v_mfma_f32_16x16x32_bf16 v[28:31], v[100:103], v[80:83], v[28:31]\n"
    "s_add_u32 m0, s40, 0\n"
    "ds_read_b64_tr_b16 v[74:75], %[rdw3] offset:2048\n"
    "ds_read_b128 v[52:55], %[rdx] offset:2048\n"
    "v_mfma_f32_16x16x32_bf16 v[24:27], v[96:99], v[80:83], v[24:27]\n"
    "ds_read_b128 v[56:59], %[rdx] offset:4096\n"
    "buffer_load_dwordx4 %[vx0], %[srdx], %[sox] offen offset:0 sc1 lds\n"
    "v_mfma_f32_16x16x32_bf16 v[20:23], v[92:95], v[80:83], v[20:23]\n"
    "buffer_load_dwordx4 %[vx1], %[srdx], %[sox] offen offset:1024 sc1 lds\n"
    "v_mfma_f32_16x16x32_bf16 v[16:19], v[88:91], v[80:83], v[16:19]\n"
    "v_mfma_f32_16x16x32_bf16 v[32:35], v[88:91], v[84:87], v[32:35]\n"
    "buffer_load_dwordx4 %[vx2], %[srdx], %[sox] offen offset:2048 sc1 lds\n"
    "v_mfma_f32_16x16x32_bf16 v[36:39], v[92:95], v[84:87], v[36:39]\n"
    "s_add_u32 m0, s41, 0\n"
    "v_mfma_f32_16x16x32_bf16 v[40:43], v[96:99], v[84:87], v[40:43]\n"
    "buffer_load_dwordx4 %[vw0], %[srdw], %[sow] offen offset:0 lds\n"
    "v_mfma_f32_16x16x32_bf16 v[44:47], v[100:103], v[84:87], v[44:47]\n"
    "s_waitcnt lgkmcnt(0)\n"
    "v_mfma_f32_16x16x32_bf16 v[0:3], v[60:63], v[48:51], v[0:3]\n"
    "ds_read_b128 v[76:79], %[rdx] offset:1024\n"
    "ds_read_b64_tr_b16 v[88:89], %[rdw0] offset:4096\n"
    "v_mfma_f32_16x16x32_bf16 v[4:7], v[64:67], v[48:51], v[4:7]\n"
    "ds_read_b64_tr_b16 v[90:91], %[rdw0] offset:6144\n"
    "ds_read_b64_tr_b16 v[92:93], %[rdw1] offset:4096\n"
    "v_mfma_f32_16x16x32_bf16 v[8:11], v[68:71], v[48:51], v[8:11]\n"
    "ds_read_b64_tr_b16 v[94:95], %[rdw1] offset:6144\n"
    "ds_read_b64_tr_b16 v[96:97], %[rdw2] offset:4096\n"
    "v_mfma_f32_16x16x32_bf16 v[12:15], v[72:75], v[48:51], v[12:15]\n"
    "ds_read_b64_tr_b16 v[98:99], %[rdw2] offset:6144\n"
    "ds_read_b64_tr_b16 v[100:101], %[rdw3] offset:4096\n"
    "v_mfma_f32_16x16x32_bf16 v[28:31], v[72:75], v[52:55], v[28:31]\n"
    "ds_read_b64_tr_b16 v[102:103], %[rdw3] offset:6144\n"
    "ds_read_b128 v[80:83], %[rdx] offset:3072\n"
    "v_mfma_f32_16x16x32_bf16 v[24:27], v[68:71], v[52:55], v[24:27]\n"
    "ds_read_b128 v[84:87], %[rdx] offset:5120\n"
    "buffer_load_dwordx4 %[vw1], %[srdw], %[sow] offen offset:1024 lds\n"
    "v_mfma_f32_16x16x32_bf16 v[20:23], v[64:67], v[52:55], v[20:23]\n"
    "s_add_u32 s43, s43, 1\n"
    "s_cmp_eq_u32 s43, 4\n"
    "s_cselect_b32 s46, s47, s48\n"
    "v_mfma_f32_16x16x32_bf16 v[16:19], v[60:63], v[52:55], v[16:19]\n"
    "s_cselect_b32 s43, 0, s43\n"
    "buffer_load_dwordx4 %[vw2], %[srdw], %[sow] offen offset:2048 lds\n"
    "v_add_u32 %[rdx], s46, %[rdx]\n"
    "v_mfma_f32_16x16x32_bf16 v[32:35], v[60:63], v[56:59], v[32:35]\n"
    "v_add_u32 %[rdw0], s46, %[rdw0]\n"
    "v_add_u32 %[rdw1], s46, %[rdw1]\n"
    "v_mfma_f32_16x16x32_bf16 v[36:39], v[64:67], v[56:59], v[36:39]\n"
    "buffer_load_dwordx4 %[vw3], %[srdw], %[sow] offen offset:3072 lds\n"
    "v_add_u32 %[rdw2], s46, %[rdw2]\n"
    "v_mfma_f32_16x16x32_bf16 v[40:43], v[68:71], v[56:59], v[40:43]\n"
    "v_add_u32 %[rdw3], s46, %[rdw3]\n"
    "s_sub_u32 %[nmid], %[nmid], 1\n"
    "v_mfma_f32_16x16x32_bf16 v[44:47], v[72:75], v[56:59], v[44:47]\n"
    "s_cmp_lg_u32 %[nmid], 0\n"
    "s_waitcnt lgkmcnt(0) vmcnt(14)\n"
    "s_barrier\n"
    "s_cbranch_scc1 LM_%=\n"
    "v_mfma_f32_16x16x32_bf16 v[0:3], v[88:91], v[76:79], v[0:3]\n"
    "ds_read_b128 v[48:51], %[rdx] offset:0\n"
    "ds_read_b64_tr_b16 v[60:61], %[rdw0] offset:0\n"
    "v_mfma_f32_16x16x32_bf16 v[4:7], v[92:95], v[76:79], v[4:7]\n"
    "ds_read_b64_tr_b16 v[62:63], %[rdw0] offset:2048\n"
    "ds_read_b64_tr_b16 v[64:65], %[rdw1] offset:0\n"
    "v_mfma_f32_16x16x32_bf16 v[8:11], v[96:99], v[76:79], v[8:11]\n"
    "ds_read_b64_tr_b16 v[66:67], %[rdw1] offset:2048\n"
    "ds_read_b64_tr_b16 v[68:69], %[rdw2] offset:0\n"
    "v_mfma_f32_16x16x32_bf16 v[12:15], v[100:103], v[76:79], v[12:15]\n"
    "ds_read_b64_tr_b16 v[70:71], %[rdw2] offset:2048\n"
    "ds_read_b64_tr_b16 v[72:73], %[rdw3] offset:0\n"
    "v_mfma_f32_16x16x32_bf16 v[28:31], v[100:103], v[80:83], v[28:31]\n"
    "ds_read_b64_tr_b16 v[74:75], %[rdw3] offset:2048\n"
    "ds_read_b128 v[52:55], %[rdx] offset:2048\n"
    "v_mfma_f32_16x16x32_bf16 v[24:27], v[96:99], v[80:83], v[24:27]\n"
    "ds_read_b128 v[56:59], %[rdx] offset:4096\n"
    "v_mfma_f32_16x16x32_bf16 v[20:23], v[92:95], v[80:83], v[20:23]\n"
    "v_mfma_f32_16x16x32_bf16 v[16:19], v[88:91], v[80:83], v[16:19]\n"
    "v_mfma_f32_16x16x32_bf16 v[32:35], v[88:91], v[84:87], v[32:35]\n"
    "v_mfma_f32_16x16x32_bf16 v[36:39], v[92:95], v[84:87], v[36:39]\n"
    "v_mfma_f32_16x16x32_bf16 v[40:43], v[96:99], v[84:87], v[40:43]\n"
    "v_mfma_f32_16x16x32_bf16 v[44:47], v[100:103], v[84:87], v[44:47]\n"
    "s_waitcnt lgkmcnt(0)\n"
    "v_mfma_f32_16x16x32_bf16 v[0:3], v[60:63], v[48:51], v[0:3]\n"
    "ds_read_b128 v[76:79], %[rdx] offset:1024\n"
    "ds_read_b64_tr_b16 v[88:89], %[rdw0] offset:4096\n"
    "v_mfma_f32_16x16x32_bf16 v[4:7], v[64:67], v[48:51], v[4:7]\n"
    "ds_read_b64_tr_b16 v[90:91], %[rdw0] offset:6144\n"
    "ds_read_b64_tr_b16 v[92:93], %[rdw1] offset:4096\n"
    "v_mfma_f32_16x16x32_bf16 v[8:11], v[68:71], v[48:51], v[8:11]\n"
    "ds_read_b64_tr_b16 v[94:95], %[rdw1] offset:6144\n"
    "ds_read_b64_tr_b16 v[96:97], %[rdw2] offset:4096\n"
    "v_mfma_f32_16x16x32_bf16 v[12:15], v[72:75], v[48:51], v[12:15]\n"
    "ds_read_b64_tr_b16 v[98:99], %[rdw2] offset:6144\n"
    "ds_read_b64_tr_b16 v[100:101], %[rdw3] offset:4096\n"
    "v_mfma_f32_16x16x32_bf16 v[28:31], v[72:75], v[52:55], v[28:31]\n"
    "ds_read_b64_tr_b16 v[102:103], %[rdw3] offset:6144\n"
    "ds_read_b128 v[80:83], %[rdx] offset:3072\n"
    "v_mfma_f32_16x16x32_bf16 v[24:27], v[68:71], v[52:55], v[24:27]\n"
    "ds_read_b128 v[84:87], %[rdx] offset:5120\n"
    "v_mfma_f32_16x16x32_bf16 v[20:23], v[64:67], v[52:55], v[20:23]\n"
    "s_add_u32 s43, s43, 1\n"
    "s_cmp_eq_u32 s43, 4\n"
    "s_cselect_b32 s46, s47, s48\n"
    "v_mfma_f32_16x16x32_bf16 v[16:19], v[60:63], v[52:55], v[16:19]\n"
    "s_cselect_b32 s43, 0, s43\n"
    "v_add_u32 %[rdx], s46, %[rdx]\n"
    "v_add_u32 %[rdw0], s46, %[rdw0]\n"
    "v_mfma_f32_16x16x32_bf16 v[32:35], v[60:63], v[56:59], v[32:35]\n"
    "v_add_u32 %[rdw1], s46, %[rdw1]\n"
    "v_mfma_f32_16x16x32_bf16 v[36:39], v[64:67], v[56:59], v[36:39]\n"
    "v_add_u32 %[rdw2], s46, %[rdw2]\n"
    "v_mfma_f32_16x16x32_bf16 v[40:43], v[68:71], v[56:59], v[40:43]\n"
    "v_add_u32 %[rdw3], s46, %[rdw3]\n"
    "v_mfma_f32_16x16x32_bf16 v[44:47], v[72:75], v[56:59], v[44:47]\n"
    "s_waitcnt lgkmcnt(0) vmcnt(7)\n"
    "s_barrier\n"
    "v_mfma_f32_16x16x32_bf16 v[0:3], v[88:91], v[76:79], v[0:3]\n"
    "ds_read_b128 v[48:51], %[rdx] offset:0\n"
    "ds_read_b64_tr_b16 v[60:61], %[rdw0] offset:0\n"
    "v_mfma_f32_16x16x32_bf16 v[4:7], v[92:95], v[76:79], v[4:7]\n"
    "ds_read_b64_tr_b16 v[62:63], %[rdw0] offset:2048\n"
    "ds_read_b64_tr_b16 v[64:65], %[rdw1] offset:0\n"
    "v_mfma_f32_16x16x32_bf16 v[8:11], v[96:99], v[76:79], v[8:11]\n"
    "ds_read_b64_tr_b16 v[66:67], %[rdw1] offset:2048\n"
    "ds_read_b64_tr_b16 v[68:69], %[rdw2] offset:0\n"
    "v_mfma_f32_16x16x32_bf16 v[12:15], v[100:103], v[76:79], v[12:15]\n"
    "ds_read_b64_tr_b16 v[70:71], %[rdw2] offset:2048\n"
    "ds_read_b64_tr_b16 v[72:73], %[rdw3] offset:0\n"
    "v_mfma_f32_16x16x32_bf16 v[28:31], v[100:103], v[80:83], v[28:31]\n"
    "ds_read_b64_tr_b16 v[74:75], %[rdw3] offset:2048\n"
    "ds_read_b128 v[52:55], %[rdx] offset:2048\n"
    "v_mfma_f32_16x16x32_bf16 v[24:27], v[96:99], v[80:83], v[24:27]\n"
    "ds_read_b128 v[56:59], %[rdx] offset:4096\n"
    "v_mfma_f32_16x16x32_bf16 v[20:23], v[92:95], v[80:83], v[20:23]\n"
    "v_mfma_f32_16x16x32_bf16 v[16:19], v[88:91], v[80:83], v[16:19]\n"
    "v_mfma_f32_16x16x32_bf16 v[32:35], v[88:91], v[84:87], v[32:35]\n"
    "v_mfma_f32_16x16x32_bf16 v[36:39], v[92:95], v[84:87], v[36:39]\n"
    "v_mfma_f32_16x16x32_bf16 v[40:43], v[96:99], v[84:87], v[40:43]\n"
    "v_mfma_f32_16x16x32_bf16 v[44:47], v[100:103], v[84:87], v[44:47]\n"
    "s_waitcnt lgkmcnt(0)\n"
    "v_mfma_f32_16x16x32_bf16 v[0:3], v[60:63], v[48:51], v[0:3]\n"
    "ds_read_b128 v[76:79], %[rdx] offset:1024\n"
    "ds_read_b64_tr_b16 v[88:89], %[rdw0] offset:4096\n"
    "v_mfma_f32_16x16x32_bf16 v[4:7], v[64:67], v[48:51], v[4:7]\n"
    "ds_read_b64_tr_b16 v[90:91], %[rdw0] offset:6144\n"
    "ds_read_b64_tr_b16 v[92:93], %[rdw1] offset:4096\n"
    "v_mfma_f32_16x16x32_bf16 v[8:11], v[68:71], v[48:51], v[8:11]\n"
    "ds_read_b64_tr_b16 v[94:95], %[rdw1] offset:6144\n"
    "ds_read_b64_tr_b16 v[96:97], %[rdw2] offset:4096\n"
    "v_mfma_f32_16x16x32_bf16 v[12:15], v[72:75], v[48:51], v[12:15]\n"
    "ds_read_b64_tr_b16 v[98:99], %[rdw2] offset:6144\n"
    "ds_read_b64_tr_b16 v[100:101], %[rdw3] offset:4096\n"
    "v_mfma_f32_16x16x32_bf16 v[28:31], v[72:75], v[52:55], v[28:31]\n"
    "ds_read_b64_tr_b16 v[102:103], %[rdw3] offset:6144\n"
    "ds_read_b128 v[80:83], %[rdx] offset:3072\n"
    "v_mfma_f32_16x16x32_bf16 v[24:27], v[68:71], v[52:55], v[24:27]\n"
    "ds_read_b128 v[84:87], %[rdx] offset:5120\n"
    "v_mfma_f32_16x16x32_bf16 v[20:23], v[64:67], v[52:55], v[20:23]\n"
    "s_add_u32 s43, s43, 1\n"
    "s_cmp_eq_u32 s43, 4\n"
    "s_cselect_b32 s46, s47, s48\n"
    "v_mfma_f32_16x16x32_bf16 v[16:19], v[60:63], v[52:55], v[16:19]\n"
    "s_cselect_b32 s43, 0, s43\n"
    "v_add_u32 %[rdx], s46, %[rdx]\n"
    "v_add_u32 %[rdw0], s46, %[rdw0]\n"
    "v_mfma_f32_16x16x32_bf16 v[32:35], v[60:63], v[56:59], v[32:35]\n"
    "v_add_u32 %[rdw1], s46, %[rdw1]\n"
    "v_mfma_f32_16x16x32_bf16 v[36:39], v[64:67], v[56:59], v[36:39]\n"
    "v_add_u32 %[rdw2], s46, %[rdw2]\n"
    "v_mfma_f32_16x16x32_bf16 v[40:43], v[68:71], v[56:59], v[40:43]\n"
    "v_add_u32 %[rdw3], s46, %[rdw3]\n"
    "v_mfma_f32_16x16x32_bf16 v[44:47], v[72:75], v[56:59], v[44:47]\n"
    "s_waitcnt lgkmcnt(0) vmcnt(0)\n"
    "s_barrier\n"
    "v_mfma_f32_16x16x32_bf16 v[0:3], v[88:91], v[76:79], v[0:3]\n"
    "ds_read_b128 v[48:51], %[rdx] offset:0\n"
    "ds_read_b64_tr_b16 v[60:61], %[rdw0] offset:0\n"
    "v_mfma_f32_16x16x32_bf16 v[4:7], v[92:95], v[76:79], v[4:7]\n"
    "ds_read_b64_tr_b16 v[62:63], %[rdw0] offset:2048\n"
    "ds_read_b64_tr_b16 v[64:65], %[rdw1] offset:0\n"
    "v_mfma_f32_16x16x32_bf16 v[8:11], v[96:99], v[76:79], v[8:11]\n"
    "ds_read_b64_tr_b16 v[66:67], %[rdw1] offset:2048\n"
    "ds_read_b64_tr_b16 v[68:69], %[rdw2] offset:0\n"
    "v_mfma_f32_16x16x32_bf16 v[12:15], v[100:103], v[76:79], v[12:15]\n"
    "ds_read_b64_tr_b16 v[70:71], %[rdw2] offset:2048\n"
    "ds_read_b64_tr_b16 v[72:73], %[rdw3] offset:0\n"
    "v_mfma_f32_16x16x32_bf16 v[28:31], v[100:103], v[80:83], v[28:31]\n"
    "ds_read_b64_tr_b16 v[74:75], %[rdw3] offset:2048\n"
    "ds_read_b128 v[52:55], %[rdx] offset:2048\n"
    "v_mfma_f32_16x16x32_bf16 v[24:27], v[96:99], v[80:83], v[24:27]\n"
    "ds_read_b128 v[56:59], %[rdx] offset:4096\n"
    "v_mfma_f32_16x16x32_bf16 v[20:23], v[92:95], v[80:83], v[20:23]\n"
    "v_mfma_f32_16x16x32_bf16 v[16:19], v[88:91], v[80:83], v[16:19]\n"
    "v_mfma_f32_16x16x32_bf16 v[32:35], v[88:91], v[84:87], v[32:35]\n"
    "v_mfma_f32_16x16x32_bf16 v[36:39], v[92:95], v[84:87], v[36:39]\n"
    "v_mfma_f32_16x16x32_bf16 v[40:43], v[96:99], v[84:87], v[40:43]\n"
    "v_mfma_f32_16x16x32_bf16 v[44:47], v[100:103], v[84:87], v[44:47]\n"
    "s_waitcnt lgkmcnt(0)\n"
    "v_mfma_f32_16x16x32_bf16 v[0:3], v[60:63], v[48:51], v[0:3]\n"
    "ds_read_b128 v[76:79], %[rdx] offset:1024\n"
    "ds_read_b64_tr_b16 v[88:89], %[rdw0] offset:4096\n"
    "v_mfma_f32_16x16x32_bf16 v[4:7], v[64:67], v[48:51], v[4:7]\n"
    "ds_read_b64_tr_b16 v[90:91], %[rdw0] offset:6144\n"
    "ds_read_b64_tr_b16 v[92:93], %[rdw1] offset:4096\n"
    "v_mfma_f32_16x16x32_bf16 v[8:11], v[68:71], v[48:51], v[8:11]\n"
    "ds_read_b64_tr_b16 v[94:95], %[rdw1] offset:6144\n"
    "ds_read_b64_tr_b16 v[96:97], %[rdw2] offset:4096\n"
    "v_mfma_f32_16x16x32_bf16 v[12:15], v[72:75], v[48:51], v[12:15]\n"
    "ds_read_b64_tr_b16 v[98:99], %[rdw2] offset:6144\n"
    "ds_read_b64_tr_b16 v[100:101], %[rdw3] offset:4096\n"
    "v_mfma_f32_16x16x32_bf16 v[28:31], v[72:75], v[52:55], v[28:31]\n"
    "ds_read_b64_tr_b16 v[102:103], %[rdw3] offset:6144\n"
    "ds_read_b128 v[80:83], %[rdx] offset:3072\n"
    "v_mfma_f32_16x16x32_bf16 v[24:27], v[68:71], v[52:55], v[24:27]\n"
    "ds_read_b128 v[84:87], %[rdx] offset:5120\n"
    "v_mfma_f32_16x16x32_bf16 v[20:23], v[64:67], v[52:55], v[20:23]\n"
    "s_add_u32 s43, s43, 1\n"
    "s_cmp_eq_u32 s43, 4\n"
    "s_cselect_b32 s46, s47, s48\n"
    "v_mfma_f32_16x16x32_bf16 v[16:19], v[60:63], v[52:55], v[16:19]\n"
    "s_cselect_b32 s43, 0, s43\n"
    "v_add_u32 %[rdx], s46, %[rdx]\n"
    "v_add_u32 %[rdw0], s46, %[rdw0]\n"
    "v_mfma_f32_16x16x32_bf16 v[32:35], v[60:63], v[56:59], v[32:35]\n"
    "v_add_u32 %[rdw1], s46, %[rdw1]\n"
    "v_mfma_f32_16x16x32_bf16 v[36:39], v[64:67], v[56:59], v[36:39]\n"
    "v_add_u32 %[rdw2], s46, %[rdw2]\n"
    "v_mfma_f32_16x16x32_bf16 v[40:43], v[68:71], v[56:59], v[40:43]\n"
    "v_add_u32 %[rdw3], s46, %[rdw3]\n"
    "v_mfma_f32_16x16x32_bf16 v[44:47], v[72:75], v[56:59], v[44:47]\n"
    "s_waitcnt lgkmcnt(0)\n"
    "s_barrier\n"
    "v_mfma_f32_16x16x32_bf16 v[0:3], v[88:91], v[76:79], v[0:3]\n"
    "s_mov_b32 s45, 0xc0135761\n"
    "s_mov_b32 s46, 0xbdd2d3e8\n"
    "v_and_b32 v107, 0xffff0000, v105\n"
    "v_mfma_f32_16x16x32_bf16 v[4:7], v[92:95], v[76:79], v[4:7]\n"
    "v_lshlrev_b32 v106, 16, v105\n"
    "v_and_b32 v105, 0xffff0000, v104\n"
    "v_mfma_f32_16x16x32_bf16 v[8:11], v[96:99], v[76:79], v[8:11]\n"
    "v_lshlrev_b32 v104, 16, v104\n"
    "v_and_b32 v111, 0xffff0000, v109\n"
    "v_mfma_f32_16x16x32_bf16 v[12:15], v[100:103], v[76:79], v[12:15]\n"
    "v_lshlrev_b32 v110, 16, v109\n"
    "v_mfma_f32_16x16x32_bf16 v[28:31], v[100:103], v[80:83], v[28:31]\n"
    "v_and_b32 v109, 0xffff0000, v108\n"
    "v_mfma_f32_16x16x32_bf16 v[24:27], v[96:99], v[80:83], v[24:27]\n"
    "v_lshlrev_b32 v108, 16, v108\n"
    "v_and_b32 v115, 0xffff0000, v113\n"
    "v_mfma_f32_16x16x32_bf16 v[20:23], v[92:95], v[80:83], v[20:23]\n"
    "v_lshlrev_b32 v114, 16, v113\n"
    "v_mfma_f32_16x16x32_bf16 v[16:19], v[88:91], v[80:83], v[16:19]\n"
    "v_and_b32 v113, 0xffff0000, v112\n"
    "v_mfma_f32_16x16x32_bf16 v[32:35], v[88:91], v[84:87], v[32:35]\n"
    "v_lshlrev_b32 v112, 16, v112\n"
    "v_and_b32 v119, 0xffff0000, v117\n"
    "v_mfma_f32_16x16x32_bf16 v[36:39], v[92:95], v[84:87], v[36:39]\n"
    "v_lshlrev_b32 v118, 16, v117\n"
    "v_mfma_f32_16x16x32_bf16 v[40:43], v[96:99], v[84:87], v[40:43]\n"
    "v_and_b32 v117, 0xffff0000, v116\n"
    "v_mfma_f32_16x16x32_bf16 v[44:47], v[100:103], v[84:87], v[44:47]\n"
    "v_lshlrev_b32 v116, 16, v116\n"
    "v_mov_b32 v128, s45\n"
    "v_add_f32 v0, v0, v104\n"
    "v_add_f32 v1, v1, v105\n"
    "v_add_f32 v2, v2, v106\n"
    "v_add_f32 v3, v3, v107\n"
    "v_add_f32 v4, v4, v108\n"
    "v_add_f32 v5, v5, v109\n"
    "v_add_f32 v6, v6, v110\n"
    "v_add_f32 v7, v7, v111\n"
    "v_mul_f32 v120, v0, v0\n"
    "v_mul_f32 v121, v1, v1\n"
    "v_mul_f32 v122, v2, v2\n"
    "v_mul_f32 v123, v3, v3\n"
    "v_mul_f32 v124, v4, v4\n"
    "v_mul_f32 v125, v5, v5\n"
    "v_mul_f32 v126, v6, v6\n"
    "v_mul_f32 v127, v7, v7\n"
    "v_fma_f32 v120, v120, s46, v128\n"
    "v_fma_f32 v121, v121, s46, v128\n"
    "v_fma_f32 v122, v122, s46, v128\n"
    "v_fma_f32 v123, v123, s46, v128\n"
    "v_fma_f32 v124, v124, s46, v128\n"
    "v_fma_f32 v125, v125, s46, v128\n"
    "v_fma_f32 v126, v126, s46, v128\n"
    "v_fma_f32 v127, v127, s46, v128\n"
    "v_mul_f32 v120, v120, v0\n"
    "v_mul_f32 v121, v121, v1\n"
    "v_mul_f32 v122, v122, v2\n"
    "v_mul_f32 v123, v123, v3\n"
    "v_mul_f32 v124, v124, v4\n"
    "v_mul_f32 v125, v125, v5\n"
    "v_mul_f32 v126, v126, v6\n"
    "v_mul_f32 v127, v127, v7\n"
    "v_exp_f32 v120, v120\n"
    "v_exp_f32 v121, v121\n"
    "v_exp_f32 v122, v122\n"
    "v_exp_f32 v123, v123\n"
    "v_exp_f32 v124, v124\n"
    "v_exp_f32 v125, v125\n"
    "v_exp_f32 v126, v126\n"
    "v_exp_f32 v127, v127\n"
    "v_add_f32 v120, 1.0, v120\n"
    "v_add_f32 v121, 1.0, v121\n"
    "v_add_f32 v122, 1.0, v122\n"
    "v_add_f32 v123, 1.0, v123\n"
    "v_add_f32 v124, 1.0, v124\n"
    "v_add_f32 v125, 1.0, v125\n"
    "v_add_f32 v126, 1.0, v126\n"
    "v_add_f32 v127, 1.0, v127\n"
    "v_rcp_f32 v120, v120\n"
    "v_rcp_f32 v121, v121\n"
    "v_rcp_f32 v122, v122\n"
    "v_rcp_f32 v123, v123\n"
    "v_rcp_f32 v124, v124\n"
    "v_rcp_f32 v125, v125\n"
    "v_rcp_f32 v126, v126\n"
    "v_rcp_f32 v127, v127\n"
    "v_mul_f32 v0, v0, v120\n"
    "v_mul_f32 v1, v1, v121\n"
    "v_mul_f32 v2, v2, v122\n"
    "v_mul_f32 v3, v3, v123\n"
    "v_mul_f32 v4, v4, v124\n"
    "v_mul_f32 v5, v5, v125\n"
    "v_mul_f32 v6, v6, v126\n"
    "v_mul_f32 v7, v7, v127\n"
    "v_cvt_pk_bf16_f32 v0, v0, v1\n"
    "v_cvt_pk_bf16_f32 v1, v2, v3\n"
    "v_cvt_pk_bf16_f32 v2, v4, v5\n"
    "v_cvt_pk_bf16_f32 v3, v6, v7\n"
    "s_add_u32 s44, %[soo], 0\n"
    "buffer_store_dwordx4 v[0:3], %[vo], %[srdo], s44 offen sc1\n"
    "v_add_f32 v8, v8, v112\n"
    "v_add_f32 v9, v9, v113\n"
    "v_add_f32 v10, v10, v114\n"
    "v_add_f32 v11, v11, v115\n"
    "v_add_f32 v12, v12, v116\n"
    "v_add_f32 v13, v13, v117\n"
    "v_add_f32 v14, v14, v118\n"
    "v_add_f32 v15, v15, v119\n"
    "v_mul_f32 v120, v8, v8\n"
    "v_mul_f32 v121, v9, v9\n"
    "v_mul_f32 v122, v10, v10\n"
    "v_mul_f32 v123, v11, v11\n"
    "v_mul_f32 v124, v12, v12\n"
    "v_mul_f32 v125, v13, v13\n"
    "v_mul_f32 v126, v14, v14\n"
    "v_mul_f32 v127, v15, v15\n"
    "v_fma_f32 v120, v120, s46, v128\n"
    "v_fma_f32 v121, v121, s46, v128\n"
    "v_fma_f32 v122, v122, s46, v128\n"
    "v_fma_f32 v123, v123, s46, v128\n"
    "v_fma_f32 v124, v124, s46, v128\n"
    "v_fma_f32 v125, v125, s46, v128\n"
    "v_fma_f32 v126, v126, s46, v128\n"
    "v_fma_f32 v127, v127, s46, v128\n"
    "v_mul_f32 v120, v120, v8\n"
    "v_mul_f32 v121, v121, v9\n"
    "v_mul_f32 v122, v122, v10\n"
    "v_mul_f32 v123, v123, v11\n"
    "v_mul_f32 v124, v124, v12\n"
    "v_mul_f32 v125, v125, v13\n"
    "v_mul_f32 v126, v126, v14\n"
    "v_mul_f32 v127, v127, v15\n"
    "v_exp_f32 v120, v120\n"
    "v_exp_f32 v121, v121\n"
    "v_exp_f32 v122, v122\n"
    "v_exp_f32 v123, v123\n"
    "v_exp_f32 v124, v124\n"
    "v_exp_f32 v125, v125\n"
    "v_exp_f32 v126, v126\n"
    "v_exp_f32 v127, v127\n"
    "v_add_f32 v120, 1.0, v120\n"
    "v_add_f32 v121, 1.0, v121\n"
    "v_add_f32 v122, 1.0, v122\n"
    "v_add_f32 v123, 1.0, v123\n"
    "v_add_f32 v124, 1.0, v124\n"
    "v_add_f32 v125, 1.0, v125\n"
    "v_add_f32 v126, 1.0, v126\n"
    "v_add_f32 v127, 1.0, v127\n"
    "v_rcp_f32 v120, v120\n"
    "v_rcp_f32 v121, v121\n"
    "v_rcp_f32 v122, v122\n"
    "v_rcp_f32 v123, v123\n"
    "v_rcp_f32 v124, v124\n"
    "v_rcp_f32 v125, v125\n"
    "v_rcp_f32 v126, v126\n"
    "v_rcp_f32 v127, v127\n"
    "v_mul_f32 v8, v8, v120\n"
    "v_mul_f32 v9, v9, v121\n"
    "v_mul_f32 v10, v10, v122\n"
    "v_mul_f32 v11, v11, v123\n"
    "v_mul_f32 v12, v12, v124\n"
    "v_mul_f32 v13, v13, v125\n"
    "v_mul_f32 v14, v14, v126\n"
    "v_mul_f32 v15, v15, v127\n"
    "v_cvt_pk_bf16_f32 v8, v8, v9\n"
    "v_cvt_pk_bf16_f32 v9, v10, v11\n"
    "v_cvt_pk_bf16_f32 v10, v12, v13\n"
    "v_cvt_pk_bf16_f32 v11, v14, v15\n"
    "s_add_u32 s44, %[soo], 1024\n"
    "buffer_store_dwordx4 v[8:11], %[vo], %[srdo], s44 offen sc1\n"
    "v_add_f32 v16, v16, v104\n"
    "v_add_f32 v17, v17, v105\n"
    "v_add_f32 v18, v18, v106\n"
    "v_add_f32 v19, v19, v107\n"
    "v_add_f32 v20, v20, v108\n"
    "v_add_f32 v21, v21, v109\n"
    "v_add_f32 v22, v22, v110\n"
    "v_add_f32 v23, v23, v111\n"
    "v_mul_f32 v120, v16, v16\n"
    "v_mul_f32 v121, v17, v17\n"
    "v_mul_f32 v122, v18, v18\n"
    "v_mul_f32 v123, v19, v19\n"
    "v_mul_f32 v124, v20, v20\n"
    "v_mul_f32 v125, v21, v21\n"
    "v_mul_f32 v126, v22, v22\n"
    "v_mul_f32 v127, v23, v23\n"
    "v_fma_f32 v120, v120, s46, v128\n"
    "v_fma_f32 v121, v121, s46, v128\n"
    "v_fma_f32 v122, v122, s46, v128\n"
    "v_fma_f32 v123, v123, s46, v128\n"
    "v_fma_f32 v124, v124, s46, v128\n"
    "v_fma_f32 v125, v125, s46, v128\n"
    "v_fma_f32 v126, v126, s46, v128\n"
    "v_fma_f32 v127, v127, s46, v128\n"
    "v_mul_f32 v120, v120, v16\n"
    "v_mul_f32 v121, v121, v17\n"
    "v_mul_f32 v122, v122, v18\n"
    "v_mul_f32 v123, v123, v19\n"
    "v_mul_f32 v124, v124, v20\n"
    "v_mul_f32 v125, v125, v21\n"
    "v_mul_f32 v126, v126, v22\n"
    "v_mul_f32 v127, v127, v23\n"
    "v_exp_f32 v120, v120\n"
    "v_exp_f32 v121, v121\n"
    "v_exp_f32 v122, v122\n"
    "v_exp_f32 v123, v123\n"
    "v_exp_f32 v124, v124\n"
    "v_exp_f32 v125, v125\n"
    "v_exp_f32 v126, v126\n"
    "v_exp_f32 v127, v127\n"
    "v_add_f32 v120, 1.0, v120\n"
    "v_add_f32 v121, 1.0, v121\n"
    "v_add_f32 v122, 1.0, v122\n"
    "v_add_f32 v123, 1.0, v123\n"
    "v_add_f32 v124, 1.0, v124\n"
    "v_add_f32 v125, 1.0, v125\n"
    "v_add_f32 v126, 1.0, v126\n"
    "v_add_f32 v127, 1.0, v127\n"
    "v_rcp_f32 v120, v120\n"
    "v_rcp_f32 v121, v121\n"
    "v_rcp_f32 v122, v122\n"
    "v_rcp_f32 v123, v123\n"
    "v_rcp_f32 v124, v124\n"
    "v_rcp_f32 v125, v125\n"
    "v_rcp_f32 v126, v126\n"
    "v_rcp_f32 v127, v127\n"
    "v_mul_f32 v16, v16, v120\n"
    "v_mul_f32 v17, v17, v121\n"
    "v_mul_f32 v18, v18, v122\n"
    "v_mul_f32 v19, v19, v123\n"
    "v_mul_f32 v20, v20, v124\n"
    "v_mul_f32 v21, v21, v125\n"
    "v_mul_f32 v22, v22, v126\n"
    "v_mul_f32 v23, v23, v127\n"
    "v_cvt_pk_bf16_f32 v16, v16, v17\n"
    "v_cvt_pk_bf16_f32 v17, v18, v19\n"
    "v_cvt_pk_bf16_f32 v18, v20, v21\n"
    "v_cvt_pk_bf16_f32 v19, v22, v23\n"
    "s_add_u32 s44, %[soo], 147456\n"
    "buffer_store_dwordx4 v[16:19], %[vo], %[srdo], s44 offen sc1\n"
    "v_add_f32 v24, v24, v112\n"
    "v_add_f32 v25, v25, v113\n"
    "v_add_f32 v26, v26, v114\n"
    "v_add_f32 v27, v27, v115\n"
    "v_add_f32 v28, v28, v116\n"
    "v_add_f32 v29, v29, v117\n"
    "v_add_f32 v30, v30, v118\n"
    "v_add_f32 v31, v31, v119\n"
    "v_mul_f32 v120, v24, v24\n"
    "v_mul_f32 v121, v25, v25\n"
    "v_mul_f32 v122, v26, v26\n"
    "v_mul_f32 v123, v27, v27\n"
    "v_mul_f32 v124, v28, v28\n"
    "v_mul_f32 v125, v29, v29\n"
    "v_mul_f32 v126, v30, v30\n"
    "v_mul_f32 v127, v31, v31\n"
    "v_fma_f32 v120, v120, s46, v128\n"
    "v_fma_f32 v121, v121, s46, v128\n"
    "v_fma_f32 v122, v122, s46, v128\n"
    "v_fma_f32 v123, v123, s46, v128\n"
    "v_fma_f32 v124, v124, s46, v128\n"
    "v_fma_f32 v125, v125, s46, v128\n"
    "v_fma_f32 v126, v126, s46, v128\n"
    "v_fma_f32 v127, v127, s46, v128\n"
    "v_mul_f32 v120, v120, v24\n"
    "v_mul_f32 v121, v121, v25\n"
    "v_mul_f32 v122, v122, v26\n"
    "v_mul_f32 v123, v123, v27\n"
    "v_mul_f32 v124, v124, v28\n"
    "v_mul_f32 v125, v125, v29\n"
    "v_mul_f32 v126, v126, v30\n"
    "v_mul_f32 v127, v127, v31\n"
    "v_exp_f32 v120, v120\n"
    "v_exp_f32 v121, v121\n"
    "v_exp_f32 v122, v122\n"
    "v_exp_f32 v123, v123\n"
    "v_exp_f32 v124, v124\n"
    "v_exp_f32 v125, v125\n"
    "v_exp_f32 v126, v126\n"
    "v_exp_f32 v127, v127\n"
    "v_add_f32 v120, 1.0, v120\n"
    "v_add_f32 v121, 1.0, v121\n"
    "v_add_f32 v122, 1.0, v122\n"
    "v_add_f32 v123, 1.0, v123\n"
    "v_add_f32 v124, 1.0, v124\n"
    "v_add_f32 v125, 1.0, v125\n"
    "v_add_f32 v126, 1.0, v126\n"
    "v_add_f32 v127, 1.0, v127\n"
    "v_rcp_f32 v120, v120\n"
    "v_rcp_f32 v121, v121\n"
    "v_rcp_f32 v122, v122\n"
    "v_rcp_f32 v123, v123\n"
    "v_rcp_f32 v124, v124\n"
    "v_rcp_f32 v125, v125\n"
    "v_rcp_f32 v126, v126\n"
    "v_rcp_f32 v127, v127\n"
    "v_mul_f32 v24, v24, v120\n"
    "v_mul_f32 v25, v25, v121\n"
    "v_mul_f32 v26, v26, v122\n"
    "v_mul_f32 v27, v27, v123\n"
    "v_mul_f32 v28, v28, v124\n"
    "v_mul_f32 v29, v29, v125\n"
    "v_mul_f32 v30, v30, v126\n"
    "v_mul_f32 v31, v31, v127\n"
    "v_cvt_pk_bf16_f32 v24, v24, v25\n"
    "v_cvt_pk_bf16_f32 v25, v26, v27\n"
    "v_cvt_pk_bf16_f32 v26, v28, v29\n"
    "v_cvt_pk_bf16_f32 v27, v30, v31\n"
    "s_add_u32 s44, %[soo], 148480\n"
    "buffer_store_dwordx4 v[24:27], %[vo], %[srdo], s44 offen sc1\n"
    "v_add_f32 v32, v32, v104\n"
    "v_add_f32 v33, v33, v105\n"
    "v_add_f32 v34, v34, v106\n"
    "v_add_f32 v35, v35, v107\n"
    "v_add_f32 v36, v36, v108\n"
    "v_add_f32 v37, v37, v109\n"
    "v_add_f32 v38, v38, v110\n"
    "v_add_f32 v39, v39, v111\n"
    "v_mul_f32 v120, v32, v32\n"
    "v_mul_f32 v121, v33, v33\n"
    "v_mul_f32 v122, v34, v34\n"
    "v_mul_f32 v123, v35, v35\n"
    "v_mul_f32 v124, v36, v36\n"
    "v_mul_f32 v125, v37, v37\n"
    "v_mul_f32 v126, v38, v38\n"
    "v_mul_f32 v127, v39, v39\n"
    "v_fma_f32 v120, v120, s46, v128\n"
    "v_fma_f32 v121, v121, s46, v128\n"
    "v_fma_f32 v122, v122, s46, v128\n"
    "v_fma_f32 v123, v123, s46, v128\n"
    "v_fma_f32 v124, v124, s46, v128\n"
    "v_fma_f32 v125, v125, s46, v128\n"
    "v_fma_f32 v126, v126, s46, v128\n"
    "v_fma_f32 v127, v127, s46, v128\n"
    "v_mul_f32 v120, v120, v32\n"
    "v_mul_f32 v121, v121, v33\n"
    "v_mul_f32 v122, v122, v34\n"
    "v_mul_f32 v123, v123, v35\n"
    "v_mul_f32 v124, v124, v36\n"
    "v_mul_f32 v125, v125, v37\n"
    "v_mul_f32 v126, v126, v38\n"
    "v_mul_f32 v127, v127, v39\n"
    "v_exp_f32 v120, v120\n"
    "v_exp_f32 v121, v121\n"
    "v_exp_f32 v122, v122\n"
    "v_exp_f32 v123, v123\n"
    "v_exp_f32 v124, v124\n"
    "v_exp_f32 v125, v125\n"
    "v_exp_f32 v126, v126\n"
    "v_exp_f32 v127, v127\n"
    "v_add_f32 v120, 1.0, v120\n"
    "v_add_f32 v121, 1.0, v121\n"
    "v_add_f32 v122, 1.0, v122\n"
    "v_add_f32 v123, 1.0, v123\n"
    "v_add_f32 v124, 1.0, v124\n"
    "v_add_f32 v125, 1.0, v125\n"
    "v_add_f32 v126, 1.0, v126\n"
    "v_add_f32 v127, 1.0, v127\n"
    "v_rcp_f32 v120, v120\n"
    "v_rcp_f32 v121, v121\n"
    "v_rcp_f32 v122, v122\n"
    "v_rcp_f32 v123, v123\n"
    "v_rcp_f32 v124, v124\n"
    "v_rcp_f32 v125, v125\n"
    "v_rcp_f32 v126, v126\n"
    "v_rcp_f32 v127, v127\n"
    "v_mul_f32 v32, v32, v120\n"
    "v_mul_f32 v33, v33, v121\n"
    "v_mul_f32 v34, v34, v122\n"
    "v_mul_f32 v35, v35, v123\n"
    "v_mul_f32 v36, v36, v124\n"
    "v_mul_f32 v37, v37, v125\n"
    "v_mul_f32 v38, v38, v126\n"
    "v_mul_f32 v39, v39, v127\n"
    "v_cvt_pk_bf16_f32 v32, v32, v33\n"
    "v_cvt_pk_bf16_f32 v33, v34, v35\n"
    "v_cvt_pk_bf16_f32 v34, v36, v37\n"
    "v_cvt_pk_bf16_f32 v35, v38, v39\n"
    "s_add_u32 s44, %[soo], 294912\n"
    "buffer_store_dwordx4 v[32:35], %[vo], %[srdo], s44 offen sc1\n"
    "v_add_f32 v40, v40, v112\n"
    "v_add_f32 v41, v41, v113\n"
    "v_add_f32 v42, v42, v114\n"
    "v_add_f32 v43, v43, v115\n"
    "v_add_f32 v44, v44, v116\n"
    "v_add_f32 v45, v45, v117\n"
    "v_add_f32 v46, v46, v118\n"
    "v_add_f32 v47, v47, v119\n"
    "v_mul_f32 v120, v40, v40\n"
    "v_mul_f32 v121, v41, v41\n"
    "v_mul_f32 v122, v42, v42\n"
    "v_mul_f32 v123, v43, v43\n"
    "v_mul_f32 v124, v44, v44\n"
    "v_mul_f32 v125, v45, v45\n"
    "v_mul_f32 v126, v46, v46\n"
    "v_mul_f32 v127, v47, v47\n"
    "v_fma_f32 v120, v120, s46, v128\n"
    "v_fma_f32 v121, v121, s46, v128\n"
    "v_fma_f32 v122, v122, s46, v128\n"
    "v_fma_f32 v123, v123, s46, v128\n"
    "v_fma_f32 v124, v124, s46, v128\n"
    "v_fma_f32 v125, v125, s46, v128\n"
    "v_fma_f32 v126, v126, s46, v128\n"
    "v_fma_f32 v127, v127, s46, v128\n"
    "v_mul_f32 v120, v120, v40\n"
    "v_mul_f32 v121, v121, v41\n"
    "v_mul_f32 v122, v122, v42\n"
    "v_mul_f32 v123, v123, v43\n"
    "v_mul_f32 v124, v124, v44\n"
    "v_mul_f32 v125, v125, v45\n"
    "v_mul_f32 v126, v126, v46\n"
    "v_mul_f32 v127, v127, v47\n"
    "v_exp_f32 v120, v120\n"
    "v_exp_f32 v121, v121\n"
    "v_exp_f32 v122, v122\n"
    "v_exp_f32 v123, v123\n"
    "v_exp_f32 v124, v124\n"
    "v_exp_f32 v125, v125\n"
    "v_exp_f32 v126, v126\n"
    "v_exp_f32 v127, v127\n"
    "v_add_f32 v120, 1.0, v120\n"
    "v_add_f32 v121, 1.0, v121\n"
    "v_add_f32 v122, 1.0, v122\n"
    "v_add_f32 v123, 1.0, v123\n"
    "v_add_f32 v124, 1.0, v124\n"
    "v_add_f32 v125, 1.0, v125\n"
    "v_add_f32 v126, 1.0, v126\n"
    "v_add_f32 v127, 1.0, v127\n"
    "v_rcp_f32 v120, v120\n"
    "v_rcp_f32 v121, v121\n"
    "v_rcp_f32 v122, v122\n"
    "v_rcp_f32 v123, v123\n"
    "v_rcp_f32 v124, v124\n"
    "v_rcp_f32 v125, v125\n"
    "v_rcp_f32 v126, v126\n"
    "v_rcp_f32 v127, v127\n"
    "v_mul_f32 v40, v40, v120\n"
    "v_mul_f32 v41, v41, v121\n"
    "v_mul_f32 v42, v42, v122\n"
    "v_mul_f32 v43, v43, v123\n"
    "v_mul_f32 v44, v44, v124\n"
    "v_mul_f32 v45, v45, v125\n"
    "v_mul_f32 v46, v46, v126\n"
    "v_mul_f32 v47, v47, v127\n"
    "v_cvt_pk_bf16_f32 v40, v40, v41\n"
    "v_cvt_pk_bf16_f32 v41, v42, v43\n"
    "v_cvt_pk_bf16_f32 v42, v44, v45\n"
    "v_cvt_pk_bf16_f32 v43, v46, v47\n"
    "s_add_u32 s44, %[soo], 295936\n"
    "buffer_store_dwordx4 v[40:43], %[vo], %[srdo], s44 offen sc1\n"
    "GE_%=:\n"
    : [rdx]"+&v"(rdx), [rdw0]"+&v"(rdw[0]), [rdw1]"+&v"(rdw[1]), [rdw2]"+&v"(rdw[2]), [rdw3]"+&v"(rdw[3]), [sox]"+&s"(sox), [sow]"+&s"(sow), [nmid]"+&s"(nmid)
    : [vw0]"v"(vw[0]), [vw1]"v"(vw[1]), [vw2]"v"(vw[2]), [vw3]"v"(vw[3]), [srdw]"s"(srdw), [dw]"s"(dw), [m0w]"s"(m0w), [vx0]"v"(vx[0]), [vx1]"v"(vx[1]), [vx2]"v"(vx[2]), [srdx]"s"(srdx), [srdo]"s"(srdo), [m0x]"s"(m0x), [vo]"v"(vo), [soo]"s"(soo), [vb]"v"(vb), [srdb]"s"(srdb)
    : "memory", "scc", "s40","s41","s42","s43","s44","s45","s46","s47","s48","s49","s50","s51","s52","s53", "v0","v1","v2","v3","v4","v5","v6","v7","v8","v9","v10","v11","v12","v13","v14","v15","v16","v17","v18","v19","v20","v21","v22","v23","v24","v25","v26","v27","v28","v29","v30","v31","v32","v33","v34","v35","v36","v37","v38","v39","v40","v41","v42","v43","v44","v45","v46","v47","v48","v49","v50","v51","v52","v53","v54","v55","v56","v57","v58","v59","v60","v61","v62","v63","v64","v65","v66","v67","v68","v69","v70","v71","v72","v73","v74","v75","v76","v77","v78","v79","v80","v81","v82","v83","v84","v85","v86","v87","v88","v89","v90","v91","v92","v93","v94","v95","v96","v97","v98","v99","v100","v101","v102","v103","v104","v105","v106","v107","v108","v109","v110","v111","v112","v113","v114","v115","v116","v117","v118","v119","v120","v121","v122","v123","v124","v125","v126","v127","v128","v129","v130","v131","v132");
  }
  if (j < 30) {
    constexpr u32 XSTRIDE = 144 * 1024, LDW = S * 2;
    const u32 st0 = sl * 72 / 5, nst = (sl + 1) * 72 / 5 - st0;
    const __amdgpu_buffer_rsrc_t srdx = __builtin_amdgcn_make_buffer_rsrc((void*)(ws + WS_UB - 4096), 0, M * F * 2 + 4096, 0x00020000), srdw = __builtin_amdgcn_make_buffer_rsrc((void*)(w2 - 4096), 0, F * S * 2 + 4096, 0x00020000),
                                  srdo = __builtin_amdgcn_make_buffer_rsrc((void*)(ws + WS_P6), 0, 5 * S * M * 2, 0x00020000);
    u32 sox = t3 * 12 * XSTRIDE + st0 * 2048, sow = st0 * 64 * LDW + tt * 256;
    u32 vw[4];
#pragma unroll
    for (int i = 0; i < 4; i++) { const u32 jj = wid * 4 + i; vw[i] = 4096 - (i & 3) * 1024 + (8 * (jj & 7) + (lane >> 3)) * LDW + (jj >> 3) * 128 + ((lane & 7) ^ (((lane >> 4) & 3) << 1)) * 16; }
    const u32 m0w = lb + 24576 + wid * 4 * 1024, dw = 64 * LDW;
    asm volatile(
    "s_nop 4\n"
    "s_mov_b32 s41, %[m0w]\n"
    "s_add_u32 m0, s41, 0\n"
    "s_nop 0\n"
    "buffer_load_dwordx4 %[vw0], %[srdw], %[sow] offen offset:0 lds\n"
    "buffer_load_dwordx4 %[vw1], %[srdw], %[sow] offen offset:1024 lds\n"
    "buffer_load_dwordx4 %[vw2], %[srdw], %[sow] offen offset:2048 lds\n"
    "buffer_load_dwordx4 %[vw3], %[srdw], %[sow] offen offset:3072 lds\n"
    "s_add_u32 s41, s41, 40960\n"
    "s_add_u32 %[sow], %[sow], %[dw]\n"
    "s_add_u32 m0, s41, 0\n"
    "s_nop 0\n"
    "buffer_load_dwordx4 %[vw0], %[srdw], %[sow] offen offset:0 lds\n"
    "buffer_load_dwordx4 %[vw1], %[srdw], %[sow] offen offset:1024 lds\n"
    "buffer_load_dwordx4 %[vw2], %[srdw], %[sow] offen offset:2048 lds\n"
    "buffer_load_dwordx4 %[vw3], %[srdw], %[sow] offen offset:3072 lds\n"
    : [sow]"+&s"(sow)
    : [vw0]"v"(vw[0]), [vw1]"v"(vw[1]), [vw2]"v"(vw[2]), [vw3]"v"(vw[3]), [srdw]"s"(srdw), [dw]"s"(dw), [m0w]"s"(m0w)
    : "memory", "scc", "s41");
    publish<8>(fl + 7 * FARR + b * FSTR, tag, tid);
    DBGT(12);
    u32 vx[6], rdw[4];
#pragma unroll
    for (int i = 0; i < 6; i++) { const u32 jj = wid * 6 + i; vx[i] = 4096 - (i & 3) * 1024 + (jj >> 1) * XSTRIDE + (jj & 1) * 1024 + lane * 16; }
    u32 rdx = lb + wx * 6 * 2048 + lane * 16;
    { const u32 r16 = lane >> 2;
#pragma unroll
      for (int w = 0; w < 4; w++) { const u32 bb = ww * 4 + w; rdw[w] = lb + 24576 + (bb >> 2) * 8192 + (r16 >> 3) * 1024 + (r16 & 7) * 128 + (((bb & 3) ^ ((r16 >> 1) & 3)) * 32) + (lane & 3) * 8; } }
    const u32 m0x = lb + wid * 6 * 1024; u32 nmid = nst - 3;
    const u32 soo = (((sl * 128 + tt * 8 + ww * 4) * 18) + t3 * 6 + wx * 3) * 1024;
    {
      const u32 f0 = st0 >> 1, nf = ((st0 + nst - 1) >> 1) - f0 + 1, k = lane < 2 * nf ? lane : 0, q = (f0 + (k >> 1)) * 6 + t3 * 2 + (k & 1);
      await<1>(fl + 7 * FARR, q / 27 + 8 * (q % 27), tag, wid); }
    DBGT(13);
    asm volatile(
    "s_nop 4\n"
    "s_mov_b32 s40, %[m0x]\n"
    "s_mov_b32 s49, 2048\n"
    "s_add_u32 m0, s40, 0\n"
    "s_nop 0\n"
    "buffer_load_dwordx4 %[vx0], %[srdx], %[sox] offen offset:0 sc1 lds\n"
    "buffer_load_dwordx4 %[vx1], %[srdx], %[sox] offen offset:1024 sc1 lds\n"
    "buffer_load_dwordx4 %[vx2], %[srdx], %[sox] offen offset:2048 sc1 lds\n"
    "buffer_load_dwordx4 %[vx3], %[srdx], %[sox] offen offset:3072 sc1 lds\n"
    "s_add_u32 m0, s40, 4096\n"
    "s_nop 0\n"
    "buffer_load_dwordx4 %[vx4], %[srdx], %[sox] offen offset:0 sc1 lds\n"
    "buffer_load_dwordx4 %[vx5], %[srdx], %[sox] offen offset:1024 sc1 lds\n"
    "s_add_u32 s40, s40, 40960\n"
    "s_add_u32 %[sox], %[sox], s49\n"
    "s_add_u32 m0, s40, 0\n"
    "s_nop 0\n"
    "buffer_load_dwordx4 %[vx0], %[srdx], %[sox] offen offset:0 sc1 lds\n"
    "buffer_load_dwordx4 %[vx1], %[srdx], %[sox] offen offset:1024 sc1 lds\n"
    "buffer_load_dwordx4 %[vx2], %[srdx], %[sox] offen offset:2048 sc1 lds\n"
    "buffer_load_dwordx4 %[vx3], %[srdx], %[sox] offen offset:3072 sc1 lds\n"
    "s_add_u32 m0, s40, 4096\n"
    "s_nop 0\n"
    "buffer_load_dwordx4 %[vx4], %[srdx], %[sox] offen offset:0 sc1 lds\n"
    "buffer_load_dwordx4 %[vx5], %[srdx], %[sox] offen offset:1024 sc1 lds\n"
    "s_getpc_b64 s[52:53]\n"
    "CT_%=:\n"
    "s_add_u32 s52, s52, LM_%=-CT_%=\n"
    "s_addc_u32 s53, s53, 0\n"
    "s_mul_i32 s44, 0, 8192\n"
    "v_lshl_add_u32 v181, %[vo], 3, s44\n"
    "v_min_u32 v182, GE_%=-LM_%=-4, v181\n"
    "global_load_dword v183, v182, s[52:53] ; dummy\n"
    "s_mov_b32 s42, 40960\n"
    "s_mov_b32 s43, 0\n"
    "s_mov_b32 s47, 4294885376\n"
    "s_mov_b32 s48, 40960\n"
    "s_mov_b32 s50, 122880\n"
    "s_waitcnt vmcnt(7)\n"
    "s_barrier\n"
    "ds_read_b128 v[96:99], %[rdx] offset:0\n"
    "ds_read_b64_tr_b16 v[120:121], %[rdw0] offset:0\n"
    "ds_read_b64_tr_b16 v[122:123], %[rdw0] offset:2048\n"
    "ds_read_b64_tr_b16 v[124:125], %[rdw1] offset:0\n"
    "ds_read_b64_tr_b16 v[126:127], %[rdw1] offset:2048\n"
    "ds_read_b64_tr_b16 v[128:129], %[rdw2] offset:0\n"
    "ds_read_b64_tr_b16 v[130:131], %[rdw2] offset:2048\n"
    "ds_read_b64_tr_b16 v[132:133], %[rdw3] offset:0\n"
    "ds_read_b64_tr_b16 v[134:135], %[rdw3] offset:2048\n"
    "ds_read_b128 v[100:103], %[rdx] offset:2048\n"
    "ds_read_b128 v[104:107], %[rdx] offset:4096\n"
    "ds_read_b128 v[108:111], %[rdx] offset:6144\n"
    "ds_read_b128 v[112:115], %[rdx] offset:8192\n"
    "ds_read_b128 v[116:119], %[rdx] offset:10240\n"
    "s_waitcnt lgkmcnt(0)\n"
    "v_mfma_f32_16x16x32_bf16 v[0:3], v[96:99], v[120:123], 0\n"
    "s_add_u32 %[sox], %[sox], s49\n"
    "ds_read_b128 v[136:139], %[rdx] offset:1024\n"
    "s_add_u32 %[sow], %[sow], %[dw]\n"
    "v_mfma_f32_16x16x32_bf16 v[4:7], v[96:99], v[124:127], 0\n"
    "s_add_u32 s42, s42, s48\n"
    "ds_read_b64_tr_b16 v[160:161], %[rdw0] offset:4096\n"
    "s_cmp_eq_u32 s42, s50\n"
    "v_mfma_f32_16x16x32_bf16 v[8:11], v[96:99], v[128:131], 0\n"
    "s_cselect_b32 s42, 0, s42\n"
    "ds_read_b64_tr_b16 v[162:163], %[rdw0] offset:6144\n"
    "s_add_u32 s40, s42, %[m0x]\n"
    "v_mfma_f32_16x16x32_bf16 v[12:15], v[96:99], v[132:135], 0\n"
    "s_add_u32 s41, s42, %[m0w]\n"
    "ds_read_b64_tr_b16 v[164:165], %[rdw1] offset:4096\n"
    "v_mfma_f32_16x16x32_bf16 v[28:31], v[100:103], v[132:135], 0\n"
    "s_add_u32 m0, s40, 0\n"
    "ds_read_b64_tr_b16 v[166:167], %[rdw1] offset:6144\n"
    "v_mfma_f32_16x16x32_bf16 v[24:27], v[100:103], v[128:131], 0\n"
    "ds_read_b64_tr_b16 v[168:169], %[rdw2] offset:4096\n"
    "buffer_load_dwordx4 %[vx0], %[srdx], %[sox] offen offset:0 sc1 lds\n"
    "v_mfma_f32_16x16x32_bf16 v[20:23], v[100:103], v[124:127], 0\n"
    "ds_read_b64_tr_b16 v[170:171], %[rdw2] offset:6144\n"
    "buffer_load_dwordx4 %[vx1], %[srdx], %[sox] offen offset:1024 sc1 lds\n"
    "v_mfma_f32_16x16x32_bf16 v[16:19], v[100:103], v[120:123], 0\n"
    "ds_read_b64_tr_b16 v[172:173], %[rdw3] offset:4096\n"
    "v_mfma_f32_16x16x32_bf16 v[32:35], v[104:107], v[120:123], 0\n"
    "ds_read_b64_tr_b16 v[174:175], %[rdw3] offset:6144\n"
    "buffer_load_dwordx4 %[vx2], %[srdx], %[sox] offen offset:2048 sc1 lds\n"
    "v_mfma_f32_16x16x32_bf16 v[36:39], v[104:107], v[124:127], 0\n"
    "ds_read_b128 v[140:143], %[rdx] offset:3072\n"
    "v_mfma_f32_16x16x32_bf16 v[40:43], v[104:107], v[128:131], 0\n"
    "ds_read_b128 v[144:147], %[rdx] offset:5120\n"
    "buffer_load_dwordx4 %[vx3], %[srdx], %[sox] offen offset:3072 sc1 lds\n"
    "v_mfma_f32_16x16x32_bf16 v[44:47], v[104:107], v[132:135], 0\n"
    "s_add_u32 m0, s40, 4096\n"
    "ds_read_b128 v[148:151], %[rdx] offset:7168\n"
    "v_mfma_f32_16x16x32_bf16 v[60:63], v[108:111], v[132:135], 0\n"
    "ds_read_b128 v[152:155], %[rdx] offset:9216\n"
    "buffer_load_dwordx4 %[vx4], %[srdx], %[sox] offen offset:0 sc1 lds\n"
    "v_mfma_f32_16x16x32_bf16 v[56:59], v[108:111], v[128:131], 0\n"
    "ds_read_b128 v[156:159], %[rdx] offset:11264\n"
    "v_mfma_f32_16x16x32_bf16 v[52:55], v[108:111], v[124:127], 0\n"
    "buffer_load_dwordx4 %[vx5], %[srdx], %[sox] offen offset:1024 sc1 lds\n"
    "s_add_u32 m0, s41, 0\n"
    "v_mfma_f32_16x16x32_bf16 v[48:51], v[108:111], v[120:123], 0\n"
    "s_add_u32 s43, s43, 1\n"
    "buffer_load_dwordx4 %[vw0], %[srdw], %[sow] offen offset:0 lds\n"
    "v_mfma_f32_16x16x32_bf16 v[64:67], v[112:115], v[120:123], 0\n"
    "s_cmp_eq_u32 s43, 3\n"
    "v_mfma_f32_16x16x32_bf16 v[68:71], v[112:115], v[124:127], 0\n"
    "s_cselect_b32 s46, s47, s48\n"
    "buffer_load_dwordx4 %[vw1], %[srdw], %[sow] offen offset:1024 lds\n"
    "v_mfma_f32_16x16x32_bf16 v[72:75], v[112:115], v[128:131], 0\n"
    "s_cselect_b32 s43, 0, s43\n"
    "v_add_u32 %[rdx], s46, %[rdx]\n"
    "v_add_u32 %[rdw0], s46, %[rdw0]\n"
    "v_mfma_f32_16x16x32_bf16 v[76:79], v[112:115], v[132:135], 0\n"
    "buffer_load_dwordx4 %[vw2], %[srdw], %[sow] offen offset:2048 lds\n"
    "v_add_u32 %[rdw1], s46, %[rdw1]\n"
    "v_mfma_f32_16x16x32_bf16 v[92:95], v[116:119], v[132:135], 0\n"
    "v_add_u32 %[rdw2], s46, %[rdw2]\n"
    "v_mfma_f32_16x16x32_bf16 v[88:91], v[116:119], v[128:131], 0\n"
    "buffer_load_dwordx4 %[vw3], %[srdw], %[sow] offen offset:3072 lds\n"
    "v_mfma_f32_16x16x32_bf16 v[84:87], v[116:119], v[124:127], 0\n"
    "v_add_u32 %[rdw3], s46, %[rdw3]\n"
    "v_mfma_f32_16x16x32_bf16 v[80:83], v[116:119], v[120:123], 0\n"
    "s_waitcnt lgkmcnt(0) vmcnt(10)\n"
    "s_barrier\n"
    "LM_%=:\n"
    "v_mfma_f32_16x16x32_bf16 v[0:3], v[136:139], v[160:163], v[0:3]\n"
    "s_add_u32 %[sox], %[sox], s49\n"
    "ds_read_b128 v[96:99], %[rdx] offset:0\n"
    "v_mfma_f32_16x16x32_bf16 v[4:7], v[136:139], v[164:167], v[4:7]\n"
    "s_add_u32 %[sow], %[sow], %[dw]\n"
    "ds_read_b64_tr_b16 v[120:121], %[rdw0] offset:0\n"
    "v_mfma_f32_16x16x32_bf16 v[8:11], v[136:139], v[168:171], v[8:11]\n"
    "s_add_u32 s42, s42, s48\n"
    "ds_read_b64_tr_b16 v[122:123], %[rdw0] offset:2048\n"
    "v_mfma_f32_16x16x32_bf16 v[12:15], v[136:139], v[172:175], v[12:15]\n"
    "s_cmp_eq_u32 s42, s50\n"
    "ds_read_b64_tr_b16 v[124:125], %[rdw1] offset:0\n"
    "v_mfma_f32_16x16x32_bf16 v[28:31], v[140:143], v[172:175], v[28:31]\n"
    "s_cselect_b32 s42, 0, s42\n"
    "ds_read_b64_tr_b16 v[126:127], %[rdw1] offset:2048\n"
    "v_mfma_f32_16x16x32_bf16 v[24:27], v[140:143], v[168:171], v[24:27]\n"
    "s_add_u32 s40, s42, %[m0x]\n"
    "ds_read_b64_tr_b16 v[128:129], %[rdw2] offset:0\n"
    "v_mfma_f32_16x16x32_bf16 v[20:23], v[140:143], v[164:167], v[20:23]\n"
    "s_add_u32 s41, s42, %[m0w]\n"
    "ds_read_b64_tr_b16 v[130:131], %[rdw2] offset:2048\n"
    "v_mfma_f32_16x16x32_bf16 v[16:19], v[140:143], v[160:163], v[16:19]\n"
    "ds_read_b64_tr_b16 v[132:133], %[rdw3] offset:0\n"
    "v_mfma_f32_16x16x32_bf16 v[32:35], v[144:147], v[160:163], v[32:35]\n"
    "ds_read_b64_tr_b16 v[134:135], %[rdw3] offset:2048\n"
    "v_mfma_f32_16x16x32_bf16 v[36:39], v[144:147], v[164:167], v[36:39]\n"
    "ds_read_b128 v[100:103], %[rdx] offset:2048\n"
    "v_mfma_f32_16x16x32_bf16 v[40:43], v[144:147], v[168:171], v[40:43]\n"
    "ds_read_b128 v[104:107], %[rdx] offset:4096\n"
    "v_mfma_f32_16x16x32_bf16 v[44:47], v[144:147], v[172:175], v[44:47]\n"
    "ds_read_b128 v[108:111], %[rdx] offset:6144\n"
    "v_mfma_f32_16x16x32_bf16 v[60:63], v[148:151], v[172:175], v[60:63]\n"
    "ds_read_b128 v[112:115], %[rdx] offset:8192\n"
    "v_mfma_f32_16x16x32_bf16 v[56:59], v[148:151], v[168:171], v[56:59]\n"
    "s_add_u32 m0, s40, 0\n"
    "ds_read_b128 v[116:119], %[rdx] offset:10240\n"
    "v_mfma_f32_16x16x32_bf16 v[52:55], v[148:151], v[164:167], v[52:55]\n"
    "buffer_load_dwordx4 %[vx0], %[srdx], %[sox] offen offset:0 sc1 lds\n"
    "v_mfma_f32_16x16x32_bf16 v[48:51], v[148:151], v[160:163], v[48:51]\n"
    "buffer_load_dwordx4 %[vx1], %[srdx], %[sox] offen offset:1024 sc1 lds\n"
    "v_mfma_f32_16x16x32_bf16 v[64:67], v[152:155], v[160:163], v[64:67]\n"
    "v_mfma_f32_16x16x32_bf16 v[68:71], v[152:155], v[164:167], v[68:71]\n"
    "buffer_load_dwordx4 %[vx2], %[srdx], %[sox] offen offset:2048 sc1 lds\n"
    "v_mfma_f32_16x16x32_bf16 v[72:75], v[152:155], v[168:171], v[72:75]\n"
    "v_mfma_f32_16x16x32_bf16 v[76:79], v[152:155], v[172:175], v[76:79]\n"
    "buffer_load_dwordx4 %[vx3], %[srdx], %[sox] offen offset:3072 sc1 lds\n"
    "v_mfma_f32_16x16x32_bf16 v[92:95], v[156:159], v[172:175], v[92:95]\n"
    "s_add_u32 m0, s40, 4096\n"
    "v_mfma_f32_16x16x32_bf16 v[88:91], v[156:159], v[168:171], v[88:91]\n"
    "buffer_load_dwordx4 %[vx4], %[srdx], %[sox] offen offset:0 sc1 lds\n"
    "v_mfma_f32_16x16x32_bf16 v[84:87], v[156:159], v[164:167], v[84:87]\n"
    "v_mfma_f32_16x16x32_bf16 v[80:83], v[156:159], v[160:163], v[80:83]\n"
    "s_waitcnt lgkmcnt(0)\n"
    "buffer_load_dwordx4 %[vx5], %[srdx], %[sox] offen offset:1024 sc1 lds\n"
    "v_mfma_f32_16x16x32_bf16 v[0:3], v[96:99], v[120:123], v[0:3]\n"
    "ds_read_b128 v[136:139], %[rdx] offset:1024\n"
    "v_mfma_f32_16x16x32_bf16 v[4:7], v[96:99], v[124:127], v[4:7]\n"
    "ds_read_b64_tr_b16 v[160:161], %[rdw0] offset:4096\n"
    "v_mfma_f32_16x16x32_bf16 v[8:11], v[96:99], v[128:131], v[8:11]\n"
    "ds_read_b64_tr_b16 v[162:163], %[rdw0] offset:6144\n"
    "v_mfma_f32_16x16x32_bf16 v[12:15], v[96:99], v[132:135], v[12:15]\n"
    "ds_read_b64_tr_b16 v[164:165], %[rdw1] offset:4096\n"
    "v_mfma_f32_16x16x32_bf16 v[28:31], v[100:103], v[132:135], v[28:31]\n"
    "ds_read_b64_tr_b16 v[166:167], %[rdw1] offset:6144\n"
    "v_mfma_f32_16x16x32_bf16 v[24:27], v[100:103], v[128:131], v[24:27]\n"
    "ds_read_b64_tr_b16 v[168:169], %[rdw2] offset:4096\n"
    "v_mfma_f32_16x16x32_bf16 v[20:23], v[100:103], v[124:127], v[20:23]\n"
    "ds_read_b64_tr_b16 v[170:171], %[rdw2] offset:6144\n"
    "v_mfma_f32_16x16x32_bf16 v[16:19], v[100:103], v[120:123], v[16:19]\n"
    "ds_read_b64_tr_b16 v[172:173], %[rdw3] offset:4096\n"
    "v_mfma_f32_16x16x32_bf16 v[32:35], v[104:107], v[120:123], v[32:35]\n"
    "ds_read_b64_tr_b16 v[174:175], %[rdw3] offset:6144\n"
    "v_mfma_f32_16x16x32_bf16 v[36:39], v[104:107], v[124:127], v[36:39]\n"
    "ds_read_b128 v[140:143], %[rdx] offset:3072\n"
    "v_mfma_f32_16x16x32_bf16 v[40:43], v[104:107], v[128:131], v[40:43]\n"
    "ds_read_b128 v[144:147], %[rdx] offset:5120\n"
    "v_mfma_f32_16x16x32_bf16 v[44:47], v[104:107], v[132:135], v[44:47]\n"
    "ds_read_b128 v[148:151], %[rdx] offset:7168\n"
    "v_mfma_f32_16x16x32_bf16 v[60:63], v[108:111], v[132:135], v[60:63]\n"
    "ds_read_b128 v[152:155], %[rdx] offset:9216\n"
    "v_mfma_f32_16x16x32_bf16 v[56:59], v[108:111], v[128:131], v[56:59]\n"
    "ds_read_b128 v[156:159], %[rdx] offset:11264\n"
    "v_mfma_f32_16x16x32_bf16 v[52:55], v[108:111], v[124:127], v[52:55]\n"
    "s_add_u32 m0, s41, 0\n"
    "v_mfma_f32_16x16x32_bf16 v[48:51], v[108:111], v[120:123], v[48:51]\n"
    "s_add_u32 s43, s43, 1\n"
    "buffer_load_dwordx4 %[vw0], %[srdw], %[sow] offen offset:0 lds\n"
    "s_cmp_eq_u32 s43, 3\n"
    "v_mfma_f32_16x16x32_bf16 v[64:67], v[112:115], v[120:123], v[64:67]\n"
    "s_cselect_b32 s46, s47, s48\n"
    "s_cselect_b32 s43, 0, s43\n"
    "v_add_u32 %[rdx], s46, %[rdx]\n"
    "v_mfma_f32_16x16x32_bf16 v[68:71], v[112:115], v[124:127], v[68:71]\n"
    "buffer_load_dwordx4 %[vw1], %[srdw], %[sow] offen offset:1024 lds\n"
    "v_add_u32 %[rdw0], s46, %[rdw0]\n"
    "v_mfma_f32_16x16x32_bf16 v[72:75], v[112:115], v[128:131], v[72:75]\n"
    "v_add_u32 %[rdw1], s46, %[rdw1]\n"
    "v_mfma_f32_16x16x32_bf16 v[76:79], v[112:115], v[132:135], v[76:79]\n"
    "buffer_load_dwordx4 %[vw2], %[srdw], %[sow] offen offset:2048 lds\n"
    "v_mfma_f32_16x16x32_bf16 v[92:95], v[116:119], v[132:135], v[92:95]\n"
    "v_add_u32 %[rdw2], s46, %[rdw2]\n"
    "v_mfma_f32_16x16x32_bf16 v[88:91], v[116:119], v[128:131], v[88:91]\n"
    "buffer_load_dwordx4 %[vw3], %[srdw], %[sow] offen offset:3072 lds\n"
    "v_mfma_f32_16x16x32_bf16 v[84:87], v[116:119], v[124:127], v[84:87]\n"
    "v_add_u32 %[rdw3], s46, %[rdw3]\n"
    "s_sub_u32 %[nmid], %[nmid], 1\n"
    "v_mfma_f32_16x16x32_bf16 v[80:83], v[116:119], v[120:123], v[80:83]\n"
    "s_cmp_lg_u32 %[nmid], 0\n"
    "s_waitcnt lgkmcnt(0) vmcnt(10)\n"
    "s_barrier\n"
    "s_cbranch_scc1 LM_%=\n"
    "v_mfma_f32_16x16x32_bf16 v[0:3], v[136:139], v[160:163], v[0:3]\n"
    "ds_read_b128 v[96:99], %[rdx] offset:0\n"
    "v_mfma_f32_16x16x32_bf16 v[4:7], v[136:139], v[164:167], v[4:7]\n"
    "ds_read_b64_tr_b16 v[120:121], %[rdw0] offset:0\n"
    "v_mfma_f32_16x16x32_bf16 v[8:11], v[136:139], v[168:171], v[8:11]\n"
    "ds_read_b64_tr_b16 v[122:123], %[rdw0] offset:2048\n"
    "v_mfma_f32_16x16x32_bf16 v[12:15], v[136:139], v[172:175], v[12:15]\n"
    "ds_read_b64_tr_b16 v[124:125], %[rdw1] offset:0\n"
    "v_mfma_f32_16x16x32_bf16 v[28:31], v[140:143], v[172:175], v[28:31]\n"
    "ds_read_b64_tr_b16 v[126:127], %[rdw1] offset:2048\n"
    "v_mfma_f32_16x16x32_bf16 v[24:27], v[140:143], v[168:171], v[24:27]\n"
    "ds_read_b64_tr_b16 v[128:129], %[rdw2] offset:0\n"
    "v_mfma_f32_16x16x32_bf16 v[20:23], v[140:143], v[164:167], v[20:23]\n"
    "ds_read_b64_tr_b16 v[130:131], %[rdw2] offset:2048\n"
    "v_mfma_f32_16x16x32_bf16 v[16:19], v[140:143], v[160:163], v[16:19]\n"
    "ds_read_b64_tr_b16 v[132:133], %[rdw3] offset:0\n"
    "v_mfma_f32_16x16x32_bf16 v[32:35], v[144:147], v[160:163], v[32:35]\n"
    "ds_read_b64_tr_b16 v[134:135], %[rdw3] offset:2048\n"
    "v_mfma_f32_16x16x32_bf16 v[36:39], v[144:147], v[164:167], v[36:39]\n"
    "ds_read_b128 v[100:103], %[rdx] offset:2048\n"
    "v_mfma_f32_16x16x32_bf16 v[40:43], v[144:147], v[168:171], v[40:43]\n"
    "ds_read_b128 v[104:107], %[rdx] offset:4096\n"
    "v_mfma_f32_16x16x32_bf16 v[44:47], v[144:147], v[172:175], v[44:47]\n"
    "ds_read_b128 v[108:111], %[rdx] offset:6144\n"
    "v_mfma_f32_16x16x32_bf16 v[60:63], v[148:151], v[172:175], v[60:63]\n"
    "ds_read_b128 v[112:115], %[rdx] offset:8192\n"
    "v_mfma_f32_16x16x32_bf16 v[56:59], v[148:151], v[168:171], v[56:59]\n"
    "ds_read_b128 v[116:119], %[rdx] offset:10240\n"
    "v_mfma_f32_16x16x32_bf16 v[52:55], v[148:151], v[164:167], v[52:55]\n"
    "v_mfma_f32_16x16x32_bf16 v[48:51], v[148:151], v[160:163], v[48:51]\n"
    "v_mfma_f32_16x16x32_bf16 v[64:67], v[152:155], v[160:163], v[64:67]\n"
    "v_mfma_f32_16x16x32_bf16 v[68:71], v[152:155], v[164:167], v[68:71]\n"
    "v_mfma_f32_16x16x32_bf16 v[72:75], v[152:155], v[168:171], v[72:75]\n"
    "v_mfma_f32_16x16x32_bf16 v[76:79], v[152:155], v[172:175], v[76:79]\n"
    "v_mfma_f32_16x16x32_bf16 v[92:95], v[156:159], v[172:175], v[92:95]\n"
    "v_mfma_f32_16x16x32_bf16 v[88:91], v[156:159], v[168:171], v[88:91]\n"
    "v_mfma_f32_16x16x32_bf16 v[84:87], v[156:159], v[164:167], v[84:87]\n"
    "v_mfma_f32_16x16x32_bf16 v[80:83], v[156:159], v[160:163], v[80:83]\n"
    "s_waitcnt lgkmcnt(0)\n"
    "v_mfma_f32_16x16x32_bf16 v[0:3], v[96:99], v[120:123], v[0:3]\n"
    "ds_read_b128 v[136:139], %[rdx] offset:1024\n"
    "v_mfma_f32_16x16x32_bf16 v[4:7], v[96:99], v[124:127], v[4:7]\n"
    "ds_read_b64_tr_b16 v[160:161], %[rdw0] offset:4096\n"
    "v_mfma_f32_16x16x32_bf16 v[8:11], v[96:99], v[128:131], v[8:11]\n"
    "ds_read_b64_tr_b16 v[162:163], %[rdw0] offset:6144\n"
    "v_mfma_f32_16x16x32_bf16 v[12:15], v[96:99], v[132:135], v[12:15]\n"
    "ds_read_b64_tr_b16 v[164:165], %[rdw1] offset:4096\n"
    "v_mfma_f32_16x16x32_bf16 v[28:31], v[100:103], v[132:135], v[28:31]\n"
    "ds_read_b64_tr_b16 v[166:167], %[rdw1] offset:6144\n"
    "v_mfma_f32_16x16x32_bf16 v[24:27], v[100:103], v[128:131], v[24:27]\n"
    "ds_read_b64_tr_b16 v[168:169], %[rdw2] offset:4096\n"
    "v_mfma_f32_16x16x32_bf16 v[20:23], v[100:103], v[124:127], v[20:23]\n"
    "ds_read_b64_tr_b16 v[170:171], %[rdw2] offset:6144\n"
    "v_mfma_f32_16x16x32_bf16 v[16:19], v[100:103], v[120:123], v[16:19]\n"
    "ds_read_b64_tr_b16 v[172:173], %[rdw3] offset:4096\n"
    "v_mfma_f32_16x16x32_bf16 v[32:35], v[104:107], v[120:123], v[32:35]\n"
    "ds_read_b64_tr_b16 v[174:175], %[rdw3] offset:6144\n"
    "v_mfma_f32_16x16x32_bf16 v[36:39], v[104:107], v[124:127], v[36:39]\n"
    "ds_read_b128 v[140:143], %[rdx] offset:3072\n"
    "v_mfma_f32_16x16x32_bf16 v[40:43], v[104:107], v[128:131], v[40:43]\n"
    "ds_read_b128 v[144:147], %[rdx] offset:5120\n"
    "v_mfma_f32_16x16x32_bf16 v[44:47], v[104:107], v[132:135], v[44:47]\n"
    "ds_read_b128 v[148:151], %[rdx] offset:7168\n"
    "v_mfma_f32_16x16x32_bf16 v[60:63], v[108:111], v[132:135], v[60:63]\n"
    "ds_read_b128 v[152:155], %[rdx] offset:9216\n"
    "v_mfma_f32_16x16x32_bf16 v[56:59], v[108:111], v[128:131], v[56:59]\n"
    "ds_read_b128 v[156:159], %[rdx] offset:11264\n"
    "v_mfma_f32_16x16x32_bf16 v[52:55], v[108:111], v[124:127], v[52:55]\n"
    "s_add_u32 s43, s43, 1\n"
    "v_mfma_f32_16x16x32_bf16 v[48:51], v[108:111], v[120:123], v[48:51]\n"
    "s_cmp_eq_u32 s43, 3\n"
    "v_mfma_f32_16x16x32_bf16 v[64:67], v[112:115], v[120:123], v[64:67]\n"
    "s_cselect_b32 s46, s47, s48\n"
    "v_mfma_f32_16x16x32_bf16 v[68:71], v[112:115], v[124:127], v[68:71]\n"
    "s_cselect_b32 s43, 0, s43\n"
    "v_add_u32 %[rdx], s46, %[rdx]\n"
    "v_add_u32 %[rdw0], s46, %[rdw0]\n"
    "v_mfma_f32_16x16x32_bf16 v[72:75], v[112:115], v[128:131], v[72:75]\n"
    "v_add_u32 %[rdw1], s46, %[rdw1]\n"
    "v_mfma_f32_16x16x32_bf16 v[76:79], v[112:115], v[132:135], v[76:79]\n"
    "v_add_u32 %[rdw2], s46, %[rdw2]\n"
    "v_mfma_f32_16x16x32_bf16 v[92:95], v[116:119], v[132:135], v[92:95]\n"
    "v_mfma_f32_16x16x32_bf16 v[88:91], v[116:119], v[128:131], v[88:91]\n"
    "v_add_u32 %[rdw3], s46, %[rdw3]\n"
    "v_mfma_f32_16x16x32_bf16 v[84:87], v[116:119], v[124:127], v[84:87]\n"
    "v_mfma_f32_16x16x32_bf16 v[80:83], v[116:119], v[120:123], v[80:83]\n"
    "s_waitcnt lgkmcnt(0) vmcnt(0)\n"
    "s_barrier\n"
    "v_mfma_f32_16x16x32_bf16 v[0:3], v[136:139], v[160:163], v[0:3]\n"
    "ds_read_b128 v[96:99], %[rdx] offset:0\n"
    "v_mfma_f32_16x16x32_bf16 v[4:7], v[136:139], v[164:167], v[4:7]\n"
    "ds_read_b64_tr_b16 v[120:121], %[rdw0] offset:0\n"
    "v_mfma_f32_16x16x32_bf16 v[8:11], v[136:139], v[168:171], v[8:11]\n"
    "ds_read_b64_tr_b16 v[122:123], %[rdw0] offset:2048\n"
    "v_mfma_f32_16x16x32_bf16 v[12:15], v[136:139], v[172:175], v[12:15]\n"
    "ds_read_b64_tr_b16 v[124:125], %[rdw1] offset:0\n"
    "v_mfma_f32_16x16x32_bf16 v[28:31], v[140:143], v[172:175], v[28:31]\n"
    "ds_read_b64_tr_b16 v[126:127], %[rdw1] offset:2048\n"
    "v_mfma_f32_16x16x32_bf16 v[24:27], v[140:143], v[168:171], v[24:27]\n"
    "ds_read_b64_tr_b16 v[128:129], %[rdw2] offset:0\n"
    "v_mfma_f32_16x16x32_bf16 v[20:23], v[140:143], v[164:167], v[20:23]\n"
    "ds_read_b64_tr_b16 v[130:131], %[rdw2] offset:2048\n"
    "v_mfma_f32_16x16x32_bf16 v[16:19], v[140:143], v[160:163], v[16:19]\n"
    "ds_read_b64_tr_b16 v[132:133], %[rdw3] offset:0\n"
    "v_mfma_f32_16x16x32_bf16 v[32:35], v[144:147], v[160:163], v[32:35]\n"
    "ds_read_b64_tr_b16 v[134:135], %[rdw3] offset:2048\n"
    "v_mfma_f32_16x16x32_bf16 v[36:39], v[144:147], v[164:167], v[36:39]\n"
    "ds_read_b128 v[100:103], %[rdx] offset:2048\n"
    "v_mfma_f32_16x16x32_bf16 v[40:43], v[144:147], v[168:171], v[40:43]\n"
    "ds_read_b128 v[104:107], %[rdx] offset:4096\n"
    "v_mfma_f32_16x16x32_bf16 v[44:47], v[144:147], v[172:175], v[44:47]\n"
    "ds_read_b128 v[108:111], %[rdx] offset:6144\n"
    "v_mfma_f32_16x16x32_bf16 v[60:63], v[148:151], v[172:175], v[60:63]\n"
    "ds_read_b128 v[112:115], %[rdx] offset:8192\n"
    "v_mfma_f32_16x16x32_bf16 v[56:59], v[148:151], v[168:171], v[56:59]\n"
    "ds_read_b128 v[116:119], %[rdx] offset:10240\n"
    "v_mfma_f32_16x16x32_bf16 v[52:55], v[148:151], v[164:167], v[52:55]\n"
    "v_mfma_f32_16x16x32_bf16 v[48:51], v[148:151], v[160:163], v[48:51]\n"
    "v_mfma_f32_16x16x32_bf16 v[64:67], v[152:155], v[160:163], v[64:67]\n"
    "v_mfma_f32_16x16x32_bf16 v[68:71], v[152:155], v[164:167], v[68:71]\n"
    "v_mfma_f32_16x16x32_bf16 v[72:75], v[152:155], v[168:171], v[72:75]\n"
    "v_mfma_f32_16x16x32_bf16 v[76:79], v[152:155], v[172:175], v[76:79]\n"
    "v_mfma_f32_16x16x32_bf16 v[92:95], v[156:159], v[172:175], v[92:95]\n"
    "v_mfma_f32_16x16x32_bf16 v[88:91], v[156:159], v[168:171], v[88:91]\n"
    "v_mfma_f32_16x16x32_bf16 v[84:87], v[156:159], v[164:167], v[84:87]\n"
    "v_mfma_f32_16x16x32_bf16 v[80:83], v[156:159], v[160:163], v[80:83]\n"
    "s_waitcnt lgkmcnt(0)\n"
    "v_mfma_f32_16x16x32_bf16 v[0:3], v[96:99], v[120:123], v[0:3]\n"
    "ds_read_b128 v[136:139], %[rdx] offset:1024\n"
    "v_mfma_f32_16x16x32_bf16 v[4:7], v[96:99], v[124:127], v[4:7]\n"
    "ds_read_b64_tr_b16 v[160:161], %[rdw0] offset:4096\n"
    "v_mfma_f32_16x16x32_bf16 v[8:11], v[96:99], v[128:131], v[8:11]\n"
    "ds_read_b64_tr_b16 v[162:163], %[rdw0] offset:6144\n"
    "v_mfma_f32_16x16x32_bf16 v[12:15], v[96:99], v[132:135], v[12:15]\n"
    "ds_read_b64_tr_b16 v[164:165], %[rdw1] offset:4096\n"
    "v_mfma_f32_16x16x32_bf16 v[28:31], v[100:103], v[132:135], v[28:31]\n"
    "ds_read_b64_tr_b16 v[166:167], %[rdw1] offset:6144\n"
    "v_mfma_f32_16x16x32_bf16 v[24:27], v[100:103], v[128:131], v[24:27]\n"
    "ds_read_b64_tr_b16 v[168:169], %[rdw2] offset:4096\n"
    "v_mfma_f32_16x16x32_bf16 v[20:23], v[100:103], v[124:127], v[20:23]\n"
    "ds_read_b64_tr_b16 v[170:171], %[rdw2] offset:6144\n"
    "v_mfma_f32_16x16x32_bf16 v[16:19], v[100:103], v[120:123], v[16:19]\n"
    "ds_read_b64_tr_b16 v[172:173], %[rdw3] offset:4096\n"
    "v_mfma_f32_16x16x32_bf16 v[32:35], v[104:107], v[120:123], v[32:35]\n"
    "ds_read_b64_tr_b16 v[174:175], %[rdw3] offset:6144\n"
    "v_mfma_f32_16x16x32_bf16 v[36:39], v[104:107], v[124:127], v[36:39]\n"
    "ds_read_b128 v[140:143], %[rdx] offset:3072\n"
    "v_mfma_f32_16x16x32_bf16 v[40:43], v[104:107], v[128:131], v[40:43]\n"
    "ds_read_b128 v[144:147], %[rdx] offset:5120\n"
    "v_mfma_f32_16x16x32_bf16 v[44:47], v[104:107], v[132:135], v[44:47]\n"
    "ds_read_b128 v[148:151], %[rdx] offset:7168\n"
    "v_mfma_f32_16x16x32_bf16 v[60:63], v[108:111], v[132:135], v[60:63]\n"
    "ds_read_b128 v[152:155], %[rdx] offset:9216\n"
    "v_mfma_f32_16x16x32_bf16 v[56:59], v[108:111], v[128:131], v[56:59]\n"
    "ds_read_b128 v[156:159], %[rdx] offset:11264\n"
    "v_mfma_f32_16x16x32_bf16 v[52:55], v[108:111], v[124:127], v[52:55]\n"
    "s_add_u32 s43, s43, 1\n"
    "v_mfma_f32_16x16x32_bf16 v[48:51], v[108:111], v[120:123], v[48:51]\n"
    "s_cmp_eq_u32 s43, 3\n"
    "v_mfma_f32_16x16x32_bf16 v[64:67], v[112:115], v[120:123], v[64:67]\n"
    "s_cselect_b32 s46, s47, s48\n"
    "v_mfma_f32_16x16x32_bf16 v[68:71], v[112:115], v[124:127], v[68:71]\n"
    "s_cselect_b32 s43, 0, s43\n"
    "v_add_u32 %[rdx], s46, %[rdx]\n"
    "v_add_u32 %[rdw0], s46, %[rdw0]\n"
    "v_mfma_f32_16x16x32_bf16 v[72:75], v[112:115], v[128:131], v[72:75]\n"
    "v_add_u32 %[rdw1], s46, %[rdw1]\n"
    "v_mfma_f32_16x16x32_bf16 v[76:79], v[112:115], v[132:135], v[76:79]\n"
    "v_add_u32 %[rdw2], s46, %[rdw2]\n"
    "v_mfma_f32_16x16x32_bf16 v[92:95], v[116:119], v[132:135], v[92:95]\n"
    "v_mfma_f32_16x16x32_bf16 v[88:91], v[116:119], v[128:131], v[88:91]\n"
    "v_add_u32 %[rdw3], s46, %[rdw3]\n"
    "v_mfma_f32_16x16x32_bf16 v[84:87], v[116:119], v[124:127], v[84:87]\n"
    "v_mfma_f32_16x16x32_bf16 v[80:83], v[116:119], v[120:123], v[80:83]\n"
    "s_waitcnt lgkmcnt(0)\n"
    "s_barrier\n"
    "v_mfma_f32_16x16x32_bf16 v[0:3], v[136:139], v[160:163], v[0:3]\n"
    "v_mfma_f32_16x16x32_bf16 v[4:7], v[136:139], v[164:167], v[4:7]\n"
    "v_mfma_f32_16x16x32_bf16 v[8:11], v[136:139], v[168:171], v[8:11]\n"
    "v_mfma_f32_16x16x32_bf16 v[12:15], v[136:139], v[172:175], v[12:15]\n"
    "v_mfma_f32_16x16x32_bf16 v[28:31], v[140:143], v[172:175], v[28:31]\n"
    "v_mfma_f32_16x16x32_bf16 v[24:27], v[140:143], v[168:171], v[24:27]\n"
    "v_mfma_f32_16x16x32_bf16 v[20:23], v[140:143], v[164:167], v[20:23]\n"
    "v_mfma_f32_16x16x32_bf16 v[16:19], v[140:143], v[160:163], v[16:19]\n"
    "v_mfma_f32_16x16x32_bf16 v[32:35], v[144:147], v[160:163], v[32:35]\n"
    "v_mfma_f32_16x16x32_bf16 v[36:39], v[144:147], v[164:167], v[36:39]\n"
    "v_mfma_f32_16x16x32_bf16 v[40:43], v[144:147], v[168:171], v[40:43]\n"
    "v_mfma_f32_16x16x32_bf16 v[44:47], v[144:147], v[172:175], v[44:47]\n"
    "v_cvt_pk_bf16_f32 v0, v0, v1\n"
    "v_cvt_pk_bf16_f32 v1, v2, v3\n"
    "v_mfma_f32_16x16x32_bf16 v[60:63], v[148:151], v[172:175], v[60:63]\n"
    "s_nop 0\n"
    "v_cvt_pk_bf16_f32 v2, v16, v17\n"
    "v_cvt_pk_bf16_f32 v3, v18, v19\n"
    "v_mfma_f32_16x16x32_bf16 v[56:59], v[148:151], v[168:171], v[56:59]\n"
    "s_add_u32 s44, %[soo], 0\n"
    "buffer_store_dwordx4 v[0:3], %[vo], %[srdo], s44 offen sc1\n"
    "v_mfma_f32_16x16x32_bf16 v[52:55], v[148:151], v[164:167], v[52:55]\n"
    "v_cvt_pk_bf16_f32 v4, v4, v5\n"
    "v_cvt_pk_bf16_f32 v5, v6, v7\n"
    "v_mfma_f32_16x16x32_bf16 v[48:51], v[148:151], v[160:163], v[48:51]\n"
    "v_cvt_pk_bf16_f32 v6, v20, v21\n"
    "v_cvt_pk_bf16_f32 v7, v22, v23\n"
    "v_mfma_f32_16x16x32_bf16 v[64:67], v[152:155], v[160:163], v[64:67]\n"
    "s_add_u32 s44, %[soo], 18432\n"
    "buffer_store_dwordx4 v[4:7], %[vo], %[srdo], s44 offen sc1\n"
    "v_mfma_f32_16x16x32_bf16 v[68:71], v[152:155], v[164:167], v[68:71]\n"
    "v_cvt_pk_bf16_f32 v8, v8, v9\n"
    "v_cvt_pk_bf16_f32 v9, v10, v11\n"
    "v_mfma_f32_16x16x32_bf16 v[72:75], v[152:155], v[168:171], v[72:75]\n"
    "v_cvt_pk_bf16_f32 v10, v24, v25\n"
    "v_cvt_pk_bf16_f32 v11, v26, v27\n"
    "v_mfma_f32_16x16x32_bf16 v[76:79], v[152:155], v[172:175], v[76:79]\n"
    "s_add_u32 s44, %[soo], 36864\n"
    "buffer_store_dwordx4 v[8:11], %[vo], %[srdo], s44 offen sc1\n"
    "v_mfma_f32_16x16x32_bf16 v[92:95], v[156:159], v[172:175], v[92:95]\n"
    "v_cvt_pk_bf16_f32 v12, v12, v13\n"
    "v_cvt_pk_bf16_f32 v13, v14, v15\n"
    "v_mfma_f32_16x16x32_bf16 v[88:91], v[156:159], v[168:171], v[88:91]\n"
    "v_cvt_pk_bf16_f32 v14, v28, v29\n"
    "v_cvt_pk_bf16_f32 v15, v30, v31\n"
    "v_mfma_f32_16x16x32_bf16 v[84:87], v[156:159], v[164:167], v[84:87]\n"
    "s_add_u32 s44, %[soo], 55296\n"
    "buffer_store_dwordx4 v[12:15], %[vo], %[srdo], s44 offen sc1\n"
    "v_mfma_f32_16x16x32_bf16 v[80:83], v[156:159], v[160:163], v[80:83]\n"
    "v_cvt_pk_bf16_f32 v32, v32, v33\n"
    "v_cvt_pk_bf16_f32 v33, v34, v35\n"
    "v_cvt_pk_bf16_f32 v34, v48, v49\n"
    "v_cvt_pk_bf16_f32 v35, v50, v51\n"
    "s_add_u32 s44, %[soo], 1024\n"
    "buffer_store_dwordx4 v[32:35], %[vo], %[srdo], s44 offen sc1\n"
    "v_cvt_pk_bf16_f32 v36, v36, v37\n"
    "v_cvt_pk_bf16_f32 v37, v38, v39\n"
    "v_cvt_pk_bf16_f32 v38, v52, v53\n"
    "v_cvt_pk_bf16_f32 v39, v54, v55\n"
    "s_add_u32 s44, %[soo], 19456\n"
    "buffer_store_dwordx4 v[36:39], %[vo], %[srdo], s44 offen sc1\n"
    "v_cvt_pk_bf16_f32 v40, v40, v41\n"
    "v_cvt_pk_bf16_f32 v41, v42, v43\n"
    "v_cvt_pk_bf16_f32 v42, v56, v57\n"
    "v_cvt_pk_bf16_f32 v43, v58, v59\n"
    "s_add_u32 s44, %[soo], 37888\n"
    "buffer_store_dwordx4 v[40:43], %[vo], %[srdo], s44 offen sc1\n"
    "v_cvt_pk_bf16_f32 v44, v44, v45\n"
    "v_cvt_pk_bf16_f32 v45, v46, v47\n"
    "v_cvt_pk_bf16_f32 v46, v60, v61\n"
    "v_cvt_pk_bf16_f32 v47, v62, v63\n"
    "s_add_u32 s44, %[soo], 56320\n"
    "buffer_store_dwordx4 v[44:47], %[vo], %[srdo], s44 offen sc1\n"
    "v_cvt_pk_bf16_f32 v64, v64, v65\n"
    "v_cvt_pk_bf16_f32 v65, v66, v67\n"
    "v_cvt_pk_bf16_f32 v66, v80, v81\n"
    "v_cvt_pk_bf16_f32 v67, v82, v83\n"
    "s_add_u32 s44, %[soo], 2048\n"
    "buffer_store_dwordx4 v[64:67], %[vo], %[srdo], s44 offen sc1\n"
    "v_cvt_pk_bf16_f32 v68, v68, v69\n"
    "v_cvt_pk_bf16_f32 v69, v70, v71\n"
    "v_cvt_pk_bf16_f32 v70, v84, v85\n"
    "v_cvt_pk_bf16_f32 v71, v86, v87\n"
    "s_add_u32 s44, %[soo], 20480\n"
    "buffer_store_dwordx4 v[68:71], %[vo], %[srdo], s44 offen sc1\n"
    "v_cvt_pk_bf16_f32 v72, v72, v73\n"
    "v_cvt_pk_bf16_f32 v73, v74, v75\n"
    "v_cvt_pk_bf16_f32 v74, v88, v89\n"
    "v_cvt_pk_bf16_f32 v75, v90, v91\n"
    "s_add_u32 s44, %[soo], 38912\n"
    "buffer_store_dwordx4 v[72:75], %[vo], %[srdo], s44 offen sc1\n"
    "v_cvt_pk_bf16_f32 v76, v76, v77\n"
    "v_cvt_pk_bf16_f32 v77, v78, v79\n"
    "v_cvt_pk_bf16_f32 v78, v92, v93\n"
    "v_cvt_pk_bf16_f32 v79, v94, v95\n"
    "s_add_u32 s44, %[soo], 57344\n"
    "buffer_store_dwordx4 v[76:79], %[vo], %[srdo], s44 offen sc1\n"
    "GE_%=:\n"
    : [rdx]"+&v"(rdx), [rdw0]"+&v"(rdw[0]), [rdw1]"+&v"(rdw[1]), [rdw2]"+&v"(rdw[2]), [rdw3]"+&v"(rdw[3]), [sox]"+&s"(sox), [sow]"+&s"(sow), [nmid]"+&s"(nmid)
    : [vw0]"v"(vw[0]), [vw1]"v"(vw[1]), [vw2]"v"(vw[2]), [vw3]"v"(vw[3]), [srdw]"s"(srdw), [dw]"s"(dw), [m0w]"s"(m0w), [vx0]"v"(vx[0]), [vx1]"v"(vx[1]), [vx2]"v"(vx[2]), [vx3]"v"(vx[3]), [vx4]"v"(vx[4]), [vx5]"v"(vx[5]), [srdx]"s"(srdx), [srdo]"s"(srdo), [m0x]"s"(m0x), [vo]"v"(vo), [soo]"s"(soo)
    : "memory", "scc", "s40","s41","s42","s43","s44","s45","s46","s47","s48","s49","s50","s51","s52","s53", "v0","v1","v2","v3","v4","v5","v6","v7","v8","v9","v10","v11","v12","v13","v14","v15","v16","v17","v18","v19","v20","v21","v22","v23","v24","v25","v26","v27","v28","v29","v30","v31","v32","v33","v34","v35","v36","v37","v38","v39","v40","v41","v42","v43","v44","v45","v46","v47","v48","v49","v50","v51","v52","v53","v54","v55","v56","v57","v58","v59","v60","v61","v62","v63","v64","v65","v66","v67","v68","v69","v70","v71","v72","v73","v74","v75","v76","v77","v78","v79","v80","v81","v82","v83","v84","v85","v86","v87","v88","v89","v90","v91","v92","v93","v94","v95","v96","v97","v98","v99","v100","v101","v102","v103","v104","v105","v106","v107","v108","v109","v110","v111","v112","v113","v114","v115","v116","v117","v118","v119","v120","v121","v122","v123","v124","v125","v126","v127","v128","v129","v130","v131","v132","v133","v134","v135","v136","v137","v138","v139","v140","v141","v142","v143","v144","v145","v146","v147","v148","v149","v150","v151","v152","v153","v154","v155","v156","v157","v158","v159","v160","v161","v162","v163","v164","v165","v166","v167","v168","v169","v170","v171","v172","v173","v174","v175","v176","v177","v178","v179","v180","v181","v182","v183");
  }
  {
    const u32 tb = b >> 1, c = lane & 15, g = lane >> 4, tok = tb * 16 + c; u32x4 xh[3][2]; f32x4 xa[3][2]; float gm[3][8]; const float bb = bf2f(b2[tok]);
    const u32x4 selt = one_hot(g == (c >> 2), 4 * (tb & 1) + (c & 3));
#pragma unroll
    for (int r = 0; r < 3; r++) { const u32 dg = (b & 1) * 9 + (r < 2 ? wid + 4 * r : 8), d0 = dg * 32 + g * 8;
#pragma unroll
      for (int h = 0; h < 2; h++) { xh[r][h] = ldb16(srdws, vo, (u32)WS_X1 + ((2 * dg + h) * 64 + (tb >> 1)) * 1024);
        const f32x4 s = *(const f32x4*)(modp + 5 * M + d0 + 4 * h) + *(const f32x4*)(modp + 3456 + 5 * M + d0 + 4 * h) + *(const f32x4*)(modp + 2 * 3456 + 5 * M + d0 + 4 * h);
#pragma unroll
        for (int e = 0; e < 4; e++) gm[r][4 * h + e] = rbf(s[e]); } }
    publish<0>(fl + 8 * FARR + b * FSTR, tag, tid);
    DBGT(14);
#pragma unroll
    for (int r = 0; r < 3; r++)
#pragma unroll
      for (int h = 0; h < 2; h++) { xa[r][h] = mfma16(xh[r][h], selt, f32x4{});
#pragma unroll
        for (int e = 0; e < 4; e++) xa[r][h][e] += gm[r][4 * h + e] * bb;
        asm volatile("" : "+v"(xa[r][h])); }
    { const u32 k = lane < 10 ? lane : 0, cmb = (k >> 1) * 16 + (tb >> 3);
      await<1>(fl + 8 * FARR, (cmb / 10) + 8 * ((cmb % 10) * 3 + (b & 1) + (k & 1)), tag, wid); }
    DBGT(15);
    u32x4 p[3][5];
#pragma unroll
    for (int r = 0; r < 3; r++) { const u32 dg = (b & 1) * 9 + (r < 2 ? wid + 4 * r : 8);
#pragma unroll
      for (int s = 0; s < 5; s++) p[r][s] = ldb16(srdws, r < 2 || wid == 0 ? vo : 0x80000000u, (u32)WS_P6 + ((s * 128 + tb) * 18 + dg) * 1024); }
#pragma unroll
    for (int r = 0; r < 3; r++) { const u32 dg = (b & 1) * 9 + (r < 2 ? wid + 4 * r : 8), d0 = dg * 32 + g * 8; f32x4 a0 = {}, a1 = {};
#pragma unroll
      for (int s = 0; s < 5; s++) { a0 = mfma16(sel0, p[r][s], a0); a1 = mfma16(sel1, p[r][s], a1); }
#pragma unroll
      for (int e = 0; e < 4; e++) { a0[e] = xa[r][0][e] + gm[r][e] * a0[e]; a1[e] = xa[r][1][e] + gm[r][4 + e] * a1[e]; }
      const u32x4 ov = {pk2(a0[0], a0[1]), pk2(a0[2], a0[3]), pk2(a1[0], a1[1]), pk2(a1[2], a1[3])};
      if (r < 2 || wid == 0) st16(out + tok * M + d0, ov); }
  }
  asm volatile("TE9002:");
  STAMP_END(2);
}

__global__ __launch_bounds__(256, 1) void k_kv(const char* __restrict__ ckv, const char* __restrict__ wuk, const char* __restrict__ wuv, char* __restrict__ kvb,
                                               const u16* __restrict__ x, const u16* __restrict__ cnd, const u16* __restrict__ wm, float* __restrict__ modp, char* __restrict__ xb,
                                               const char* __restrict__ wq) {
  STAMP_BEGIN();
  asm volatile("" :: "s"(ckv), "s"(wuk), "s"(wuv), "s"(kvb), "s"(x), "s"(cnd), "s"(wm), "s"(modp), "s"(xb), "s"(wq));
  __shared__ __attribute__((aligned(1024))) char lds[163840];
  const u32 lb = (u32)(uintptr_t)(__attribute__((address_space(3))) char*)lds;
  const u32 b = blockIdx.x, j = b >> 3, nt = (b & 7) * 4 + (j >> 3), tt = j & 7, isv = nt >> 4, rot = 0;
  const u32 tid = threadIdx.x, lane = tid & 63, wid = __builtin_amdgcn_readfirstlane(tid >> 6);
  const __amdgpu_buffer_rsrc_t srdx = __builtin_amdgcn_make_buffer_rsrc((void*)(ckv - 4096), 0, S * DC * 2 + 4096, 0x00020000), srdw = __builtin_amdgcn_make_buffer_rsrc((void*)(wuk + (wuv - wuk) * (long)isv - 4096), 0, DC * HD * 2 + 4096, 0x00020000);
  u32 vx[4], vw[4], rdx[4], rdw[8];
  { const u32 r = lane >> 3, c = lane & 7;
#pragma unroll
    for (int p = 0; p < 4; p++) { vx[p] = 4096 - p * 1024 + (8 * (4 * wid + p) + r) * 1024 + (c ^ (4 * (p & 1) + (r >> 1))) * 16;
      vw[p] = 4096 - p * 1024 + (8 * p + r) * (HD * 2) + wid * 128 + (c ^ ((((r >> 1) & 1) | ((p & 1) << 1)) << 1)) * 16; } }
  const u32 m0x = lb + wid * 4096, m0w = lb + 16384 + wid * 8192, sox = tt * 256 * 1024, sow = (nt & 15) * 512;
  const u32 row = b * 8 + wid * 2 + (lane >> 5), l5 = lane & 31, OOB = 0x80000000u, kq = (b * 1214) >> 16, cg = b - kq * 54, gm = (161 - b) & OOB;
  const u32 vxr = row * (M * 2) + l5 * 16, vxr2 = (vxr + 1024) | ((7 - l5) & OOB);
  const u32 vxs = ((row >> 4) * 18 + (l5 >> 2)) * 1024 + ((row & 15) + 16 * (l5 & 3)) * 16, vxs2 = (vxs + 16384) | ((7 - l5) & OOB);
  const u32 vwm = ((kq * 192 + (tid >> 3) * 6) * 6912 + cg * 128 + (tid & 7) * 16) | gm;
  const u32 vcn = ((kq * 192 + (tid >> 3) * 6) * 2) | gm;
  const u32 lbr = lb + 147456, vred = lbr + wid * 256 + ((lane & 7) * 8 + (lane & 8) / 2 + (lane & 16) / 8 + (lane >> 5)) * 4, vmo = ((kq * 3456 + cg * 64 + lane) * 4) | gm | ((0 - wid) & OOB);
  const __amdgpu_buffer_rsrc_t srdwm = __builtin_amdgcn_make_buffer_rsrc((void*)wm, 0, M * 3456 * 2, 0x00020000), srdc = __builtin_amdgcn_make_buffer_rsrc((void*)cnd, 0, M * 2, 0x00020000), srdxx = __builtin_amdgcn_make_buffer_rsrc((void*)x, 0, S * M * 2, 0x00020000),
                                srdxb = __builtin_amdgcn_make_buffer_rsrc((void*)xb, 0, 128 * 18 * 1024, 0x00020000), srdmo = __builtin_amdgcn_make_buffer_rsrc((void*)modp, 0, 3 * 3456 * 4, 0x00020000);
  const u32 vo = lane * 16;
  asm volatile(
    "s_nop 4\n"
    "KH90001:\n"
    "s_getpc_b64 s[56:57]\n"
    "CU_%=:\n"
    "s_add_u32 s56, s56, KH90001-CU_%=\n"
    "s_addc_u32 s57, s57, -1\n"
    "s_mul_i32 s44, 0, 8192\n"
    "v_lshl_add_u32 v186, %[vo], 3, s44\n"
    "v_min_u32 v187, KK90001-KH90001-4, v186\n"
    "global_load_dword v188, v187, s[56:57] ; dummy\n"
    "s_sub_u32 s42, %[rot], 1\n"
    "s_add_u32 s42, s42, 1\n"
    "s_and_b32 s42, s42, 7\n"
    "s_lshl_b32 s43, s42, 7\n"
    "s_add_u32 s40, s43, %[sox]\n"
    "s_lshl_b32 s43, s42, 19\n"
    "s_add_u32 s41, s43, %[sow]\n"
    "s_add_u32 s47, s41, 262144\n"
    "buffer_load_dwordx3 v[170:172], %[vcn], %[srdc], 0 offen\n"
    "s_mov_b32 s48, 0\n"
    "buffer_load_dwordx4 v[140:143], %[vwm], %[srdwm], s48 offen nt\n"
    "s_mov_b32 s48, 6912\n"
    "buffer_load_dwordx4 v[144:147], %[vwm], %[srdwm], s48 offen nt\n"
    "s_mov_b32 s48, 13824\n"
    "buffer_load_dwordx4 v[148:151], %[vwm], %[srdwm], s48 offen nt\n"
    "s_mov_b32 s48, 20736\n"
    "buffer_load_dwordx4 v[152:155], %[vwm], %[srdwm], s48 offen nt\n"
    "s_mov_b32 s48, 27648\n"
    "buffer_load_dwordx4 v[156:159], %[vwm], %[srdwm], s48 offen nt\n"
    "s_mov_b32 s48, 34560\n"
    "buffer_load_dwordx4 v[160:163], %[vwm], %[srdwm], s48 offen nt\n"
    "buffer_load_dwordx4 v[128:131], %[vxr], %[srdxx], 0 offen offset:0\n"
    "buffer_load_dwordx4 v[132:135], %[vxr], %[srdxx], 0 offen offset:512\n"
    "buffer_load_dwordx4 v[136:139], %[vxr2], %[srdxx], 0 offen offset:0\n"
    "s_mul_i32 s48, %[isv], 5\n"
    "s_lshr_b32 s48, s48, 1\n"
    "s_add_u32 s48, s48, %[wid]\n"
    "s_getpc_b64 s[56:57]\n"
    "CT_%=:\n"
    "s_add_u32 s56, s56, KK90001-CT_%=\n"
    "s_addc_u32 s57, s57, 0\n"
    "s_mul_i32 s44, s48, 8192\n"
    "v_lshl_add_u32 v189, %[vo], 3, s44\n"
    "v_min_u32 v190, KE90001-KK90001-4, v189\n"
    "global_load_dword v191, v190, s[56:57] ; dummy\n"
    "s_add_u32 m0, %[m0x], 0\n"
    "s_nop 0\n"
    "buffer_load_dwordx4 %[vx0], %[srdx], s40 offen offset:0 lds\n"
    "buffer_load_dwordx4 %[vx1], %[srdx], s40 offen offset:1024 lds\n"
    "buffer_load_dwordx4 %[vx2], %[srdx], s40 offen offset:2048 lds\n"
    "buffer_load_dwordx4 %[vx3], %[srdx], s40 offen offset:3072 lds\n"
    "s_add_u32 m0, %[m0w], 0\n"
    "s_nop 0\n"
    "buffer_load_dwordx4 %[vw0], %[srdw], s41 offen offset:0 nt lds\n"
    "buffer_load_dwordx4 %[vw1], %[srdw], s41 offen offset:1024 nt lds\n"
    "buffer_load_dwordx4 %[vw2], %[srdw], s41 offen offset:2048 nt lds\n"
    "buffer_load_dwordx4 %[vw3], %[srdw], s41 offen offset:3072 nt lds\n"
    "s_add_u32 m0, %[m0w], 4096\n"
    "s_nop 0\n"
    "buffer_load_dwordx4 %[vw0], %[srdw], s47 offen offset:0 nt lds\n"
    "buffer_load_dwordx4 %[vw1], %[srdw], s47 offen offset:1024 nt lds\n"
    "buffer_load_dwordx4 %[vw2], %[srdw], s47 offen offset:2048 nt lds\n"
    "buffer_load_dwordx4 %[vw3], %[srdw], s47 offen offset:3072 nt lds\n"
    "s_waitcnt vmcnt(13)\n"
    "v_lshlrev_b32 v164, 16, v170\n"
    "v_mul_f32 v182, 0xbfb8aa3b, v164\n"
    "v_exp_f32 v182, v182\n"
    "s_nop 0\n"
    "v_add_f32 v182, 1.0, v182\n"
    "v_rcp_f32 v182, v182\n"
    "s_nop 0\n"
    "v_mul_f32 v164, v164, v182\n"
    "v_cvt_pk_bf16_f32 v164, v164, v164\n"
    "v_and_b32 v164, 0xffff0000, v164\n"
    "v_lshlrev_b32 v183, 16, v140\n"
    "v_and_b32 v140, 0xffff0000, v140\n"
    "v_mul_f32 v174, v164, v183\n"
    "v_mul_f32 v175, v164, v140\n"
    "v_lshlrev_b32 v183, 16, v141\n"
    "v_and_b32 v141, 0xffff0000, v141\n"
    "v_mul_f32 v176, v164, v183\n"
    "v_mul_f32 v177, v164, v141\n"
    "v_lshlrev_b32 v183, 16, v142\n"
    "v_and_b32 v142, 0xffff0000, v142\n"
    "v_mul_f32 v178, v164, v183\n"
    "v_mul_f32 v179, v164, v142\n"
    "v_lshlrev_b32 v183, 16, v143\n"
    "v_and_b32 v143, 0xffff0000, v143\n"
    "v_mul_f32 v180, v164, v183\n"
    "v_mul_f32 v181, v164, v143\n"
    "v_and_b32 v165, 0xffff0000, v170\n"
    "v_mul_f32 v182, 0xbfb8aa3b, v165\n"
    "v_exp_f32 v182, v182\n"
    "s_nop 0\n"
    "v_add_f32 v182, 1.0, v182\n"
    "v_rcp_f32 v182, v182\n"
    "s_nop 0\n"
    "v_mul_f32 v165, v165, v182\n"
    "v_cvt_pk_bf16_f32 v165, v165, v165\n"
    "v_and_b32 v165, 0xffff0000, v165\n"
    "v_lshlrev_b32 v183, 16, v144\n"
    "v_and_b32 v144, 0xffff0000, v144\n"
    "v_fmac_f32 v174, v165, v183\n"
    "v_fmac_f32 v175, v165, v144\n"
    "v_lshlrev_b32 v183, 16, v145\n"
    "v_and_b32 v145, 0xffff0000, v145\n"
    "v_fmac_f32 v176, v165, v183\n"
    "v_fmac_f32 v177, v165, v145\n"
    "v_lshlrev_b32 v183, 16, v146\n"
    "v_and_b32 v146, 0xffff0000, v146\n"
    "v_fmac_f32 v178, v165, v183\n"
    "v_fmac_f32 v179, v165, v146\n"
    "v_lshlrev_b32 v183, 16, v147\n"
    "v_and_b32 v147, 0xffff0000, v147\n"
    "v_fmac_f32 v180, v165, v183\n"
    "v_fmac_f32 v181, v165, v147\n"
    "v_lshlrev_b32 v166, 16, v171\n"
    "v_mul_f32 v182, 0xbfb8aa3b, v166\n"
    "v_exp_f32 v182, v182\n"
    "s_nop 0\n"
    "v_add_f32 v182, 1.0, v182\n"
    "v_rcp_f32 v182, v182\n"
    "s_nop 0\n"
    "v_mul_f32 v166, v166, v182\n"
    "v_cvt_pk_bf16_f32 v166, v166, v166\n"
    "v_and_b32 v166, 0xffff0000, v166\n"
    "v_lshlrev_b32 v183, 16, v148\n"
    "v_and_b32 v148, 0xffff0000, v148\n"
    "v_fmac_f32 v174, v166, v183\n"
    "v_fmac_f32 v175, v166, v148\n"
    "v_lshlrev_b32 v183, 16, v149\n"
    "v_and_b32 v149, 0xffff0000, v149\n"
    "v_fmac_f32 v176, v166, v183\n"
    "v_fmac_f32 v177, v166, v149\n"
    "v_lshlrev_b32 v183, 16, v150\n"
    "v_and_b32 v150, 0xffff0000, v150\n"
    "v_fmac_f32 v178, v166, v183\n"
    "v_fmac_f32 v179, v166, v150\n"
    "v_lshlrev_b32 v183, 16, v151\n"
    "v_and_b32 v151, 0xffff0000, v151\n"
    "v_fmac_f32 v180, v166, v183\n"
    "v_fmac_f32 v181, v166, v151\n"
    "v_and_b32 v167, 0xffff0000, v171\n"
    "v_mul_f32 v182, 0xbfb8aa3b, v167\n"
    "v_exp_f32 v182, v182\n"
    "s_nop 0\n"
    "v_add_f32 v182, 1.0, v182\n"
    "v_rcp_f32 v182, v182\n"
    "s_nop 0\n"
    "v_mul_f32 v167, v167, v182\n"
    "v_cvt_pk_bf16_f32 v167, v167, v167\n"
    "v_and_b32 v167, 0xffff0000, v167\n"
    "v_lshlrev_b32 v183, 16, v152\n"
    "v_and_b32 v152, 0xffff0000, v152\n"
    "v_fmac_f32 v174, v167, v183\n"
    "v_fmac_f32 v175, v167, v152\n"
    "v_lshlrev_b32 v183, 16, v153\n"
    "v_and_b32 v153, 0xffff0000, v153\n"
    "v_fmac_f32 v176, v167, v183\n"
    "v_fmac_f32 v177, v167, v153\n"
    "v_lshlrev_b32 v183, 16, v154\n"
    "v_and_b32 v154, 0xffff0000, v154\n"
    "v_fmac_f32 v178, v167, v183\n"
    "v_fmac_f32 v179, v167, v154\n"
    "v_lshlrev_b32 v183, 16, v155\n"
    "v_and_b32 v155, 0xffff0000, v155\n"
    "v_fmac_f32 v180, v167, v183\n"
    "v_fmac_f32 v181, v167, v155\n"
    "v_lshlrev_b32 v168, 16, v172\n"
    "v_mul_f32 v182, 0xbfb8aa3b, v168\n"
    "v_exp_f32 v182, v182\n"
    "s_nop 0\n"
    "v_add_f32 v182, 1.0, v182\n"
    "v_rcp_f32 v182, v182\n"
    "s_nop 0\n"
    "v_mul_f32 v168, v168, v182\n"
    "v_cvt_pk_bf16_f32 v168, v168, v168\n"
    "v_and_b32 v168, 0xffff0000, v168\n"
    "v_lshlrev_b32 v183, 16, v156\n"
    "v_and_b32 v156, 0xffff0000, v156\n"
    "v_fmac_f32 v174, v168, v183\n"
    "v_fmac_f32 v175, v168, v156\n"
    "v_lshlrev_b32 v183, 16, v157\n"
    "v_and_b32 v157, 0xffff0000, v157\n"
    "v_fmac_f32 v176, v168, v183\n"
    "v_fmac_f32 v177, v168, v157\n"
    "v_lshlrev_b32 v183, 16, v158\n"
    "v_and_b32 v158, 0xffff0000, v158\n"
    "v_fmac_f32 v178, v168, v183\n"
    "v_fmac_f32 v179, v168, v158\n"
    "v_lshlrev_b32 v183, 16, v159\n"
    "v_and_b32 v159, 0xffff0000, v159\n"
    "v_fmac_f32 v180, v168, v183\n"
    "v_fmac_f32 v181, v168, v159\n"
    "v_and_b32 v169, 0xffff0000, v172\n"
    "v_mul_f32 v182, 0xbfb8aa3b, v169\n"
    "v_exp_f32 v182, v182\n"
    "s_nop 0\n"
    "v_add_f32 v182, 1.0, v182\n"
    "v_rcp_f32 v182, v182\n"
    "s_nop 0\n"
    "v_mul_f32 v169, v169, v182\n"
    "v_cvt_pk_bf16_f32 v169, v169, v169\n"
    "v_and_b32 v169, 0xffff0000, v169\n"
    "v_lshlrev_b32 v183, 16, v160\n"
    "v_and_b32 v160, 0xffff0000, v160\n"
    "v_fmac_f32 v174, v169, v183\n"
    "v_fmac_f32 v175, v169, v160\n"
    "v_lshlrev_b32 v183, 16, v161\n"
    "v_and_b32 v161, 0xffff0000, v161\n"
    "v_fmac_f32 v176, v169, v183\n"
    "v_fmac_f32 v177, v169, v161\n"
    "v_lshlrev_b32 v183, 16, v162\n"
    "v_and_b32 v162, 0xffff0000, v162\n"
    "v_fmac_f32 v178, v169, v183\n"
    "v_fmac_f32 v179, v169, v162\n"
    "v_lshlrev_b32 v183, 16, v163\n"
    "v_and_b32 v163, 0xffff0000, v163\n"
    "v_fmac_f32 v180, v169, v183\n"
    "v_fmac_f32 v181, v169, v163\n"
    "v_permlane32_swap_b32 v174, v175\n"
    "v_permlane32_swap_b32 v176, v177\n"
    "v_permlane32_swap_b32 v178, v179\n"
    "v_permlane32_swap_b32 v180, v181\n"
    "v_add_f32 v174, v174, v175\n"
    "v_add_f32 v176, v176, v177\n"
    "v_add_f32 v178, v178, v179\n"
    "v_add_f32 v180, v180, v181\n"
    "v_permlane16_swap_b32 v174, v176\n"
    "s_nop 0\n"
    "v_permlane16_swap_b32 v178, v180\n"
    "v_add_f32 v174, v174, v176\n"
    "v_add_f32 v178, v178, v180\n"
    "v_and_b32 v182, 128, %[vo]\n"
    "v_cmp_ne_u32 vcc, 0, v182\n"
    "v_add_f32_dpp v175, v174, v174 row_ror:8 row_mask:0xf bank_mask:0xf\n"
    "v_add_f32_dpp v179, v178, v178 row_ror:8 row_mask:0xf bank_mask:0xf\n"
    "v_cndmask_b32 v174, v175, v179, vcc\n"
    "v_lshrrev_b32 v182, 2, %[vo]\n"
    "v_add_u32 v182, %[lb], v182\n"
    "ds_write_b32 %[vred], v174\n"
    "s_add_u32 s42, s42, 1\n"
    "s_and_b32 s42, s42, 7\n"
    "s_lshl_b32 s43, s42, 7\n"
    "s_add_u32 s40, s43, %[sox]\n"
    "s_lshl_b32 s43, s42, 19\n"
    "s_add_u32 s41, s43, %[sow]\n"
    "s_add_u32 s47, s41, 262144\n"
    "s_add_u32 m0, %[m0x], 49152\n"
    "v_lshlrev_b32 v140, 16, v128\n"
    "buffer_load_dwordx4 %[vx0], %[srdx], s40 offen offset:0 lds\n"
    "v_and_b32 v141, 0xffff0000, v128\n"
    "v_lshlrev_b32 v142, 16, v129\n"
    "v_and_b32 v143, 0xffff0000, v129\n"
    "v_lshlrev_b32 v144, 16, v130\n"
    "v_and_b32 v145, 0xffff0000, v130\n"
    "v_lshlrev_b32 v146, 16, v131\n"
    "v_and_b32 v147, 0xffff0000, v131\n"
    "v_lshlrev_b32 v148, 16, v132\n"
    "v_and_b32 v149, 0xffff0000, v132\n"
    "v_lshlrev_b32 v150, 16, v133\n"
    "v_and_b32 v151, 0xffff0000, v133\n"
    "buffer_load_dwordx4 %[vx1], %[srdx], s40 offen offset:1024 lds\n"
    "v_lshlrev_b32 v152, 16, v134\n"
    "v_and_b32 v153, 0xffff0000, v134\n"
    "v_lshlrev_b32 v154, 16, v135\n"
    "v_and_b32 v155, 0xffff0000, v135\n"
    "v_lshlrev_b32 v156, 16, v136\n"
    "v_and_b32 v157, 0xffff0000, v136\n"
    "v_lshlrev_b32 v158, 16, v137\n"
    "v_and_b32 v159, 0xffff0000, v137\n"
    "v_lshlrev_b32 v160, 16, v138\n"
    "v_and_b32 v161, 0xffff0000, v138\n"
    "v_lshlrev_b32 v162, 16, v139\n"
    "buffer_load_dwordx4 %[vx2], %[srdx], s40 offen offset:2048 lds\n"
    "v_and_b32 v163, 0xffff0000, v139\n"
    "v_add_f32 v184, v140, v141\n"
    "v_mul_f32 v185, v140, v140\n"
    "v_fmac_f32 v185, v141, v141\n"
    "v_add_f32 v184, v184, v142\n"
    "v_fmac_f32 v185, v142, v142\n"
    "v_add_f32 v184, v184, v143\n"
    "v_fmac_f32 v185, v143, v143\n"
    "v_add_f32 v184, v184, v144\n"
    "v_fmac_f32 v185, v144, v144\n"
    "buffer_load_dwordx4 %[vx3], %[srdx], s40 offen offset:3072 lds\n"
    "v_add_f32 v184, v184, v145\n"
    "v_fmac_f32 v185, v145, v145\n"
    "v_add_f32 v184, v184, v146\n"
    "v_fmac_f32 v185, v146, v146\n"
    "v_add_f32 v184, v184, v147\n"
    "v_fmac_f32 v185, v147, v147\n"
    "v_add_f32 v184, v184, v148\n"
    "v_fmac_f32 v185, v148, v148\n"
    "v_add_f32 v184, v184, v149\n"
    "v_fmac_f32 v185, v149, v149\n"
    "s_add_u32 m0, %[m0w], 49152\n"
    "v_add_f32 v184, v184, v150\n"
    "buffer_load_dwordx4 %[vw0], %[srdw], s41 offen offset:0 nt lds\n"
    "v_fmac_f32 v185, v150, v150\n"
    "v_add_f32 v184, v184, v151\n"
    "v_fmac_f32 v185, v151, v151\n"
    "v_add_f32 v184, v184, v152\n"
    "v_fmac_f32 v185, v152, v152\n"
    "v_add_f32 v184, v184, v153\n"
    "v_fmac_f32 v185, v153, v153\n"
    "v_add_f32 v184, v184, v154\n"
    "v_fmac_f32 v185, v154, v154\n"
    "v_add_f32 v184, v184, v155\n"
    "v_fmac_f32 v185, v155, v155\n"
    "buffer_load_dwordx4 %[vw1], %[srdw], s41 offen offset:1024 nt lds\n"
    "v_add_f32 v184, v184, v156\n"
    "v_fmac_f32 v185, v156, v156\n"
    "v_add_f32 v184, v184, v157\n"
    "v_fmac_f32 v185, v157, v157\n"
    "v_add_f32 v184, v184, v158\n"
    "v_fmac_f32 v185, v158, v158\n"
    "v_add_f32 v184, v184, v159\n"
    "v_fmac_f32 v185, v159, v159\n"
    "v_add_f32 v184, v184, v160\n"
    "v_fmac_f32 v185, v160, v160\n"
    "buffer_load_dwordx4 %[vw2], %[srdw], s41 offen offset:2048 nt lds\n"
    "v_add_f32 v184, v184, v161\n"
    "v_fmac_f32 v185, v161, v161\n"
    "v_add_f32 v184, v184, v162\n"
    "v_fmac_f32 v185, v162, v162\n"
    "v_add_f32 v184, v184, v163\n"
    "v_fmac_f32 v185, v163, v163\n"
    "s_nop 0\n"
    "v_add_f32_dpp v184, v184, v184 quad_perm:[1,0,3,2] row_mask:0xf bank_mask:0xf\n"
    "v_add_f32_dpp v185, v185, v185 quad_perm:[1,0,3,2] row_mask:0xf bank_mask:0xf\n"
    "s_nop 0\n"
    "v_add_f32_dpp v184, v184, v184 quad_perm:[2,3,0,1] row_mask:0xf bank_mask:0xf\n"
    "v_add_f32_dpp v185, v185, v185 quad_perm:[2,3,0,1] row_mask:0xf bank_mask:0xf\n"
    "s_nop 0\n"
    "v_add_f32_dpp v184, v184, v184 row_half_mirror row_mask:0xf bank_mask:0xf\n"
    "buffer_load_dwordx4 %[vw3], %[srdw], s41 offen offset:3072 nt lds\n"
    "v_add_f32_dpp v185, v185, v185 row_half_mirror row_mask:0xf bank_mask:0xf\n"
    "v_add_f32_dpp v184, v184, v184 row_mirror row_mask:0xf bank_mask:0xf\n"
    "s_nop 0\n"
    "v_add_f32_dpp v185, v185, v185 row_mirror row_mask:0xf bank_mask:0xf\n"
    "v_mov_b32 v186, v184\n"
    "v_mov_b32 v187, v185\n"
    "s_nop 0\n"
    "v_permlane16_swap_b32 v184, v186\n"
    "v_permlane16_swap_b32 v185, v187\n"
    "v_add_f32 v184, v184, v186\n"
    "v_add_f32 v185, v185, v187\n"
    "v_mul_f32 v184, 0x3ae38e39, v184\n"
    "s_add_u32 m0, %[m0w], 53248\n"
    "v_mul_f32 v185, 0x3ae38e39, v185\n"
    "buffer_load_dwordx4 %[vw0], %[srdw], s47 offen offset:0 nt lds\n"
    "v_fma_f32 v185, -v184, v184, v185\n"
    "v_max_f32 v185, 0, v185\n"
    "v_add_f32 v185, 0x3727c5ac, v185\n"
    "v_rsq_f32 v185, v185\n"
    "s_nop 0\n"
    "v_mul_f32 v184, v184, v185\n"
    "v_fma_f32 v140, v140, v185, -v184\n"
    "v_fma_f32 v141, v141, v185, -v184\n"
    "v_fma_f32 v142, v142, v185, -v184\n"
    "v_fma_f32 v143, v143, v185, -v184\n"
    "v_fma_f32 v144, v144, v185, -v184\n"
    "buffer_load_dwordx4 %[vw1], %[srdw], s47 offen offset:1024 nt lds\n"
    "v_fma_f32 v145, v145, v185, -v184\n"
    "v_fma_f32 v146, v146, v185, -v184\n"
    "v_fma_f32 v147, v147, v185, -v184\n"
    "v_cvt_pk_f16_f32 v128, v140, v141\n"
    "v_cvt_pk_f16_f32 v129, v142, v143\n"
    "v_cvt_pk_f16_f32 v130, v144, v145\n"
    "v_cvt_pk_f16_f32 v131, v146, v147\n"
    "v_fma_f32 v148, v148, v185, -v184\n"
    "v_fma_f32 v149, v149, v185, -v184\n"
    "v_fma_f32 v150, v150, v185, -v184\n"
    "v_fma_f32 v151, v151, v185, -v184\n"
    "buffer_load_dwordx4 %[vw2], %[srdw], s47 offen offset:2048 nt lds\n"
    "v_fma_f32 v152, v152, v185, -v184\n"
    "v_fma_f32 v153, v153, v185, -v184\n"
    "v_fma_f32 v154, v154, v185, -v184\n"
    "v_fma_f32 v155, v155, v185, -v184\n"
    "v_cvt_pk_f16_f32 v132, v148, v149\n"
    "v_cvt_pk_f16_f32 v133, v150, v151\n"
    "v_cvt_pk_f16_f32 v134, v152, v153\n"
    "v_cvt_pk_f16_f32 v135, v154, v155\n"
    "v_fma_f32 v156, v156, v185, -v184\n"
    "v_fma_f32 v157, v157, v185, -v184\n"
    "v_fma_f32 v158, v158, v185, -v184\n"
    "buffer_load_dwordx4 %[vw3], %[srdw], s47 offen offset:3072 nt lds\n"
    "v_fma_f32 v159, v159, v185, -v184\n"
    "v_fma_f32 v160, v160, v185, -v184\n"
    "v_fma_f32 v161, v161, v185, -v184\n"
    "v_fma_f32 v162, v162, v185, -v184\n"
    "v_fma_f32 v163, v163, v185, -v184\n"
    "v_cvt_pk_f16_f32 v136, v156, v157\n"
    "v_cvt_pk_f16_f32 v137, v158, v159\n"
    "v_cvt_pk_f16_f32 v138, v160, v161\n"
    "v_cvt_pk_f16_f32 v139, v162, v163\n"
    "s_mov_b32 s48, 0\n"
    "buffer_store_dwordx4 v[128:131], %[vxs], %[srdxb], s48 offen sc1\n"
    "s_mov_b32 s48, 8192\n"
    "buffer_store_dwordx4 v[132:135], %[vxs], %[srdxb], s48 offen sc1\n"
    "s_mov_b32 s48, 0\n"
    "buffer_store_dwordx4 v[136:139], %[vxs2], %[srdxb], s48 offen sc1\n"
    "s_waitcnt vmcnt(15) lgkmcnt(0)\n"
    "s_barrier\n"
    "ds_read_b32 v174, v182 offset:0\n"
    "ds_read_b32 v175, v182 offset:256\n"
    "ds_read_b32 v176, v182 offset:512\n"
    "ds_read_b32 v177, v182 offset:768\n"
    "s_waitcnt lgkmcnt(0)\n"
    "v_add_f32 v174, v174, v175\n"
    "v_add_f32 v176, v176, v177\n"
    "v_add_f32 v174, v174, v176\n"
    "buffer_store_dword v174, %[vmo], %[srdmo], 0 offen sc1\n"
    :
    : [vx0]"v"(vx[0]), [vx1]"v"(vx[1]), [vx2]"v"(vx[2]), [vx3]"v"(vx[3]), [vw0]"v"(vw[0]), [vw1]"v"(vw[1]), [vw2]"v"(vw[2]), [vw3]"v"(vw[3]), [srdx]"s"(srdx), [srdw]"s"(srdw), [m0x]"s"(m0x), [m0w]"s"(m0w), [sox]"s"(sox), [sow]"s"(sow), [rot]"s"(rot), [vo]"v"(vo), [isv]"s"(isv), [wid]"s"(wid), [lb]"s"(lbr), [vwm]"v"(vwm), [vcn]"v"(vcn), [vxr]"v"(vxr), [vxr2]"v"(vxr2), [vxs]"v"(vxs), [vxs2]"v"(vxs2), [vred]"v"(vred), [vmo]"v"(vmo), [srdwm]"s"(srdwm), [srdc]"s"(srdc), [srdxx]"s"(srdxx), [srdxb]"s"(srdxb), [srdmo]"s"(srdmo)
    : "memory", "scc", "vcc", "s40","s41","s42","s43","s44","s45","s46","s47","s48","s49","s50","s51","s52","s53","s54","s55","s56","s57", "v128","v129","v130","v131","v132","v133","v134","v135","v136","v137","v138","v139","v140","v141","v142","v143","v144","v145","v146","v147","v148","v149","v150","v151","v152","v153","v154","v155","v156","v157","v158","v159","v160","v161","v162","v163","v164","v165","v166","v167","v168","v169","v170","v171","v172","v173","v174","v175","v176","v177","v178","v179","v180","v181","v182","v183","v184","v185","v186","v187","v188","v189","v190","v191");
  { const u32 i = lane & 15, g = lane >> 4, q = (lane >> 2) & 3;
#pragma unroll
    for (int k = 0; k < 4; k++) rdx[k] = lb + (k >> 1) * 98304 + (i >> 3) * 1024 + (i & 7) * 128 + ((4 * (k & 1) + g) ^ ((i >> 1) & 7)) * 16;
#pragma unroll
    for (int k = 0; k < 8; k++) rdw[k] = lb + (k >> 2) * 98304 + 16384 + wid * 8192 + g * 1024 + q * 128 + (((k & 3) ^ ((q >> 1) | ((g & 1) << 1))) * 32) + (lane & 3) * 8; }
  const u32 h = (nt & 15) * 2 + (wid >> 1), m0t = lb + wid * 2048, sot0 = sox + 128 * 1024 - wid * 16384, sot1 = sot0 + 64 * 1024;
  const u32 soo = isv * (S * HD * 2) + h * 524288 + tt * 65536 + ((wid & 1) << (11 + isv));
  const __amdgpu_buffer_rsrc_t srdo = __builtin_amdgcn_make_buffer_rsrc((void*)kvb, 0, 2 * S * HD * 2, 0x00020000);
  const u32 vpf = (b * 4 + wid) * 4608 + lane * 64;
  const __amdgpu_buffer_rsrc_t srdp = __builtin_amdgcn_make_buffer_rsrc((void*)wq, 0, M * HD * 2, 0x00020000);
  u32 vdum;
  asm volatile(
    "s_nop 4\n"
    "s_mov_b32 s49, 4096\n"
    "s_sub_u32 s45, %[rot], 1\n"
    "s_lshl_b32 s45, s45, 7\n"
    "s_add_u32 s42, %[rot], 1\n"
    "s_cmp_lg_u32 %[isv], 0\n"
    "s_cbranch_scc1 KV_%=\n"
    "KK90001:\n"
    "ds_read_b64_tr_b16 a[0:1], %[rdw0] offset:0\n"
    "ds_read_b64_tr_b16 a[2:3], %[rdw0] offset:512\n"
    "ds_read_b64_tr_b16 a[4:5], %[rdw1] offset:0\n"
    "ds_read_b64_tr_b16 a[6:7], %[rdw1] offset:512\n"
    "ds_read_b64_tr_b16 a[8:9], %[rdw2] offset:0\n"
    "ds_read_b64_tr_b16 a[10:11], %[rdw2] offset:512\n"
    "ds_read_b64_tr_b16 a[12:13], %[rdw3] offset:0\n"
    "ds_read_b64_tr_b16 a[14:15], %[rdw3] offset:512\n"
    "ds_read_b128 v[192:195], %[rdx0] offset:0\n"
    "ds_read_b128 v[196:199], %[rdx0] offset:2048\n"
    "ds_read_b128 v[200:203], %[rdx0] offset:4096\n"
    "ds_read_b128 v[204:207], %[rdx0] offset:6144\n"
    "ds_read_b128 v[208:211], %[rdx0] offset:8192\n"
    "ds_read_b128 v[212:215], %[rdx0] offset:10240\n"
    "s_waitcnt lgkmcnt(4)\n"
    "v_mfma_f32_16x16x32_bf16 v[0:3], a[0:3], v[192:195], 0\n"
    "ds_read_b128 v[216:219], %[rdx0] offset:12288\n"
    "v_mfma_f32_16x16x32_bf16 v[4:7], a[4:7], v[192:195], 0\n"
    "ds_read_b64_tr_b16 a[16:17], %[rdw0] offset:4096\n"
    "v_mfma_f32_16x16x32_bf16 v[8:11], a[8:11], v[192:195], 0\n"
    "ds_read_b64_tr_b16 a[18:19], %[rdw0] offset:4608\n"
    "v_mfma_f32_16x16x32_bf16 v[12:15], a[12:15], v[192:195], 0\n"
    "ds_read_b64_tr_b16 a[20:21], %[rdw1] offset:4096\n"
    "v_mfma_f32_16x16x32_bf16 v[28:31], a[12:15], v[196:199], 0\n"
    "ds_read_b128 v[220:223], %[rdx0] offset:14336\n"
    "v_mfma_f32_16x16x32_bf16 v[24:27], a[8:11], v[196:199], 0\n"
    "ds_read_b64_tr_b16 a[22:23], %[rdw1] offset:4608\n"
    "v_mfma_f32_16x16x32_bf16 v[20:23], a[4:7], v[196:199], 0\n"
    "ds_read_b64_tr_b16 a[24:25], %[rdw2] offset:4096\n"
    "v_mfma_f32_16x16x32_bf16 v[16:19], a[0:3], v[196:199], 0\n"
    "s_add_u32 s42, s42, 1\n"
    "s_and_b32 s42, s42, 7\n"
    "ds_read_b64_tr_b16 a[26:27], %[rdw2] offset:4608\n"
    "s_waitcnt lgkmcnt(10)\n"
    "v_mfma_f32_16x16x32_bf16 v[32:35], a[0:3], v[200:203], 0\n"
    "s_lshl_b32 s43, s42, 7\n"
    "s_add_u32 s40, s43, %[sox]\n"
    "ds_read_b128 v[192:195], %[rdx1] offset:0\n"
    "v_mfma_f32_16x16x32_bf16 v[36:39], a[4:7], v[200:203], 0\n"
    "s_lshl_b32 s43, s42, 19\n"
    "s_add_u32 s41, s43, %[sow]\n"
    "ds_read_b64_tr_b16 a[28:29], %[rdw3] offset:4096\n"
    "v_mfma_f32_16x16x32_bf16 v[40:43], a[8:11], v[200:203], 0\n"
    "s_add_u32 s47, s41, 262144\n"
    "s_add_u32 m0, %[m0x], 98304\n"
    "ds_read_b64_tr_b16 a[30:31], %[rdw3] offset:4608\n"
    "v_mfma_f32_16x16x32_bf16 v[44:47], a[12:15], v[200:203], 0\n"
    "buffer_load_dwordx4 %[vx0], %[srdx], s40 offen offset:0 lds\n"
    "v_mfma_f32_16x16x32_bf16 v[60:63], a[12:15], v[204:207], 0\n"
    "ds_read_b128 v[196:199], %[rdx1] offset:2048\n"
    "v_mfma_f32_16x16x32_bf16 v[56:59], a[8:11], v[204:207], 0\n"
    "buffer_load_dwordx4 %[vx1], %[srdx], s40 offen offset:1024 lds\n"
    "v_mfma_f32_16x16x32_bf16 v[52:55], a[4:7], v[204:207], 0\n"
    "buffer_load_dwordx4 %[vx2], %[srdx], s40 offen offset:2048 lds\n"
    "v_mfma_f32_16x16x32_bf16 v[48:51], a[0:3], v[204:207], 0\n"
    "buffer_load_dwordx4 %[vx3], %[srdx], s40 offen offset:3072 lds\n"
    "s_waitcnt lgkmcnt(12)\n"
    "v_mfma_f32_16x16x32_bf16 v[64:67], a[0:3], v[208:211], 0\n"
    "s_add_u32 m0, %[m0w], 98304\n"
    "ds_read_b128 v[200:203], %[rdx1] offset:4096\n"
    "v_mfma_f32_16x16x32_bf16 v[68:71], a[4:7], v[208:211], 0\n"
    "buffer_load_dwordx4 %[vw0], %[srdw], s41 offen offset:0 nt lds\n"
    "v_mfma_f32_16x16x32_bf16 v[72:75], a[8:11], v[208:211], 0\n"
    "buffer_load_dwordx4 %[vw1], %[srdw], s41 offen offset:1024 nt lds\n"
    "v_mfma_f32_16x16x32_bf16 v[76:79], a[12:15], v[208:211], 0\n"
    "buffer_load_dwordx4 %[vw2], %[srdw], s41 offen offset:2048 nt lds\n"
    "v_mfma_f32_16x16x32_bf16 v[92:95], a[12:15], v[212:215], 0\n"
    "ds_read_b128 v[204:207], %[rdx1] offset:6144\n"
    "v_mfma_f32_16x16x32_bf16 v[88:91], a[8:11], v[212:215], 0\n"
    "buffer_load_dwordx4 %[vw3], %[srdw], s41 offen offset:3072 nt lds\n"
    "v_mfma_f32_16x16x32_bf16 v[84:87], a[4:7], v[212:215], 0\n"
    "s_add_u32 m0, %[m0w], 102400\n"
    "v_mfma_f32_16x16x32_bf16 v[80:83], a[0:3], v[212:215], 0\n"
    "buffer_load_dwordx4 %[vw0], %[srdw], s47 offen offset:0 nt lds\n"
    "s_waitcnt lgkmcnt(12)\n"
    "v_mfma_f32_16x16x32_bf16 v[96:99], a[0:3], v[216:219], 0\n"
    "ds_read_b128 v[208:211], %[rdx1] offset:8192\n"
    "v_mfma_f32_16x16x32_bf16 v[100:103], a[4:7], v[216:219], 0\n"
    "v_mfma_f32_16x16x32_bf16 v[104:107], a[8:11], v[216:219], 0\n"
    "buffer_load_dwordx4 %[vw1], %[srdw], s47 offen offset:1024 nt lds\n"
    "v_mfma_f32_16x16x32_bf16 v[108:111], a[12:15], v[216:219], 0\n"
    "s_waitcnt lgkmcnt(9)\n"
    "v_mfma_f32_16x16x32_bf16 v[124:127], a[12:15], v[220:223], 0\n"
    "ds_read_b128 v[212:215], %[rdx1] offset:10240\n"
    "v_mfma_f32_16x16x32_bf16 v[120:123], a[8:11], v[220:223], 0\n"
    "buffer_load_dwordx4 %[vw2], %[srdw], s47 offen offset:2048 nt lds\n"
    "v_mfma_f32_16x16x32_bf16 v[116:119], a[4:7], v[220:223], 0\n"
    "v_mfma_f32_16x16x32_bf16 v[112:115], a[0:3], v[220:223], 0\n"
    "s_waitcnt lgkmcnt(6)\n"
    "v_mfma_f32_16x16x32_bf16 v[0:3], a[16:19], v[192:195], v[0:3]\n"
    "ds_read_b128 v[216:219], %[rdx1] offset:12288\n"
    "v_mfma_f32_16x16x32_bf16 v[4:7], a[20:23], v[192:195], v[4:7]\n"
    "buffer_load_dwordx4 %[vw3], %[srdw], s47 offen offset:3072 nt lds\n"
    "v_mfma_f32_16x16x32_bf16 v[8:11], a[24:27], v[192:195], v[8:11]\n"
    "s_waitcnt lgkmcnt(5)\n"
    "v_mfma_f32_16x16x32_bf16 v[12:15], a[28:31], v[192:195], v[12:15]\n"
    "v_mfma_f32_16x16x32_bf16 v[28:31], a[28:31], v[196:199], v[28:31]\n"
    "ds_read_b128 v[220:223], %[rdx1] offset:14336\n"
    "v_mfma_f32_16x16x32_bf16 v[24:27], a[24:27], v[196:199], v[24:27]\n"
    "v_mfma_f32_16x16x32_bf16 v[20:23], a[20:23], v[196:199], v[20:23]\n"
    "v_mfma_f32_16x16x32_bf16 v[16:19], a[16:19], v[196:199], v[16:19]\n"
    "s_waitcnt lgkmcnt(4)\n"
    "v_mfma_f32_16x16x32_bf16 v[32:35], a[16:19], v[200:203], v[32:35]\n"
    "v_mfma_f32_16x16x32_bf16 v[36:39], a[20:23], v[200:203], v[36:39]\n"
    "v_mfma_f32_16x16x32_bf16 v[40:43], a[24:27], v[200:203], v[40:43]\n"
    "v_mfma_f32_16x16x32_bf16 v[44:47], a[28:31], v[200:203], v[44:47]\n"
    "v_mfma_f32_16x16x32_bf16 v[60:63], a[28:31], v[204:207], v[60:63]\n"
    "v_mfma_f32_16x16x32_bf16 v[56:59], a[24:27], v[204:207], v[56:59]\n"
    "v_mfma_f32_16x16x32_bf16 v[52:55], a[20:23], v[204:207], v[52:55]\n"
    "v_mfma_f32_16x16x32_bf16 v[48:51], a[16:19], v[204:207], v[48:51]\n"
    "s_waitcnt vmcnt(16) lgkmcnt(0)\n"
    "s_barrier\n"
    "v_mfma_f32_16x16x32_bf16 v[64:67], a[16:19], v[208:211], v[64:67]\n"
    "ds_read_b64_tr_b16 a[32:33], %[rdw0] offset:49152\n"
    "v_mfma_f32_16x16x32_bf16 v[68:71], a[20:23], v[208:211], v[68:71]\n"
    "ds_read_b64_tr_b16 a[34:35], %[rdw0] offset:49664\n"
    "v_mfma_f32_16x16x32_bf16 v[72:75], a[24:27], v[208:211], v[72:75]\n"
    "ds_read_b128 v[192:195], %[rdx0] offset:49152\n"
    "v_mfma_f32_16x16x32_bf16 v[76:79], a[28:31], v[208:211], v[76:79]\n"
    "ds_read_b64_tr_b16 a[36:37], %[rdw1] offset:49152\n"
    "v_mfma_f32_16x16x32_bf16 v[92:95], a[28:31], v[212:215], v[92:95]\n"
    "ds_read_b64_tr_b16 a[38:39], %[rdw1] offset:49664\n"
    "v_mfma_f32_16x16x32_bf16 v[88:91], a[24:27], v[212:215], v[88:91]\n"
    "ds_read_b128 v[196:199], %[rdx0] offset:51200\n"
    "v_mfma_f32_16x16x32_bf16 v[84:87], a[20:23], v[212:215], v[84:87]\n"
    "ds_read_b64_tr_b16 a[40:41], %[rdw2] offset:49152\n"
    "v_mfma_f32_16x16x32_bf16 v[80:83], a[16:19], v[212:215], v[80:83]\n"
    "ds_read_b64_tr_b16 a[42:43], %[rdw2] offset:49664\n"
    "v_mfma_f32_16x16x32_bf16 v[96:99], a[16:19], v[216:219], v[96:99]\n"
    "ds_read_b128 v[200:203], %[rdx0] offset:53248\n"
    "v_mfma_f32_16x16x32_bf16 v[100:103], a[20:23], v[216:219], v[100:103]\n"
    "ds_read_b64_tr_b16 a[44:45], %[rdw3] offset:49152\n"
    "v_mfma_f32_16x16x32_bf16 v[104:107], a[24:27], v[216:219], v[104:107]\n"
    "s_add_u32 s42, s42, 1\n"
    "s_and_b32 s42, s42, 7\n"
    "ds_read_b64_tr_b16 a[46:47], %[rdw3] offset:49664\n"
    "v_mfma_f32_16x16x32_bf16 v[108:111], a[28:31], v[216:219], v[108:111]\n"
    "s_lshl_b32 s43, s42, 7\n"
    "s_add_u32 s40, s43, %[sox]\n"
    "ds_read_b128 v[204:207], %[rdx0] offset:55296\n"
    "v_mfma_f32_16x16x32_bf16 v[124:127], a[28:31], v[220:223], v[124:127]\n"
    "s_lshl_b32 s43, s42, 19\n"
    "s_add_u32 s41, s43, %[sow]\n"
    "ds_read_b128 v[208:211], %[rdx0] offset:57344\n"
    "v_mfma_f32_16x16x32_bf16 v[120:123], a[24:27], v[220:223], v[120:123]\n"
    "s_add_u32 s47, s41, 262144\n"
    "s_add_u32 m0, %[m0x], 0\n"
    "ds_read_b128 v[212:215], %[rdx0] offset:59392\n"
    "v_mfma_f32_16x16x32_bf16 v[116:119], a[20:23], v[220:223], v[116:119]\n"
    "buffer_load_dwordx4 %[vx0], %[srdx], s40 offen offset:0 lds\n"
    "v_mfma_f32_16x16x32_bf16 v[112:115], a[16:19], v[220:223], v[112:115]\n"
    "buffer_load_dwordx4 %[vx1], %[srdx], s40 offen offset:1024 lds\n"
    "s_waitcnt lgkmcnt(10)\n"
    "v_mfma_f32_16x16x32_bf16 v[0:3], a[32:35], v[192:195], v[0:3]\n"
    "ds_read_b128 v[216:219], %[rdx0] offset:61440\n"
    "s_waitcnt lgkmcnt(9)\n"
    "v_mfma_f32_16x16x32_bf16 v[4:7], a[36:39], v[192:195], v[4:7]\n"
    "ds_read_b64_tr_b16 a[48:49], %[rdw0] offset:53248\n"
    "s_waitcnt lgkmcnt(7)\n"
    "v_mfma_f32_16x16x32_bf16 v[8:11], a[40:43], v[192:195], v[8:11]\n"
    "ds_read_b64_tr_b16 a[50:51], %[rdw0] offset:53760\n"
    "s_waitcnt lgkmcnt(5)\n"
    "v_mfma_f32_16x16x32_bf16 v[12:15], a[44:47], v[192:195], v[12:15]\n"
    "ds_read_b64_tr_b16 a[52:53], %[rdw1] offset:53248\n"
    "v_mfma_f32_16x16x32_bf16 v[28:31], a[44:47], v[196:199], v[28:31]\n"
    "ds_read_b128 v[220:223], %[rdx0] offset:63488\n"
    "v_mfma_f32_16x16x32_bf16 v[24:27], a[40:43], v[196:199], v[24:27]\n"
    "ds_read_b64_tr_b16 a[54:55], %[rdw1] offset:53760\n"
    "v_mfma_f32_16x16x32_bf16 v[20:23], a[36:39], v[196:199], v[20:23]\n"
    "ds_read_b64_tr_b16 a[56:57], %[rdw2] offset:53248\n"
    "v_mfma_f32_16x16x32_bf16 v[16:19], a[32:35], v[196:199], v[16:19]\n"
    "ds_read_b64_tr_b16 a[58:59], %[rdw2] offset:53760\n"
    "v_mfma_f32_16x16x32_bf16 v[32:35], a[32:35], v[200:203], v[32:35]\n"
    "ds_read_b128 v[192:195], %[rdx1] offset:49152\n"
    "v_mfma_f32_16x16x32_bf16 v[36:39], a[36:39], v[200:203], v[36:39]\n"
    "ds_read_b64_tr_b16 a[60:61], %[rdw3] offset:53248\n"
    "v_mfma_f32_16x16x32_bf16 v[40:43], a[40:43], v[200:203], v[40:43]\n"
    "ds_read_b64_tr_b16 a[62:63], %[rdw3] offset:53760\n"
    "v_mfma_f32_16x16x32_bf16 v[44:47], a[44:47], v[200:203], v[44:47]\n"
    "buffer_load_dwordx4 %[vx2], %[srdx], s40 offen offset:2048 lds\n"
    "v_mfma_f32_16x16x32_bf16 v[60:63], a[44:47], v[204:207], v[60:63]\n"
    "ds_read_b128 v[196:199], %[rdx1] offset:51200\n"
    "v_mfma_f32_16x16x32_bf16 v[56:59], a[40:43], v[204:207], v[56:59]\n"
    "buffer_load_dwordx4 %[vx3], %[srdx], s40 offen offset:3072 lds\n"
    "v_mfma_f32_16x16x32_bf16 v[52:55], a[36:39], v[204:207], v[52:55]\n"
    "s_add_u32 m0, %[m0w], 0\n"
    "v_mfma_f32_16x16x32_bf16 v[48:51], a[32:35], v[204:207], v[48:51]\n"
    "buffer_load_dwordx4 %[vw0], %[srdw], s41 offen offset:0 nt lds\n"
    "s_waitcnt lgkmcnt(12)\n"
    "v_mfma_f32_16x16x32_bf16 v[64:67], a[32:35], v[208:211], v[64:67]\n"
    "ds_read_b128 v[200:203], %[rdx1] offset:53248\n"
    "v_mfma_f32_16x16x32_bf16 v[68:71], a[36:39], v[208:211], v[68:71]\n"
    "buffer_load_dwordx4 %[vw1], %[srdw], s41 offen offset:1024 nt lds\n"
    "v_mfma_f32_16x16x32_bf16 v[72:75], a[40:43], v[208:211], v[72:75]\n"
    "buffer_load_dwordx4 %[vw2], %[srdw], s41 offen offset:2048 nt lds\n"
    "v_mfma_f32_16x16x32_bf16 v[76:79], a[44:47], v[208:211], v[76:79]\n"
    "buffer_load_dwordx4 %[vw3], %[srdw], s41 offen offset:3072 nt lds\n"
    "v_mfma_f32_16x16x32_bf16 v[92:95], a[44:47], v[212:215], v[92:95]\n"
    "s_add_u32 m0, %[m0w], 4096\n"
    "ds_read_b128 v[204:207], %[rdx1] offset:55296\n"
    "v_mfma_f32_16x16x32_bf16 v[88:91], a[40:43], v[212:215], v[88:91]\n"
    "buffer_load_dwordx4 %[vw0], %[srdw], s47 offen offset:0 nt lds\n"
    "v_mfma_f32_16x16x32_bf16 v[84:87], a[36:39], v[212:215], v[84:87]\n"
    "buffer_load_dwordx4 %[vw1], %[srdw], s47 offen offset:1024 nt lds\n"
    "v_mfma_f32_16x16x32_bf16 v[80:83], a[32:35], v[212:215], v[80:83]\n"
    "s_waitcnt lgkmcnt(12)\n"
    "v_mfma_f32_16x16x32_bf16 v[96:99], a[32:35], v[216:219], v[96:99]\n"
    "ds_read_b128 v[208:211], %[rdx1] offset:57344\n"
    "v_mfma_f32_16x16x32_bf16 v[100:103], a[36:39], v[216:219], v[100:103]\n"
    "v_mfma_f32_16x16x32_bf16 v[104:107], a[40:43], v[216:219], v[104:107]\n"
    "buffer_load_dwordx4 %[vw2], %[srdw], s47 offen offset:2048 nt lds\n"
    "v_mfma_f32_16x16x32_bf16 v[108:111], a[44:47], v[216:219], v[108:111]\n"
    "s_waitcnt lgkmcnt(9)\n"
    "v_mfma_f32_16x16x32_bf16 v[124:127], a[44:47], v[220:223], v[124:127]\n"
    "ds_read_b128 v[212:215], %[rdx1] offset:59392\n"
    "v_mfma_f32_16x16x32_bf16 v[120:123], a[40:43], v[220:223], v[120:123]\n"
    "v_mfma_f32_16x16x32_bf16 v[116:119], a[36:39], v[220:223], v[116:119]\n"
    "buffer_load_dwordx4 %[vw3], %[srdw], s47 offen offset:3072 nt lds\n"
    "v_mfma_f32_16x16x32_bf16 v[112:115], a[32:35], v[220:223], v[112:115]\n"
    "s_waitcnt lgkmcnt(6)\n"
    "v_mfma_f32_16x16x32_bf16 v[0:3], a[48:51], v[192:195], v[0:3]\n"
    "ds_read_b128 v[216:219], %[rdx1] offset:61440\n"
    "v_mfma_f32_16x16x32_bf16 v[4:7], a[52:55], v[192:195], v[4:7]\n"
    "v_mfma_f32_16x16x32_bf16 v[8:11], a[56:59], v[192:195], v[8:11]\n"
    "s_waitcnt lgkmcnt(5)\n"
    "v_mfma_f32_16x16x32_bf16 v[12:15], a[60:63], v[192:195], v[12:15]\n"
    "v_mfma_f32_16x16x32_bf16 v[28:31], a[60:63], v[196:199], v[28:31]\n"
    "ds_read_b128 v[220:223], %[rdx1] offset:63488\n"
    "v_mfma_f32_16x16x32_bf16 v[24:27], a[56:59], v[196:199], v[24:27]\n"
    "v_mfma_f32_16x16x32_bf16 v[20:23], a[52:55], v[196:199], v[20:23]\n"
    "v_mfma_f32_16x16x32_bf16 v[16:19], a[48:51], v[196:199], v[16:19]\n"
    "s_waitcnt lgkmcnt(4)\n"
    "v_mfma_f32_16x16x32_bf16 v[32:35], a[48:51], v[200:203], v[32:35]\n"
    "v_mfma_f32_16x16x32_bf16 v[36:39], a[52:55], v[200:203], v[36:39]\n"
    "v_mfma_f32_16x16x32_bf16 v[40:43], a[56:59], v[200:203], v[40:43]\n"
    "v_mfma_f32_16x16x32_bf16 v[44:47], a[60:63], v[200:203], v[44:47]\n"
    "v_mfma_f32_16x16x32_bf16 v[60:63], a[60:63], v[204:207], v[60:63]\n"
    "v_mfma_f32_16x16x32_bf16 v[56:59], a[56:59], v[204:207], v[56:59]\n"
    "v_mfma_f32_16x16x32_bf16 v[52:55], a[52:55], v[204:207], v[52:55]\n"
    "v_mfma_f32_16x16x32_bf16 v[48:51], a[48:51], v[204:207], v[48:51]\n"
    "s_waitcnt vmcnt(12) lgkmcnt(0)\n"
    "s_barrier\n"
    "v_mfma_f32_16x16x32_bf16 v[64:67], a[48:51], v[208:211], v[64:67]\n"
    "ds_read_b64_tr_b16 a[64:65], %[rdw4] offset:0\n"
    "v_mfma_f32_16x16x32_bf16 v[68:71], a[52:55], v[208:211], v[68:71]\n"
    "ds_read_b64_tr_b16 a[66:67], %[rdw4] offset:512\n"
    "v_mfma_f32_16x16x32_bf16 v[72:75], a[56:59], v[208:211], v[72:75]\n"
    "ds_read_b128 v[192:195], %[rdx2] offset:0\n"
    "v_mfma_f32_16x16x32_bf16 v[76:79], a[60:63], v[208:211], v[76:79]\n"
    "ds_read_b64_tr_b16 a[68:69], %[rdw5] offset:0\n"
    "v_mfma_f32_16x16x32_bf16 v[92:95], a[60:63], v[212:215], v[92:95]\n"
    "ds_read_b64_tr_b16 a[70:71], %[rdw5] offset:512\n"
    "v_mfma_f32_16x16x32_bf16 v[88:91], a[56:59], v[212:215], v[88:91]\n"
    "ds_read_b128 v[196:199], %[rdx2] offset:2048\n"
    "v_mfma_f32_16x16x32_bf16 v[84:87], a[52:55], v[212:215], v[84:87]\n"
    "ds_read_b64_tr_b16 a[72:73], %[rdw6] offset:0\n"
    "v_mfma_f32_16x16x32_bf16 v[80:83], a[48:51], v[212:215], v[80:83]\n"
    "ds_read_b64_tr_b16 a[74:75], %[rdw6] offset:512\n"
    "v_mfma_f32_16x16x32_bf16 v[96:99], a[48:51], v[216:219], v[96:99]\n"
    "ds_read_b128 v[200:203], %[rdx2] offset:4096\n"
    "v_mfma_f32_16x16x32_bf16 v[100:103], a[52:55], v[216:219], v[100:103]\n"
    "ds_read_b64_tr_b16 a[76:77], %[rdw7] offset:0\n"
    "v_mfma_f32_16x16x32_bf16 v[104:107], a[56:59], v[216:219], v[104:107]\n"
    "s_add_u32 s42, s42, 1\n"
    "s_and_b32 s42, s42, 7\n"
    "ds_read_b64_tr_b16 a[78:79], %[rdw7] offset:512\n"
    "v_mfma_f32_16x16x32_bf16 v[108:111], a[60:63], v[216:219], v[108:111]\n"
    "s_lshl_b32 s43, s42, 7\n"
    "s_add_u32 s40, s43, %[sox]\n"
    "ds_read_b128 v[204:207], %[rdx2] offset:6144\n"
    "v_mfma_f32_16x16x32_bf16 v[124:127], a[60:63], v[220:223], v[124:127]\n"
    "s_lshl_b32 s43, s42, 19\n"
    "s_add_u32 s41, s43, %[sow]\n"
    "ds_read_b128 v[208:211], %[rdx2] offset:8192\n"
    "v_mfma_f32_16x16x32_bf16 v[120:123], a[56:59], v[220:223], v[120:123]\n"
    "s_add_u32 s47, s41, 262144\n"
    "s_add_u32 m0, %[m0x], 49152\n"
    "ds_read_b128 v[212:215], %[rdx2] offset:10240\n"
    "v_mfma_f32_16x16x32_bf16 v[116:119], a[52:55], v[220:223], v[116:119]\n"
    "buffer_load_dwordx4 %[vx0], %[srdx], s40 offen offset:0 lds\n"
    "v_mfma_f32_16x16x32_bf16 v[112:115], a[48:51], v[220:223], v[112:115]\n"
    "buffer_load_dwordx4 %[vx1], %[srdx], s40 offen offset:1024 lds\n"
    "s_waitcnt lgkmcnt(10)\n"
    "v_mfma_f32_16x16x32_bf16 v[0:3], a[64:67], v[192:195], v[0:3]\n"
    "ds_read_b128 v[216:219], %[rdx2] offset:12288\n"
    "s_waitcnt lgkmcnt(9)\n"
    "v_mfma_f32_16x16x32_bf16 v[4:7], a[68:71], v[192:195], v[4:7]\n"
    "ds_read_b64_tr_b16 a[80:81], %[rdw4] offset:4096\n"
    "s_waitcnt lgkmcnt(7)\n"
    "v_mfma_f32_16x16x32_bf16 v[8:11], a[72:75], v[192:195], v[8:11]\n"
    "ds_read_b64_tr_b16 a[82:83], %[rdw4] offset:4608\n"
    "s_waitcnt lgkmcnt(5)\n"
    "v_mfma_f32_16x16x32_bf16 v[12:15], a[76:79], v[192:195], v[12:15]\n"
    "ds_read_b64_tr_b16 a[84:85], %[rdw5] offset:4096\n"
    "v_mfma_f32_16x16x32_bf16 v[28:31], a[76:79], v[196:199], v[28:31]\n"
    "ds_read_b128 v[220:223], %[rdx2] offset:14336\n"
    "v_mfma_f32_16x16x32_bf16 v[24:27], a[72:75], v[196:199], v[24:27]\n"
    "ds_read_b64_tr_b16 a[86:87], %[rdw5] offset:4608\n"
    "v_mfma_f32_16x16x32_bf16 v[20:23], a[68:71], v[196:199], v[20:23]\n"
    "ds_read_b64_tr_b16 a[88:89], %[rdw6] offset:4096\n"
    "v_mfma_f32_16x16x32_bf16 v[16:19], a[64:67], v[196:199], v[16:19]\n"
    "ds_read_b64_tr_b16 a[90:91], %[rdw6] offset:4608\n"
    "v_mfma_f32_16x16x32_bf16 v[32:35], a[64:67], v[200:203], v[32:35]\n"
    "ds_read_b128 v[192:195], %[rdx3] offset:0\n"
    "v_mfma_f32_16x16x32_bf16 v[36:39], a[68:71], v[200:203], v[36:39]\n"
    "ds_read_b64_tr_b16 a[92:93], %[rdw7] offset:4096\n"
    "v_mfma_f32_16x16x32_bf16 v[40:43], a[72:75], v[200:203], v[40:43]\n"
    "ds_read_b64_tr_b16 a[94:95], %[rdw7] offset:4608\n"
    "v_mfma_f32_16x16x32_bf16 v[44:47], a[76:79], v[200:203], v[44:47]\n"
    "buffer_load_dwordx4 %[vx2], %[srdx], s40 offen offset:2048 lds\n"
    "v_mfma_f32_16x16x32_bf16 v[60:63], a[76:79], v[204:207], v[60:63]\n"
    "ds_read_b128 v[196:199], %[rdx3] offset:2048\n"
    "v_mfma_f32_16x16x32_bf16 v[56:59], a[72:75], v[204:207], v[56:59]\n"
    "buffer_load_dwordx4 %[vx3], %[srdx], s40 offen offset:3072 lds\n"
    "v_mfma_f32_16x16x32_bf16 v[52:55], a[68:71], v[204:207], v[52:55]\n"
    "s_add_u32 m0, %[m0w], 49152\n"
    "v_mfma_f32_16x16x32_bf16 v[48:51], a[64:67], v[204:207], v[48:51]\n"
    "buffer_load_dwordx4 %[vw0], %[srdw], s41 offen offset:0 nt lds\n"
    "s_waitcnt lgkmcnt(12)\n"
    "v_mfma_f32_16x16x32_bf16 v[64:67], a[64:67], v[208:211], v[64:67]\n"
    "ds_read_b128 v[200:203], %[rdx3] offset:4096\n"
    "v_mfma_f32_16x16x32_bf16 v[68:71], a[68:71], v[208:211], v[68:71]\n"
    "buffer_load_dwordx4 %[vw1], %[srdw], s41 offen offset:1024 nt lds\n"
    "v_mfma_f32_16x16x32_bf16 v[72:75], a[72:75], v[208:211], v[72:75]\n"
    "buffer_load_dwordx4 %[vw2], %[srdw], s41 offen offset:2048 nt lds\n"
    "v_mfma_f32_16x16x32_bf16 v[76:79], a[76:79], v[208:211], v[76:79]\n"
    "buffer_load_dwordx4 %[vw3], %[srdw], s41 offen offset:3072 nt lds\n"
    "v_mfma_f32_16x16x32_bf16 v[92:95], a[76:79], v[212:215], v[92:95]\n"
    "s_add_u32 m0, %[m0w], 53248\n"
    "ds_read_b128 v[204:207], %[rdx3] offset:6144\n"
    "v_mfma_f32_16x16x32_bf16 v[88:91], a[72:75], v[212:215], v[88:91]\n"
    "buffer_load_dwordx4 %[vw0], %[srdw], s47 offen offset:0 nt lds\n"
    "v_mfma_f32_16x16x32_bf16 v[84:87], a[68:71], v[212:215], v[84:87]\n"
    "buffer_load_dwordx4 %[vw1], %[srdw], s47 offen offset:1024 nt lds\n"
    "v_mfma_f32_16x16x32_bf16 v[80:83], a[64:67], v[212:215], v[80:83]\n"
    "s_waitcnt lgkmcnt(12)\n"
    "v_mfma_f32_16x16x32_bf16 v[96:99], a[64:67], v[216:219], v[96:99]\n"
    "ds_read_b128 v[208:211], %[rdx3] offset:8192\n"
    "v_mfma_f32_16x16x32_bf16 v[100:103], a[68:71], v[216:219], v[100:103]\n"
    "v_mfma_f32_16x16x32_bf16 v[104:107], a[72:75], v[216:219], v[104:107]\n"
    "buffer_load_dwordx4 %[vw2], %[srdw], s47 offen offset:2048 nt lds\n"
    "v_mfma_f32_16x16x32_bf16 v[108:111], a[76:79], v[216:219], v[108:111]\n"
    "s_waitcnt lgkmcnt(9)\n"
    "v_mfma_f32_16x16x32_bf16 v[124:127], a[76:79], v[220:223], v[124:127]\n"
    "ds_read_b128 v[212:215], %[rdx3] offset:10240\n"
    "v_mfma_f32_16x16x32_bf16 v[120:123], a[72:75], v[220:223], v[120:123]\n"
    "v_mfma_f32_16x16x32_bf16 v[116:119], a[68:71], v[220:223], v[116:119]\n"
    "buffer_load_dwordx4 %[vw3], %[srdw], s47 offen offset:3072 nt lds\n"
    "v_mfma_f32_16x16x32_bf16 v[112:115], a[64:67], v[220:223], v[112:115]\n"
    "s_waitcnt lgkmcnt(6)\n"
    "v_mfma_f32_16x16x32_bf16 v[0:3], a[80:83], v[192:195], v[0:3]\n"
    "ds_read_b128 v[216:219], %[rdx3] offset:12288\n"
    "v_mfma_f32_16x16x32_bf16 v[4:7], a[84:87], v[192:195], v[4:7]\n"
    "v_mfma_f32_16x16x32_bf16 v[8:11], a[88:91], v[192:195], v[8:11]\n"
    "s_waitcnt lgkmcnt(5)\n"
    "v_mfma_f32_16x16x32_bf16 v[12:15], a[92:95], v[192:195], v[12:15]\n"
    "v_mfma_f32_16x16x32_bf16 v[28:31], a[92:95], v[196:199], v[28:31]\n"
    "ds_read_b128 v[220:223], %[rdx3] offset:14336\n"
    "v_mfma_f32_16x16x32_bf16 v[24:27], a[88:91], v[196:199], v[24:27]\n"
    "v_mfma_f32_16x16x32_bf16 v[20:23], a[84:87], v[196:199], v[20:23]\n"
    "v_mfma_f32_16x16x32_bf16 v[16:19], a[80:83], v[196:199], v[16:19]\n"
    "s_waitcnt lgkmcnt(4)\n"
    "v_mfma_f32_16x16x32_bf16 v[32:35], a[80:83], v[200:203], v[32:35]\n"
    "v_mfma_f32_16x16x32_bf16 v[36:39], a[84:87], v[200:203], v[36:39]\n"
    "v_mfma_f32_16x16x32_bf16 v[40:43], a[88:91], v[200:203], v[40:43]\n"
    "v_mfma_f32_16x16x32_bf16 v[44:47], a[92:95], v[200:203], v[44:47]\n"
    "v_mfma_f32_16x16x32_bf16 v[60:63], a[92:95], v[204:207], v[60:63]\n"
    "v_mfma_f32_16x16x32_bf16 v[56:59], a[88:91], v[204:207], v[56:59]\n"
    "v_mfma_f32_16x16x32_bf16 v[52:55], a[84:87], v[204:207], v[52:55]\n"
    "v_mfma_f32_16x16x32_bf16 v[48:51], a[80:83], v[204:207], v[48:51]\n"
    "s_waitcnt vmcnt(12) lgkmcnt(0)\n"
    "s_barrier\n"
    "v_mfma_f32_16x16x32_bf16 v[64:67], a[80:83], v[208:211], v[64:67]\n"
    "ds_read_b64_tr_b16 a[96:97], %[rdw0] offset:0\n"
    "v_mfma_f32_16x16x32_bf16 v[68:71], a[84:87], v[208:211], v[68:71]\n"
    "ds_read_b64_tr_b16 a[98:99], %[rdw0] offset:512\n"
    "v_mfma_f32_16x16x32_bf16 v[72:75], a[88:91], v[208:211], v[72:75]\n"
    "ds_read_b128 v[192:195], %[rdx0] offset:0\n"
    "v_mfma_f32_16x16x32_bf16 v[76:79], a[92:95], v[208:211], v[76:79]\n"
    "ds_read_b64_tr_b16 a[100:101], %[rdw1] offset:0\n"
    "v_mfma_f32_16x16x32_bf16 v[92:95], a[92:95], v[212:215], v[92:95]\n"
    "ds_read_b64_tr_b16 a[102:103], %[rdw1] offset:512\n"
    "v_mfma_f32_16x16x32_bf16 v[88:91], a[88:91], v[212:215], v[88:91]\n"
    "ds_read_b128 v[196:199], %[rdx0] offset:2048\n"
    "v_mfma_f32_16x16x32_bf16 v[84:87], a[84:87], v[212:215], v[84:87]\n"
    "ds_read_b64_tr_b16 a[104:105], %[rdw2] offset:0\n"
    "v_mfma_f32_16x16x32_bf16 v[80:83], a[80:83], v[212:215], v[80:83]\n"
    "ds_read_b64_tr_b16 a[106:107], %[rdw2] offset:512\n"
    "v_mfma_f32_16x16x32_bf16 v[96:99], a[80:83], v[216:219], v[96:99]\n"
    "ds_read_b128 v[200:203], %[rdx0] offset:4096\n"
    "v_mfma_f32_16x16x32_bf16 v[100:103], a[84:87], v[216:219], v[100:103]\n"
    "ds_read_b64_tr_b16 a[108:109], %[rdw3] offset:0\n"
    "v_mfma_f32_16x16x32_bf16 v[104:107], a[88:91], v[216:219], v[104:107]\n"
    "s_add_u32 s42, s42, 1\n"
    "s_and_b32 s42, s42, 7\n"
    "ds_read_b64_tr_b16 a[110:111], %[rdw3] offset:512\n"
    "v_mfma_f32_16x16x32_bf16 v[108:111], a[92:95], v[216:219], v[108:111]\n"
    "s_lshl_b32 s43, s42, 7\n"
    "s_add_u32 s40, s43, %[sox]\n"
    "ds_read_b128 v[204:207], %[rdx0] offset:6144\n"
    "v_mfma_f32_16x16x32_bf16 v[124:127], a[92:95], v[220:223], v[124:127]\n"
    "s_lshl_b32 s43, s42, 19\n"
    "s_add_u32 s41, s43, %[sow]\n"
    "ds_read_b128 v[208:211], %[rdx0] offset:8192\n"
    "v_mfma_f32_16x16x32_bf16 v[120:123], a[88:91], v[220:223], v[120:123]\n"
    "s_add_u32 s47, s41, 262144\n"
    "s_add_u32 m0, %[m0x], 98304\n"
    "ds_read_b128 v[212:215], %[rdx0] offset:10240\n"
    "v_mfma_f32_16x16x32_bf16 v[116:119], a[84:87], v[220:223], v[116:119]\n"
    "buffer_load_dwordx4 %[vx0], %[srdx], s40 offen offset:0 lds\n"
    "v_mfma_f32_16x16x32_bf16 v[112:115], a[80:83], v[220:223], v[112:115]\n"
    "buffer_load_dwordx4 %[vx1], %[srdx], s40 offen offset:1024 lds\n"
    "s_waitcnt lgkmcnt(10)\n"
    "v_mfma_f32_16x16x32_bf16 v[0:3], a[96:99], v[192:195], v[0:3]\n"
    "ds_read_b128 v[216:219], %[rdx0] offset:12288\n"
    "s_waitcnt lgkmcnt(9)\n"
    "v_mfma_f32_16x16x32_bf16 v[4:7], a[100:103], v[192:195], v[4:7]\n"
    "ds_read_b64_tr_b16 a[112:113], %[rdw0] offset:4096\n"
    "s_waitcnt lgkmcnt(7)\n"
    "v_mfma_f32_16x16x32_bf16 v[8:11], a[104:107], v[192:195], v[8:11]\n"
    "ds_read_b64_tr_b16 a[114:115], %[rdw0] offset:4608\n"
    "s_waitcnt lgkmcnt(5)\n"
    "v_mfma_f32_16x16x32_bf16 v[12:15], a[108:111], v[192:195], v[12:15]\n"
    "ds_read_b64_tr_b16 a[116:117], %[rdw1] offset:4096\n"
    "v_mfma_f32_16x16x32_bf16 v[28:31], a[108:111], v[196:199], v[28:31]\n"
    "ds_read_b128 v[220:223], %[rdx0] offset:14336\n"
    "v_mfma_f32_16x16x32_bf16 v[24:27], a[104:107], v[196:199], v[24:27]\n"
    "ds_read_b64_tr_b16 a[118:119], %[rdw1] offset:4608\n"
    "v_mfma_f32_16x16x32_bf16 v[20:23], a[100:103], v[196:199], v[20:23]\n"
    "ds_read_b64_tr_b16 a[120:121], %[rdw2] offset:4096\n"
    "v_mfma_f32_16x16x32_bf16 v[16:19], a[96:99], v[196:199], v[16:19]\n"
    "ds_read_b64_tr_b16 a[122:123], %[rdw2] offset:4608\n"
    "v_mfma_f32_16x16x32_bf16 v[32:35], a[96:99], v[200:203], v[32:35]\n"
    "ds_read_b128 v[192:195], %[rdx1] offset:0\n"
    "v_mfma_f32_16x16x32_bf16 v[36:39], a[100:103], v[200:203], v[36:39]\n"
    "ds_read_b64_tr_b16 a[124:125], %[rdw3] offset:4096\n"
    "v_mfma_f32_16x16x32_bf16 v[40:43], a[104:107], v[200:203], v[40:43]\n"
    "ds_read_b64_tr_b16 a[126:127], %[rdw3] offset:4608\n"
    "v_mfma_f32_16x16x32_bf16 v[44:47], a[108:111], v[200:203], v[44:47]\n"
    "buffer_load_dwordx4 %[vx2], %[srdx], s40 offen offset:2048 lds\n"
    "v_mfma_f32_16x16x32_bf16 v[60:63], a[108:111], v[204:207], v[60:63]\n"
    "ds_read_b128 v[196:199], %[rdx1] offset:2048\n"
    "v_mfma_f32_16x16x32_bf16 v[56:59], a[104:107], v[204:207], v[56:59]\n"
    "buffer_load_dwordx4 %[vx3], %[srdx], s40 offen offset:3072 lds\n"
    "v_mfma_f32_16x16x32_bf16 v[52:55], a[100:103], v[204:207], v[52:55]\n"
    "s_add_u32 m0, %[m0w], 98304\n"
    "v_mfma_f32_16x16x32_bf16 v[48:51], a[96:99], v[204:207], v[48:51]\n"
    "buffer_load_dwordx4 %[vw0], %[srdw], s41 offen offset:0 nt lds\n"
    "s_waitcnt lgkmcnt(12)\n"
    "v_mfma_f32_16x16x32_bf16 v[64:67], a[96:99], v[208:211], v[64:67]\n"
    "ds_read_b128 v[200:203], %[rdx1] offset:4096\n"
    "v_mfma_f32_16x16x32_bf16 v[68:71], a[100:103], v[208:211], v[68:71]\n"
    "buffer_load_dwordx4 %[vw1], %[srdw], s41 offen offset:1024 nt lds\n"
    "v_mfma_f32_16x16x32_bf16 v[72:75], a[104:107], v[208:211], v[72:75]\n"
    "buffer_load_dwordx4 %[vw2], %[srdw], s41 offen offset:2048 nt lds\n"
    "v_mfma_f32_16x16x32_bf16 v[76:79], a[108:111], v[208:211], v[76:79]\n"
    "buffer_load_dwordx4 %[vw3], %[srdw], s41 offen offset:3072 nt lds\n"
    "v_mfma_f32_16x16x32_bf16 v[92:95], a[108:111], v[212:215], v[92:95]\n"
    "s_add_u32 m0, %[m0w], 102400\n"
    "ds_read_b128 v[204:207], %[rdx1] offset:6144\n"
    "v_mfma_f32_16x16x32_bf16 v[88:91], a[104:107], v[212:215], v[88:91]\n"
    "buffer_load_dwordx4 %[vw0], %[srdw], s47 offen offset:0 nt lds\n"
    "v_mfma_f32_16x16x32_bf16 v[84:87], a[100:103], v[212:215], v[84:87]\n"
    "buffer_load_dwordx4 %[vw1], %[srdw], s47 offen offset:1024 nt lds\n"
    "v_mfma_f32_16x16x32_bf16 v[80:83], a[96:99], v[212:215], v[80:83]\n"
    "s_waitcnt lgkmcnt(12)\n"
    "v_mfma_f32_16x16x32_bf16 v[96:99], a[96:99], v[216:219], v[96:99]\n"
    "ds_read_b128 v[208:211], %[rdx1] offset:8192\n"
    "v_mfma_f32_16x16x32_bf16 v[100:103], a[100:103], v[216:219], v[100:103]\n"
    "v_mfma_f32_16x16x32_bf16 v[104:107], a[104:107], v[216:219], v[104:107]\n"
    "buffer_load_dwordx4 %[vw2], %[srdw], s47 offen offset:2048 nt lds\n"
    "v_mfma_f32_16x16x32_bf16 v[108:111], a[108:111], v[216:219], v[108:111]\n"
    "s_waitcnt lgkmcnt(9)\n"
    "v_mfma_f32_16x16x32_bf16 v[124:127], a[108:111], v[220:223], v[124:127]\n"
    "ds_read_b128 v[212:215], %[rdx1] offset:10240\n"
    "v_mfma_f32_16x16x32_bf16 v[120:123], a[104:107], v[220:223], v[120:123]\n"
    "v_mfma_f32_16x16x32_bf16 v[116:119], a[100:103], v[220:223], v[116:119]\n"
    "buffer_load_dwordx4 %[vw3], %[srdw], s47 offen offset:3072 nt lds\n"
    "v_mfma_f32_16x16x32_bf16 v[112:115], a[96:99], v[220:223], v[112:115]\n"
    "s_waitcnt lgkmcnt(6)\n"
    "v_mfma_f32_16x16x32_bf16 v[0:3], a[112:115], v[192:195], v[0:3]\n"
    "ds_read_b128 v[216:219], %[rdx1] offset:12288\n"
    "v_mfma_f32_16x16x32_bf16 v[4:7], a[116:119], v[192:195], v[4:7]\n"
    "v_mfma_f32_16x16x32_bf16 v[8:11], a[120:123], v[192:195], v[8:11]\n"
    "s_waitcnt lgkmcnt(5)\n"
    "v_mfma_f32_16x16x32_bf16 v[12:15], a[124:127], v[192:195], v[12:15]\n"
    "v_mfma_f32_16x16x32_bf16 v[28:31], a[124:127], v[196:199], v[28:31]\n"
    "ds_read_b128 v[220:223], %[rdx1] offset:14336\n"
    "v_mfma_f32_16x16x32_bf16 v[24:27], a[120:123], v[196:199], v[24:27]\n"
    "v_mfma_f32_16x16x32_bf16 v[20:23], a[116:119], v[196:199], v[20:23]\n"
    "v_mfma_f32_16x16x32_bf16 v[16:19], a[112:115], v[196:199], v[16:19]\n"
    "s_waitcnt lgkmcnt(4)\n"
    "v_mfma_f32_16x16x32_bf16 v[32:35], a[112:115], v[200:203], v[32:35]\n"
    "v_mfma_f32_16x16x32_bf16 v[36:39], a[116:119], v[200:203], v[36:39]\n"
    "v_mfma_f32_16x16x32_bf16 v[40:43], a[120:123], v[200:203], v[40:43]\n"
    "v_mfma_f32_16x16x32_bf16 v[44:47], a[124:127], v[200:203], v[44:47]\n"
    "v_mfma_f32_16x16x32_bf16 v[60:63], a[124:127], v[204:207], v[60:63]\n"
    "v_mfma_f32_16x16x32_bf16 v[56:59], a[120:123], v[204:207], v[56:59]\n"
    "v_mfma_f32_16x16x32_bf16 v[52:55], a[116:119], v[204:207], v[52:55]\n"
    "v_mfma_f32_16x16x32_bf16 v[48:51], a[112:115], v[204:207], v[48:51]\n"
    "s_waitcnt vmcnt(12) lgkmcnt(0)\n"
    "s_barrier\n"
    "v_mfma_f32_16x16x32_bf16 v[64:67], a[112:115], v[208:211], v[64:67]\n"
    "ds_read_b64_tr_b16 a[128:129], %[rdw0] offset:49152\n"
    "v_mfma_f32_16x16x32_bf16 v[68:71], a[116:119], v[208:211], v[68:71]\n"
    "ds_read_b64_tr_b16 a[130:131], %[rdw0] offset:49664\n"
    "v_mfma_f32_16x16x32_bf16 v[72:75], a[120:123], v[208:211], v[72:75]\n"
    "ds_read_b128 v[192:195], %[rdx0] offset:49152\n"
    "v_mfma_f32_16x16x32_bf16 v[76:79], a[124:127], v[208:211], v[76:79]\n"
    "ds_read_b64_tr_b16 a[132:133], %[rdw1] offset:49152\n"
    "v_mfma_f32_16x16x32_bf16 v[92:95], a[124:127], v[212:215], v[92:95]\n"
    "ds_read_b64_tr_b16 a[134:135], %[rdw1] offset:49664\n"
    "v_mfma_f32_16x16x32_bf16 v[88:91], a[120:123], v[212:215], v[88:91]\n"
    "ds_read_b128 v[196:199], %[rdx0] offset:51200\n"
    "v_mfma_f32_16x16x32_bf16 v[84:87], a[116:119], v[212:215], v[84:87]\n"
    "ds_read_b64_tr_b16 a[136:137], %[rdw2] offset:49152\n"
    "v_mfma_f32_16x16x32_bf16 v[80:83], a[112:115], v[212:215], v[80:83]\n"
    "ds_read_b64_tr_b16 a[138:139], %[rdw2] offset:49664\n"
    "v_mfma_f32_16x16x32_bf16 v[96:99], a[112:115], v[216:219], v[96:99]\n"
    "ds_read_b128 v[200:203], %[rdx0] offset:53248\n"
    "v_mfma_f32_16x16x32_bf16 v[100:103], a[116:119], v[216:219], v[100:103]\n"
    "ds_read_b64_tr_b16 a[140:141], %[rdw3] offset:49152\n"
    "v_mfma_f32_16x16x32_bf16 v[104:107], a[120:123], v[216:219], v[104:107]\n"
    "s_add_u32 s42, s42, 1\n"
    "s_and_b32 s42, s42, 7\n"
    "ds_read_b64_tr_b16 a[142:143], %[rdw3] offset:49664\n"
    "v_mfma_f32_16x16x32_bf16 v[108:111], a[124:127], v[216:219], v[108:111]\n"
    "s_lshl_b32 s43, s42, 7\n"
    "s_add_u32 s40, s43, %[sox]\n"
    "ds_read_b128 v[204:207], %[rdx0] offset:55296\n"
    "v_mfma_f32_16x16x32_bf16 v[124:127], a[124:127], v[220:223], v[124:127]\n"
    "s_lshl_b32 s43, s42, 19\n"
    "s_add_u32 s41, s43, %[sow]\n"
    "ds_read_b128 v[208:211], %[rdx0] offset:57344\n"
    "v_mfma_f32_16x16x32_bf16 v[120:123], a[120:123], v[220:223], v[120:123]\n"
    "s_add_u32 s47, s41, 262144\n"
    "s_add_u32 m0, %[m0x], 0\n"
    "ds_read_b128 v[212:215], %[rdx0] offset:59392\n"
    "v_mfma_f32_16x16x32_bf16 v[116:119], a[116:119], v[220:223], v[116:119]\n"
    "buffer_load_dwordx4 %[vx0], %[srdx], s40 offen offset:0 lds\n"
    "v_mfma_f32_16x16x32_bf16 v[112:115], a[112:115], v[220:223], v[112:115]\n"
    "buffer_load_dwordx4 %[vx1], %[srdx], s40 offen offset:1024 lds\n"
    "s_waitcnt lgkmcnt(10)\n"
    "v_mfma_f32_16x16x32_bf16 v[0:3], a[128:131], v[192:195], v[0:3]\n"
    "ds_read_b128 v[216:219], %[rdx0] offset:61440\n"
    "s_waitcnt lgkmcnt(9)\n"
    "v_mfma_f32_16x16x32_bf16 v[4:7], a[132:135], v[192:195], v[4:7]\n"
    "ds_read_b64_tr_b16 a[144:145], %[rdw0] offset:53248\n"
    "s_waitcnt lgkmcnt(7)\n"
    "v_mfma_f32_16x16x32_bf16 v[8:11], a[136:139], v[192:195], v[8:11]\n"
    "ds_read_b64_tr_b16 a[146:147], %[rdw0] offset:53760\n"
    "s_waitcnt lgkmcnt(5)\n"
    "v_mfma_f32_16x16x32_bf16 v[12:15], a[140:143], v[192:195], v[12:15]\n"
    "ds_read_b64_tr_b16 a[148:149], %[rdw1] offset:53248\n"
    "v_mfma_f32_16x16x32_bf16 v[28:31], a[140:143], v[196:199], v[28:31]\n"
    "ds_read_b128 v[220:223], %[rdx0] offset:63488\n"
    "v_mfma_f32_16x16x32_bf16 v[24:27], a[136:139], v[196:199], v[24:27]\n"
    "ds_read_b64_tr_b16 a[150:151], %[rdw1] offset:53760\n"
    "v_mfma_f32_16x16x32_bf16 v[20:23], a[132:135], v[196:199], v[20:23]\n"
    "ds_read_b64_tr_b16 a[152:153], %[rdw2] offset:53248\n"
    "v_mfma_f32_16x16x32_bf16 v[16:19], a[128:131], v[196:199], v[16:19]\n"
    "ds_read_b64_tr_b16 a[154:155], %[rdw2] offset:53760\n"
    "v_mfma_f32_16x16x32_bf16 v[32:35], a[128:131], v[200:203], v[32:35]\n"
    "ds_read_b128 v[192:195], %[rdx1] offset:49152\n"
    "v_mfma_f32_16x16x32_bf16 v[36:39], a[132:135], v[200:203], v[36:39]\n"
    "ds_read_b64_tr_b16 a[156:157], %[rdw3] offset:53248\n"
    "v_mfma_f32_16x16x32_bf16 v[40:43], a[136:139], v[200:203], v[40:43]\n"
    "ds_read_b64_tr_b16 a[158:159], %[rdw3] offset:53760\n"
    "v_mfma_f32_16x16x32_bf16 v[44:47], a[140:143], v[200:203], v[44:47]\n"
    "buffer_load_dwordx4 %[vx2], %[srdx], s40 offen offset:2048 lds\n"
    "v_mfma_f32_16x16x32_bf16 v[60:63], a[140:143], v[204:207], v[60:63]\n"
    "ds_read_b128 v[196:199], %[rdx1] offset:51200\n"
    "v_mfma_f32_16x16x32_bf16 v[56:59], a[136:139], v[204:207], v[56:59]\n"
    "buffer_load_dwordx4 %[vx3], %[srdx], s40 offen offset:3072 lds\n"
    "v_mfma_f32_16x16x32_bf16 v[52:55], a[132:135], v[204:207], v[52:55]\n"
    "s_add_u32 m0, %[m0w], 0\n"
    "v_mfma_f32_16x16x32_bf16 v[48:51], a[128:131], v[204:207], v[48:51]\n"
    "buffer_load_dwordx4 %[vw0], %[srdw], s41 offen offset:0 nt lds\n"
    "s_waitcnt lgkmcnt(12)\n"
    "v_mfma_f32_16x16x32_bf16 v[64:67], a[128:131], v[208:211], v[64:67]\n"
    "ds_read_b128 v[200:203], %[rdx1] offset:53248\n"
    "v_mfma_f32_16x16x32_bf16 v[68:71], a[132:135], v[208:211], v[68:71]\n"
    "buffer_load_dwordx4 %[vw1], %[srdw], s41 offen offset:1024 nt lds\n"
    "v_mfma_f32_16x16x32_bf16 v[72:75], a[136:139], v[208:211], v[72:75]\n"
    "buffer_load_dwordx4 %[vw2], %[srdw], s41 offen offset:2048 nt lds\n"
    "v_mfma_f32_16x16x32_bf16 v[76:79], a[140:143], v[208:211], v[76:79]\n"
    "buffer_load_dwordx4 %[vw3], %[srdw], s41 offen offset:3072 nt lds\n"
    "v_mfma_f32_16x16x32_bf16 v[92:95], a[140:143], v[212:215], v[92:95]\n"
    "s_add_u32 m0, %[m0w], 4096\n"
    "ds_read_b128 v[204:207], %[rdx1] offset:55296\n"
    "v_mfma_f32_16x16x32_bf16 v[88:91], a[136:139], v[212:215], v[88:91]\n"
    "buffer_load_dwordx4 %[vw0], %[srdw], s47 offen offset:0 nt lds\n"
    "v_mfma_f32_16x16x32_bf16 v[84:87], a[132:135], v[212:215], v[84:87]\n"
    "buffer_load_dwordx4 %[vw1], %[srdw], s47 offen offset:1024 nt lds\n"
    "v_mfma_f32_16x16x32_bf16 v[80:83], a[128:131], v[212:215], v[80:83]\n"
    "s_waitcnt lgkmcnt(12)\n"
    "v_mfma_f32_16x16x32_bf16 v[96:99], a[128:131], v[216:219], v[96:99]\n"
    "ds_read_b128 v[208:211], %[rdx1] offset:57344\n"
    "v_mfma_f32_16x16x32_bf16 v[100:103], a[132:135], v[216:219], v[100:103]\n"
    "v_mfma_f32_16x16x32_bf16 v[104:107], a[136:139], v[216:219], v[104:107]\n"
    "buffer_load_dwordx4 %[vw2], %[srdw], s47 offen offset:2048 nt lds\n"
    "v_mfma_f32_16x16x32_bf16 v[108:111], a[140:143], v[216:219], v[108:111]\n"
    "s_waitcnt lgkmcnt(9)\n"
    "v_mfma_f32_16x16x32_bf16 v[124:127], a[140:143], v[220:223], v[124:127]\n"
    "ds_read_b128 v[212:215], %[rdx1] offset:59392\n"
    "v_mfma_f32_16x16x32_bf16 v[120:123], a[136:139], v[220:223], v[120:123]\n"
    "v_mfma_f32_16x16x32_bf16 v[116:119], a[132:135], v[220:223], v[116:119]\n"
    "buffer_load_dwordx4 %[vw3], %[srdw], s47 offen offset:3072 nt lds\n"
    "v_mfma_f32_16x16x32_bf16 v[112:115], a[128:131], v[220:223], v[112:115]\n"
    "s_waitcnt lgkmcnt(6)\n"
    "v_mfma_f32_16x16x32_bf16 v[0:3], a[144:147], v[192:195], v[0:3]\n"
    "ds_read_b128 v[216:219], %[rdx1] offset:61440\n"
    "v_mfma_f32_16x16x32_bf16 v[4:7], a[148:151], v[192:195], v[4:7]\n"
    "v_mfma_f32_16x16x32_bf16 v[8:11], a[152:155], v[192:195], v[8:11]\n"
    "s_waitcnt lgkmcnt(5)\n"
    "v_mfma_f32_16x16x32_bf16 v[12:15], a[156:159], v[192:195], v[12:15]\n"
    "v_mfma_f32_16x16x32_bf16 v[28:31], a[156:159], v[196:199], v[28:31]\n"
    "ds_read_b128 v[220:223], %[rdx1] offset:63488\n"
    "v_mfma_f32_16x16x32_bf16 v[24:27], a[152:155], v[196:199], v[24:27]\n"
    "v_mfma_f32_16x16x32_bf16 v[20:23], a[148:151], v[196:199], v[20:23]\n"
    "v_mfma_f32_16x16x32_bf16 v[16:19], a[144:147], v[196:199], v[16:19]\n"
    "s_waitcnt lgkmcnt(4)\n"
    "v_mfma_f32_16x16x32_bf16 v[32:35], a[144:147], v[200:203], v[32:35]\n"
    "v_mfma_f32_16x16x32_bf16 v[36:39], a[148:151], v[200:203], v[36:39]\n"
    "v_mfma_f32_16x16x32_bf16 v[40:43], a[152:155], v[200:203], v[40:43]\n"
    "v_mfma_f32_16x16x32_bf16 v[44:47], a[156:159], v[200:203], v[44:47]\n"
    "v_mfma_f32_16x16x32_bf16 v[60:63], a[156:159], v[204:207], v[60:63]\n"
    "v_mfma_f32_16x16x32_bf16 v[56:59], a[152:155], v[204:207], v[56:59]\n"
    "v_mfma_f32_16x16x32_bf16 v[52:55], a[148:151], v[204:207], v[52:55]\n"
    "v_mfma_f32_16x16x32_bf16 v[48:51], a[144:147], v[204:207], v[48:51]\n"
    "s_waitcnt vmcnt(12) lgkmcnt(0)\n"
    "s_barrier\n"
    "v_mfma_f32_16x16x32_bf16 v[64:67], a[144:147], v[208:211], v[64:67]\n"
    "ds_read_b64_tr_b16 a[160:161], %[rdw4] offset:0\n"
    "v_mfma_f32_16x16x32_bf16 v[68:71], a[148:151], v[208:211], v[68:71]\n"
    "ds_read_b64_tr_b16 a[162:163], %[rdw4] offset:512\n"
    "v_mfma_f32_16x16x32_bf16 v[72:75], a[152:155], v[208:211], v[72:75]\n"
    "ds_read_b128 v[192:195], %[rdx2] offset:0\n"
    "v_mfma_f32_16x16x32_bf16 v[76:79], a[156:159], v[208:211], v[76:79]\n"
    "ds_read_b64_tr_b16 a[164:165], %[rdw5] offset:0\n"
    "v_mfma_f32_16x16x32_bf16 v[92:95], a[156:159], v[212:215], v[92:95]\n"
    "ds_read_b64_tr_b16 a[166:167], %[rdw5] offset:512\n"
    "v_mfma_f32_16x16x32_bf16 v[88:91], a[152:155], v[212:215], v[88:91]\n"
    "ds_read_b128 v[196:199], %[rdx2] offset:2048\n"
    "v_mfma_f32_16x16x32_bf16 v[84:87], a[148:151], v[212:215], v[84:87]\n"
    "ds_read_b64_tr_b16 a[168:169], %[rdw6] offset:0\n"
    "v_mfma_f32_16x16x32_bf16 v[80:83], a[144:147], v[212:215], v[80:83]\n"
    "ds_read_b64_tr_b16 a[170:171], %[rdw6] offset:512\n"
    "v_mfma_f32_16x16x32_bf16 v[96:99], a[144:147], v[216:219], v[96:99]\n"
    "ds_read_b128 v[200:203], %[rdx2] offset:4096\n"
    "v_mfma_f32_16x16x32_bf16 v[100:103], a[148:151], v[216:219], v[100:103]\n"
    "ds_read_b64_tr_b16 a[172:173], %[rdw7] offset:0\n"
    "v_mfma_f32_16x16x32_bf16 v[104:107], a[152:155], v[216:219], v[104:107]\n"
    "s_add_u32 s42, s42, 1\n"
    "s_and_b32 s42, s42, 7\n"
    "ds_read_b64_tr_b16 a[174:175], %[rdw7] offset:512\n"
    "v_mfma_f32_16x16x32_bf16 v[108:111], a[156:159], v[216:219], v[108:111]\n"
    "s_lshl_b32 s43, s42, 7\n"
    "s_add_u32 s40, s43, %[sox]\n"
    "ds_read_b128 v[204:207], %[rdx2] offset:6144\n"
    "v_mfma_f32_16x16x32_bf16 v[124:127], a[156:159], v[220:223], v[124:127]\n"
    "s_lshl_b32 s43, s42, 19\n"
    "s_add_u32 s41, s43, %[sow]\n"
    "ds_read_b128 v[208:211], %[rdx2] offset:8192\n"
    "v_mfma_f32_16x16x32_bf16 v[120:123], a[152:155], v[220:223], v[120:123]\n"
    "s_add_u32 s47, s41, 262144\n"
    "s_add_u32 m0, %[m0x], 49152\n"
    "ds_read_b128 v[212:215], %[rdx2] offset:10240\n"
    "v_mfma_f32_16x16x32_bf16 v[116:119], a[148:151], v[220:223], v[116:119]\n"
    "buffer_load_dwordx4 %[vx0], %[srdx], s40 offen offset:0 lds\n"
    "v_mfma_f32_16x16x32_bf16 v[112:115], a[144:147], v[220:223], v[112:115]\n"
    "buffer_load_dwordx4 %[vx1], %[srdx], s40 offen offset:1024 lds\n"
    "s_waitcnt lgkmcnt(10)\n"
    "v_mfma_f32_16x16x32_bf16 v[0:3], a[160:163], v[192:195], v[0:3]\n"
    "ds_read_b128 v[216:219], %[rdx2] offset:12288\n"
    "s_waitcnt lgkmcnt(9)\n"
    "v_mfma_f32_16x16x32_bf16 v[4:7], a[164:167], v[192:195], v[4:7]\n"
    "ds_read_b64_tr_b16 a[176:177], %[rdw4] offset:4096\n"
    "s_waitcnt lgkmcnt(7)\n"
    "v_mfma_f32_16x16x32_bf16 v[8:11], a[168:171], v[192:195], v[8:11]\n"
    "ds_read_b64_tr_b16 a[178:179], %[rdw4] offset:4608\n"
    "s_waitcnt lgkmcnt(5)\n"
    "v_mfma_f32_16x16x32_bf16 v[12:15], a[172:175], v[192:195], v[12:15]\n"
    "ds_read_b64_tr_b16 a[180:181], %[rdw5] offset:4096\n"
    "v_mfma_f32_16x16x32_bf16 v[28:31], a[172:175], v[196:199], v[28:31]\n"
    "ds_read_b128 v[220:223], %[rdx2] offset:14336\n"
    "v_mfma_f32_16x16x32_bf16 v[24:27], a[168:171], v[196:199], v[24:27]\n"
    "ds_read_b64_tr_b16 a[182:183], %[rdw5] offset:4608\n"
    "v_mfma_f32_16x16x32_bf16 v[20:23], a[164:167], v[196:199], v[20:23]\n"
    "ds_read_b64_tr_b16 a[184:185], %[rdw6] offset:4096\n"
    "v_mfma_f32_16x16x32_bf16 v[16:19], a[160:163], v[196:199], v[16:19]\n"
    "ds_read_b64_tr_b16 a[186:187], %[rdw6] offset:4608\n"
    "v_mfma_f32_16x16x32_bf16 v[32:35], a[160:163], v[200:203], v[32:35]\n"
    "ds_read_b128 v[192:195], %[rdx3] offset:0\n"
    "v_mfma_f32_16x16x32_bf16 v[36:39], a[164:167], v[200:203], v[36:39]\n"
    "ds_read_b64_tr_b16 a[188:189], %[rdw7] offset:4096\n"
    "v_mfma_f32_16x16x32_bf16 v[40:43], a[168:171], v[200:203], v[40:43]\n"
    "ds_read_b64_tr_b16 a[190:191], %[rdw7] offset:4608\n"
    "v_mfma_f32_16x16x32_bf16 v[44:47], a[172:175], v[200:203], v[44:47]\n"
    "buffer_load_dwordx4 %[vx2], %[srdx], s40 offen offset:2048 lds\n"
    "v_mfma_f32_16x16x32_bf16 v[60:63], a[172:175], v[204:207], v[60:63]\n"
    "ds_read_b128 v[196:199], %[rdx3] offset:2048\n"
    "v_mfma_f32_16x16x32_bf16 v[56:59], a[168:171], v[204:207], v[56:59]\n"
    "buffer_load_dwordx4 %[vx3], %[srdx], s40 offen offset:3072 lds\n"
    "v_mfma_f32_16x16x32_bf16 v[52:55], a[164:167], v[204:207], v[52:55]\n"
    "s_add_u32 m0, %[m0w], 49152\n"
    "v_mfma_f32_16x16x32_bf16 v[48:51], a[160:163], v[204:207], v[48:51]\n"
    "buffer_load_dwordx4 %[vw0], %[srdw], s41 offen offset:0 nt lds\n"
    "s_waitcnt lgkmcnt(12)\n"
    "v_mfma_f32_16x16x32_bf16 v[64:67], a[160:163], v[208:211], v[64:67]\n"
    "ds_read_b128 v[200:203], %[rdx3] offset:4096\n"
    "v_mfma_f32_16x16x32_bf16 v[68:71], a[164:167], v[208:211], v[68:71]\n"
    "buffer_load_dwordx4 %[vw1], %[srdw], s41 offen offset:1024 nt lds\n"
    "v_mfma_f32_16x16x32_bf16 v[72:75], a[168:171], v[208:211], v[72:75]\n"
    "buffer_load_dwordx4 %[vw2], %[srdw], s41 offen offset:2048 nt lds\n"
    "v_mfma_f32_16x16x32_bf16 v[76:79], a[172:175], v[208:211], v[76:79]\n"
    "buffer_load_dwordx4 %[vw3], %[srdw], s41 offen offset:3072 nt lds\n"
    "v_mfma_f32_16x16x32_bf16 v[92:95], a[172:175], v[212:215], v[92:95]\n"
    "s_add_u32 m0, %[m0w], 53248\n"
    "ds_read_b128 v[204:207], %[rdx3] offset:6144\n"
    "v_mfma_f32_16x16x32_bf16 v[88:91], a[168:171], v[212:215], v[88:91]\n"
    "buffer_load_dwordx4 %[vw0], %[srdw], s47 offen offset:0 nt lds\n"
    "v_mfma_f32_16x16x32_bf16 v[84:87], a[164:167], v[212:215], v[84:87]\n"
    "buffer_load_dwordx4 %[vw1], %[srdw], s47 offen offset:1024 nt lds\n"
    "v_mfma_f32_16x16x32_bf16 v[80:83], a[160:163], v[212:215], v[80:83]\n"
    "s_waitcnt lgkmcnt(12)\n"
    "v_mfma_f32_16x16x32_bf16 v[96:99], a[160:163], v[216:219], v[96:99]\n"
    "ds_read_b128 v[208:211], %[rdx3] offset:8192\n"
    "v_mfma_f32_16x16x32_bf16 v[100:103], a[164:167], v[216:219], v[100:103]\n"
    "v_mfma_f32_16x16x32_bf16 v[104:107], a[168:171], v[216:219], v[104:107]\n"
    "buffer_load_dwordx4 %[vw2], %[srdw], s47 offen offset:2048 nt lds\n"
    "v_mfma_f32_16x16x32_bf16 v[108:111], a[172:175], v[216:219], v[108:111]\n"
    "s_waitcnt lgkmcnt(9)\n"
    "v_mfma_f32_16x16x32_bf16 v[124:127], a[172:175], v[220:223], v[124:127]\n"
    "ds_read_b128 v[212:215], %[rdx3] offset:10240\n"
    "v_mfma_f32_16x16x32_bf16 v[120:123], a[168:171], v[220:223], v[120:123]\n"
    "v_mfma_f32_16x16x32_bf16 v[116:119], a[164:167], v[220:223], v[116:119]\n"
    "buffer_load_dwordx4 %[vw3], %[srdw], s47 offen offset:3072 nt lds\n"
    "v_mfma_f32_16x16x32_bf16 v[112:115], a[160:163], v[220:223], v[112:115]\n"
    "s_waitcnt lgkmcnt(6)\n"
    "v_mfma_f32_16x16x32_bf16 v[0:3], a[176:179], v[192:195], v[0:3]\n"
    "ds_read_b128 v[216:219], %[rdx3] offset:12288\n"
    "v_mfma_f32_16x16x32_bf16 v[4:7], a[180:183], v[192:195], v[4:7]\n"
    "v_mfma_f32_16x16x32_bf16 v[8:11], a[184:187], v[192:195], v[8:11]\n"
    "s_waitcnt lgkmcnt(5)\n"
    "v_mfma_f32_16x16x32_bf16 v[12:15], a[188:191], v[192:195], v[12:15]\n"
    "v_mfma_f32_16x16x32_bf16 v[28:31], a[188:191], v[196:199], v[28:31]\n"
    "ds_read_b128 v[220:223], %[rdx3] offset:14336\n"
    "v_mfma_f32_16x16x32_bf16 v[24:27], a[184:187], v[196:199], v[24:27]\n"
    "v_mfma_f32_16x16x32_bf16 v[20:23], a[180:183], v[196:199], v[20:23]\n"
    "v_mfma_f32_16x16x32_bf16 v[16:19], a[176:179], v[196:199], v[16:19]\n"
    "s_waitcnt lgkmcnt(4)\n"
    "v_mfma_f32_16x16x32_bf16 v[32:35], a[176:179], v[200:203], v[32:35]\n"
    "v_mfma_f32_16x16x32_bf16 v[36:39], a[180:183], v[200:203], v[36:39]\n"
    "v_mfma_f32_16x16x32_bf16 v[40:43], a[184:187], v[200:203], v[40:43]\n"
    "v_mfma_f32_16x16x32_bf16 v[44:47], a[188:191], v[200:203], v[44:47]\n"
    "v_mfma_f32_16x16x32_bf16 v[60:63], a[188:191], v[204:207], v[60:63]\n"
    "v_mfma_f32_16x16x32_bf16 v[56:59], a[184:187], v[204:207], v[56:59]\n"
    "v_mfma_f32_16x16x32_bf16 v[52:55], a[180:183], v[204:207], v[52:55]\n"
    "v_mfma_f32_16x16x32_bf16 v[48:51], a[176:179], v[204:207], v[48:51]\n"
    "s_waitcnt vmcnt(12) lgkmcnt(0)\n"
    "s_barrier\n"
    "v_mfma_f32_16x16x32_bf16 v[64:67], a[176:179], v[208:211], v[64:67]\n"
    "ds_read_b64_tr_b16 a[192:193], %[rdw0] offset:0\n"
    "v_mfma_f32_16x16x32_bf16 v[68:71], a[180:183], v[208:211], v[68:71]\n"
    "ds_read_b64_tr_b16 a[194:195], %[rdw0] offset:512\n"
    "v_mfma_f32_16x16x32_bf16 v[72:75], a[184:187], v[208:211], v[72:75]\n"
    "ds_read_b128 v[192:195], %[rdx0] offset:0\n"
    "v_mfma_f32_16x16x32_bf16 v[76:79], a[188:191], v[208:211], v[76:79]\n"
    "ds_read_b64_tr_b16 a[196:197], %[rdw1] offset:0\n"
    "v_mfma_f32_16x16x32_bf16 v[92:95], a[188:191], v[212:215], v[92:95]\n"
    "ds_read_b64_tr_b16 a[198:199], %[rdw1] offset:512\n"
    "v_mfma_f32_16x16x32_bf16 v[88:91], a[184:187], v[212:215], v[88:91]\n"
    "ds_read_b128 v[196:199], %[rdx0] offset:2048\n"
    "v_mfma_f32_16x16x32_bf16 v[84:87], a[180:183], v[212:215], v[84:87]\n"
    "ds_read_b64_tr_b16 a[200:201], %[rdw2] offset:0\n"
    "v_mfma_f32_16x16x32_bf16 v[80:83], a[176:179], v[212:215], v[80:83]\n"
    "ds_read_b64_tr_b16 a[202:203], %[rdw2] offset:512\n"
    "v_mfma_f32_16x16x32_bf16 v[96:99], a[176:179], v[216:219], v[96:99]\n"
    "ds_read_b128 v[200:203], %[rdx0] offset:4096\n"
    "v_mfma_f32_16x16x32_bf16 v[100:103], a[180:183], v[216:219], v[100:103]\n"
    "ds_read_b64_tr_b16 a[204:205], %[rdw3] offset:0\n"
    "v_mfma_f32_16x16x32_bf16 v[104:107], a[184:187], v[216:219], v[104:107]\n"
    "ds_read_b64_tr_b16 a[206:207], %[rdw3] offset:512\n"
    "v_mfma_f32_16x16x32_bf16 v[108:111], a[188:191], v[216:219], v[108:111]\n"
    "ds_read_b128 v[204:207], %[rdx0] offset:6144\n"
    "v_mfma_f32_16x16x32_bf16 v[124:127], a[188:191], v[220:223], v[124:127]\n"
    "s_add_u32 s45, s45, 128\n"
    "s_and_b32 s45, s45, 1023\n"
    "ds_read_b128 v[208:211], %[rdx0] offset:8192\n"
    "v_mfma_f32_16x16x32_bf16 v[120:123], a[184:187], v[220:223], v[120:123]\n"
    "s_add_u32 s44, s45, %[sot0]\n"
    "s_add_u32 m0, %[m0t], 98304\n"
    "ds_read_b128 v[212:215], %[rdx0] offset:10240\n"
    "v_mfma_f32_16x16x32_bf16 v[116:119], a[180:183], v[220:223], v[116:119]\n"
    "buffer_load_dwordx4 %[vx0], %[srdx], s44 offen lds\n"
    "v_mfma_f32_16x16x32_bf16 v[112:115], a[176:179], v[220:223], v[112:115]\n"
    "buffer_load_dwordx4 %[vx1], %[srdx], s44 offen offset:1024 lds\n"
    "s_waitcnt lgkmcnt(10)\n"
    "v_mfma_f32_16x16x32_bf16 v[0:3], a[192:195], v[192:195], v[0:3]\n"
    "ds_read_b128 v[216:219], %[rdx0] offset:12288\n"
    "s_waitcnt lgkmcnt(9)\n"
    "v_mfma_f32_16x16x32_bf16 v[4:7], a[196:199], v[192:195], v[4:7]\n"
    "ds_read_b64_tr_b16 a[208:209], %[rdw0] offset:4096\n"
    "s_waitcnt lgkmcnt(7)\n"
    "v_mfma_f32_16x16x32_bf16 v[8:11], a[200:203], v[192:195], v[8:11]\n"
    "ds_read_b64_tr_b16 a[210:211], %[rdw0] offset:4608\n"
    "s_waitcnt lgkmcnt(5)\n"
    "v_mfma_f32_16x16x32_bf16 v[12:15], a[204:207], v[192:195], v[12:15]\n"
    "ds_read_b64_tr_b16 a[212:213], %[rdw1] offset:4096\n"
    "v_mfma_f32_16x16x32_bf16 v[28:31], a[204:207], v[196:199], v[28:31]\n"
    "ds_read_b128 v[220:223], %[rdx0] offset:14336\n"
    "v_mfma_f32_16x16x32_bf16 v[24:27], a[200:203], v[196:199], v[24:27]\n"
    "ds_read_b64_tr_b16 a[214:215], %[rdw1] offset:4608\n"
    "v_mfma_f32_16x16x32_bf16 v[20:23], a[196:199], v[196:199], v[20:23]\n"
    "ds_read_b64_tr_b16 a[216:217], %[rdw2] offset:4096\n"
    "v_mfma_f32_16x16x32_bf16 v[16:19], a[192:195], v[196:199], v[16:19]\n"
    "ds_read_b64_tr_b16 a[218:219], %[rdw2] offset:4608\n"
    "v_mfma_f32_16x16x32_bf16 v[32:35], a[192:195], v[200:203], v[32:35]\n"
    "ds_read_b128 v[192:195], %[rdx1] offset:0\n"
    "v_mfma_f32_16x16x32_bf16 v[36:39], a[196:199], v[200:203], v[36:39]\n"
    "s_add_u32 s45, s45, 128\n"
    "s_and_b32 s45, s45, 1023\n"
    "ds_read_b64_tr_b16 a[220:221], %[rdw3] offset:4096\n"
    "v_mfma_f32_16x16x32_bf16 v[40:43], a[200:203], v[200:203], v[40:43]\n"
    "s_add_u32 s44, s45, %[sot0]\n"
    "s_add_u32 m0, %[m0t], 106496\n"
    "ds_read_b64_tr_b16 a[222:223], %[rdw3] offset:4608\n"
    "v_mfma_f32_16x16x32_bf16 v[44:47], a[204:207], v[200:203], v[44:47]\n"
    "buffer_load_dwordx4 %[vx0], %[srdx], s44 offen lds\n"
    "v_mfma_f32_16x16x32_bf16 v[60:63], a[204:207], v[204:207], v[60:63]\n"
    "ds_read_b128 v[196:199], %[rdx1] offset:2048\n"
    "v_mfma_f32_16x16x32_bf16 v[56:59], a[200:203], v[204:207], v[56:59]\n"
    "buffer_load_dwordx4 %[vx1], %[srdx], s44 offen offset:1024 lds\n"
    "v_mfma_f32_16x16x32_bf16 v[52:55], a[196:199], v[204:207], v[52:55]\n"
    "v_mfma_f32_16x16x32_bf16 v[48:51], a[192:195], v[204:207], v[48:51]\n"
    "s_add_u32 s45, s45, 128\n"
    "s_and_b32 s45, s45, 1023\n"
    "s_waitcnt lgkmcnt(12)\n"
    "v_mfma_f32_16x16x32_bf16 v[64:67], a[192:195], v[208:211], v[64:67]\n"
    "s_add_u32 s44, s45, %[sot0]\n"
    "s_add_u32 m0, %[m0t], 114688\n"
    "ds_read_b128 v[200:203], %[rdx1] offset:4096\n"
    "v_mfma_f32_16x16x32_bf16 v[68:71], a[196:199], v[208:211], v[68:71]\n"
    "buffer_load_dwordx4 %[vx0], %[srdx], s44 offen lds\n"
    "v_mfma_f32_16x16x32_bf16 v[72:75], a[200:203], v[208:211], v[72:75]\n"
    "buffer_load_dwordx4 %[vx1], %[srdx], s44 offen offset:1024 lds\n"
    "v_mfma_f32_16x16x32_bf16 v[76:79], a[204:207], v[208:211], v[76:79]\n"
    "s_add_u32 s45, s45, 128\n"
    "s_and_b32 s45, s45, 1023\n"
    "v_mfma_f32_16x16x32_bf16 v[92:95], a[204:207], v[212:215], v[92:95]\n"
    "s_add_u32 s44, s45, %[sot0]\n"
    "s_add_u32 m0, %[m0t], 122880\n"
    "ds_read_b128 v[204:207], %[rdx1] offset:6144\n"
    "v_mfma_f32_16x16x32_bf16 v[88:91], a[200:203], v[212:215], v[88:91]\n"
    "buffer_load_dwordx4 %[vx0], %[srdx], s44 offen lds\n"
    "v_mfma_f32_16x16x32_bf16 v[84:87], a[196:199], v[212:215], v[84:87]\n"
    "buffer_load_dwordx4 %[vx1], %[srdx], s44 offen offset:1024 lds\n"
    "v_mfma_f32_16x16x32_bf16 v[80:83], a[192:195], v[212:215], v[80:83]\n"
    "s_add_u32 s45, s45, 128\n"
    "s_and_b32 s45, s45, 1023\n"
    "s_waitcnt lgkmcnt(12)\n"
    "v_mfma_f32_16x16x32_bf16 v[96:99], a[192:195], v[216:219], v[96:99]\n"
    "s_add_u32 s44, s45, %[sot0]\n"
    "s_add_u32 m0, %[m0t], 131072\n"
    "ds_read_b128 v[208:211], %[rdx1] offset:8192\n"
    "v_mfma_f32_16x16x32_bf16 v[100:103], a[196:199], v[216:219], v[100:103]\n"
    "buffer_load_dwordx4 %[vx0], %[srdx], s44 offen lds\n"
    "v_mfma_f32_16x16x32_bf16 v[104:107], a[200:203], v[216:219], v[104:107]\n"
    "buffer_load_dwordx4 %[vx1], %[srdx], s44 offen offset:1024 lds\n"
    "v_mfma_f32_16x16x32_bf16 v[108:111], a[204:207], v[216:219], v[108:111]\n"
    "s_add_u32 s45, s45, 128\n"
    "s_and_b32 s45, s45, 1023\n"
    "s_waitcnt lgkmcnt(9)\n"
    "v_mfma_f32_16x16x32_bf16 v[124:127], a[204:207], v[220:223], v[124:127]\n"
    "s_add_u32 s44, s45, %[sot0]\n"
    "s_add_u32 m0, %[m0t], 139264\n"
    "ds_read_b128 v[212:215], %[rdx1] offset:10240\n"
    "v_mfma_f32_16x16x32_bf16 v[120:123], a[200:203], v[220:223], v[120:123]\n"
    "buffer_load_dwordx4 %[vx0], %[srdx], s44 offen lds\n"
    "v_mfma_f32_16x16x32_bf16 v[116:119], a[196:199], v[220:223], v[116:119]\n"
    "buffer_load_dwordx4 %[vx1], %[srdx], s44 offen offset:1024 lds\n"
    "v_mfma_f32_16x16x32_bf16 v[112:115], a[192:195], v[220:223], v[112:115]\n"
    "s_add_u32 s45, s45, 128\n"
    "s_and_b32 s45, s45, 1023\n"
    "s_waitcnt lgkmcnt(6)\n"
    "v_mfma_f32_16x16x32_bf16 v[0:3], a[208:211], v[192:195], v[0:3]\n"
    "s_add_u32 s44, s45, %[sot0]\n"
    "s_add_u32 m0, %[m0t], 147456\n"
    "ds_read_b128 v[216:219], %[rdx1] offset:12288\n"
    "v_mfma_f32_16x16x32_bf16 v[4:7], a[212:215], v[192:195], v[4:7]\n"
    "buffer_load_dwordx4 %[vx0], %[srdx], s44 offen lds\n"
    "v_mfma_f32_16x16x32_bf16 v[8:11], a[216:219], v[192:195], v[8:11]\n"
    "buffer_load_dwordx4 %[vx1], %[srdx], s44 offen offset:1024 lds\n"
    "s_waitcnt lgkmcnt(5)\n"
    "v_mfma_f32_16x16x32_bf16 v[12:15], a[220:223], v[192:195], v[12:15]\n"
    "s_add_u32 s45, s45, 128\n"
    "s_and_b32 s45, s45, 1023\n"
    "v_mfma_f32_16x16x32_bf16 v[28:31], a[220:223], v[196:199], v[28:31]\n"
    "s_add_u32 s44, s45, %[sot0]\n"
    "s_add_u32 m0, %[m0t], 155648\n"
    "ds_read_b128 v[220:223], %[rdx1] offset:14336\n"
    "v_mfma_f32_16x16x32_bf16 v[24:27], a[216:219], v[196:199], v[24:27]\n"
    "buffer_load_dwordx4 %[vx0], %[srdx], s44 offen lds\n"
    "v_mfma_f32_16x16x32_bf16 v[20:23], a[212:215], v[196:199], v[20:23]\n"
    "buffer_load_dwordx4 %[vx1], %[srdx], s44 offen offset:1024 lds\n"
    "v_mfma_f32_16x16x32_bf16 v[16:19], a[208:211], v[196:199], v[16:19]\n"
    "s_waitcnt lgkmcnt(4)\n"
    "v_mfma_f32_16x16x32_bf16 v[32:35], a[208:211], v[200:203], v[32:35]\n"
    "v_mfma_f32_16x16x32_bf16 v[36:39], a[212:215], v[200:203], v[36:39]\n"
    "v_mfma_f32_16x16x32_bf16 v[40:43], a[216:219], v[200:203], v[40:43]\n"
    "v_mfma_f32_16x16x32_bf16 v[44:47], a[220:223], v[200:203], v[44:47]\n"
    "v_mfma_f32_16x16x32_bf16 v[60:63], a[220:223], v[204:207], v[60:63]\n"
    "v_mfma_f32_16x16x32_bf16 v[56:59], a[216:219], v[204:207], v[56:59]\n"
    "v_mfma_f32_16x16x32_bf16 v[52:55], a[212:215], v[204:207], v[52:55]\n"
    "v_mfma_f32_16x16x32_bf16 v[48:51], a[208:211], v[204:207], v[48:51]\n"
    "s_waitcnt vmcnt(16) lgkmcnt(0)\n"
    "s_barrier\n"
    "v_mfma_f32_16x16x32_bf16 v[64:67], a[208:211], v[208:211], v[64:67]\n"
    "ds_read_b64_tr_b16 a[224:225], %[rdw0] offset:49152\n"
    "v_mfma_f32_16x16x32_bf16 v[68:71], a[212:215], v[208:211], v[68:71]\n"
    "ds_read_b64_tr_b16 a[226:227], %[rdw0] offset:49664\n"
    "v_mfma_f32_16x16x32_bf16 v[72:75], a[216:219], v[208:211], v[72:75]\n"
    "ds_read_b128 v[192:195], %[rdx0] offset:49152\n"
    "v_mfma_f32_16x16x32_bf16 v[76:79], a[220:223], v[208:211], v[76:79]\n"
    "ds_read_b64_tr_b16 a[228:229], %[rdw1] offset:49152\n"
    "v_mfma_f32_16x16x32_bf16 v[92:95], a[220:223], v[212:215], v[92:95]\n"
    "ds_read_b64_tr_b16 a[230:231], %[rdw1] offset:49664\n"
    "v_mfma_f32_16x16x32_bf16 v[88:91], a[216:219], v[212:215], v[88:91]\n"
    "ds_read_b128 v[196:199], %[rdx0] offset:51200\n"
    "v_mfma_f32_16x16x32_bf16 v[84:87], a[212:215], v[212:215], v[84:87]\n"
    "ds_read_b64_tr_b16 a[232:233], %[rdw2] offset:49152\n"
    "v_mfma_f32_16x16x32_bf16 v[80:83], a[208:211], v[212:215], v[80:83]\n"
    "ds_read_b64_tr_b16 a[234:235], %[rdw2] offset:49664\n"
    "v_mfma_f32_16x16x32_bf16 v[96:99], a[208:211], v[216:219], v[96:99]\n"
    "ds_read_b128 v[200:203], %[rdx0] offset:53248\n"
    "v_mfma_f32_16x16x32_bf16 v[100:103], a[212:215], v[216:219], v[100:103]\n"
    "ds_read_b64_tr_b16 a[236:237], %[rdw3] offset:49152\n"
    "v_mfma_f32_16x16x32_bf16 v[104:107], a[216:219], v[216:219], v[104:107]\n"
    "ds_read_b64_tr_b16 a[238:239], %[rdw3] offset:49664\n"
    "v_mfma_f32_16x16x32_bf16 v[108:111], a[220:223], v[216:219], v[108:111]\n"
    "ds_read_b128 v[204:207], %[rdx0] offset:55296\n"
    "v_mfma_f32_16x16x32_bf16 v[124:127], a[220:223], v[220:223], v[124:127]\n"
    "s_add_u32 s45, s45, 128\n"
    "s_and_b32 s45, s45, 1023\n"
    "ds_read_b128 v[208:211], %[rdx0] offset:57344\n"
    "v_mfma_f32_16x16x32_bf16 v[120:123], a[216:219], v[220:223], v[120:123]\n"
    "s_add_u32 s44, s45, %[sot1]\n"
    "s_add_u32 m0, %[m0t], 0\n"
    "ds_read_b128 v[212:215], %[rdx0] offset:59392\n"
    "v_mfma_f32_16x16x32_bf16 v[116:119], a[212:215], v[220:223], v[116:119]\n"
    "buffer_load_dwordx4 %[vx0], %[srdx], s44 offen lds\n"
    "v_mfma_f32_16x16x32_bf16 v[112:115], a[208:211], v[220:223], v[112:115]\n"
    "buffer_load_dwordx4 %[vx1], %[srdx], s44 offen offset:1024 lds\n"
    "s_waitcnt lgkmcnt(10)\n"
    "v_mfma_f32_16x16x32_bf16 v[0:3], a[224:227], v[192:195], v[0:3]\n"
    "ds_read_b128 v[216:219], %[rdx0] offset:61440\n"
    "s_waitcnt lgkmcnt(9)\n"
    "v_mfma_f32_16x16x32_bf16 v[4:7], a[228:231], v[192:195], v[4:7]\n"
    "ds_read_b64_tr_b16 a[240:241], %[rdw0] offset:53248\n"
    "s_waitcnt lgkmcnt(7)\n"
    "v_mfma_f32_16x16x32_bf16 v[8:11], a[232:235], v[192:195], v[8:11]\n"
    "ds_read_b64_tr_b16 a[242:243], %[rdw0] offset:53760\n"
    "s_waitcnt lgkmcnt(5)\n"
    "v_mfma_f32_16x16x32_bf16 v[12:15], a[236:239], v[192:195], v[12:15]\n"
    "ds_read_b64_tr_b16 a[244:245], %[rdw1] offset:53248\n"
    "v_mfma_f32_16x16x32_bf16 v[28:31], a[236:239], v[196:199], v[28:31]\n"
    "ds_read_b128 v[220:223], %[rdx0] offset:63488\n"
    "v_mfma_f32_16x16x32_bf16 v[24:27], a[232:235], v[196:199], v[24:27]\n"
    "ds_read_b64_tr_b16 a[246:247], %[rdw1] offset:53760\n"
    "v_mfma_f32_16x16x32_bf16 v[20:23], a[228:231], v[196:199], v[20:23]\n"
    "ds_read_b64_tr_b16 a[248:249], %[rdw2] offset:53248\n"
    "v_mfma_f32_16x16x32_bf16 v[16:19], a[224:227], v[196:199], v[16:19]\n"
    "ds_read_b64_tr_b16 a[250:251], %[rdw2] offset:53760\n"
    "v_mfma_f32_16x16x32_bf16 v[32:35], a[224:227], v[200:203], v[32:35]\n"
    "ds_read_b128 v[192:195], %[rdx1] offset:49152\n"
    "v_mfma_f32_16x16x32_bf16 v[36:39], a[228:231], v[200:203], v[36:39]\n"
    "s_add_u32 s45, s45, 128\n"
    "s_and_b32 s45, s45, 1023\n"
    "ds_read_b64_tr_b16 a[252:253], %[rdw3] offset:53248\n"
    "v_mfma_f32_16x16x32_bf16 v[40:43], a[232:235], v[200:203], v[40:43]\n"
    "s_add_u32 s44, s45, %[sot1]\n"
    "s_add_u32 m0, %[m0t], 8192\n"
    "ds_read_b64_tr_b16 a[254:255], %[rdw3] offset:53760\n"
    "v_mfma_f32_16x16x32_bf16 v[44:47], a[236:239], v[200:203], v[44:47]\n"
    "buffer_load_dwordx4 %[vx0], %[srdx], s44 offen lds\n"
    "v_mfma_f32_16x16x32_bf16 v[60:63], a[236:239], v[204:207], v[60:63]\n"
    "ds_read_b128 v[196:199], %[rdx1] offset:51200\n"
    "v_mfma_f32_16x16x32_bf16 v[56:59], a[232:235], v[204:207], v[56:59]\n"
    "buffer_load_dwordx4 %[vx1], %[srdx], s44 offen offset:1024 lds\n"
    "v_mfma_f32_16x16x32_bf16 v[52:55], a[228:231], v[204:207], v[52:55]\n"
    "v_mfma_f32_16x16x32_bf16 v[48:51], a[224:227], v[204:207], v[48:51]\n"
    "s_add_u32 s45, s45, 128\n"
    "s_and_b32 s45, s45, 1023\n"
    "s_waitcnt lgkmcnt(12)\n"
    "v_mfma_f32_16x16x32_bf16 v[64:67], a[224:227], v[208:211], v[64:67]\n"
    "s_add_u32 s44, s45, %[sot1]\n"
    "s_add_u32 m0, %[m0t], 16384\n"
    "ds_read_b128 v[200:203], %[rdx1] offset:53248\n"
    "v_mfma_f32_16x16x32_bf16 v[68:71], a[228:231], v[208:211], v[68:71]\n"
    "buffer_load_dwordx4 %[vx0], %[srdx], s44 offen lds\n"
    "v_mfma_f32_16x16x32_bf16 v[72:75], a[232:235], v[208:211], v[72:75]\n"
    "buffer_load_dwordx4 %[vx1], %[srdx], s44 offen offset:1024 lds\n"
    "v_mfma_f32_16x16x32_bf16 v[76:79], a[236:239], v[208:211], v[76:79]\n"
    "s_add_u32 s45, s45, 128\n"
    "s_and_b32 s45, s45, 1023\n"
    "v_mfma_f32_16x16x32_bf16 v[92:95], a[236:239], v[212:215], v[92:95]\n"
    "s_add_u32 s44, s45, %[sot1]\n"
    "s_add_u32 m0, %[m0t], 24576\n"
    "ds_read_b128 v[204:207], %[rdx1] offset:55296\n"
    "v_mfma_f32_16x16x32_bf16 v[88:91], a[232:235], v[212:215], v[88:91]\n"
    "buffer_load_dwordx4 %[vx0], %[srdx], s44 offen lds\n"
    "v_mfma_f32_16x16x32_bf16 v[84:87], a[228:231], v[212:215], v[84:87]\n"
    "buffer_load_dwordx4 %[vx1], %[srdx], s44 offen offset:1024 lds\n"
    "v_mfma_f32_16x16x32_bf16 v[80:83], a[224:227], v[212:215], v[80:83]\n"
    "s_add_u32 s45, s45, 128\n"
    "s_and_b32 s45, s45, 1023\n"
    "s_waitcnt lgkmcnt(12)\n"
    "v_mfma_f32_16x16x32_bf16 v[96:99], a[224:227], v[216:219], v[96:99]\n"
    "s_add_u32 s44, s45, %[sot1]\n"
    "s_add_u32 m0, %[m0t], 32768\n"
    "ds_read_b128 v[208:211], %[rdx1] offset:57344\n"
    "v_mfma_f32_16x16x32_bf16 v[100:103], a[228:231], v[216:219], v[100:103]\n"
    "buffer_load_dwordx4 %[vx0], %[srdx], s44 offen lds\n"
    "v_mfma_f32_16x16x32_bf16 v[104:107], a[232:235], v[216:219], v[104:107]\n"
    "buffer_load_dwordx4 %[vx1], %[srdx], s44 offen offset:1024 lds\n"
    "v_mfma_f32_16x16x32_bf16 v[108:111], a[236:239], v[216:219], v[108:111]\n"
    "s_add_u32 s45, s45, 128\n"
    "s_and_b32 s45, s45, 1023\n"
    "s_waitcnt lgkmcnt(9)\n"
    "v_mfma_f32_16x16x32_bf16 v[124:127], a[236:239], v[220:223], v[124:127]\n"
    "s_add_u32 s44, s45, %[sot1]\n"
    "s_add_u32 m0, %[m0t], 40960\n"
    "ds_read_b128 v[212:215], %[rdx1] offset:59392\n"
    "v_mfma_f32_16x16x32_bf16 v[120:123], a[232:235], v[220:223], v[120:123]\n"
    "buffer_load_dwordx4 %[vx0], %[srdx], s44 offen lds\n"
    "v_mfma_f32_16x16x32_bf16 v[116:119], a[228:231], v[220:223], v[116:119]\n"
    "buffer_load_dwordx4 %[vx1], %[srdx], s44 offen offset:1024 lds\n"
    "v_mfma_f32_16x16x32_bf16 v[112:115], a[224:227], v[220:223], v[112:115]\n"
    "s_waitcnt lgkmcnt(6)\n"
    "v_mfma_f32_16x16x32_bf16 v[0:3], a[240:243], v[192:195], v[0:3]\n"
    "ds_read_b128 v[216:219], %[rdx1] offset:61440\n"
    "v_mfma_f32_16x16x32_bf16 v[4:7], a[244:247], v[192:195], v[4:7]\n"
    "v_mfma_f32_16x16x32_bf16 v[8:11], a[248:251], v[192:195], v[8:11]\n"
    "s_waitcnt lgkmcnt(5)\n"
    "v_mfma_f32_16x16x32_bf16 v[12:15], a[252:255], v[192:195], v[12:15]\n"
    "v_mfma_f32_16x16x32_bf16 v[28:31], a[252:255], v[196:199], v[28:31]\n"
    "ds_read_b128 v[220:223], %[rdx1] offset:63488\n"
    "v_mfma_f32_16x16x32_bf16 v[24:27], a[248:251], v[196:199], v[24:27]\n"
    "v_mfma_f32_16x16x32_bf16 v[20:23], a[244:247], v[196:199], v[20:23]\n"
    "v_mfma_f32_16x16x32_bf16 v[16:19], a[240:243], v[196:199], v[16:19]\n"
    "s_waitcnt lgkmcnt(4)\n"
    "v_mfma_f32_16x16x32_bf16 v[32:35], a[240:243], v[200:203], v[32:35]\n"
    "v_mfma_f32_16x16x32_bf16 v[36:39], a[244:247], v[200:203], v[36:39]\n"
    "v_mfma_f32_16x16x32_bf16 v[40:43], a[248:251], v[200:203], v[40:43]\n"
    "v_mfma_f32_16x16x32_bf16 v[44:47], a[252:255], v[200:203], v[44:47]\n"
    "v_mfma_f32_16x16x32_bf16 v[60:63], a[252:255], v[204:207], v[60:63]\n"
    "v_mfma_f32_16x16x32_bf16 v[56:59], a[248:251], v[204:207], v[56:59]\n"
    "v_mfma_f32_16x16x32_bf16 v[52:55], a[244:247], v[204:207], v[52:55]\n"
    "v_mfma_f32_16x16x32_bf16 v[48:51], a[240:243], v[204:207], v[48:51]\n"
    "s_waitcnt vmcnt(12) lgkmcnt(0)\n"
    "s_barrier\n"
    "v_mfma_f32_16x16x32_bf16 v[64:67], a[240:243], v[208:211], v[64:67]\n"
    "ds_read_b128 v[192:195], %[rdx2] offset:0\n"
    "v_mfma_f32_16x16x32_bf16 v[68:71], a[244:247], v[208:211], v[68:71]\n"
    "s_add_u32 s45, s45, 128\n"
    "s_and_b32 s45, s45, 1023\n"
    "v_mfma_f32_16x16x32_bf16 v[72:75], a[248:251], v[208:211], v[72:75]\n"
    "s_add_u32 s44, s45, %[sot1]\n"
    "s_add_u32 m0, %[m0t], 49152\n"
    "ds_read_b128 v[196:199], %[rdx2] offset:2048\n"
    "v_mfma_f32_16x16x32_bf16 v[76:79], a[252:255], v[208:211], v[76:79]\n"
    "buffer_load_dwordx4 %[vx0], %[srdx], s44 offen lds\n"
    "v_mfma_f32_16x16x32_bf16 v[92:95], a[252:255], v[212:215], v[92:95]\n"
    "ds_read_b128 v[200:203], %[rdx2] offset:4096\n"
    "v_mfma_f32_16x16x32_bf16 v[88:91], a[248:251], v[212:215], v[88:91]\n"
    "v_mfma_f32_16x16x32_bf16 v[84:87], a[244:247], v[212:215], v[84:87]\n"
    "ds_read_b128 v[204:207], %[rdx2] offset:6144\n"
    "v_mfma_f32_16x16x32_bf16 v[80:83], a[240:243], v[212:215], v[80:83]\n"
    "buffer_load_dwordx4 %[vx1], %[srdx], s44 offen offset:1024 lds\n"
    "v_mfma_f32_16x16x32_bf16 v[96:99], a[240:243], v[216:219], v[96:99]\n"
    "ds_read_b128 v[208:211], %[rdx3] offset:0\n"
    "v_mfma_f32_16x16x32_bf16 v[100:103], a[244:247], v[216:219], v[100:103]\n"
    "v_mfma_f32_16x16x32_bf16 v[104:107], a[248:251], v[216:219], v[104:107]\n"
    "v_mfma_f32_16x16x32_bf16 v[108:111], a[252:255], v[216:219], v[108:111]\n"
    "s_add_u32 s45, s45, 128\n"
    "s_and_b32 s45, s45, 1023\n"
    "ds_read_b128 v[212:215], %[rdx3] offset:2048\n"
    "v_mfma_f32_16x16x32_bf16 v[124:127], a[252:255], v[220:223], v[124:127]\n"
    "s_add_u32 s44, s45, %[sot1]\n"
    "s_add_u32 m0, %[m0t], 57344\n"
    "v_mfma_f32_16x16x32_bf16 v[120:123], a[248:251], v[220:223], v[120:123]\n"
    "buffer_load_dwordx4 %[vx0], %[srdx], s44 offen lds\n"
    "v_mfma_f32_16x16x32_bf16 v[116:119], a[244:247], v[220:223], v[116:119]\n"
    "v_mfma_f32_16x16x32_bf16 v[112:115], a[240:243], v[220:223], v[112:115]\n"
    "s_waitcnt lgkmcnt(4)\n"
    "v_mfma_f32_16x16x32_bf16 v[128:131], a[0:3], v[192:195], 0\n"
    "ds_read_b128 v[216:219], %[rdx3] offset:4096\n"
    "v_mfma_f32_16x16x32_bf16 v[132:135], a[4:7], v[192:195], 0\n"
    "v_mfma_f32_16x16x32_bf16 v[136:139], a[8:11], v[192:195], 0\n"
    "v_mfma_f32_16x16x32_bf16 v[140:143], a[12:15], v[192:195], 0\n"
    "buffer_load_dwordx4 %[vx1], %[srdx], s44 offen offset:1024 lds\n"
    "v_mfma_f32_16x16x32_bf16 v[156:159], a[12:15], v[196:199], 0\n"
    "ds_read_b128 v[220:223], %[rdx3] offset:6144\n"
    "v_mfma_f32_16x16x32_bf16 v[152:155], a[8:11], v[196:199], 0\n"
    "v_mfma_f32_16x16x32_bf16 v[148:151], a[4:7], v[196:199], 0\n"
    "v_mfma_f32_16x16x32_bf16 v[144:147], a[0:3], v[196:199], 0\n"
    "s_waitcnt lgkmcnt(4)\n"
    "v_mfma_f32_16x16x32_bf16 v[160:163], a[0:3], v[200:203], 0\n"
    "ds_read_b128 v[192:195], %[rdx2] offset:8192\n"
    "v_mfma_f32_16x16x32_bf16 v[164:167], a[4:7], v[200:203], 0\n"
    "v_mfma_f32_16x16x32_bf16 v[168:171], a[8:11], v[200:203], 0\n"
    "v_mfma_f32_16x16x32_bf16 v[172:175], a[12:15], v[200:203], 0\n"
    "v_mfma_f32_16x16x32_bf16 v[188:191], a[12:15], v[204:207], 0\n"
    "ds_read_b128 v[196:199], %[rdx2] offset:10240\n"
    "v_mfma_f32_16x16x32_bf16 v[184:187], a[8:11], v[204:207], 0\n"
    "v_mfma_f32_16x16x32_bf16 v[180:183], a[4:7], v[204:207], 0\n"
    "v_mfma_f32_16x16x32_bf16 v[176:179], a[0:3], v[204:207], 0\n"
    "s_waitcnt lgkmcnt(4)\n"
    "v_cvt_pk_bf16_f32 v0, v0, v1\n"
    "v_mfma_f32_16x16x32_bf16 v[128:131], a[16:19], v[208:211], v[128:131]\n"
    "ds_read_b128 v[200:203], %[rdx2] offset:12288\n"
    "v_mfma_f32_16x16x32_bf16 v[132:135], a[20:23], v[208:211], v[132:135]\n"
    "v_cvt_pk_bf16_f32 v1, v2, v3\n"
    "v_mfma_f32_16x16x32_bf16 v[136:139], a[24:27], v[208:211], v[136:139]\n"
    "v_cvt_pk_bf16_f32 v2, v4, v5\n"
    "v_mfma_f32_16x16x32_bf16 v[140:143], a[28:31], v[208:211], v[140:143]\n"
    "v_mfma_f32_16x16x32_bf16 v[156:159], a[28:31], v[212:215], v[156:159]\n"
    "ds_read_b128 v[204:207], %[rdx2] offset:14336\n"
    "v_mfma_f32_16x16x32_bf16 v[152:155], a[24:27], v[212:215], v[152:155]\n"
    "v_cvt_pk_bf16_f32 v3, v6, v7\n"
    "v_mfma_f32_16x16x32_bf16 v[148:151], a[20:23], v[212:215], v[148:151]\n"
    "v_mfma_f32_16x16x32_bf16 v[144:147], a[16:19], v[212:215], v[144:147]\n"
    "s_add_u32 s46, %[soo], 0\n"
    "buffer_store_dwordx4 v[0:3], %[vo], %[srdo], s46 offen sc1\n"
    "s_waitcnt lgkmcnt(4)\n"
    "v_mfma_f32_16x16x32_bf16 v[160:163], a[16:19], v[216:219], v[160:163]\n"
    "ds_read_b128 v[208:211], %[rdx3] offset:8192\n"
    "v_mfma_f32_16x16x32_bf16 v[164:167], a[20:23], v[216:219], v[164:167]\n"
    "v_mfma_f32_16x16x32_bf16 v[168:171], a[24:27], v[216:219], v[168:171]\n"
    "v_mfma_f32_16x16x32_bf16 v[172:175], a[28:31], v[216:219], v[172:175]\n"
    "v_mfma_f32_16x16x32_bf16 v[188:191], a[28:31], v[220:223], v[188:191]\n"
    "ds_read_b128 v[212:215], %[rdx3] offset:10240\n"
    "v_mfma_f32_16x16x32_bf16 v[184:187], a[24:27], v[220:223], v[184:187]\n"
    "v_mfma_f32_16x16x32_bf16 v[180:183], a[20:23], v[220:223], v[180:183]\n"
    "v_mfma_f32_16x16x32_bf16 v[176:179], a[16:19], v[220:223], v[176:179]\n"
    "s_waitcnt lgkmcnt(4)\n"
    "v_mfma_f32_16x16x32_bf16 v[128:131], a[32:35], v[192:195], v[128:131]\n"
    "ds_read_b128 v[216:219], %[rdx3] offset:12288\n"
    "v_mfma_f32_16x16x32_bf16 v[132:135], a[36:39], v[192:195], v[132:135]\n"
    "v_mfma_f32_16x16x32_bf16 v[136:139], a[40:43], v[192:195], v[136:139]\n"
    "v_mfma_f32_16x16x32_bf16 v[140:143], a[44:47], v[192:195], v[140:143]\n"
    "v_mfma_f32_16x16x32_bf16 v[156:159], a[44:47], v[196:199], v[156:159]\n"
    "ds_read_b128 v[220:223], %[rdx3] offset:14336\n"
    "v_cvt_pk_bf16_f32 v8, v8, v9\n"
    "v_mfma_f32_16x16x32_bf16 v[152:155], a[40:43], v[196:199], v[152:155]\n"
    "v_mfma_f32_16x16x32_bf16 v[148:151], a[36:39], v[196:199], v[148:151]\n"
    "v_cvt_pk_bf16_f32 v9, v10, v11\n"
    "v_mfma_f32_16x16x32_bf16 v[144:147], a[32:35], v[196:199], v[144:147]\n"
    "s_waitcnt lgkmcnt(4)\n"
    "v_cvt_pk_bf16_f32 v10, v12, v13\n"
    "v_mfma_f32_16x16x32_bf16 v[160:163], a[32:35], v[200:203], v[160:163]\n"
    "ds_read_b128 v[192:195], %[rdx2] offset:16384\n"
    "v_mfma_f32_16x16x32_bf16 v[164:167], a[36:39], v[200:203], v[164:167]\n"
    "v_mfma_f32_16x16x32_bf16 v[168:171], a[40:43], v[200:203], v[168:171]\n"
    "v_cvt_pk_bf16_f32 v11, v14, v15\n"
    "v_mfma_f32_16x16x32_bf16 v[172:175], a[44:47], v[200:203], v[172:175]\n"
    "v_mfma_f32_16x16x32_bf16 v[188:191], a[44:47], v[204:207], v[188:191]\n"
    "s_add_u32 s46, %[soo], 1024\n"
    "ds_read_b128 v[196:199], %[rdx2] offset:18432\n"
    "buffer_store_dwordx4 v[8:11], %[vo], %[srdo], s46 offen sc1\n"
    "v_mfma_f32_16x16x32_bf16 v[184:187], a[40:43], v[204:207], v[184:187]\n"
    "v_mfma_f32_16x16x32_bf16 v[180:183], a[36:39], v[204:207], v[180:183]\n"
    "v_mfma_f32_16x16x32_bf16 v[176:179], a[32:35], v[204:207], v[176:179]\n"
    "s_waitcnt lgkmcnt(4)\n"
    "v_mfma_f32_16x16x32_bf16 v[128:131], a[48:51], v[208:211], v[128:131]\n"
    "ds_read_b128 v[200:203], %[rdx2] offset:20480\n"
    "v_mfma_f32_16x16x32_bf16 v[132:135], a[52:55], v[208:211], v[132:135]\n"
    "v_mfma_f32_16x16x32_bf16 v[136:139], a[56:59], v[208:211], v[136:139]\n"
    "v_mfma_f32_16x16x32_bf16 v[140:143], a[60:63], v[208:211], v[140:143]\n"
    "v_mfma_f32_16x16x32_bf16 v[156:159], a[60:63], v[212:215], v[156:159]\n"
    "ds_read_b128 v[204:207], %[rdx2] offset:22528\n"
    "v_mfma_f32_16x16x32_bf16 v[152:155], a[56:59], v[212:215], v[152:155]\n"
    "v_mfma_f32_16x16x32_bf16 v[148:151], a[52:55], v[212:215], v[148:151]\n"
    "v_mfma_f32_16x16x32_bf16 v[144:147], a[48:51], v[212:215], v[144:147]\n"
    "s_waitcnt lgkmcnt(4)\n"
    "v_mfma_f32_16x16x32_bf16 v[160:163], a[48:51], v[216:219], v[160:163]\n"
    "ds_read_b128 v[208:211], %[rdx3] offset:16384\n"
    "v_mfma_f32_16x16x32_bf16 v[164:167], a[52:55], v[216:219], v[164:167]\n"
    "v_cvt_pk_bf16_f32 v16, v16, v17\n"
    "v_mfma_f32_16x16x32_bf16 v[168:171], a[56:59], v[216:219], v[168:171]\n"
    "v_cvt_pk_bf16_f32 v17, v18, v19\n"
    "v_mfma_f32_16x16x32_bf16 v[172:175], a[60:63], v[216:219], v[172:175]\n"
    "v_mfma_f32_16x16x32_bf16 v[188:191], a[60:63], v[220:223], v[188:191]\n"
    "ds_read_b128 v[212:215], %[rdx3] offset:18432\n"
    "v_cvt_pk_bf16_f32 v18, v20, v21\n"
    "v_mfma_f32_16x16x32_bf16 v[184:187], a[56:59], v[220:223], v[184:187]\n"
    "v_mfma_f32_16x16x32_bf16 v[180:183], a[52:55], v[220:223], v[180:183]\n"
    "v_cvt_pk_bf16_f32 v19, v22, v23\n"
    "v_mfma_f32_16x16x32_bf16 v[176:179], a[48:51], v[220:223], v[176:179]\n"
    "s_waitcnt lgkmcnt(4)\n"
    "v_mfma_f32_16x16x32_bf16 v[128:131], a[64:67], v[192:195], v[128:131]\n"
    "ds_read_b128 v[216:219], %[rdx3] offset:20480\n"
    "v_mfma_f32_16x16x32_bf16 v[132:135], a[68:71], v[192:195], v[132:135]\n"
    "s_add_u32 s46, %[soo], 4096\n"
    "buffer_store_dwordx4 v[16:19], %[vo], %[srdo], s46 offen sc1\n"
    "v_mfma_f32_16x16x32_bf16 v[136:139], a[72:75], v[192:195], v[136:139]\n"
    "v_mfma_f32_16x16x32_bf16 v[140:143], a[76:79], v[192:195], v[140:143]\n"
    "v_mfma_f32_16x16x32_bf16 v[156:159], a[76:79], v[196:199], v[156:159]\n"
    "ds_read_b128 v[220:223], %[rdx3] offset:22528\n"
    "v_mfma_f32_16x16x32_bf16 v[152:155], a[72:75], v[196:199], v[152:155]\n"
    "v_mfma_f32_16x16x32_bf16 v[148:151], a[68:71], v[196:199], v[148:151]\n"
    "v_mfma_f32_16x16x32_bf16 v[144:147], a[64:67], v[196:199], v[144:147]\n"
    "s_waitcnt lgkmcnt(4)\n"
    "v_mfma_f32_16x16x32_bf16 v[160:163], a[64:67], v[200:203], v[160:163]\n"
    "ds_read_b128 v[192:195], %[rdx2] offset:24576\n"
    "v_mfma_f32_16x16x32_bf16 v[164:167], a[68:71], v[200:203], v[164:167]\n"
    "v_mfma_f32_16x16x32_bf16 v[168:171], a[72:75], v[200:203], v[168:171]\n"
    "v_mfma_f32_16x16x32_bf16 v[172:175], a[76:79], v[200:203], v[172:175]\n"
    "v_mfma_f32_16x16x32_bf16 v[188:191], a[76:79], v[204:207], v[188:191]\n"
    "ds_read_b128 v[196:199], %[rdx2] offset:26624\n"
    "v_mfma_f32_16x16x32_bf16 v[184:187], a[72:75], v[204:207], v[184:187]\n"
    "v_cvt_pk_bf16_f32 v24, v24, v25\n"
    "v_mfma_f32_16x16x32_bf16 v[180:183], a[68:71], v[204:207], v[180:183]\n"
    "v_cvt_pk_bf16_f32 v25, v26, v27\n"
    "v_mfma_f32_16x16x32_bf16 v[176:179], a[64:67], v[204:207], v[176:179]\n"
    "s_waitcnt lgkmcnt(4)\n"
    "v_mfma_f32_16x16x32_bf16 v[128:131], a[80:83], v[208:211], v[128:131]\n"
    "ds_read_b128 v[200:203], %[rdx2] offset:28672\n"
    "v_cvt_pk_bf16_f32 v26, v28, v29\n"
    "v_mfma_f32_16x16x32_bf16 v[132:135], a[84:87], v[208:211], v[132:135]\n"
    "v_mfma_f32_16x16x32_bf16 v[136:139], a[88:91], v[208:211], v[136:139]\n"
    "v_cvt_pk_bf16_f32 v27, v30, v31\n"
    "v_mfma_f32_16x16x32_bf16 v[140:143], a[92:95], v[208:211], v[140:143]\n"
    "v_mfma_f32_16x16x32_bf16 v[156:159], a[92:95], v[212:215], v[156:159]\n"
    "ds_read_b128 v[204:207], %[rdx2] offset:30720\n"
    "v_mfma_f32_16x16x32_bf16 v[152:155], a[88:91], v[212:215], v[152:155]\n"
    "s_add_u32 s46, %[soo], 5120\n"
    "buffer_store_dwordx4 v[24:27], %[vo], %[srdo], s46 offen sc1\n"
    "v_mfma_f32_16x16x32_bf16 v[148:151], a[84:87], v[212:215], v[148:151]\n"
    "v_mfma_f32_16x16x32_bf16 v[144:147], a[80:83], v[212:215], v[144:147]\n"
    "s_waitcnt lgkmcnt(4)\n"
    "v_mfma_f32_16x16x32_bf16 v[160:163], a[80:83], v[216:219], v[160:163]\n"
    "ds_read_b128 v[208:211], %[rdx3] offset:24576\n"
    "v_mfma_f32_16x16x32_bf16 v[164:167], a[84:87], v[216:219], v[164:167]\n"
    "buffer_load_dword %[vdum], %[vpf], %[srdp], 0 offen ; dummy\n"
    "v_mfma_f32_16x16x32_bf16 v[168:171], a[88:91], v[216:219], v[168:171]\n"
    "v_mfma_f32_16x16x32_bf16 v[172:175], a[92:95], v[216:219], v[172:175]\n"
    "v_mfma_f32_16x16x32_bf16 v[188:191], a[92:95], v[220:223], v[188:191]\n"
    "ds_read_b128 v[212:215], %[rdx3] offset:26624\n"
    "v_mfma_f32_16x16x32_bf16 v[184:187], a[88:91], v[220:223], v[184:187]\n"
    "v_mfma_f32_16x16x32_bf16 v[180:183], a[84:87], v[220:223], v[180:183]\n"
    "v_mfma_f32_16x16x32_bf16 v[176:179], a[80:83], v[220:223], v[176:179]\n"
    "s_waitcnt lgkmcnt(4)\n"
    "v_mfma_f32_16x16x32_bf16 v[128:131], a[96:99], v[192:195], v[128:131]\n"
    "ds_read_b128 v[216:219], %[rdx3] offset:28672\n"
    "v_mfma_f32_16x16x32_bf16 v[132:135], a[100:103], v[192:195], v[132:135]\n"
    "v_mfma_f32_16x16x32_bf16 v[136:139], a[104:107], v[192:195], v[136:139]\n"
    "v_cvt_pk_bf16_f32 v32, v32, v33\n"
    "v_mfma_f32_16x16x32_bf16 v[140:143], a[108:111], v[192:195], v[140:143]\n"
    "v_cvt_pk_bf16_f32 v33, v34, v35\n"
    "v_mfma_f32_16x16x32_bf16 v[156:159], a[108:111], v[196:199], v[156:159]\n"
    "ds_read_b128 v[220:223], %[rdx3] offset:30720\n"
    "v_mfma_f32_16x16x32_bf16 v[152:155], a[104:107], v[196:199], v[152:155]\n"
    "v_cvt_pk_bf16_f32 v34, v36, v37\n"
    "v_mfma_f32_16x16x32_bf16 v[148:151], a[100:103], v[196:199], v[148:151]\n"
    "v_mfma_f32_16x16x32_bf16 v[144:147], a[96:99], v[196:199], v[144:147]\n"
    "s_waitcnt lgkmcnt(4)\n"
    "v_cvt_pk_bf16_f32 v35, v38, v39\n"
    "v_mfma_f32_16x16x32_bf16 v[160:163], a[96:99], v[200:203], v[160:163]\n"
    "ds_read_b128 v[192:195], %[rdx2] offset:32768\n"
    "v_mfma_f32_16x16x32_bf16 v[164:167], a[100:103], v[200:203], v[164:167]\n"
    "v_mfma_f32_16x16x32_bf16 v[168:171], a[104:107], v[200:203], v[168:171]\n"
    "s_add_u32 s46, %[soo], 8192\n"
    "buffer_store_dwordx4 v[32:35], %[vo], %[srdo], s46 offen sc1\n"
    "v_mfma_f32_16x16x32_bf16 v[172:175], a[108:111], v[200:203], v[172:175]\n"
    "v_mfma_f32_16x16x32_bf16 v[188:191], a[108:111], v[204:207], v[188:191]\n"
    "ds_read_b128 v[196:199], %[rdx2] offset:34816\n"
    "v_mfma_f32_16x16x32_bf16 v[184:187], a[104:107], v[204:207], v[184:187]\n"
    "v_mfma_f32_16x16x32_bf16 v[180:183], a[100:103], v[204:207], v[180:183]\n"
    "v_mfma_f32_16x16x32_bf16 v[176:179], a[96:99], v[204:207], v[176:179]\n"
    "s_waitcnt lgkmcnt(4)\n"
    "v_mfma_f32_16x16x32_bf16 v[128:131], a[112:115], v[208:211], v[128:131]\n"
    "ds_read_b128 v[200:203], %[rdx2] offset:36864\n"
    "v_mfma_f32_16x16x32_bf16 v[132:135], a[116:119], v[208:211], v[132:135]\n"
    "v_mfma_f32_16x16x32_bf16 v[136:139], a[120:123], v[208:211], v[136:139]\n"
    "v_mfma_f32_16x16x32_bf16 v[140:143], a[124:127], v[208:211], v[140:143]\n"
    "v_mfma_f32_16x16x32_bf16 v[156:159], a[124:127], v[212:215], v[156:159]\n"
    "ds_read_b128 v[204:207], %[rdx2] offset:38912\n"
    "v_mfma_f32_16x16x32_bf16 v[152:155], a[120:123], v[212:215], v[152:155]\n"
    "v_mfma_f32_16x16x32_bf16 v[148:151], a[116:119], v[212:215], v[148:151]\n"
    "v_mfma_f32_16x16x32_bf16 v[144:147], a[112:115], v[212:215], v[144:147]\n"
    "s_waitcnt lgkmcnt(4)\n"
    "v_cvt_pk_bf16_f32 v40, v40, v41\n"
    "v_mfma_f32_16x16x32_bf16 v[160:163], a[112:115], v[216:219], v[160:163]\n"
    "ds_read_b128 v[208:211], %[rdx3] offset:32768\n"
    "v_mfma_f32_16x16x32_bf16 v[164:167], a[116:119], v[216:219], v[164:167]\n"
    "v_cvt_pk_bf16_f32 v41, v42, v43\n"
    "v_mfma_f32_16x16x32_bf16 v[168:171], a[120:123], v[216:219], v[168:171]\n"
    "v_cvt_pk_bf16_f32 v42, v44, v45\n"
    "v_mfma_f32_16x16x32_bf16 v[172:175], a[124:127], v[216:219], v[172:175]\n"
    "v_mfma_f32_16x16x32_bf16 v[188:191], a[124:127], v[220:223], v[188:191]\n"
    "ds_read_b128 v[212:215], %[rdx3] offset:34816\n"
    "v_mfma_f32_16x16x32_bf16 v[184:187], a[120:123], v[220:223], v[184:187]\n"
    "v_cvt_pk_bf16_f32 v43, v46, v47\n"
    "v_mfma_f32_16x16x32_bf16 v[180:183], a[116:119], v[220:223], v[180:183]\n"
    "v_mfma_f32_16x16x32_bf16 v[176:179], a[112:115], v[220:223], v[176:179]\n"
    "s_add_u32 s46, %[soo], 9216\n"
    "buffer_store_dwordx4 v[40:43], %[vo], %[srdo], s46 offen sc1\n"
    "s_waitcnt lgkmcnt(4)\n"
    "v_mfma_f32_16x16x32_bf16 v[128:131], a[128:131], v[192:195], v[128:131]\n"
    "ds_read_b128 v[216:219], %[rdx3] offset:36864\n"
    "v_mfma_f32_16x16x32_bf16 v[132:135], a[132:135], v[192:195], v[132:135]\n"
    "buffer_load_dword %[vdum], %[vpf], %[srdp], s49 offen ; dummy\n"
    "v_mfma_f32_16x16x32_bf16 v[136:139], a[136:139], v[192:195], v[136:139]\n"
    "v_mfma_f32_16x16x32_bf16 v[140:143], a[140:143], v[192:195], v[140:143]\n"
    "v_mfma_f32_16x16x32_bf16 v[156:159], a[140:143], v[196:199], v[156:159]\n"
    "ds_read_b128 v[220:223], %[rdx3] offset:38912\n"
    "v_mfma_f32_16x16x32_bf16 v[152:155], a[136:139], v[196:199], v[152:155]\n"
    "v_mfma_f32_16x16x32_bf16 v[148:151], a[132:135], v[196:199], v[148:151]\n"
    "v_mfma_f32_16x16x32_bf16 v[144:147], a[128:131], v[196:199], v[144:147]\n"
    "s_waitcnt lgkmcnt(4)\n"
    "v_mfma_f32_16x16x32_bf16 v[160:163], a[128:131], v[200:203], v[160:163]\n"
    "ds_read_b128 v[192:195], %[rdx2] offset:40960\n"
    "v_mfma_f32_16x16x32_bf16 v[164:167], a[132:135], v[200:203], v[164:167]\n"
    "v_mfma_f32_16x16x32_bf16 v[168:171], a[136:139], v[200:203], v[168:171]\n"
    "v_mfma_f32_16x16x32_bf16 v[172:175], a[140:143], v[200:203], v[172:175]\n"
    "v_cvt_pk_bf16_f32 v48, v48, v49\n"
    "v_mfma_f32_16x16x32_bf16 v[188:191], a[140:143], v[204:207], v[188:191]\n"
    "ds_read_b128 v[196:199], %[rdx2] offset:43008\n"
    "v_mfma_f32_16x16x32_bf16 v[184:187], a[136:139], v[204:207], v[184:187]\n"
    "v_cvt_pk_bf16_f32 v49, v50, v51\n"
    "v_mfma_f32_16x16x32_bf16 v[180:183], a[132:135], v[204:207], v[180:183]\n"
    "v_cvt_pk_bf16_f32 v50, v52, v53\n"
    "v_mfma_f32_16x16x32_bf16 v[176:179], a[128:131], v[204:207], v[176:179]\n"
    "s_waitcnt lgkmcnt(4)\n"
    "v_mfma_f32_16x16x32_bf16 v[128:131], a[144:147], v[208:211], v[128:131]\n"
    "ds_read_b128 v[200:203], %[rdx2] offset:45056\n"
    "v_mfma_f32_16x16x32_bf16 v[132:135], a[148:151], v[208:211], v[132:135]\n"
    "v_cvt_pk_bf16_f32 v51, v54, v55\n"
    "v_mfma_f32_16x16x32_bf16 v[136:139], a[152:155], v[208:211], v[136:139]\n"
    "v_mfma_f32_16x16x32_bf16 v[140:143], a[156:159], v[208:211], v[140:143]\n"
    "s_add_u32 s46, %[soo], 12288\n"
    "buffer_store_dwordx4 v[48:51], %[vo], %[srdo], s46 offen sc1\n"
    "v_mfma_f32_16x16x32_bf16 v[156:159], a[156:159], v[212:215], v[156:159]\n"
    "ds_read_b128 v[204:207], %[rdx2] offset:47104\n"
    "v_mfma_f32_16x16x32_bf16 v[152:155], a[152:155], v[212:215], v[152:155]\n"
    "v_mfma_f32_16x16x32_bf16 v[148:151], a[148:151], v[212:215], v[148:151]\n"
    "v_mfma_f32_16x16x32_bf16 v[144:147], a[144:147], v[212:215], v[144:147]\n"
    "s_waitcnt lgkmcnt(4)\n"
    "v_mfma_f32_16x16x32_bf16 v[160:163], a[144:147], v[216:219], v[160:163]\n"
    "ds_read_b128 v[208:211], %[rdx3] offset:40960\n"
    "v_mfma_f32_16x16x32_bf16 v[164:167], a[148:151], v[216:219], v[164:167]\n"
    "v_mfma_f32_16x16x32_bf16 v[168:171], a[152:155], v[216:219], v[168:171]\n"
    "v_mfma_f32_16x16x32_bf16 v[172:175], a[156:159], v[216:219], v[172:175]\n"
    "v_mfma_f32_16x16x32_bf16 v[188:191], a[156:159], v[220:223], v[188:191]\n"
    "ds_read_b128 v[212:215], %[rdx3] offset:43008\n"
    "v_mfma_f32_16x16x32_bf16 v[184:187], a[152:155], v[220:223], v[184:187]\n"
    "v_mfma_f32_16x16x32_bf16 v[180:183], a[148:151], v[220:223], v[180:183]\n"
    "v_mfma_f32_16x16x32_bf16 v[176:179], a[144:147], v[220:223], v[176:179]\n"
    "s_waitcnt lgkmcnt(4)\n"
    "v_mfma_f32_16x16x32_bf16 v[128:131], a[160:163], v[192:195], v[128:131]\n"
    "ds_read_b128 v[216:219], %[rdx3] offset:45056\n"
    "v_cvt_pk_bf16_f32 v56, v56, v57\n"
    "v_mfma_f32_16x16x32_bf16 v[132:135], a[164:167], v[192:195], v[132:135]\n"
    "v_mfma_f32_16x16x32_bf16 v[136:139], a[168:171], v[192:195], v[136:139]\n"
    "v_cvt_pk_bf16_f32 v57, v58, v59\n"
    "v_mfma_f32_16x16x32_bf16 v[140:143], a[172:175], v[192:195], v[140:143]\n"
    "v_cvt_pk_bf16_f32 v58, v60, v61\n"
    "v_mfma_f32_16x16x32_bf16 v[156:159], a[172:175], v[196:199], v[156:159]\n"
    "ds_read_b128 v[220:223], %[rdx3] offset:47104\n"
    "v_mfma_f32_16x16x32_bf16 v[152:155], a[168:171], v[196:199], v[152:155]\n"
    "v_mfma_f32_16x16x32_bf16 v[148:151], a[164:167], v[196:199], v[148:151]\n"
    "v_cvt_pk_bf16_f32 v59, v62, v63\n"
    "v_mfma_f32_16x16x32_bf16 v[144:147], a[160:163], v[196:199], v[144:147]\n"
    "s_waitcnt lgkmcnt(4)\n"
    "v_mfma_f32_16x16x32_bf16 v[160:163], a[160:163], v[200:203], v[160:163]\n"
    "s_add_u32 s46, %[soo], 13312\n"
    "ds_read_b128 v[192:195], %[rdx2] offset:49152\n"
    "buffer_store_dwordx4 v[56:59], %[vo], %[srdo], s46 offen sc1\n"
    "v_mfma_f32_16x16x32_bf16 v[164:167], a[164:167], v[200:203], v[164:167]\n"
    "v_mfma_f32_16x16x32_bf16 v[168:171], a[168:171], v[200:203], v[168:171]\n"
    "v_mfma_f32_16x16x32_bf16 v[172:175], a[172:175], v[200:203], v[172:175]\n"
    "v_mfma_f32_16x16x32_bf16 v[188:191], a[172:175], v[204:207], v[188:191]\n"
    "ds_read_b128 v[196:199], %[rdx2] offset:51200\n"
    "v_mfma_f32_16x16x32_bf16 v[184:187], a[168:171], v[204:207], v[184:187]\n"
    "v_mfma_f32_16x16x32_bf16 v[180:183], a[164:167], v[204:207], v[180:183]\n"
    "v_mfma_f32_16x16x32_bf16 v[176:179], a[160:163], v[204:207], v[176:179]\n"
    "s_waitcnt lgkmcnt(4)\n"
    "v_mfma_f32_16x16x32_bf16 v[128:131], a[176:179], v[208:211], v[128:131]\n"
    "ds_read_b128 v[200:203], %[rdx2] offset:53248\n"
    "v_mfma_f32_16x16x32_bf16 v[132:135], a[180:183], v[208:211], v[132:135]\n"
    "v_mfma_f32_16x16x32_bf16 v[136:139], a[184:187], v[208:211], v[136:139]\n"
    "v_mfma_f32_16x16x32_bf16 v[140:143], a[188:191], v[208:211], v[140:143]\n"
    "v_mfma_f32_16x16x32_bf16 v[156:159], a[188:191], v[212:215], v[156:159]\n"
    "ds_read_b128 v[204:207], %[rdx2] offset:55296\n"
    "v_mfma_f32_16x16x32_bf16 v[152:155], a[184:187], v[212:215], v[152:155]\n"
    "v_cvt_pk_bf16_f32 v64, v64, v65\n"
    "v_mfma_f32_16x16x32_bf16 v[148:151], a[180:183], v[212:215], v[148:151]\n"
    "v_cvt_pk_bf16_f32 v65, v66, v67\n"
    "v_mfma_f32_16x16x32_bf16 v[144:147], a[176:179], v[212:215], v[144:147]\n"
    "s_waitcnt lgkmcnt(4)\n"
    "v_mfma_f32_16x16x32_bf16 v[160:163], a[176:179], v[216:219], v[160:163]\n"
    "ds_read_b128 v[208:211], %[rdx3] offset:49152\n"
    "v_cvt_pk_bf16_f32 v66, v68, v69\n"
    "v_mfma_f32_16x16x32_bf16 v[164:167], a[180:183], v[216:219], v[164:167]\n"
    "v_mfma_f32_16x16x32_bf16 v[168:171], a[184:187], v[216:219], v[168:171]\n"
    "v_cvt_pk_bf16_f32 v67, v70, v71\n"
    "v_mfma_f32_16x16x32_bf16 v[172:175], a[188:191], v[216:219], v[172:175]\n"
    "v_mfma_f32_16x16x32_bf16 v[188:191], a[188:191], v[220:223], v[188:191]\n"
    "ds_read_b128 v[212:215], %[rdx3] offset:51200\n"
    "v_mfma_f32_16x16x32_bf16 v[184:187], a[184:187], v[220:223], v[184:187]\n"
    "s_add_u32 s46, %[soo], 16384\n"
    "buffer_store_dwordx4 v[64:67], %[vo], %[srdo], s46 offen sc1\n"
    "v_mfma_f32_16x16x32_bf16 v[180:183], a[180:183], v[220:223], v[180:183]\n"
    "v_mfma_f32_16x16x32_bf16 v[176:179], a[176:179], v[220:223], v[176:179]\n"
    "s_waitcnt lgkmcnt(4)\n"
    "v_mfma_f32_16x16x32_bf16 v[128:131], a[192:195], v[192:195], v[128:131]\n"
    "ds_read_b128 v[216:219], %[rdx3] offset:53248\n"
    "v_mfma_f32_16x16x32_bf16 v[132:135], a[196:199], v[192:195], v[132:135]\n"
    "v_mfma_f32_16x16x32_bf16 v[136:139], a[200:203], v[192:195], v[136:139]\n"
    "v_mfma_f32_16x16x32_bf16 v[140:143], a[204:207], v[192:195], v[140:143]\n"
    "v_mfma_f32_16x16x32_bf16 v[156:159], a[204:207], v[196:199], v[156:159]\n"
    "ds_read_b128 v[220:223], %[rdx3] offset:55296\n"
    "v_mfma_f32_16x16x32_bf16 v[152:155], a[200:203], v[196:199], v[152:155]\n"
    "v_mfma_f32_16x16x32_bf16 v[148:151], a[196:199], v[196:199], v[148:151]\n"
    "v_mfma_f32_16x16x32_bf16 v[144:147], a[192:195], v[196:199], v[144:147]\n"
    "s_waitcnt lgkmcnt(4)\n"
    "v_mfma_f32_16x16x32_bf16 v[160:163], a[192:195], v[200:203], v[160:163]\n"
    "ds_read_b128 v[192:195], %[rdx2] offset:57344\n"
    "v_mfma_f32_16x16x32_bf16 v[164:167], a[196:199], v[200:203], v[164:167]\n"
    "v_cvt_pk_bf16_f32 v72, v72, v73\n"
    "v_mfma_f32_16x16x32_bf16 v[168:171], a[200:203], v[200:203], v[168:171]\n"
    "v_cvt_pk_bf16_f32 v73, v74, v75\n"
    "v_mfma_f32_16x16x32_bf16 v[172:175], a[204:207], v[200:203], v[172:175]\n"
    "v_mfma_f32_16x16x32_bf16 v[188:191], a[204:207], v[204:207], v[188:191]\n"
    "ds_read_b128 v[196:199], %[rdx2] offset:59392\n"
    "v_cvt_pk_bf16_f32 v74, v76, v77\n"
    "v_mfma_f32_16x16x32_bf16 v[184:187], a[200:203], v[204:207], v[184:187]\n"
    "v_mfma_f32_16x16x32_bf16 v[180:183], a[196:199], v[204:207], v[180:183]\n"
    "v_cvt_pk_bf16_f32 v75, v78, v79\n"
    "v_mfma_f32_16x16x32_bf16 v[176:179], a[192:195], v[204:207], v[176:179]\n"
    "s_waitcnt lgkmcnt(4)\n"
    "v_mfma_f32_16x16x32_bf16 v[128:131], a[208:211], v[208:211], v[128:131]\n"
    "ds_read_b128 v[200:203], %[rdx2] offset:61440\n"
    "v_mfma_f32_16x16x32_bf16 v[132:135], a[212:215], v[208:211], v[132:135]\n"
    "s_add_u32 s46, %[soo], 17408\n"
    "buffer_store_dwordx4 v[72:75], %[vo], %[srdo], s46 offen sc1\n"
    "v_mfma_f32_16x16x32_bf16 v[136:139], a[216:219], v[208:211], v[136:139]\n"
    "v_mfma_f32_16x16x32_bf16 v[140:143], a[220:223], v[208:211], v[140:143]\n"
    "v_mfma_f32_16x16x32_bf16 v[156:159], a[220:223], v[212:215], v[156:159]\n"
    "ds_read_b128 v[204:207], %[rdx2] offset:63488\n"
    "v_mfma_f32_16x16x32_bf16 v[152:155], a[216:219], v[212:215], v[152:155]\n"
    "v_mfma_f32_16x16x32_bf16 v[148:151], a[212:215], v[212:215], v[148:151]\n"
    "v_mfma_f32_16x16x32_bf16 v[144:147], a[208:211], v[212:215], v[144:147]\n"
    "s_waitcnt lgkmcnt(4)\n"
    "v_mfma_f32_16x16x32_bf16 v[160:163], a[208:211], v[216:219], v[160:163]\n"
    "ds_read_b128 v[208:211], %[rdx3] offset:57344\n"
    "v_mfma_f32_16x16x32_bf16 v[164:167], a[212:215], v[216:219], v[164:167]\n"
    "v_mfma_f32_16x16x32_bf16 v[168:171], a[216:219], v[216:219], v[168:171]\n"
    "v_mfma_f32_16x16x32_bf16 v[172:175], a[220:223], v[216:219], v[172:175]\n"
    "v_mfma_f32_16x16x32_bf16 v[188:191], a[220:223], v[220:223], v[188:191]\n"
    "ds_read_b128 v[212:215], %[rdx3] offset:59392\n"
    "v_mfma_f32_16x16x32_bf16 v[184:187], a[216:219], v[220:223], v[184:187]\n"
    "v_mfma_f32_16x16x32_bf16 v[180:183], a[212:215], v[220:223], v[180:183]\n"
    "v_cvt_pk_bf16_f32 v80, v80, v81\n"
    "v_mfma_f32_16x16x32_bf16 v[176:179], a[208:211], v[220:223], v[176:179]\n"
    "s_waitcnt vmcnt(12)\n"
    "s_barrier\n"
    "s_waitcnt lgkmcnt(4)\n"
    "v_mfma_f32_16x16x32_bf16 v[128:131], a[224:227], v[192:195], v[128:131]\n"
    "ds_read_b128 v[216:219], %[rdx3] offset:61440\n"
    "v_cvt_pk_bf16_f32 v81, v82, v83\n"
    "v_mfma_f32_16x16x32_bf16 v[132:135], a[228:231], v[192:195], v[132:135]\n"
    "v_mfma_f32_16x16x32_bf16 v[136:139], a[232:235], v[192:195], v[136:139]\n"
    "v_cvt_pk_bf16_f32 v82, v84, v85\n"
    "v_mfma_f32_16x16x32_bf16 v[140:143], a[236:239], v[192:195], v[140:143]\n"
    "v_cvt_pk_bf16_f32 v83, v86, v87\n"
    "v_mfma_f32_16x16x32_bf16 v[156:159], a[236:239], v[196:199], v[156:159]\n"
    "ds_read_b128 v[220:223], %[rdx3] offset:63488\n"
    "v_mfma_f32_16x16x32_bf16 v[152:155], a[232:235], v[196:199], v[152:155]\n"
    "v_mfma_f32_16x16x32_bf16 v[148:151], a[228:231], v[196:199], v[148:151]\n"
    "s_add_u32 s46, %[soo], 20480\n"
    "buffer_store_dwordx4 v[80:83], %[vo], %[srdo], s46 offen sc1\n"
    "v_mfma_f32_16x16x32_bf16 v[144:147], a[224:227], v[196:199], v[144:147]\n"
    "s_waitcnt lgkmcnt(4)\n"
    "v_mfma_f32_16x16x32_bf16 v[160:163], a[224:227], v[200:203], v[160:163]\n"
    "ds_read_b128 v[192:195], %[rdx0] offset:0\n"
    "v_mfma_f32_16x16x32_bf16 v[164:167], a[228:231], v[200:203], v[164:167]\n"
    "v_mfma_f32_16x16x32_bf16 v[168:171], a[232:235], v[200:203], v[168:171]\n"
    "v_mfma_f32_16x16x32_bf16 v[172:175], a[236:239], v[200:203], v[172:175]\n"
    "v_mfma_f32_16x16x32_bf16 v[188:191], a[236:239], v[204:207], v[188:191]\n"
    "ds_read_b128 v[196:199], %[rdx0] offset:2048\n"
    "v_mfma_f32_16x16x32_bf16 v[184:187], a[232:235], v[204:207], v[184:187]\n"
    "v_mfma_f32_16x16x32_bf16 v[180:183], a[228:231], v[204:207], v[180:183]\n"
    "v_mfma_f32_16x16x32_bf16 v[176:179], a[224:227], v[204:207], v[176:179]\n"
    "s_waitcnt lgkmcnt(4)\n"
    "v_mfma_f32_16x16x32_bf16 v[128:131], a[240:243], v[208:211], v[128:131]\n"
    "ds_read_b128 v[200:203], %[rdx0] offset:4096\n"
    "v_mfma_f32_16x16x32_bf16 v[132:135], a[244:247], v[208:211], v[132:135]\n"
    "v_mfma_f32_16x16x32_bf16 v[136:139], a[248:251], v[208:211], v[136:139]\n"
    "v_mfma_f32_16x16x32_bf16 v[140:143], a[252:255], v[208:211], v[140:143]\n"
    "v_cvt_pk_bf16_f32 v88, v88, v89\n"
    "v_mfma_f32_16x16x32_bf16 v[156:159], a[252:255], v[212:215], v[156:159]\n"
    "ds_read_b128 v[204:207], %[rdx0] offset:6144\n"
    "v_mfma_f32_16x16x32_bf16 v[152:155], a[248:251], v[212:215], v[152:155]\n"
    "v_cvt_pk_bf16_f32 v89, v90, v91\n"
    "v_mfma_f32_16x16x32_bf16 v[148:151], a[244:247], v[212:215], v[148:151]\n"
    "v_cvt_pk_bf16_f32 v90, v92, v93\n"
    "v_mfma_f32_16x16x32_bf16 v[144:147], a[240:243], v[212:215], v[144:147]\n"
    "s_waitcnt lgkmcnt(4)\n"
    "v_mfma_f32_16x16x32_bf16 v[160:163], a[240:243], v[216:219], v[160:163]\n"
    "ds_read_b128 v[208:211], %[rdx1] offset:0\n"
    "v_mfma_f32_16x16x32_bf16 v[164:167], a[244:247], v[216:219], v[164:167]\n"
    "v_cvt_pk_bf16_f32 v91, v94, v95\n"
    "v_mfma_f32_16x16x32_bf16 v[168:171], a[248:251], v[216:219], v[168:171]\n"
    "v_mfma_f32_16x16x32_bf16 v[172:175], a[252:255], v[216:219], v[172:175]\n"
    "s_add_u32 s46, %[soo], 21504\n"
    "buffer_store_dwordx4 v[88:91], %[vo], %[srdo], s46 offen sc1\n"
    "v_mfma_f32_16x16x32_bf16 v[188:191], a[252:255], v[220:223], v[188:191]\n"
    "ds_read_b128 v[212:215], %[rdx1] offset:2048\n"
    "v_mfma_f32_16x16x32_bf16 v[184:187], a[248:251], v[220:223], v[184:187]\n"
    "v_mfma_f32_16x16x32_bf16 v[180:183], a[244:247], v[220:223], v[180:183]\n"
    "v_mfma_f32_16x16x32_bf16 v[176:179], a[240:243], v[220:223], v[176:179]\n"
    "s_waitcnt lgkmcnt(4)\n"
    "v_mfma_f32_16x16x32_bf16 v[0:3], a[0:3], v[192:195], 0\n"
    "ds_read_b128 v[216:219], %[rdx1] offset:4096\n"
    "v_mfma_f32_16x16x32_bf16 v[4:7], a[4:7], v[192:195], 0\n"
    "v_mfma_f32_16x16x32_bf16 v[8:11], a[8:11], v[192:195], 0\n"
    "v_mfma_f32_16x16x32_bf16 v[12:15], a[12:15], v[192:195], 0\n"
    "v_mfma_f32_16x16x32_bf16 v[28:31], a[12:15], v[196:199], 0\n"
    "ds_read_b128 v[220:223], %[rdx1] offset:6144\n"
    "v_mfma_f32_16x16x32_bf16 v[24:27], a[8:11], v[196:199], 0\n"
    "v_mfma_f32_16x16x32_bf16 v[20:23], a[4:7], v[196:199], 0\n"
    "v_mfma_f32_16x16x32_bf16 v[16:19], a[0:3], v[196:199], 0\n"
    "s_waitcnt lgkmcnt(4)\n"
    "v_mfma_f32_16x16x32_bf16 v[32:35], a[0:3], v[200:203], 0\n"
    "ds_read_b128 v[192:195], %[rdx0] offset:8192\n"
    "v_mfma_f32_16x16x32_bf16 v[36:39], a[4:7], v[200:203], 0\n"
    "v_mfma_f32_16x16x32_bf16 v[40:43], a[8:11], v[200:203], 0\n"
    "v_mfma_f32_16x16x32_bf16 v[44:47], a[12:15], v[200:203], 0\n"
    "v_mfma_f32_16x16x32_bf16 v[60:63], a[12:15], v[204:207], 0\n"
    "ds_read_b128 v[196:199], %[rdx0] offset:10240\n"
    "v_mfma_f32_16x16x32_bf16 v[56:59], a[8:11], v[204:207], 0\n"
    "v_cvt_pk_bf16_f32 v96, v96, v97\n"
    "v_mfma_f32_16x16x32_bf16 v[52:55], a[4:7], v[204:207], 0\n"
    "v_cvt_pk_bf16_f32 v97, v98, v99\n"
    "v_mfma_f32_16x16x32_bf16 v[48:51], a[0:3], v[204:207], 0\n"
    "s_waitcnt lgkmcnt(4)\n"
    "v_mfma_f32_16x16x32_bf16 v[0:3], a[16:19], v[208:211], v[0:3]\n"
    "ds_read_b128 v[200:203], %[rdx0] offset:12288\n"
    "v_cvt_pk_bf16_f32 v98, v100, v101\n"
    "v_mfma_f32_16x16x32_bf16 v[4:7], a[20:23], v[208:211], v[4:7]\n"
    "v_mfma_f32_16x16x32_bf16 v[8:11], a[24:27], v[208:211], v[8:11]\n"
    "v_cvt_pk_bf16_f32 v99, v102, v103\n"
    "v_mfma_f32_16x16x32_bf16 v[12:15], a[28:31], v[208:211], v[12:15]\n"
    "v_mfma_f32_16x16x32_bf16 v[28:31], a[28:31], v[212:215], v[28:31]\n"
    "ds_read_b128 v[204:207], %[rdx0] offset:14336\n"
    "v_mfma_f32_16x16x32_bf16 v[24:27], a[24:27], v[212:215], v[24:27]\n"
    "s_add_u32 s46, %[soo], 24576\n"
    "buffer_store_dwordx4 v[96:99], %[vo], %[srdo], s46 offen sc1\n"
    "v_mfma_f32_16x16x32_bf16 v[20:23], a[20:23], v[212:215], v[20:23]\n"
    "v_mfma_f32_16x16x32_bf16 v[16:19], a[16:19], v[212:215], v[16:19]\n"
    "s_waitcnt lgkmcnt(4)\n"
    "v_mfma_f32_16x16x32_bf16 v[32:35], a[16:19], v[216:219], v[32:35]\n"
    "ds_read_b128 v[208:211], %[rdx1] offset:8192\n"
    "v_mfma_f32_16x16x32_bf16 v[36:39], a[20:23], v[216:219], v[36:39]\n"
    "v_mfma_f32_16x16x32_bf16 v[40:43], a[24:27], v[216:219], v[40:43]\n"
    "v_mfma_f32_16x16x32_bf16 v[44:47], a[28:31], v[216:219], v[44:47]\n"
    "v_mfma_f32_16x16x32_bf16 v[60:63], a[28:31], v[220:223], v[60:63]\n"
    "ds_read_b128 v[212:215], %[rdx1] offset:10240\n"
    "v_mfma_f32_16x16x32_bf16 v[56:59], a[24:27], v[220:223], v[56:59]\n"
    "v_mfma_f32_16x16x32_bf16 v[52:55], a[20:23], v[220:223], v[52:55]\n"
    "v_mfma_f32_16x16x32_bf16 v[48:51], a[16:19], v[220:223], v[48:51]\n"
    "s_waitcnt lgkmcnt(4)\n"
    "v_mfma_f32_16x16x32_bf16 v[0:3], a[32:35], v[192:195], v[0:3]\n"
    "ds_read_b128 v[216:219], %[rdx1] offset:12288\n"
    "v_mfma_f32_16x16x32_bf16 v[4:7], a[36:39], v[192:195], v[4:7]\n"
    "v_mfma_f32_16x16x32_bf16 v[8:11], a[40:43], v[192:195], v[8:11]\n"
    "v_cvt_pk_bf16_f32 v104, v104, v105\n"
    "v_mfma_f32_16x16x32_bf16 v[12:15], a[44:47], v[192:195], v[12:15]\n"
    "v_cvt_pk_bf16_f32 v105, v106, v107\n"
    "v_mfma_f32_16x16x32_bf16 v[28:31], a[44:47], v[196:199], v[28:31]\n"
    "ds_read_b128 v[220:223], %[rdx1] offset:14336\n"
    "v_mfma_f32_16x16x32_bf16 v[24:27], a[40:43], v[196:199], v[24:27]\n"
    "v_cvt_pk_bf16_f32 v106, v108, v109\n"
    "v_mfma_f32_16x16x32_bf16 v[20:23], a[36:39], v[196:199], v[20:23]\n"
    "v_mfma_f32_16x16x32_bf16 v[16:19], a[32:35], v[196:199], v[16:19]\n"
    "s_waitcnt lgkmcnt(4)\n"
    "v_cvt_pk_bf16_f32 v107, v110, v111\n"
    "v_mfma_f32_16x16x32_bf16 v[32:35], a[32:35], v[200:203], v[32:35]\n"
    "ds_read_b128 v[192:195], %[rdx0] offset:16384\n"
    "v_mfma_f32_16x16x32_bf16 v[36:39], a[36:39], v[200:203], v[36:39]\n"
    "v_mfma_f32_16x16x32_bf16 v[40:43], a[40:43], v[200:203], v[40:43]\n"
    "s_add_u32 s46, %[soo], 25600\n"
    "buffer_store_dwordx4 v[104:107], %[vo], %[srdo], s46 offen sc1\n"
    "v_mfma_f32_16x16x32_bf16 v[44:47], a[44:47], v[200:203], v[44:47]\n"
    "v_mfma_f32_16x16x32_bf16 v[60:63], a[44:47], v[204:207], v[60:63]\n"
    "ds_read_b128 v[196:199], %[rdx0] offset:18432\n"
    "v_mfma_f32_16x16x32_bf16 v[56:59], a[40:43], v[204:207], v[56:59]\n"
    "v_mfma_f32_16x16x32_bf16 v[52:55], a[36:39], v[204:207], v[52:55]\n"
    "v_mfma_f32_16x16x32_bf16 v[48:51], a[32:35], v[204:207], v[48:51]\n"
    "s_waitcnt lgkmcnt(4)\n"
    "v_mfma_f32_16x16x32_bf16 v[0:3], a[48:51], v[208:211], v[0:3]\n"
    "ds_read_b128 v[200:203], %[rdx0] offset:20480\n"
    "v_mfma_f32_16x16x32_bf16 v[4:7], a[52:55], v[208:211], v[4:7]\n"
    "v_mfma_f32_16x16x32_bf16 v[8:11], a[56:59], v[208:211], v[8:11]\n"
    "v_mfma_f32_16x16x32_bf16 v[12:15], a[60:63], v[208:211], v[12:15]\n"
    "v_mfma_f32_16x16x32_bf16 v[28:31], a[60:63], v[212:215], v[28:31]\n"
    "ds_read_b128 v[204:207], %[rdx0] offset:22528\n"
    "v_mfma_f32_16x16x32_bf16 v[24:27], a[56:59], v[212:215], v[24:27]\n"
    "v_mfma_f32_16x16x32_bf16 v[20:23], a[52:55], v[212:215], v[20:23]\n"
    "v_cvt_pk_bf16_f32 v112, v112, v113\n"
    "v_mfma_f32_16x16x32_bf16 v[16:19], a[48:51], v[212:215], v[16:19]\n"
    "s_waitcnt lgkmcnt(4)\n"
    "v_cvt_pk_bf16_f32 v113, v114, v115\n"
    "v_mfma_f32_16x16x32_bf16 v[32:35], a[48:51], v[216:219], v[32:35]\n"
    "ds_read_b128 v[208:211], %[rdx1] offset:16384\n"
    "v_mfma_f32_16x16x32_bf16 v[36:39], a[52:55], v[216:219], v[36:39]\n"
    "v_cvt_pk_bf16_f32 v114, v116, v117\n"
    "v_mfma_f32_16x16x32_bf16 v[40:43], a[56:59], v[216:219], v[40:43]\n"
    "v_mfma_f32_16x16x32_bf16 v[44:47], a[60:63], v[216:219], v[44:47]\n"
    "v_cvt_pk_bf16_f32 v115, v118, v119\n"
    "v_mfma_f32_16x16x32_bf16 v[60:63], a[60:63], v[220:223], v[60:63]\n"
    "ds_read_b128 v[212:215], %[rdx1] offset:18432\n"
    "v_mfma_f32_16x16x32_bf16 v[56:59], a[56:59], v[220:223], v[56:59]\n"
    "v_mfma_f32_16x16x32_bf16 v[52:55], a[52:55], v[220:223], v[52:55]\n"
    "s_add_u32 s46, %[soo], 28672\n"
    "buffer_store_dwordx4 v[112:115], %[vo], %[srdo], s46 offen sc1\n"
    "v_mfma_f32_16x16x32_bf16 v[48:51], a[48:51], v[220:223], v[48:51]\n"
    "s_waitcnt lgkmcnt(4)\n"
    "v_mfma_f32_16x16x32_bf16 v[0:3], a[64:67], v[192:195], v[0:3]\n"
    "ds_read_b128 v[216:219], %[rdx1] offset:20480\n"
    "v_mfma_f32_16x16x32_bf16 v[4:7], a[68:71], v[192:195], v[4:7]\n"
    "v_mfma_f32_16x16x32_bf16 v[8:11], a[72:75], v[192:195], v[8:11]\n"
    "v_mfma_f32_16x16x32_bf16 v[12:15], a[76:79], v[192:195], v[12:15]\n"
    "v_mfma_f32_16x16x32_bf16 v[28:31], a[76:79], v[196:199], v[28:31]\n"
    "ds_read_b128 v[220:223], %[rdx1] offset:22528\n"
    "v_mfma_f32_16x16x32_bf16 v[24:27], a[72:75], v[196:199], v[24:27]\n"
    "v_mfma_f32_16x16x32_bf16 v[20:23], a[68:71], v[196:199], v[20:23]\n"
    "v_mfma_f32_16x16x32_bf16 v[16:19], a[64:67], v[196:199], v[16:19]\n"
    "s_waitcnt lgkmcnt(4)\n"
    "v_mfma_f32_16x16x32_bf16 v[32:35], a[64:67], v[200:203], v[32:35]\n"
    "ds_read_b128 v[192:195], %[rdx0] offset:24576\n"
    "v_mfma_f32_16x16x32_bf16 v[36:39], a[68:71], v[200:203], v[36:39]\n"
    "v_mfma_f32_16x16x32_bf16 v[40:43], a[72:75], v[200:203], v[40:43]\n"
    "v_mfma_f32_16x16x32_bf16 v[44:47], a[76:79], v[200:203], v[44:47]\n"
    "v_cvt_pk_bf16_f32 v120, v120, v121\n"
    "v_mfma_f32_16x16x32_bf16 v[60:63], a[76:79], v[204:207], v[60:63]\n"
    "ds_read_b128 v[196:199], %[rdx0] offset:26624\n"
    "v_mfma_f32_16x16x32_bf16 v[56:59], a[72:75], v[204:207], v[56:59]\n"
    "v_cvt_pk_bf16_f32 v121, v122, v123\n"
    "v_mfma_f32_16x16x32_bf16 v[52:55], a[68:71], v[204:207], v[52:55]\n"
    "v_cvt_pk_bf16_f32 v122, v124, v125\n"
    "v_mfma_f32_16x16x32_bf16 v[48:51], a[64:67], v[204:207], v[48:51]\n"
    "s_waitcnt lgkmcnt(4)\n"
    "v_mfma_f32_16x16x32_bf16 v[0:3], a[80:83], v[208:211], v[0:3]\n"
    "ds_read_b128 v[200:203], %[rdx0] offset:28672\n"
    "v_mfma_f32_16x16x32_bf16 v[4:7], a[84:87], v[208:211], v[4:7]\n"
    "v_cvt_pk_bf16_f32 v123, v126, v127\n"
    "v_mfma_f32_16x16x32_bf16 v[8:11], a[88:91], v[208:211], v[8:11]\n"
    "v_mfma_f32_16x16x32_bf16 v[12:15], a[92:95], v[208:211], v[12:15]\n"
    "s_add_u32 s46, %[soo], 29696\n"
    "buffer_store_dwordx4 v[120:123], %[vo], %[srdo], s46 offen sc1\n"
    "v_mfma_f32_16x16x32_bf16 v[28:31], a[92:95], v[212:215], v[28:31]\n"
    "ds_read_b128 v[204:207], %[rdx0] offset:30720\n"
    "v_mfma_f32_16x16x32_bf16 v[24:27], a[88:91], v[212:215], v[24:27]\n"
    "v_mfma_f32_16x16x32_bf16 v[20:23], a[84:87], v[212:215], v[20:23]\n"
    "v_mfma_f32_16x16x32_bf16 v[16:19], a[80:83], v[212:215], v[16:19]\n"
    "s_waitcnt lgkmcnt(4)\n"
    "v_mfma_f32_16x16x32_bf16 v[32:35], a[80:83], v[216:219], v[32:35]\n"
    "ds_read_b128 v[208:211], %[rdx1] offset:24576\n"
    "v_mfma_f32_16x16x32_bf16 v[36:39], a[84:87], v[216:219], v[36:39]\n"
    "v_mfma_f32_16x16x32_bf16 v[40:43], a[88:91], v[216:219], v[40:43]\n"
    "v_mfma_f32_16x16x32_bf16 v[44:47], a[92:95], v[216:219], v[44:47]\n"
    "v_mfma_f32_16x16x32_bf16 v[60:63], a[92:95], v[220:223], v[60:63]\n"
    "ds_read_b128 v[212:215], %[rdx1] offset:26624\n"
    "v_mfma_f32_16x16x32_bf16 v[56:59], a[88:91], v[220:223], v[56:59]\n"
    "v_mfma_f32_16x16x32_bf16 v[52:55], a[84:87], v[220:223], v[52:55]\n"
    "v_mfma_f32_16x16x32_bf16 v[48:51], a[80:83], v[220:223], v[48:51]\n"
    "s_waitcnt lgkmcnt(4)\n"
    "v_cvt_pk_bf16_f32 v128, v128, v129\n"
    "v_mfma_f32_16x16x32_bf16 v[0:3], a[96:99], v[192:195], v[0:3]\n"
    "ds_read_b128 v[216:219], %[rdx1] offset:28672\n"
    "v_mfma_f32_16x16x32_bf16 v[4:7], a[100:103], v[192:195], v[4:7]\n"
    "v_cvt_pk_bf16_f32 v129, v130, v131\n"
    "v_mfma_f32_16x16x32_bf16 v[8:11], a[104:107], v[192:195], v[8:11]\n"
    "v_cvt_pk_bf16_f32 v130, v132, v133\n"
    "v_mfma_f32_16x16x32_bf16 v[12:15], a[108:111], v[192:195], v[12:15]\n"
    "v_mfma_f32_16x16x32_bf16 v[28:31], a[108:111], v[196:199], v[28:31]\n"
    "ds_read_b128 v[220:223], %[rdx1] offset:30720\n"
    "v_mfma_f32_16x16x32_bf16 v[24:27], a[104:107], v[196:199], v[24:27]\n"
    "v_cvt_pk_bf16_f32 v131, v134, v135\n"
    "v_mfma_f32_16x16x32_bf16 v[20:23], a[100:103], v[196:199], v[20:23]\n"
    "v_mfma_f32_16x16x32_bf16 v[16:19], a[96:99], v[196:199], v[16:19]\n"
    "s_add_u32 s46, %[soo], 32768\n"
    "buffer_store_dwordx4 v[128:131], %[vo], %[srdo], s46 offen sc1\n"
    "s_waitcnt lgkmcnt(4)\n"
    "v_mfma_f32_16x16x32_bf16 v[32:35], a[96:99], v[200:203], v[32:35]\n"
    "ds_read_b128 v[192:195], %[rdx0] offset:32768\n"
    "v_mfma_f32_16x16x32_bf16 v[36:39], a[100:103], v[200:203], v[36:39]\n"
    "v_mfma_f32_16x16x32_bf16 v[40:43], a[104:107], v[200:203], v[40:43]\n"
    "v_mfma_f32_16x16x32_bf16 v[44:47], a[108:111], v[200:203], v[44:47]\n"
    "v_mfma_f32_16x16x32_bf16 v[60:63], a[108:111], v[204:207], v[60:63]\n"
    "ds_read_b128 v[196:199], %[rdx0] offset:34816\n"
    "v_mfma_f32_16x16x32_bf16 v[56:59], a[104:107], v[204:207], v[56:59]\n"
    "v_mfma_f32_16x16x32_bf16 v[52:55], a[100:103], v[204:207], v[52:55]\n"
    "v_mfma_f32_16x16x32_bf16 v[48:51], a[96:99], v[204:207], v[48:51]\n"
    "s_waitcnt lgkmcnt(4)\n"
    "v_mfma_f32_16x16x32_bf16 v[0:3], a[112:115], v[208:211], v[0:3]\n"
    "ds_read_b128 v[200:203], %[rdx0] offset:36864\n"
    "v_mfma_f32_16x16x32_bf16 v[4:7], a[116:119], v[208:211], v[4:7]\n"
    "v_mfma_f32_16x16x32_bf16 v[8:11], a[120:123], v[208:211], v[8:11]\n"
    "v_mfma_f32_16x16x32_bf16 v[12:15], a[124:127], v[208:211], v[12:15]\n"
    "v_mfma_f32_16x16x32_bf16 v[28:31], a[124:127], v[212:215], v[28:31]\n"
    "ds_read_b128 v[204:207], %[rdx0] offset:38912\n"
    "v_cvt_pk_bf16_f32 v136, v136, v137\n"
    "v_mfma_f32_16x16x32_bf16 v[24:27], a[120:123], v[212:215], v[24:27]\n"
    "v_mfma_f32_16x16x32_bf16 v[20:23], a[116:119], v[212:215], v[20:23]\n"
    "v_cvt_pk_bf16_f32 v137, v138, v139\n"
    "v_mfma_f32_16x16x32_bf16 v[16:19], a[112:115], v[212:215], v[16:19]\n"
    "s_waitcnt lgkmcnt(4)\n"
    "v_cvt_pk_bf16_f32 v138, v140, v141\n"
    "v_mfma_f32_16x16x32_bf16 v[32:35], a[112:115], v[216:219], v[32:35]\n"
    "ds_read_b128 v[208:211], %[rdx1] offset:32768\n"
    "v_mfma_f32_16x16x32_bf16 v[36:39], a[116:119], v[216:219], v[36:39]\n"
    "v_mfma_f32_16x16x32_bf16 v[40:43], a[120:123], v[216:219], v[40:43]\n"
    "v_cvt_pk_bf16_f32 v139, v142, v143\n"
    "v_mfma_f32_16x16x32_bf16 v[44:47], a[124:127], v[216:219], v[44:47]\n"
    "v_mfma_f32_16x16x32_bf16 v[60:63], a[124:127], v[220:223], v[60:63]\n"
    "s_add_u32 s46, %[soo], 33792\n"
    "ds_read_b128 v[212:215], %[rdx1] offset:34816\n"
    "buffer_store_dwordx4 v[136:139], %[vo], %[srdo], s46 offen sc1\n"
    "v_mfma_f32_16x16x32_bf16 v[56:59], a[120:123], v[220:223], v[56:59]\n"
    "v_mfma_f32_16x16x32_bf16 v[52:55], a[116:119], v[220:223], v[52:55]\n"
    "v_mfma_f32_16x16x32_bf16 v[48:51], a[112:115], v[220:223], v[48:51]\n"
    "s_waitcnt lgkmcnt(4)\n"
    "v_mfma_f32_16x16x32_bf16 v[0:3], a[128:131], v[192:195], v[0:3]\n"
    "ds_read_b128 v[216:219], %[rdx1] offset:36864\n"
    "v_mfma_f32_16x16x32_bf16 v[4:7], a[132:135], v[192:195], v[4:7]\n"
    "v_mfma_f32_16x16x32_bf16 v[8:11], a[136:139], v[192:195], v[8:11]\n"
    "v_mfma_f32_16x16x32_bf16 v[12:15], a[140:143], v[192:195], v[12:15]\n"
    "v_mfma_f32_16x16x32_bf16 v[28:31], a[140:143], v[196:199], v[28:31]\n"
    "ds_read_b128 v[220:223], %[rdx1] offset:38912\n"
    "v_mfma_f32_16x16x32_bf16 v[24:27], a[136:139], v[196:199], v[24:27]\n"
    "v_mfma_f32_16x16x32_bf16 v[20:23], a[132:135], v[196:199], v[20:23]\n"
    "v_mfma_f32_16x16x32_bf16 v[16:19], a[128:131], v[196:199], v[16:19]\n"
    "s_waitcnt lgkmcnt(4)\n"
    "v_mfma_f32_16x16x32_bf16 v[32:35], a[128:131], v[200:203], v[32:35]\n"
    "ds_read_b128 v[192:195], %[rdx0] offset:40960\n"
    "v_cvt_pk_bf16_f32 v144, v144, v145\n"
    "v_mfma_f32_16x16x32_bf16 v[36:39], a[132:135], v[200:203], v[36:39]\n"
    "v_mfma_f32_16x16x32_bf16 v[40:43], a[136:139], v[200:203], v[40:43]\n"
    "v_cvt_pk_bf16_f32 v145, v146, v147\n"
    "v_mfma_f32_16x16x32_bf16 v[44:47], a[140:143], v[200:203], v[44:47]\n"
    "v_cvt_pk_bf16_f32 v146, v148, v149\n"
    "v_mfma_f32_16x16x32_bf16 v[60:63], a[140:143], v[204:207], v[60:63]\n"
    "ds_read_b128 v[196:199], %[rdx0] offset:43008\n"
    "v_mfma_f32_16x16x32_bf16 v[56:59], a[136:139], v[204:207], v[56:59]\n"
    "v_mfma_f32_16x16x32_bf16 v[52:55], a[132:135], v[204:207], v[52:55]\n"
    "v_cvt_pk_bf16_f32 v147, v150, v151\n"
    "v_mfma_f32_16x16x32_bf16 v[48:51], a[128:131], v[204:207], v[48:51]\n"
    "s_waitcnt lgkmcnt(4)\n"
    "v_mfma_f32_16x16x32_bf16 v[0:3], a[144:147], v[208:211], v[0:3]\n"
    "s_add_u32 s46, %[soo], 36864\n"
    "ds_read_b128 v[200:203], %[rdx0] offset:45056\n"
    "buffer_store_dwordx4 v[144:147], %[vo], %[srdo], s46 offen sc1\n"
    "v_mfma_f32_16x16x32_bf16 v[4:7], a[148:151], v[208:211], v[4:7]\n"
    "v_mfma_f32_16x16x32_bf16 v[8:11], a[152:155], v[208:211], v[8:11]\n"
    "v_mfma_f32_16x16x32_bf16 v[12:15], a[156:159], v[208:211], v[12:15]\n"
    "v_mfma_f32_16x16x32_bf16 v[28:31], a[156:159], v[212:215], v[28:31]\n"
    "ds_read_b128 v[204:207], %[rdx0] offset:47104\n"
    "v_mfma_f32_16x16x32_bf16 v[24:27], a[152:155], v[212:215], v[24:27]\n"
    "v_mfma_f32_16x16x32_bf16 v[20:23], a[148:151], v[212:215], v[20:23]\n"
    "v_mfma_f32_16x16x32_bf16 v[16:19], a[144:147], v[212:215], v[16:19]\n"
    "s_waitcnt lgkmcnt(4)\n"
    "v_mfma_f32_16x16x32_bf16 v[32:35], a[144:147], v[216:219], v[32:35]\n"
    "ds_read_b128 v[208:211], %[rdx1] offset:40960\n"
    "v_mfma_f32_16x16x32_bf16 v[36:39], a[148:151], v[216:219], v[36:39]\n"
    "v_mfma_f32_16x16x32_bf16 v[40:43], a[152:155], v[216:219], v[40:43]\n"
    "v_mfma_f32_16x16x32_bf16 v[44:47], a[156:159], v[216:219], v[44:47]\n"
    "v_mfma_f32_16x16x32_bf16 v[60:63], a[156:159], v[220:223], v[60:63]\n"
    "ds_read_b128 v[212:215], %[rdx1] offset:43008\n"
    "v_mfma_f32_16x16x32_bf16 v[56:59], a[152:155], v[220:223], v[56:59]\n"
    "v_cvt_pk_bf16_f32 v152, v152, v153\n"
    "v_mfma_f32_16x16x32_bf16 v[52:55], a[148:151], v[220:223], v[52:55]\n"
    "v_cvt_pk_bf16_f32 v153, v154, v155\n"
    "v_mfma_f32_16x16x32_bf16 v[48:51], a[144:147], v[220:223], v[48:51]\n"
    "s_waitcnt lgkmcnt(4)\n"
    "v_mfma_f32_16x16x32_bf16 v[0:3], a[160:163], v[192:195], v[0:3]\n"
    "ds_read_b128 v[216:219], %[rdx1] offset:45056\n"
    "v_cvt_pk_bf16_f32 v154, v156, v157\n"
    "v_mfma_f32_16x16x32_bf16 v[4:7], a[164:167], v[192:195], v[4:7]\n"
    "v_mfma_f32_16x16x32_bf16 v[8:11], a[168:171], v[192:195], v[8:11]\n"
    "v_cvt_pk_bf16_f32 v155, v158, v159\n"
    "v_mfma_f32_16x16x32_bf16 v[12:15], a[172:175], v[192:195], v[12:15]\n"
    "v_mfma_f32_16x16x32_bf16 v[28:31], a[172:175], v[196:199], v[28:31]\n"
    "ds_read_b128 v[220:223], %[rdx1] offset:47104\n"
    "v_mfma_f32_16x16x32_bf16 v[24:27], a[168:171], v[196:199], v[24:27]\n"
    "s_add_u32 s46, %[soo], 37888\n"
    "buffer_store_dwordx4 v[152:155], %[vo], %[srdo], s46 offen sc1\n"
    "v_mfma_f32_16x16x32_bf16 v[20:23], a[164:167], v[196:199], v[20:23]\n"
    "v_mfma_f32_16x16x32_bf16 v[16:19], a[160:163], v[196:199], v[16:19]\n"
    "s_waitcnt lgkmcnt(4)\n"
    "v_mfma_f32_16x16x32_bf16 v[32:35], a[160:163], v[200:203], v[32:35]\n"
    "ds_read_b128 v[192:195], %[rdx0] offset:49152\n"
    "v_mfma_f32_16x16x32_bf16 v[36:39], a[164:167], v[200:203], v[36:39]\n"
    "v_mfma_f32_16x16x32_bf16 v[40:43], a[168:171], v[200:203], v[40:43]\n"
    "v_mfma_f32_16x16x32_bf16 v[44:47], a[172:175], v[200:203], v[44:47]\n"
    "v_mfma_f32_16x16x32_bf16 v[60:63], a[172:175], v[204:207], v[60:63]\n"
    "ds_read_b128 v[196:199], %[rdx0] offset:51200\n"
    "v_mfma_f32_16x16x32_bf16 v[56:59], a[168:171], v[204:207], v[56:59]\n"
    "v_mfma_f32_16x16x32_bf16 v[52:55], a[164:167], v[204:207], v[52:55]\n"
    "v_mfma_f32_16x16x32_bf16 v[48:51], a[160:163], v[204:207], v[48:51]\n"
    "s_waitcnt lgkmcnt(4)\n"
    "v_mfma_f32_16x16x32_bf16 v[0:3], a[176:179], v[208:211], v[0:3]\n"
    "ds_read_b128 v[200:203], %[rdx0] offset:53248\n"
    "v_mfma_f32_16x16x32_bf16 v[4:7], a[180:183], v[208:211], v[4:7]\n"
    "v_cvt_pk_bf16_f32 v160, v160, v161\n"
    "v_mfma_f32_16x16x32_bf16 v[8:11], a[184:187], v[208:211], v[8:11]\n"
    "v_cvt_pk_bf16_f32 v161, v162, v163\n"
    "v_mfma_f32_16x16x32_bf16 v[12:15], a[188:191], v[208:211], v[12:15]\n"
    "v_mfma_f32_16x16x32_bf16 v[28:31], a[188:191], v[212:215], v[28:31]\n"
    "ds_read_b128 v[204:207], %[rdx0] offset:55296\n"
    "v_cvt_pk_bf16_f32 v162, v164, v165\n"
    "v_mfma_f32_16x16x32_bf16 v[24:27], a[184:187], v[212:215], v[24:27]\n"
    "v_mfma_f32_16x16x32_bf16 v[20:23], a[180:183], v[212:215], v[20:23]\n"
    "v_cvt_pk_bf16_f32 v163, v166, v167\n"
    "v_mfma_f32_16x16x32_bf16 v[16:19], a[176:179], v[212:215], v[16:19]\n"
    "s_waitcnt lgkmcnt(4)\n"
    "v_mfma_f32_16x16x32_bf16 v[32:35], a[176:179], v[216:219], v[32:35]\n"
    "ds_read_b128 v[208:211], %[rdx1] offset:49152\n"
    "v_mfma_f32_16x16x32_bf16 v[36:39], a[180:183], v[216:219], v[36:39]\n"
    "s_add_u32 s46, %[soo], 40960\n"
    "buffer_store_dwordx4 v[160:163], %[vo], %[srdo], s46 offen sc1\n"
    "v_mfma_f32_16x16x32_bf16 v[40:43], a[184:187], v[216:219], v[40:43]\n"
    "v_mfma_f32_16x16x32_bf16 v[44:47], a[188:191], v[216:219], v[44:47]\n"
    "v_mfma_f32_16x16x32_bf16 v[60:63], a[188:191], v[220:223], v[60:63]\n"
    "ds_read_b128 v[212:215], %[rdx1] offset:51200\n"
    "v_mfma_f32_16x16x32_bf16 v[56:59], a[184:187], v[220:223], v[56:59]\n"
    "v_mfma_f32_16x16x32_bf16 v[52:55], a[180:183], v[220:223], v[52:55]\n"
    "v_mfma_f32_16x16x32_bf16 v[48:51], a[176:179], v[220:223], v[48:51]\n"
    "s_waitcnt lgkmcnt(4)\n"
    "v_mfma_f32_16x16x32_bf16 v[0:3], a[192:195], v[192:195], v[0:3]\n"
    "ds_read_b128 v[216:219], %[rdx1] offset:53248\n"
    "v_mfma_f32_16x16x32_bf16 v[4:7], a[196:199], v[192:195], v[4:7]\n"
    "v_mfma_f32_16x16x32_bf16 v[8:11], a[200:203], v[192:195], v[8:11]\n"
    "v_mfma_f32_16x16x32_bf16 v[12:15], a[204:207], v[192:195], v[12:15]\n"
    "v_mfma_f32_16x16x32_bf16 v[28:31], a[204:207], v[196:199], v[28:31]\n"
    "ds_read_b128 v[220:223], %[rdx1] offset:55296\n"
    "v_mfma_f32_16x16x32_bf16 v[24:27], a[200:203], v[196:199], v[24:27]\n"
    "v_mfma_f32_16x16x32_bf16 v[20:23], a[196:199], v[196:199], v[20:23]\n"
    "v_cvt_pk_bf16_f32 v168, v168, v169\n"
    "v_mfma_f32_16x16x32_bf16 v[16:19], a[192:195], v[196:199], v[16:19]\n"
    "s_waitcnt lgkmcnt(4)\n"
    "v_cvt_pk_bf16_f32 v169, v170, v171\n"
    "v_mfma_f32_16x16x32_bf16 v[32:35], a[192:195], v[200:203], v[32:35]\n"
    "ds_read_b128 v[192:195], %[rdx0] offset:57344\n"
    "v_mfma_f32_16x16x32_bf16 v[36:39], a[196:199], v[200:203], v[36:39]\n"
    "v_cvt_pk_bf16_f32 v170, v172, v173\n"
    "v_mfma_f32_16x16x32_bf16 v[40:43], a[200:203], v[200:203], v[40:43]\n"
    "v_mfma_f32_16x16x32_bf16 v[44:47], a[204:207], v[200:203], v[44:47]\n"
    "v_cvt_pk_bf16_f32 v171, v174, v175\n"
    "v_mfma_f32_16x16x32_bf16 v[60:63], a[204:207], v[204:207], v[60:63]\n"
    "ds_read_b128 v[196:199], %[rdx0] offset:59392\n"
    "v_mfma_f32_16x16x32_bf16 v[56:59], a[200:203], v[204:207], v[56:59]\n"
    "v_mfma_f32_16x16x32_bf16 v[52:55], a[196:199], v[204:207], v[52:55]\n"
    "s_add_u32 s46, %[soo], 41984\n"
    "buffer_store_dwordx4 v[168:171], %[vo], %[srdo], s46 offen sc1\n"
    "v_mfma_f32_16x16x32_bf16 v[48:51], a[192:195], v[204:207], v[48:51]\n"
    "s_waitcnt lgkmcnt(4)\n"
    "v_mfma_f32_16x16x32_bf16 v[0:3], a[208:211], v[208:211], v[0:3]\n"
    "ds_read_b128 v[200:203], %[rdx0] offset:61440\n"
    "v_mfma_f32_16x16x32_bf16 v[4:7], a[212:215], v[208:211], v[4:7]\n"
    "v_mfma_f32_16x16x32_bf16 v[8:11], a[216:219], v[208:211], v[8:11]\n"
    "v_mfma_f32_16x16x32_bf16 v[12:15], a[220:223], v[208:211], v[12:15]\n"
    "v_mfma_f32_16x16x32_bf16 v[28:31], a[220:223], v[212:215], v[28:31]\n"
    "ds_read_b128 v[204:207], %[rdx0] offset:63488\n"
    "v_mfma_f32_16x16x32_bf16 v[24:27], a[216:219], v[212:215], v[24:27]\n"
    "v_mfma_f32_16x16x32_bf16 v[20:23], a[212:215], v[212:215], v[20:23]\n"
    "v_mfma_f32_16x16x32_bf16 v[16:19], a[208:211], v[212:215], v[16:19]\n"
    "s_waitcnt lgkmcnt(4)\n"
    "v_mfma_f32_16x16x32_bf16 v[32:35], a[208:211], v[216:219], v[32:35]\n"
    "ds_read_b128 v[208:211], %[rdx1] offset:57344\n"
    "v_mfma_f32_16x16x32_bf16 v[36:39], a[212:215], v[216:219], v[36:39]\n"
    "v_mfma_f32_16x16x32_bf16 v[40:43], a[216:219], v[216:219], v[40:43]\n"
    "v_cvt_pk_bf16_f32 v176, v176, v177\n"
    "v_mfma_f32_16x16x32_bf16 v[44:47], a[220:223], v[216:219], v[44:47]\n"
    "v_cvt_pk_bf16_f32 v177, v178, v179\n"
    "v_mfma_f32_16x16x32_bf16 v[60:63], a[220:223], v[220:223], v[60:63]\n"
    "ds_read_b128 v[212:215], %[rdx1] offset:59392\n"
    "v_mfma_f32_16x16x32_bf16 v[56:59], a[216:219], v[220:223], v[56:59]\n"
    "v_cvt_pk_bf16_f32 v178, v180, v181\n"
    "v_mfma_f32_16x16x32_bf16 v[52:55], a[212:215], v[220:223], v[52:55]\n"
    "v_mfma_f32_16x16x32_bf16 v[48:51], a[208:211], v[220:223], v[48:51]\n"
    "s_waitcnt lgkmcnt(4)\n"
    "v_cvt_pk_bf16_f32 v179, v182, v183\n"
    "v_mfma_f32_16x16x32_bf16 v[0:3], a[224:227], v[192:195], v[0:3]\n"
    "ds_read_b128 v[216:219], %[rdx1] offset:61440\n"
    "v_mfma_f32_16x16x32_bf16 v[4:7], a[228:231], v[192:195], v[4:7]\n"
    "v_mfma_f32_16x16x32_bf16 v[8:11], a[232:235], v[192:195], v[8:11]\n"
    "s_add_u32 s46, %[soo], 45056\n"
    "buffer_store_dwordx4 v[176:179], %[vo], %[srdo], s46 offen sc1\n"
    "v_mfma_f32_16x16x32_bf16 v[12:15], a[236:239], v[192:195], v[12:15]\n"
    "v_mfma_f32_16x16x32_bf16 v[28:31], a[236:239], v[196:199], v[28:31]\n"
    "ds_read_b128 v[220:223], %[rdx1] offset:63488\n"
    "v_mfma_f32_16x16x32_bf16 v[24:27], a[232:235], v[196:199], v[24:27]\n"
    "v_mfma_f32_16x16x32_bf16 v[20:23], a[228:231], v[196:199], v[20:23]\n"
    "v_mfma_f32_16x16x32_bf16 v[16:19], a[224:227], v[196:199], v[16:19]\n"
    "s_waitcnt lgkmcnt(4)\n"
    "v_mfma_f32_16x16x32_bf16 v[32:35], a[224:227], v[200:203], v[32:35]\n"
    "v_mfma_f32_16x16x32_bf16 v[36:39], a[228:231], v[200:203], v[36:39]\n"
    "v_mfma_f32_16x16x32_bf16 v[40:43], a[232:235], v[200:203], v[40:43]\n"
    "v_mfma_f32_16x16x32_bf16 v[44:47], a[236:239], v[200:203], v[44:47]\n"
    "v_mfma_f32_16x16x32_bf16 v[60:63], a[236:239], v[204:207], v[60:63]\n"
    "v_mfma_f32_16x16x32_bf16 v[56:59], a[232:235], v[204:207], v[56:59]\n"
    "v_mfma_f32_16x16x32_bf16 v[52:55], a[228:231], v[204:207], v[52:55]\n"
    "v_mfma_f32_16x16x32_bf16 v[48:51], a[224:227], v[204:207], v[48:51]\n"
    "s_waitcnt lgkmcnt(3)\n"
    "v_cvt_pk_bf16_f32 v184, v184, v185\n"
    "v_mfma_f32_16x16x32_bf16 v[0:3], a[240:243], v[208:211], v[0:3]\n"
    "v_cvt_pk_bf16_f32 v185, v186, v187\n"
    "v_mfma_f32_16x16x32_bf16 v[4:7], a[244:247], v[208:211], v[4:7]\n"
    "v_mfma_f32_16x16x32_bf16 v[8:11], a[248:251], v[208:211], v[8:11]\n"
    "v_cvt_pk_bf16_f32 v186, v188, v189\n"
    "v_mfma_f32_16x16x32_bf16 v[12:15], a[252:255], v[208:211], v[12:15]\n"
    "s_waitcnt lgkmcnt(2)\n"
    "v_mfma_f32_16x16x32_bf16 v[28:31], a[252:255], v[212:215], v[28:31]\n"
    "v_cvt_pk_bf16_f32 v187, v190, v191\n"
    "v_mfma_f32_16x16x32_bf16 v[24:27], a[248:251], v[212:215], v[24:27]\n"
    "v_mfma_f32_16x16x32_bf16 v[20:23], a[244:247], v[212:215], v[20:23]\n"
    "v_mfma_f32_16x16x32_bf16 v[16:19], a[240:243], v[212:215], v[16:19]\n"
    "s_add_u32 s46, %[soo], 46080\n"
    "buffer_store_dwordx4 v[184:187], %[vo], %[srdo], s46 offen sc1\n"
    "s_waitcnt lgkmcnt(1)\n"
    "v_mfma_f32_16x16x32_bf16 v[32:35], a[240:243], v[216:219], v[32:35]\n"
    "v_mfma_f32_16x16x32_bf16 v[36:39], a[244:247], v[216:219], v[36:39]\n"
    "v_mfma_f32_16x16x32_bf16 v[40:43], a[248:251], v[216:219], v[40:43]\n"
    "v_mfma_f32_16x16x32_bf16 v[44:47], a[252:255], v[216:219], v[44:47]\n"
    "s_waitcnt lgkmcnt(0)\n"
    "v_mfma_f32_16x16x32_bf16 v[60:63], a[252:255], v[220:223], v[60:63]\n"
    "v_mfma_f32_16x16x32_bf16 v[56:59], a[248:251], v[220:223], v[56:59]\n"
    "v_mfma_f32_16x16x32_bf16 v[52:55], a[244:247], v[220:223], v[52:55]\n"
    "v_mfma_f32_16x16x32_bf16 v[48:51], a[240:243], v[220:223], v[48:51]\n"
    "v_cvt_pk_bf16_f32 v0, v0, v1\n"
    "v_cvt_pk_bf16_f32 v1, v2, v3\n"
    "v_cvt_pk_bf16_f32 v2, v4, v5\n"
    "v_cvt_pk_bf16_f32 v3, v6, v7\n"
    "s_add_u32 s46, %[soo], 49152\n"
    "buffer_store_dwordx4 v[0:3], %[vo], %[srdo], s46 offen sc1\n"
    "v_cvt_pk_bf16_f32 v8, v8, v9\n"
    "v_cvt_pk_bf16_f32 v9, v10, v11\n"
    "v_cvt_pk_bf16_f32 v10, v12, v13\n"
    "v_cvt_pk_bf16_f32 v11, v14, v15\n"
    "s_add_u32 s46, %[soo], 50176\n"
    "buffer_store_dwordx4 v[8:11], %[vo], %[srdo], s46 offen sc1\n"
    "v_cvt_pk_bf16_f32 v16, v16, v17\n"
    "v_cvt_pk_bf16_f32 v17, v18, v19\n"
    "v_cvt_pk_bf16_f32 v18, v20, v21\n"
    "v_cvt_pk_bf16_f32 v19, v22, v23\n"
    "s_add_u32 s46, %[soo], 53248\n"
    "buffer_store_dwordx4 v[16:19], %[vo], %[srdo], s46 offen sc1\n"
    "v_cvt_pk_bf16_f32 v24, v24, v25\n"
    "v_cvt_pk_bf16_f32 v25, v26, v27\n"
    "v_cvt_pk_bf16_f32 v26, v28, v29\n"
    "v_cvt_pk_bf16_f32 v27, v30, v31\n"
    "s_add_u32 s46, %[soo], 54272\n"
    "buffer_store_dwordx4 v[24:27], %[vo], %[srdo], s46 offen sc1\n"
    "v_cvt_pk_bf16_f32 v32, v32, v33\n"
    "v_cvt_pk_bf16_f32 v33, v34, v35\n"
    "v_cvt_pk_bf16_f32 v34, v36, v37\n"
    "v_cvt_pk_bf16_f32 v35, v38, v39\n"
    "s_add_u32 s46, %[soo], 57344\n"
    "buffer_store_dwordx4 v[32:35], %[vo], %[srdo], s46 offen sc1\n"
    "v_cvt_pk_bf16_f32 v40, v40, v41\n"
    "v_cvt_pk_bf16_f32 v41, v42, v43\n"
    "v_cvt_pk_bf16_f32 v42, v44, v45\n"
    "v_cvt_pk_bf16_f32 v43, v46, v47\n"
    "s_add_u32 s46, %[soo], 58368\n"
    "buffer_store_dwordx4 v[40:43], %[vo], %[srdo], s46 offen sc1\n"
    "v_cvt_pk_bf16_f32 v48, v48, v49\n"
    "v_cvt_pk_bf16_f32 v49, v50, v51\n"
    "v_cvt_pk_bf16_f32 v50, v52, v53\n"
    "v_cvt_pk_bf16_f32 v51, v54, v55\n"
    "s_add_u32 s46, %[soo], 61440\n"
    "buffer_store_dwordx4 v[48:51], %[vo], %[srdo], s46 offen sc1\n"
    "v_cvt_pk_bf16_f32 v56, v56, v57\n"
    "v_cvt_pk_bf16_f32 v57, v58, v59\n"
    "v_cvt_pk_bf16_f32 v58, v60, v61\n"
    "v_cvt_pk_bf16_f32 v59, v62, v63\n"
    "s_add_u32 s46, %[soo], 62464\n"
    "buffer_store_dwordx4 v[56:59], %[vo], %[srdo], s46 offen sc1\n"
    "s_branch KE90001\n"
    ".p2align 6\n"
    "KV_%=:\n"
    "ds_read_b64_tr_b16 a[0:1], %[rdw0] offset:0\n"
    "ds_read_b64_tr_b16 a[2:3], %[rdw0] offset:512\n"
    "ds_read_b64_tr_b16 a[4:5], %[rdw1] offset:0\n"
    "ds_read_b64_tr_b16 a[6:7], %[rdw1] offset:512\n"
    "ds_read_b64_tr_b16 a[8:9], %[rdw2] offset:0\n"
    "ds_read_b64_tr_b16 a[10:11], %[rdw2] offset:512\n"
    "ds_read_b64_tr_b16 a[12:13], %[rdw3] offset:0\n"
    "ds_read_b64_tr_b16 a[14:15], %[rdw3] offset:512\n"
    "ds_read_b128 v[192:195], %[rdx0] offset:0\n"
    "ds_read_b128 v[196:199], %[rdx0] offset:2048\n"
    "ds_read_b128 v[200:203], %[rdx0] offset:4096\n"
    "ds_read_b128 v[204:207], %[rdx0] offset:6144\n"
    "ds_read_b128 v[208:211], %[rdx0] offset:8192\n"
    "ds_read_b128 v[212:215], %[rdx0] offset:10240\n"
    "s_waitcnt lgkmcnt(4)\n"
    "v_mfma_f32_16x16x32_bf16 v[0:3], v[192:195], a[0:3], 0\n"
    "ds_read_b128 v[216:219], %[rdx0] offset:12288\n"
    "v_mfma_f32_16x16x32_bf16 v[4:7], v[192:195], a[4:7], 0\n"
    "ds_read_b64_tr_b16 a[16:17], %[rdw0] offset:4096\n"
    "v_mfma_f32_16x16x32_bf16 v[8:11], v[192:195], a[8:11], 0\n"
    "ds_read_b64_tr_b16 a[18:19], %[rdw0] offset:4608\n"
    "v_mfma_f32_16x16x32_bf16 v[12:15], v[192:195], a[12:15], 0\n"
    "ds_read_b64_tr_b16 a[20:21], %[rdw1] offset:4096\n"
    "v_mfma_f32_16x16x32_bf16 v[28:31], v[196:199], a[12:15], 0\n"
    "ds_read_b128 v[220:223], %[rdx0] offset:14336\n"
    "v_mfma_f32_16x16x32_bf16 v[24:27], v[196:199], a[8:11], 0\n"
    "ds_read_b64_tr_b16 a[22:23], %[rdw1] offset:4608\n"
    "v_mfma_f32_16x16x32_bf16 v[20:23], v[196:199], a[4:7], 0\n"
    "ds_read_b64_tr_b16 a[24:25], %[rdw2] offset:4096\n"
    "v_mfma_f32_16x16x32_bf16 v[16:19], v[196:199], a[0:3], 0\n"
    "s_add_u32 s42, s42, 1\n"
    "s_and_b32 s42, s42, 7\n"
    "ds_read_b64_tr_b16 a[26:27], %[rdw2] offset:4608\n"
    "s_waitcnt lgkmcnt(10)\n"
    "v_mfma_f32_16x16x32_bf16 v[32:35], v[200:203], a[0:3], 0\n"
    "s_lshl_b32 s43, s42, 7\n"
    "s_add_u32 s40, s43, %[sox]\n"
    "ds_read_b128 v[192:195], %[rdx1] offset:0\n"
    "v_mfma_f32_16x16x32_bf16 v[36:39], v[200:203], a[4:7], 0\n"
    "s_lshl_b32 s43, s42, 19\n"
    "s_add_u32 s41, s43, %[sow]\n"
    "ds_read_b64_tr_b16 a[28:29], %[rdw3] offset:4096\n"
    "v_mfma_f32_16x16x32_bf16 v[40:43], v[200:203], a[8:11], 0\n"
    "s_add_u32 s47, s41, 262144\n"
    "s_add_u32 m0, %[m0x], 98304\n"
    "ds_read_b64_tr_b16 a[30:31], %[rdw3] offset:4608\n"
    "v_mfma_f32_16x16x32_bf16 v[44:47], v[200:203], a[12:15], 0\n"
    "buffer_load_dwordx4 %[vx0], %[srdx], s40 offen offset:0 lds\n"
    "v_mfma_f32_16x16x32_bf16 v[60:63], v[204:207], a[12:15], 0\n"
    "ds_read_b128 v[196:199], %[rdx1] offset:2048\n"
    "v_mfma_f32_16x16x32_bf16 v[56:59], v[204:207], a[8:11], 0\n"
    "buffer_load_dwordx4 %[vx1], %[srdx], s40 offen offset:1024 lds\n"
    "v_mfma_f32_16x16x32_bf16 v[52:55], v[204:207], a[4:7], 0\n"
    "buffer_load_dwordx4 %[vx2], %[srdx], s40 offen offset:2048 lds\n"
    "v_mfma_f32_16x16x32_bf16 v[48:51], v[204:207], a[0:3], 0\n"
    "buffer_load_dwordx4 %[vx3], %[srdx], s40 offen offset:3072 lds\n"
    "s_waitcnt lgkmcnt(12)\n"
    "v_mfma_f32_16x16x32_bf16 v[64:67], v[208:211], a[0:3], 0\n"
    "s_add_u32 m0, %[m0w], 98304\n"
    "ds_read_b128 v[200:203], %[rdx1] offset:4096\n"
    "v_mfma_f32_16x16x32_bf16 v[68:71], v[208:211], a[4:7], 0\n"
    "buffer_load_dwordx4 %[vw0], %[srdw], s41 offen offset:0 nt lds\n"
    "v_mfma_f32_16x16x32_bf16 v[72:75], v[208:211], a[8:11], 0\n"
    "buffer_load_dwordx4 %[vw1], %[srdw], s41 offen offset:1024 nt lds\n"
    "v_mfma_f32_16x16x32_bf16 v[76:79], v[208:211], a[12:15], 0\n"
    "buffer_load_dwordx4 %[vw2], %[srdw], s41 offen offset:2048 nt lds\n"
    "v_mfma_f32_16x16x32_bf16 v[92:95], v[212:215], a[12:15], 0\n"
    "ds_read_b128 v[204:207], %[rdx1] offset:6144\n"
    "v_mfma_f32_16x16x32_bf16 v[88:91], v[212:215], a[8:11], 0\n"
    "buffer_load_dwordx4 %[vw3], %[srdw], s41 offen offset:3072 nt lds\n"
    "v_mfma_f32_16x16x32_bf16 v[84:87], v[212:215], a[4:7], 0\n"
    "s_add_u32 m0, %[m0w], 102400\n"
    "v_mfma_f32_16x16x32_bf16 v[80:83], v[212:215], a[0:3], 0\n"
    "buffer_load_dwordx4 %[vw0], %[srdw], s47 offen offset:0 nt lds\n"
    "s_waitcnt lgkmcnt(12)\n"
    "v_mfma_f32_16x16x32_bf16 v[96:99], v[216:219], a[0:3], 0\n"
    "ds_read_b128 v[208:211], %[rdx1] offset:8192\n"
    "v_mfma_f32_16x16x32_bf16 v[100:103], v[216:219], a[4:7], 0\n"
    "v_mfma_f32_16x16x32_bf16 v[104:107], v[216:219], a[8:11], 0\n"
    "buffer_load_dwordx4 %[vw1], %[srdw], s47 offen offset:1024 nt lds\n"
    "v_mfma_f32_16x16x32_bf16 v[108:111], v[216:219], a[12:15], 0\n"
    "s_waitcnt lgkmcnt(9)\n"
    "v_mfma_f32_16x16x32_bf16 v[124:127], v[220:223], a[12:15], 0\n"
    "ds_read_b128 v[212:215], %[rdx1] offset:10240\n"
    "v_mfma_f32_16x16x32_bf16 v[120:123], v[220:223], a[8:11], 0\n"
    "buffer_load_dwordx4 %[vw2], %[srdw], s47 offen offset:2048 nt lds\n"
    "v_mfma_f32_16x16x32_bf16 v[116:119], v[220:223], a[4:7], 0\n"
    "v_mfma_f32_16x16x32_bf16 v[112:115], v[220:223], a[0:3], 0\n"
    "s_waitcnt lgkmcnt(6)\n"
    "v_mfma_f32_16x16x32_bf16 v[0:3], v[192:195], a[16:19], v[0:3]\n"
    "ds_read_b128 v[216:219], %[rdx1] offset:12288\n"
    "v_mfma_f32_16x16x32_bf16 v[4:7], v[192:195], a[20:23], v[4:7]\n"
    "buffer_load_dwordx4 %[vw3], %[srdw], s47 offen offset:3072 nt lds\n"
    "v_mfma_f32_16x16x32_bf16 v[8:11], v[192:195], a[24:27], v[8:11]\n"
    "s_waitcnt lgkmcnt(5)\n"
    "v_mfma_f32_16x16x32_bf16 v[12:15], v[192:195], a[28:31], v[12:15]\n"
    "v_mfma_f32_16x16x32_bf16 v[28:31], v[196:199], a[28:31], v[28:31]\n"
    "ds_read_b128 v[220:223], %[rdx1] offset:14336\n"
    "v_mfma_f32_16x16x32_bf16 v[24:27], v[196:199], a[24:27], v[24:27]\n"
    "v_mfma_f32_16x16x32_bf16 v[20:23], v[196:199], a[20:23], v[20:23]\n"
    "v_mfma_f32_16x16x32_bf16 v[16:19], v[196:199], a[16:19], v[16:19]\n"
    "s_waitcnt lgkmcnt(4)\n"
    "v_mfma_f32_16x16x32_bf16 v[32:35], v[200:203], a[16:19], v[32:35]\n"
    "v_mfma_f32_16x16x32_bf16 v[36:39], v[200:203], a[20:23], v[36:39]\n"
    "v_mfma_f32_16x16x32_bf16 v[40:43], v[200:203], a[24:27], v[40:43]\n"
    "v_mfma_f32_16x16x32_bf16 v[44:47], v[200:203], a[28:31], v[44:47]\n"
    "v_mfma_f32_16x16x32_bf16 v[60:63], v[204:207], a[28:31], v[60:63]\n"
    "v_mfma_f32_16x16x32_bf16 v[56:59], v[204:207], a[24:27], v[56:59]\n"
    "v_mfma_f32_16x16x32_bf16 v[52:55], v[204:207], a[20:23], v[52:55]\n"
    "v_mfma_f32_16x16x32_bf16 v[48:51], v[204:207], a[16:19], v[48:51]\n"
    "s_waitcnt vmcnt(16) lgkmcnt(0)\n"
    "s_barrier\n"
    "v_mfma_f32_16x16x32_bf16 v[64:67], v[208:211], a[16:19], v[64:67]\n"
    "ds_read_b64_tr_b16 a[32:33], %[rdw0] offset:49152\n"
    "v_mfma_f32_16x16x32_bf16 v[68:71], v[208:211], a[20:23], v[68:71]\n"
    "ds_read_b64_tr_b16 a[34:35], %[rdw0] offset:49664\n"
    "v_mfma_f32_16x16x32_bf16 v[72:75], v[208:211], a[24:27], v[72:75]\n"
    "ds_read_b128 v[192:195], %[rdx0] offset:49152\n"
    "v_mfma_f32_16x16x32_bf16 v[76:79], v[208:211], a[28:31], v[76:79]\n"
    "ds_read_b64_tr_b16 a[36:37], %[rdw1] offset:49152\n"
    "v_mfma_f32_16x16x32_bf16 v[92:95], v[212:215], a[28:31], v[92:95]\n"
    "ds_read_b64_tr_b16 a[38:39], %[rdw1] offset:49664\n"
    "v_mfma_f32_16x16x32_bf16 v[88:91], v[212:215], a[24:27], v[88:91]\n"
    "ds_read_b128 v[196:199], %[rdx0] offset:51200\n"
    "v_mfma_f32_16x16x32_bf16 v[84:87], v[212:215], a[20:23], v[84:87]\n"
    "ds_read_b64_tr_b16 a[40:41], %[rdw2] offset:49152\n"
    "v_mfma_f32_16x16x32_bf16 v[80:83], v[212:215], a[16:19], v[80:83]\n"
    "ds_read_b64_tr_b16 a[42:43], %[rdw2] offset:49664\n"
    "v_mfma_f32_16x16x32_bf16 v[96:99], v[216:219], a[16:19], v[96:99]\n"
    "ds_read_b128 v[200:203], %[rdx0] offset:53248\n"
    "v_mfma_f32_16x16x32_bf16 v[100:103], v[216:219], a[20:23], v[100:103]\n"
    "ds_read_b64_tr_b16 a[44:45], %[rdw3] offset:49152\n"
    "v_mfma_f32_16x16x32_bf16 v[104:107], v[216:219], a[24:27], v[104:107]\n"
    "s_add_u32 s42, s42, 1\n"
    "s_and_b32 s42, s42, 7\n"
    "ds_read_b64_tr_b16 a[46:47], %[rdw3] offset:49664\n"
    "v_mfma_f32_16x16x32_bf16 v[108:111], v[216:219], a[28:31], v[108:111]\n"
    "s_lshl_b32 s43, s42, 7\n"
    "s_add_u32 s40, s43, %[sox]\n"
    "ds_read_b128 v[204:207], %[rdx0] offset:55296\n"
    "v_mfma_f32_16x16x32_bf16 v[124:127], v[220:223], a[28:31], v[124:127]\n"
    "s_lshl_b32 s43, s42, 19\n"
    "s_add_u32 s41, s43, %[sow]\n"
    "ds_read_b128 v[208:211], %[rdx0] offset:57344\n"
    "v_mfma_f32_16x16x32_bf16 v[120:123], v[220:223], a[24:27], v[120:123]\n"
    "s_add_u32 s47, s41, 262144\n"
    "s_add_u32 m0, %[m0x], 0\n"
    "ds_read_b128 v[212:215], %[rdx0] offset:59392\n"
    "v_mfma_f32_16x16x32_bf16 v[116:119], v[220:223], a[20:23], v[116:119]\n"
    "buffer_load_dwordx4 %[vx0], %[srdx], s40 offen offset:0 lds\n"
    "v_mfma_f32_16x16x32_bf16 v[112:115], v[220:223], a[16:19], v[112:115]\n"
    "buffer_load_dwordx4 %[vx1], %[srdx], s40 offen offset:1024 lds\n"
    "s_waitcnt lgkmcnt(10)\n"
    "v_mfma_f32_16x16x32_bf16 v[0:3], v[192:195], a[32:35], v[0:3]\n"
    "ds_read_b128 v[216:219], %[rdx0] offset:61440\n"
    "s_waitcnt lgkmcnt(9)\n"
    "v_mfma_f32_16x16x32_bf16 v[4:7], v[192:195], a[36:39], v[4:7]\n"
    "ds_read_b64_tr_b16 a[48:49], %[rdw0] offset:53248\n"
    "s_waitcnt lgkmcnt(7)\n"
    "v_mfma_f32_16x16x32_bf16 v[8:11], v[192:195], a[40:43], v[8:11]\n"
    "ds_read_b64_tr_b16 a[50:51], %[rdw0] offset:53760\n"
    "s_waitcnt lgkmcnt(5)\n"
    "v_mfma_f32_16x16x32_bf16 v[12:15], v[192:195], a[44:47], v[12:15]\n"
    "ds_read_b64_tr_b16 a[52:53], %[rdw1] offset:53248\n"
    "v_mfma_f32_16x16x32_bf16 v[28:31], v[196:199], a[44:47], v[28:31]\n"
    "ds_read_b128 v[220:223], %[rdx0] offset:63488\n"
    "v_mfma_f32_16x16x32_bf16 v[24:27], v[196:199], a[40:43], v[24:27]\n"
    "ds_read_b64_tr_b16 a[54:55], %[rdw1] offset:53760\n"
    "v_mfma_f32_16x16x32_bf16 v[20:23], v[196:199], a[36:39], v[20:23]\n"
    "ds_read_b64_tr_b16 a[56:57], %[rdw2] offset:53248\n"
    "v_mfma_f32_16x16x32_bf16 v[16:19], v[196:199], a[32:35], v[16:19]\n"
    "ds_read_b64_tr_b16 a[58:59], %[rdw2] offset:53760\n"
    "v_mfma_f32_16x16x32_bf16 v[32:35], v[200:203], a[32:35], v[32:35]\n"
    "ds_read_b128 v[192:195], %[rdx1] offset:49152\n"
    "v_mfma_f32_16x16x32_bf16 v[36:39], v[200:203], a[36:39], v[36:39]\n"
    "ds_read_b64_tr_b16 a[60:61], %[rdw3] offset:53248\n"
    "v_mfma_f32_16x16x32_bf16 v[40:43], v[200:203], a[40:43], v[40:43]\n"
    "ds_read_b64_tr_b16 a[62:63], %[rdw3] offset:53760\n"
    "v_mfma_f32_16x16x32_bf16 v[44:47], v[200:203], a[44:47], v[44:47]\n"
    "buffer_load_dwordx4 %[vx2], %[srdx], s40 offen offset:2048 lds\n"
    "v_mfma_f32_16x16x32_bf16 v[60:63], v[204:207], a[44:47], v[60:63]\n"
    "ds_read_b128 v[196:199], %[rdx1] offset:51200\n"
    "v_mfma_f32_16x16x32_bf16 v[56:59], v[204:207], a[40:43], v[56:59]\n"
    "buffer_load_dwordx4 %[vx3], %[srdx], s40 offen offset:3072 lds\n"
    "v_mfma_f32_16x16x32_bf16 v[52:55], v[204:207], a[36:39], v[52:55]\n"
    "s_add_u32 m0, %[m0w], 0\n"
    "v_mfma_f32_16x16x32_bf16 v[48:51], v[204:207], a[32:35], v[48:51]\n"
    "buffer_load_dwordx4 %[vw0], %[srdw], s41 offen offset:0 nt lds\n"
    "s_waitcnt lgkmcnt(12)\n"
    "v_mfma_f32_16x16x32_bf16 v[64:67], v[208:211], a[32:35], v[64:67]\n"
    "ds_read_b128 v[200:203], %[rdx1] offset:53248\n"
    "v_mfma_f32_16x16x32_bf16 v[68:71], v[208:211], a[36:39], v[68:71]\n"
    "buffer_load_dwordx4 %[vw1], %[srdw], s41 offen offset:1024 nt lds\n"
    "v_mfma_f32_16x16x32_bf16 v[72:75], v[208:211], a[40:43], v[72:75]\n"
    "buffer_load_dwordx4 %[vw2], %[srdw], s41 offen offset:2048 nt lds\n"
    "v_mfma_f32_16x16x32_bf16 v[76:79], v[208:211], a[44:47], v[76:79]\n"
    "buffer_load_dwordx4 %[vw3], %[srdw], s41 offen offset:3072 nt lds\n"
    "v_mfma_f32_16x16x32_bf16 v[92:95], v[212:215], a[44:47], v[92:95]\n"
    "s_add_u32 m0, %[m0w], 4096\n"
    "ds_read_b128 v[204:207], %[rdx1] offset:55296\n"
    "v_mfma_f32_16x16x32_bf16 v[88:91], v[212:215], a[40:43], v[88:91]\n"
    "buffer_load_dwordx4 %[vw0], %[srdw], s47 offen offset:0 nt lds\n"
    "v_mfma_f32_16x16x32_bf16 v[84:87], v[212:215], a[36:39], v[84:87]\n"
    "buffer_load_dwordx4 %[vw1], %[srdw], s47 offen offset:1024 nt lds\n"
    "v_mfma_f32_16x16x32_bf16 v[80:83], v[212:215], a[32:35], v[80:83]\n"
    "s_waitcnt lgkmcnt(12)\n"
    "v_mfma_f32_16x16x32_bf16 v[96:99], v[216:219], a[32:35], v[96:99]\n"
    "ds_read_b128 v[208:211], %[rdx1] offset:57344\n"
    "v_mfma_f32_16x16x32_bf16 v[100:103], v[216:219], a[36:39], v[100:103]\n"
    "v_mfma_f32_16x16x32_bf16 v[104:107], v[216:219], a[40:43], v[104:107]\n"
    "buffer_load_dwordx4 %[vw2], %[srdw], s47 offen offset:2048 nt lds\n"
    "v_mfma_f32_16x16x32_bf16 v[108:111], v[216:219], a[44:47], v[108:111]\n"
    "s_waitcnt lgkmcnt(9)\n"
    "v_mfma_f32_16x16x32_bf16 v[124:127], v[220:223], a[44:47], v[124:127]\n"
    "ds_read_b128 v[212:215], %[rdx1] offset:59392\n"
    "v_mfma_f32_16x16x32_bf16 v[120:123], v[220:223], a[40:43], v[120:123]\n"
    "v_mfma_f32_16x16x32_bf16 v[116:119], v[220:223], a[36:39], v[116:119]\n"
    "buffer_load_dwordx4 %[vw3], %[srdw], s47 offen offset:3072 nt lds\n"
    "v_mfma_f32_16x16x32_bf16 v[112:115], v[220:223], a[32:35], v[112:115]\n"
    "s_waitcnt lgkmcnt(6)\n"
    "v_mfma_f32_16x16x32_bf16 v[0:3], v[192:195], a[48:51], v[0:3]\n"
    "ds_read_b128 v[216:219], %[rdx1] offset:61440\n"
    "v_mfma_f32_16x16x32_bf16 v[4:7], v[192:195], a[52:55], v[4:7]\n"
    "v_mfma_f32_16x16x32_bf16 v[8:11], v[192:195], a[56:59], v[8:11]\n"
    "s_waitcnt lgkmcnt(5)\n"
    "v_mfma_f32_16x16x32_bf16 v[12:15], v[192:195], a[60:63], v[12:15]\n"
    "v_mfma_f32_16x16x32_bf16 v[28:31], v[196:199], a[60:63], v[28:31]\n"
    "ds_read_b128 v[220:223], %[rdx1] offset:63488\n"
    "v_mfma_f32_16x16x32_bf16 v[24:27], v[196:199], a[56:59], v[24:27]\n"
    "v_mfma_f32_16x16x32_bf16 v[20:23], v[196:199], a[52:55], v[20:23]\n"
    "v_mfma_f32_16x16x32_bf16 v[16:19], v[196:199], a[48:51], v[16:19]\n"
    "s_waitcnt lgkmcnt(4)\n"
    "v_mfma_f32_16x16x32_bf16 v[32:35], v[200:203], a[48:51], v[32:35]\n"
    "v_mfma_f32_16x16x32_bf16 v[36:39], v[200:203], a[52:55], v[36:39]\n"
    "v_mfma_f32_16x16x32_bf16 v[40:43], v[200:203], a[56:59], v[40:43]\n"
    "v_mfma_f32_16x16x32_bf16 v[44:47], v[200:203], a[60:63], v[44:47]\n"
    "v_mfma_f32_16x16x32_bf16 v[60:63], v[204:207], a[60:63], v[60:63]\n"
    "v_mfma_f32_16x16x32_bf16 v[56:59], v[204:207], a[56:59], v[56:59]\n"
    "v_mfma_f32_16x16x32_bf16 v[52:55], v[204:207], a[52:55], v[52:55]\n"
    "v_mfma_f32_16x16x32_bf16 v[48:51], v[204:207], a[48:51], v[48:51]\n"
    "s_waitcnt vmcnt(12) lgkmcnt(0)\n"
    "s_barrier\n"
    "v_mfma_f32_16x16x32_bf16 v[64:67], v[208:211], a[48:51], v[64:67]\n"
    "ds_read_b64_tr_b16 a[64:65], %[rdw4] offset:0\n"
    "v_mfma_f32_16x16x32_bf16 v[68:71], v[208:211], a[52:55], v[68:71]\n"
    "ds_read_b64_tr_b16 a[66:67], %[rdw4] offset:512\n"
    "v_mfma_f32_16x16x32_bf16 v[72:75], v[208:211], a[56:59], v[72:75]\n"
    "ds_read_b128 v[192:195], %[rdx2] offset:0\n"
    "v_mfma_f32_16x16x32_bf16 v[76:79], v[208:211], a[60:63], v[76:79]\n"
    "ds_read_b64_tr_b16 a[68:69], %[rdw5] offset:0\n"
    "v_mfma_f32_16x16x32_bf16 v[92:95], v[212:215], a[60:63], v[92:95]\n"
    "ds_read_b64_tr_b16 a[70:71], %[rdw5] offset:512\n"
    "v_mfma_f32_16x16x32_bf16 v[88:91], v[212:215], a[56:59], v[88:91]\n"
    "ds_read_b128 v[196:199], %[rdx2] offset:2048\n"
    "v_mfma_f32_16x16x32_bf16 v[84:87], v[212:215], a[52:55], v[84:87]\n"
    "ds_read_b64_tr_b16 a[72:73], %[rdw6] offset:0\n"
    "v_mfma_f32_16x16x32_bf16 v[80:83], v[212:215], a[48:51], v[80:83]\n"
    "ds_read_b64_tr_b16 a[74:75], %[rdw6] offset:512\n"
    "v_mfma_f32_16x16x32_bf16 v[96:99], v[216:219], a[48:51], v[96:99]\n"
    "ds_read_b128 v[200:203], %[rdx2] offset:4096\n"
    "v_mfma_f32_16x16x32_bf16 v[100:103], v[216:219], a[52:55], v[100:103]\n"
    "ds_read_b64_tr_b16 a[76:77], %[rdw7] offset:0\n"
    "v_mfma_f32_16x16x32_bf16 v[104:107], v[216:219], a[56:59], v[104:107]\n"
    "s_add_u32 s42, s42, 1\n"
    "s_and_b32 s42, s42, 7\n"
    "ds_read_b64_tr_b16 a[78:79], %[rdw7] offset:512\n"
    "v_mfma_f32_16x16x32_bf16 v[108:111], v[216:219], a[60:63], v[108:111]\n"
    "s_lshl_b32 s43, s42, 7\n"
    "s_add_u32 s40, s43, %[sox]\n"
    "ds_read_b128 v[204:207], %[rdx2] offset:6144\n"
    "v_mfma_f32_16x16x32_bf16 v[124:127], v[220:223], a[60:63], v[124:127]\n"
    "s_lshl_b32 s43, s42, 19\n"
    "s_add_u32 s41, s43, %[sow]\n"
    "ds_read_b128 v[208:211], %[rdx2] offset:8192\n"
    "v_mfma_f32_16x16x32_bf16 v[120:123], v[220:223], a[56:59], v[120:123]\n"
    "s_add_u32 s47, s41, 262144\n"
    "s_add_u32 m0, %[m0x], 49152\n"
    "ds_read_b128 v[212:215], %[rdx2] offset:10240\n"
    "v_mfma_f32_16x16x32_bf16 v[116:119], v[220:223], a[52:55], v[116:119]\n"
    "buffer_load_dwordx4 %[vx0], %[srdx], s40 offen offset:0 lds\n"
    "v_mfma_f32_16x16x32_bf16 v[112:115], v[220:223], a[48:51], v[112:115]\n"
    "buffer_load_dwordx4 %[vx1], %[srdx], s40 offen offset:1024 lds\n"
    "s_waitcnt lgkmcnt(10)\n"
    "v_mfma_f32_16x16x32_bf16 v[0:3], v[192:195], a[64:67], v[0:3]\n"
    "ds_read_b128 v[216:219], %[rdx2] offset:12288\n"
    "s_waitcnt lgkmcnt(9)\n"
    "v_mfma_f32_16x16x32_bf16 v[4:7], v[192:195], a[68:71], v[4:7]\n"
    "ds_read_b64_tr_b16 a[80:81], %[rdw4] offset:4096\n"
    "s_waitcnt lgkmcnt(7)\n"
    "v_mfma_f32_16x16x32_bf16 v[8:11], v[192:195], a[72:75], v[8:11]\n"
    "ds_read_b64_tr_b16 a[82:83], %[rdw4] offset:4608\n"
    "s_waitcnt lgkmcnt(5)\n"
    "v_mfma_f32_16x16x32_bf16 v[12:15], v[192:195], a[76:79], v[12:15]\n"
    "ds_read_b64_tr_b16 a[84:85], %[rdw5] offset:4096\n"
    "v_mfma_f32_16x16x32_bf16 v[28:31], v[196:199], a[76:79], v[28:31]\n"
    "ds_read_b128 v[220:223], %[rdx2] offset:14336\n"
    "v_mfma_f32_16x16x32_bf16 v[24:27], v[196:199], a[72:75], v[24:27]\n"
    "ds_read_b64_tr_b16 a[86:87], %[rdw5] offset:4608\n"
    "v_mfma_f32_16x16x32_bf16 v[20:23], v[196:199], a[68:71], v[20:23]\n"
    "ds_read_b64_tr_b16 a[88:89], %[rdw6] offset:4096\n"
    "v_mfma_f32_16x16x32_bf16 v[16:19], v[196:199], a[64:67], v[16:19]\n"
    "ds_read_b64_tr_b16 a[90:91], %[rdw6] offset:4608\n"
    "v_mfma_f32_16x16x32_bf16 v[32:35], v[200:203], a[64:67], v[32:35]\n"
    "ds_read_b128 v[192:195], %[rdx3] offset:0\n"
    "v_mfma_f32_16x16x32_bf16 v[36:39], v[200:203], a[68:71], v[36:39]\n"
    "ds_read_b64_tr_b16 a[92:93], %[rdw7] offset:4096\n"
    "v_mfma_f32_16x16x32_bf16 v[40:43], v[200:203], a[72:75], v[40:43]\n"
    "ds_read_b64_tr_b16 a[94:95], %[rdw7] offset:4608\n"
    "v_mfma_f32_16x16x32_bf16 v[44:47], v[200:203], a[76:79], v[44:47]\n"
    "buffer_load_dwordx4 %[vx2], %[srdx], s40 offen offset:2048 lds\n"
    "v_mfma_f32_16x16x32_bf16 v[60:63], v[204:207], a[76:79], v[60:63]\n"
    "ds_read_b128 v[196:199], %[rdx3] offset:2048\n"
    "v_mfma_f32_16x16x32_bf16 v[56:59], v[204:207], a[72:75], v[56:59]\n"
    "buffer_load_dwordx4 %[vx3], %[srdx], s40 offen offset:3072 lds\n"
    "v_mfma_f32_16x16x32_bf16 v[52:55], v[204:207], a[68:71], v[52:55]\n"
    "s_add_u32 m0, %[m0w], 49152\n"
    "v_mfma_f32_16x16x32_bf16 v[48:51], v[204:207], a[64:67], v[48:51]\n"
    "buffer_load_dwordx4 %[vw0], %[srdw], s41 offen offset:0 nt lds\n"
    "s_waitcnt lgkmcnt(12)\n"
    "v_mfma_f32_16x16x32_bf16 v[64:67], v[208:211], a[64:67], v[64:67]\n"
    "ds_read_b128 v[200:203], %[rdx3] offset:4096\n"
    "v_mfma_f32_16x16x32_bf16 v[68:71], v[208:211], a[68:71], v[68:71]\n"
    "buffer_load_dwordx4 %[vw1], %[srdw], s41 offen offset:1024 nt lds\n"
    "v_mfma_f32_16x16x32_bf16 v[72:75], v[208:211], a[72:75], v[72:75]\n"
    "buffer_load_dwordx4 %[vw2], %[srdw], s41 offen offset:2048 nt lds\n"
    "v_mfma_f32_16x16x32_bf16 v[76:79], v[208:211], a[76:79], v[76:79]\n"
    "buffer_load_dwordx4 %[vw3], %[srdw], s41 offen offset:3072 nt lds\n"
    "v_mfma_f32_16x16x32_bf16 v[92:95], v[212:215], a[76:79], v[92:95]\n"
    "s_add_u32 m0, %[m0w], 53248\n"
    "ds_read_b128 v[204:207], %[rdx3] offset:6144\n"
    "v_mfma_f32_16x16x32_bf16 v[88:91], v[212:215], a[72:75], v[88:91]\n"
    "buffer_load_dwordx4 %[vw0], %[srdw], s47 offen offset:0 nt lds\n"
    "v_mfma_f32_16x16x32_bf16 v[84:87], v[212:215], a[68:71], v[84:87]\n"
    "buffer_load_dwordx4 %[vw1], %[srdw], s47 offen offset:1024 nt lds\n"
    "v_mfma_f32_16x16x32_bf16 v[80:83], v[212:215], a[64:67], v[80:83]\n"
    "s_waitcnt lgkmcnt(12)\n"
    "v_mfma_f32_16x16x32_bf16 v[96:99], v[216:219], a[64:67], v[96:99]\n"
    "ds_read_b128 v[208:211], %[rdx3] offset:8192\n"
    "v_mfma_f32_16x16x32_bf16 v[100:103], v[216:219], a[68:71], v[100:103]\n"
    "v_mfma_f32_16x16x32_bf16 v[104:107], v[216:219], a[72:75], v[104:107]\n"
    "buffer_load_dwordx4 %[vw2], %[srdw], s47 offen offset:2048 nt lds\n"
    "v_mfma_f32_16x16x32_bf16 v[108:111], v[216:219], a[76:79], v[108:111]\n"
    "s_waitcnt lgkmcnt(9)\n"
    "v_mfma_f32_16x16x32_bf16 v[124:127], v[220:223], a[76:79], v[124:127]\n"
    "ds_read_b128 v[212:215], %[rdx3] offset:10240\n"
    "v_mfma_f32_16x16x32_bf16 v[120:123], v[220:223], a[72:75], v[120:123]\n"
    "v_mfma_f32_16x16x32_bf16 v[116:119], v[220:223], a[68:71], v[116:119]\n"
    "buffer_load_dwordx4 %[vw3], %[srdw], s47 offen offset:3072 nt lds\n"
    "v_mfma_f32_16x16x32_bf16 v[112:115], v[220:223], a[64:67], v[112:115]\n"
    "s_waitcnt lgkmcnt(6)\n"
    "v_mfma_f32_16x16x32_bf16 v[0:3], v[192:195], a[80:83], v[0:3]\n"
    "ds_read_b128 v[216:219], %[rdx3] offset:12288\n"
    "v_mfma_f32_16x16x32_bf16 v[4:7], v[192:195], a[84:87], v[4:7]\n"
    "v_mfma_f32_16x16x32_bf16 v[8:11], v[192:195], a[88:91], v[8:11]\n"
    "s_waitcnt lgkmcnt(5)\n"
    "v_mfma_f32_16x16x32_bf16 v[12:15], v[192:195], a[92:95], v[12:15]\n"
    "v_mfma_f32_16x16x32_bf16 v[28:31], v[196:199], a[92:95], v[28:31]\n"
    "ds_read_b128 v[220:223], %[rdx3] offset:14336\n"
    "v_mfma_f32_16x16x32_bf16 v[24:27], v[196:199], a[88:91], v[24:27]\n"
    "v_mfma_f32_16x16x32_bf16 v[20:23], v[196:199], a[84:87], v[20:23]\n"
    "v_mfma_f32_16x16x32_bf16 v[16:19], v[196:199], a[80:83], v[16:19]\n"
    "s_waitcnt lgkmcnt(4)\n"
    "v_mfma_f32_16x16x32_bf16 v[32:35], v[200:203], a[80:83], v[32:35]\n"
    "v_mfma_f32_16x16x32_bf16 v[36:39], v[200:203], a[84:87], v[36:39]\n"
    "v_mfma_f32_16x16x32_bf16 v[40:43], v[200:203], a[88:91], v[40:43]\n"
    "v_mfma_f32_16x16x32_bf16 v[44:47], v[200:203], a[92:95], v[44:47]\n"
    "v_mfma_f32_16x16x32_bf16 v[60:63], v[204:207], a[92:95], v[60:63]\n"
    "v_mfma_f32_16x16x32_bf16 v[56:59], v[204:207], a[88:91], v[56:59]\n"
    "v_mfma_f32_16x16x32_bf16 v[52:55], v[204:207], a[84:87], v[52:55]\n"
    "v_mfma_f32_16x16x32_bf16 v[48:51], v[204:207], a[80:83], v[48:51]\n"
    "s_waitcnt vmcnt(12) lgkmcnt(0)\n"
    "s_barrier\n"
    "v_mfma_f32_16x16x32_bf16 v[64:67], v[208:211], a[80:83], v[64:67]\n"
    "ds_read_b64_tr_b16 a[96:97], %[rdw0] offset:0\n"
    "v_mfma_f32_16x16x32_bf16 v[68:71], v[208:211], a[84:87], v[68:71]\n"
    "ds_read_b64_tr_b16 a[98:99], %[rdw0] offset:512\n"
    "v_mfma_f32_16x16x32_bf16 v[72:75], v[208:211], a[88:91], v[72:75]\n"
    "ds_read_b128 v[192:195], %[rdx0] offset:0\n"
    "v_mfma_f32_16x16x32_bf16 v[76:79], v[208:211], a[92:95], v[76:79]\n"
    "ds_read_b64_tr_b16 a[100:101], %[rdw1] offset:0\n"
    "v_mfma_f32_16x16x32_bf16 v[92:95], v[212:215], a[92:95], v[92:95]\n"
    "ds_read_b64_tr_b16 a[102:103], %[rdw1] offset:512\n"
    "v_mfma_f32_16x16x32_bf16 v[88:91], v[212:215], a[88:91], v[88:91]\n"
    "ds_read_b128 v[196:199], %[rdx0] offset:2048\n"
    "v_mfma_f32_16x16x32_bf16 v[84:87], v[212:215], a[84:87], v[84:87]\n"
    "ds_read_b64_tr_b16 a[104:105], %[rdw2] offset:0\n"
    "v_mfma_f32_16x16x32_bf16 v[80:83], v[212:215], a[80:83], v[80:83]\n"
    "ds_read_b64_tr_b16 a[106:107], %[rdw2] offset:512\n"
    "v_mfma_f32_16x16x32_bf16 v[96:99], v[216:219], a[80:83], v[96:99]\n"
    "ds_read_b128 v[200:203], %[rdx0] offset:4096\n"
    "v_mfma_f32_16x16x32_bf16 v[100:103], v[216:219], a[84:87], v[100:103]\n"
    "ds_read_b64_tr_b16 a[108:109], %[rdw3] offset:0\n"
    "v_mfma_f32_16x16x32_bf16 v[104:107], v[216:219], a[88:91], v[104:107]\n"
    "s_add_u32 s42, s42, 1\n"
    "s_and_b32 s42, s42, 7\n"
    "ds_read_b64_tr_b16 a[110:111], %[rdw3] offset:512\n"
    "v_mfma_f32_16x16x32_bf16 v[108:111], v[216:219], a[92:95], v[108:111]\n"
    "s_lshl_b32 s43, s42, 7\n"
    "s_add_u32 s40, s43, %[sox]\n"
    "ds_read_b128 v[204:207], %[rdx0] offset:6144\n"
    "v_mfma_f32_16x16x32_bf16 v[124:127], v[220:223], a[92:95], v[124:127]\n"
    "s_lshl_b32 s43, s42, 19\n"
    "s_add_u32 s41, s43, %[sow]\n"
    "ds_read_b128 v[208:211], %[rdx0] offset:8192\n"
    "v_mfma_f32_16x16x32_bf16 v[120:123], v[220:223], a[88:91], v[120:123]\n"
    "s_add_u32 s47, s41, 262144\n"
    "s_add_u32 m0, %[m0x], 98304\n"
    "ds_read_b128 v[212:215], %[rdx0] offset:10240\n"
    "v_mfma_f32_16x16x32_bf16 v[116:119], v[220:223], a[84:87], v[116:119]\n"
    "buffer_load_dwordx4 %[vx0], %[srdx], s40 offen offset:0 lds\n"
    "v_mfma_f32_16x16x32_bf16 v[112:115], v[220:223], a[80:83], v[112:115]\n"
    "buffer_load_dwordx4 %[vx1], %[srdx], s40 offen offset:1024 lds\n"
    "s_waitcnt lgkmcnt(10)\n"
    "v_mfma_f32_16x16x32_bf16 v[0:3], v[192:195], a[96:99], v[0:3]\n"
    "ds_read_b128 v[216:219], %[rdx0] offset:12288\n"
    "s_waitcnt lgkmcnt(9)\n"
    "v_mfma_f32_16x16x32_bf16 v[4:7], v[192:195], a[100:103], v[4:7]\n"
    "ds_read_b64_tr_b16 a[112:113], %[rdw0] offset:4096\n"
    "s_waitcnt lgkmcnt(7)\n"
    "v_mfma_f32_16x16x32_bf16 v[8:11], v[192:195], a[104:107], v[8:11]\n"
    "ds_read_b64_tr_b16 a[114:115], %[rdw0] offset:4608\n"
    "s_waitcnt lgkmcnt(5)\n"
    "v_mfma_f32_16x16x32_bf16 v[12:15], v[192:195], a[108:111], v[12:15]\n"
    "ds_read_b64_tr_b16 a[116:117], %[rdw1] offset:4096\n"
    "v_mfma_f32_16x16x32_bf16 v[28:31], v[196:199], a[108:111], v[28:31]\n"
    "ds_read_b128 v[220:223], %[rdx0] offset:14336\n"
    "v_mfma_f32_16x16x32_bf16 v[24:27], v[196:199], a[104:107], v[24:27]\n"
    "ds_read_b64_tr_b16 a[118:119], %[rdw1] offset:4608\n"
    "v_mfma_f32_16x16x32_bf16 v[20:23], v[196:199], a[100:103], v[20:23]\n"
    "ds_read_b64_tr_b16 a[120:121], %[rdw2] offset:4096\n"
    "v_mfma_f32_16x16x32_bf16 v[16:19], v[196:199], a[96:99], v[16:19]\n"
    "ds_read_b64_tr_b16 a[122:123], %[rdw2] offset:4608\n"
    "v_mfma_f32_16x16x32_bf16 v[32:35], v[200:203], a[96:99], v[32:35]\n"
    "ds_read_b128 v[192:195], %[rdx1] offset:0\n"
    "v_mfma_f32_16x16x32_bf16 v[36:39], v[200:203], a[100:103], v[36:39]\n"
    "ds_read_b64_tr_b16 a[124:125], %[rdw3] offset:4096\n"
    "v_mfma_f32_16x16x32_bf16 v[40:43], v[200:203], a[104:107], v[40:43]\n"
    "ds_read_b64_tr_b16 a[126:127], %[rdw3] offset:4608\n"
    "v_mfma_f32_16x16x32_bf16 v[44:47], v[200:203], a[108:111], v[44:47]\n"
    "buffer_load_dwordx4 %[vx2], %[srdx], s40 offen offset:2048 lds\n"
    "v_mfma_f32_16x16x32_bf16 v[60:63], v[204:207], a[108:111], v[60:63]\n"
    "ds_read_b128 v[196:199], %[rdx1] offset:2048\n"
    "v_mfma_f32_16x16x32_bf16 v[56:59], v[204:207], a[104:107], v[56:59]\n"
    "buffer_load_dwordx4 %[vx3], %[srdx], s40 offen offset:3072 lds\n"
    "v_mfma_f32_16x16x32_bf16 v[52:55], v[204:207], a[100:103], v[52:55]\n"
    "s_add_u32 m0, %[m0w], 98304\n"
    "v_mfma_f32_16x16x32_bf16 v[48:51], v[204:207], a[96:99], v[48:51]\n"
    "buffer_load_dwordx4 %[vw0], %[srdw], s41 offen offset:0 nt lds\n"
    "s_waitcnt lgkmcnt(12)\n"
    "v_mfma_f32_16x16x32_bf16 v[64:67], v[208:211], a[96:99], v[64:67]\n"
    "ds_read_b128 v[200:203], %[rdx1] offset:4096\n"
    "v_mfma_f32_16x16x32_bf16 v[68:71], v[208:211], a[100:103], v[68:71]\n"
    "buffer_load_dwordx4 %[vw1], %[srdw], s41 offen offset:1024 nt lds\n"
    "v_mfma_f32_16x16x32_bf16 v[72:75], v[208:211], a[104:107], v[72:75]\n"
    "buffer_load_dwordx4 %[vw2], %[srdw], s41 offen offset:2048 nt lds\n"
    "v_mfma_f32_16x16x32_bf16 v[76:79], v[208:211], a[108:111], v[76:79]\n"
    "buffer_load_dwordx4 %[vw3], %[srdw], s41 offen offset:3072 nt lds\n"
    "v_mfma_f32_16x16x32_bf16 v[92:95], v[212:215], a[108:111], v[92:95]\n"
    "s_add_u32 m0, %[m0w], 102400\n"
    "ds_read_b128 v[204:207], %[rdx1] offset:6144\n"
    "v_mfma_f32_16x16x32_bf16 v[88:91], v[212:215], a[104:107], v[88:91]\n"
    "buffer_load_dwordx4 %[vw0], %[srdw], s47 offen offset:0 nt lds\n"
    "v_mfma_f32_16x16x32_bf16 v[84:87], v[212:215], a[100:103], v[84:87]\n"
    "buffer_load_dwordx4 %[vw1], %[srdw], s47 offen offset:1024 nt lds\n"
    "v_mfma_f32_16x16x32_bf16 v[80:83], v[212:215], a[96:99], v[80:83]\n"
    "s_waitcnt lgkmcnt(12)\n"
    "v_mfma_f32_16x16x32_bf16 v[96:99], v[216:219], a[96:99], v[96:99]\n"
    "ds_read_b128 v[208:211], %[rdx1] offset:8192\n"
    "v_mfma_f32_16x16x32_bf16 v[100:103], v[216:219], a[100:103], v[100:103]\n"
    "v_mfma_f32_16x16x32_bf16 v[104:107], v[216:219], a[104:107], v[104:107]\n"
    "buffer_load_dwordx4 %[vw2], %[srdw], s47 offen offset:2048 nt lds\n"
    "v_mfma_f32_16x16x32_bf16 v[108:111], v[216:219], a[108:111], v[108:111]\n"
    "s_waitcnt lgkmcnt(9)\n"
    "v_mfma_f32_16x16x32_bf16 v[124:127], v[220:223], a[108:111], v[124:127]\n"
    "ds_read_b128 v[212:215], %[rdx1] offset:10240\n"
    "v_mfma_f32_16x16x32_bf16 v[120:123], v[220:223], a[104:107], v[120:123]\n"
    "v_mfma_f32_16x16x32_bf16 v[116:119], v[220:223], a[100:103], v[116:119]\n"
    "buffer_load_dwordx4 %[vw3], %[srdw], s47 offen offset:3072 nt lds\n"
    "v_mfma_f32_16x16x32_bf16 v[112:115], v[220:223], a[96:99], v[112:115]\n"
    "s_waitcnt lgkmcnt(6)\n"
    "v_mfma_f32_16x16x32_bf16 v[0:3], v[192:195], a[112:115], v[0:3]\n"
    "ds_read_b128 v[216:219], %[rdx1] offset:12288\n"
    "v_mfma_f32_16x16x32_bf16 v[4:7], v[192:195], a[116:119], v[4:7]\n"
    "v_mfma_f32_16x16x32_bf16 v[8:11], v[192:195], a[120:123], v[8:11]\n"
    "s_waitcnt lgkmcnt(5)\n"
    "v_mfma_f32_16x16x32_bf16 v[12:15], v[192:195], a[124:127], v[12:15]\n"
    "v_mfma_f32_16x16x32_bf16 v[28:31], v[196:199], a[124:127], v[28:31]\n"
    "ds_read_b128 v[220:223], %[rdx1] offset:14336\n"
    "v_mfma_f32_16x16x32_bf16 v[24:27], v[196:199], a[120:123], v[24:27]\n"
    "v_mfma_f32_16x16x32_bf16 v[20:23], v[196:199], a[116:119], v[20:23]\n"
    "v_mfma_f32_16x16x32_bf16 v[16:19], v[196:199], a[112:115], v[16:19]\n"
    "s_waitcnt lgkmcnt(4)\n"
    "v_mfma_f32_16x16x32_bf16 v[32:35], v[200:203], a[112:115], v[32:35]\n"
    "v_mfma_f32_16x16x32_bf16 v[36:39], v[200:203], a[116:119], v[36:39]\n"
    "v_mfma_f32_16x16x32_bf16 v[40:43], v[200:203], a[120:123], v[40:43]\n"
    "v_mfma_f32_16x16x32_bf16 v[44:47], v[200:203], a[124:127], v[44:47]\n"
    "v_mfma_f32_16x16x32_bf16 v[60:63], v[204:207], a[124:127], v[60:63]\n"
    "v_mfma_f32_16x16x32_bf16 v[56:59], v[204:207], a[120:123], v[56:59]\n"
    "v_mfma_f32_16x16x32_bf16 v[52:55], v[204:207], a[116:119], v[52:55]\n"
    "v_mfma_f32_16x16x32_bf16 v[48:51], v[204:207], a[112:115], v[48:51]\n"
    "s_waitcnt vmcnt(12) lgkmcnt(0)\n"
    "s_barrier\n"
    "v_mfma_f32_16x16x32_bf16 v[64:67], v[208:211], a[112:115], v[64:67]\n"
    "ds_read_b64_tr_b16 a[128:129], %[rdw0] offset:49152\n"
    "v_mfma_f32_16x16x32_bf16 v[68:71], v[208:211], a[116:119], v[68:71]\n"
    "ds_read_b64_tr_b16 a[130:131], %[rdw0] offset:49664\n"
    "v_mfma_f32_16x16x32_bf16 v[72:75], v[208:211], a[120:123], v[72:75]\n"
    "ds_read_b128 v[192:195], %[rdx0] offset:49152\n"
    "v_mfma_f32_16x16x32_bf16 v[76:79], v[208:211], a[124:127], v[76:79]\n"
    "ds_read_b64_tr_b16 a[132:133], %[rdw1] offset:49152\n"
    "v_mfma_f32_16x16x32_bf16 v[92:95], v[212:215], a[124:127], v[92:95]\n"
    "ds_read_b64_tr_b16 a[134:135], %[rdw1] offset:49664\n"
    "v_mfma_f32_16x16x32_bf16 v[88:91], v[212:215], a[120:123], v[88:91]\n"
    "ds_read_b128 v[196:199], %[rdx0] offset:51200\n"
    "v_mfma_f32_16x16x32_bf16 v[84:87], v[212:215], a[116:119], v[84:87]\n"
    "ds_read_b64_tr_b16 a[136:137], %[rdw2] offset:49152\n"
    "v_mfma_f32_16x16x32_bf16 v[80:83], v[212:215], a[112:115], v[80:83]\n"
    "ds_read_b64_tr_b16 a[138:139], %[rdw2] offset:49664\n"
    "v_mfma_f32_16x16x32_bf16 v[96:99], v[216:219], a[112:115], v[96:99]\n"
    "ds_read_b128 v[200:203], %[rdx0] offset:53248\n"
    "v_mfma_f32_16x16x32_bf16 v[100:103], v[216:219], a[116:119], v[100:103]\n"
    "ds_read_b64_tr_b16 a[140:141], %[rdw3] offset:49152\n"
    "v_mfma_f32_16x16x32_bf16 v[104:107], v[216:219], a[120:123], v[104:107]\n"
    "s_add_u32 s42, s42, 1\n"
    "s_and_b32 s42, s42, 7\n"
    "ds_read_b64_tr_b16 a[142:143], %[rdw3] offset:49664\n"
    "v_mfma_f32_16x16x32_bf16 v[108:111], v[216:219], a[124:127], v[108:111]\n"
    "s_lshl_b32 s43, s42, 7\n"
    "s_add_u32 s40, s43, %[sox]\n"
    "ds_read_b128 v[204:207], %[rdx0] offset:55296\n"
    "v_mfma_f32_16x16x32_bf16 v[124:127], v[220:223], a[124:127], v[124:127]\n"
    "s_lshl_b32 s43, s42, 19\n"
    "s_add_u32 s41, s43, %[sow]\n"
    "ds_read_b128 v[208:211], %[rdx0] offset:57344\n"
    "v_mfma_f32_16x16x32_bf16 v[120:123], v[220:223], a[120:123], v[120:123]\n"
    "s_add_u32 s47, s41, 262144\n"
    "s_add_u32 m0, %[m0x], 0\n"
    "ds_read_b128 v[212:215], %[rdx0] offset:59392\n"
    "v_mfma_f32_16x16x32_bf16 v[116:119], v[220:223], a[116:119], v[116:119]\n"
    "buffer_load_dwordx4 %[vx0], %[srdx], s40 offen offset:0 lds\n"
    "v_mfma_f32_16x16x32_bf16 v[112:115], v[220:223], a[112:115], v[112:115]\n"
    "buffer_load_dwordx4 %[vx1], %[srdx], s40 offen offset:1024 lds\n"
    "s_waitcnt lgkmcnt(10)\n"
    "v_mfma_f32_16x16x32_bf16 v[0:3], v[192:195], a[128:131], v[0:3]\n"
    "ds_read_b128 v[216:219], %[rdx0] offset:61440\n"
    "s_waitcnt lgkmcnt(9)\n"
    "v_mfma_f32_16x16x32_bf16 v[4:7], v[192:195], a[132:135], v[4:7]\n"
    "ds_read_b64_tr_b16 a[144:145], %[rdw0] offset:53248\n"
    "s_waitcnt lgkmcnt(7)\n"
    "v_mfma_f32_16x16x32_bf16 v[8:11], v[192:195], a[136:139], v[8:11]\n"
    "ds_read_b64_tr_b16 a[146:147], %[rdw0] offset:53760\n"
    "s_waitcnt lgkmcnt(5)\n"
    "v_mfma_f32_16x16x32_bf16 v[12:15], v[192:195], a[140:143], v[12:15]\n"
    "ds_read_b64_tr_b16 a[148:149], %[rdw1] offset:53248\n"
    "v_mfma_f32_16x16x32_bf16 v[28:31], v[196:199], a[140:143], v[28:31]\n"
    "ds_read_b128 v[220:223], %[rdx0] offset:63488\n"
    "v_mfma_f32_16x16x32_bf16 v[24:27], v[196:199], a[136:139], v[24:27]\n"
    "ds_read_b64_tr_b16 a[150:151], %[rdw1] offset:53760\n"
    "v_mfma_f32_16x16x32_bf16 v[20:23], v[196:199], a[132:135], v[20:23]\n"
    "ds_read_b64_tr_b16 a[152:153], %[rdw2] offset:53248\n"
    "v_mfma_f32_16x16x32_bf16 v[16:19], v[196:199], a[128:131], v[16:19]\n"
    "ds_read_b64_tr_b16 a[154:155], %[rdw2] offset:53760\n"
    "v_mfma_f32_16x16x32_bf16 v[32:35], v[200:203], a[128:131], v[32:35]\n"
    "ds_read_b128 v[192:195], %[rdx1] offset:49152\n"
    "v_mfma_f32_16x16x32_bf16 v[36:39], v[200:203], a[132:135], v[36:39]\n"
    "ds_read_b64_tr_b16 a[156:157], %[rdw3] offset:53248\n"
    "v_mfma_f32_16x16x32_bf16 v[40:43], v[200:203], a[136:139], v[40:43]\n"
    "ds_read_b64_tr_b16 a[158:159], %[rdw3] offset:53760\n"
    "v_mfma_f32_16x16x32_bf16 v[44:47], v[200:203], a[140:143], v[44:47]\n"
    "buffer_load_dwordx4 %[vx2], %[srdx], s40 offen offset:2048 lds\n"
    "v_mfma_f32_16x16x32_bf16 v[60:63], v[204:207], a[140:143], v[60:63]\n"
    "ds_read_b128 v[196:199], %[rdx1] offset:51200\n"
    "v_mfma_f32_16x16x32_bf16 v[56:59], v[204:207], a[136:139], v[56:59]\n"
    "buffer_load_dwordx4 %[vx3], %[srdx], s40 offen offset:3072 lds\n"
    "v_mfma_f32_16x16x32_bf16 v[52:55], v[204:207], a[132:135], v[52:55]\n"
    "s_add_u32 m0, %[m0w], 0\n"
    "v_mfma_f32_16x16x32_bf16 v[48:51], v[204:207], a[128:131], v[48:51]\n"
    "buffer_load_dwordx4 %[vw0], %[srdw], s41 offen offset:0 nt lds\n"
    "s_waitcnt lgkmcnt(12)\n"
    "v_mfma_f32_16x16x32_bf16 v[64:67], v[208:211], a[128:131], v[64:67]\n"
    "ds_read_b128 v[200:203], %[rdx1] offset:53248\n"
    "v_mfma_f32_16x16x32_bf16 v[68:71], v[208:211], a[132:135], v[68:71]\n"
    "buffer_load_dwordx4 %[vw1], %[srdw], s41 offen offset:1024 nt lds\n"
    "v_mfma_f32_16x16x32_bf16 v[72:75], v[208:211], a[136:139], v[72:75]\n"
    "buffer_load_dwordx4 %[vw2], %[srdw], s41 offen offset:2048 nt lds\n"
    "v_mfma_f32_16x16x32_bf16 v[76:79], v[208:211], a[140:143], v[76:79]\n"
    "buffer_load_dwordx4 %[vw3], %[srdw], s41 offen offset:3072 nt lds\n"
    "v_mfma_f32_16x16x32_bf16 v[92:95], v[212:215], a[140:143], v[92:95]\n"
    "s_add_u32 m0, %[m0w], 4096\n"
    "ds_read_b128 v[204:207], %[rdx1] offset:55296\n"
    "v_mfma_f32_16x16x32_bf16 v[88:91], v[212:215], a[136:139], v[88:91]\n"
    "buffer_load_dwordx4 %[vw0], %[srdw], s47 offen offset:0 nt lds\n"
    "v_mfma_f32_16x16x32_bf16 v[84:87], v[212:215], a[132:135], v[84:87]\n"
    "buffer_load_dwordx4 %[vw1], %[srdw], s47 offen offset:1024 nt lds\n"
    "v_mfma_f32_16x16x32_bf16 v[80:83], v[212:215], a[128:131], v[80:83]\n"
    "s_waitcnt lgkmcnt(12)\n"
    "v_mfma_f32_16x16x32_bf16 v[96:99], v[216:219], a[128:131], v[96:99]\n"
    "ds_read_b128 v[208:211], %[rdx1] offset:57344\n"
    "v_mfma_f32_16x16x32_bf16 v[100:103], v[216:219], a[132:135], v[100:103]\n"
    "v_mfma_f32_16x16x32_bf16 v[104:107], v[216:219], a[136:139], v[104:107]\n"
    "buffer_load_dwordx4 %[vw2], %[srdw], s47 offen offset:2048 nt lds\n"
    "v_mfma_f32_16x16x32_bf16 v[108:111], v[216:219], a[140:143], v[108:111]\n"
    "s_waitcnt lgkmcnt(9)\n"
    "v_mfma_f32_16x16x32_bf16 v[124:127], v[220:223], a[140:143], v[124:127]\n"
    "ds_read_b128 v[212:215], %[rdx1] offset:59392\n"
    "v_mfma_f32_16x16x32_bf16 v[120:123], v[220:223], a[136:139], v[120:123]\n"
    "v_mfma_f32_16x16x32_bf16 v[116:119], v[220:223], a[132:135], v[116:119]\n"
    "buffer_load_dwordx4 %[vw3], %[srdw], s47 offen offset:3072 nt lds\n"
    "v_mfma_f32_16x16x32_bf16 v[112:115], v[220:223], a[128:131], v[112:115]\n"
    "s_waitcnt lgkmcnt(6)\n"
    "v_mfma_f32_16x16x32_bf16 v[0:3], v[192:195], a[144:147], v[0:3]\n"
    "ds_read_b128 v[216:219], %[rdx1] offset:61440\n"
    "v_mfma_f32_16x16x32_bf16 v[4:7], v[192:195], a[148:151], v[4:7]\n"
    "v_mfma_f32_16x16x32_bf16 v[8:11], v[192:195], a[152:155], v[8:11]\n"
    "s_waitcnt lgkmcnt(5)\n"
    "v_mfma_f32_16x16x32_bf16 v[12:15], v[192:195], a[156:159], v[12:15]\n"
    "v_mfma_f32_16x16x32_bf16 v[28:31], v[196:199], a[156:159], v[28:31]\n"
    "ds_read_b128 v[220:223], %[rdx1] offset:63488\n"
    "v_mfma_f32_16x16x32_bf16 v[24:27], v[196:199], a[152:155], v[24:27]\n"
    "v_mfma_f32_16x16x32_bf16 v[20:23], v[196:199], a[148:151], v[20:23]\n"
    "v_mfma_f32_16x16x32_bf16 v[16:19], v[196:199], a[144:147], v[16:19]\n"
    "s_waitcnt lgkmcnt(4)\n"
    "v_mfma_f32_16x16x32_bf16 v[32:35], v[200:203], a[144:147], v[32:35]\n"
    "v_mfma_f32_16x16x32_bf16 v[36:39], v[200:203], a[148:151], v[36:39]\n"
    "v_mfma_f32_16x16x32_bf16 v[40:43], v[200:203], a[152:155], v[40:43]\n"
    "v_mfma_f32_16x16x32_bf16 v[44:47], v[200:203], a[156:159], v[44:47]\n"
    "v_mfma_f32_16x16x32_bf16 v[60:63], v[204:207], a[156:159], v[60:63]\n"
    "v_mfma_f32_16x16x32_bf16 v[56:59], v[204:207], a[152:155], v[56:59]\n"
    "v_mfma_f32_16x16x32_bf16 v[52:55], v[204:207], a[148:151], v[52:55]\n"
    "v_mfma_f32_16x16x32_bf16 v[48:51], v[204:207], a[144:147], v[48:51]\n"
    "s_waitcnt vmcnt(12) lgkmcnt(0)\n"
    "s_barrier\n"
    "v_mfma_f32_16x16x32_bf16 v[64:67], v[208:211], a[144:147], v[64:67]\n"
    "ds_read_b64_tr_b16 a[160:161], %[rdw4] offset:0\n"
    "v_mfma_f32_16x16x32_bf16 v[68:71], v[208:211], a[148:151], v[68:71]\n"
    "ds_read_b64_tr_b16 a[162:163], %[rdw4] offset:512\n"
    "v_mfma_f32_16x16x32_bf16 v[72:75], v[208:211], a[152:155], v[72:75]\n"
    "ds_read_b128 v[192:195], %[rdx2] offset:0\n"
    "v_mfma_f32_16x16x32_bf16 v[76:79], v[208:211], a[156:159], v[76:79]\n"
    "ds_read_b64_tr_b16 a[164:165], %[rdw5] offset:0\n"
    "v_mfma_f32_16x16x32_bf16 v[92:95], v[212:215], a[156:159], v[92:95]\n"
    "ds_read_b64_tr_b16 a[166:167], %[rdw5] offset:512\n"
    "v_mfma_f32_16x16x32_bf16 v[88:91], v[212:215], a[152:155], v[88:91]\n"
    "ds_read_b128 v[196:199], %[rdx2] offset:2048\n"
    "v_mfma_f32_16x16x32_bf16 v[84:87], v[212:215], a[148:151], v[84:87]\n"
    "ds_read_b64_tr_b16 a[168:169], %[rdw6] offset:0\n"
    "v_mfma_f32_16x16x32_bf16 v[80:83], v[212:215], a[144:147], v[80:83]\n"
    "ds_read_b64_tr_b16 a[170:171], %[rdw6] offset:512\n"
    "v_mfma_f32_16x16x32_bf16 v[96:99], v[216:219], a[144:147], v[96:99]\n"
    "ds_read_b128 v[200:203], %[rdx2] offset:4096\n"
    "v_mfma_f32_16x16x32_bf16 v[100:103], v[216:219], a[148:151], v[100:103]\n"
    "ds_read_b64_tr_b16 a[172:173], %[rdw7] offset:0\n"
    "v_mfma_f32_16x16x32_bf16 v[104:107], v[216:219], a[152:155], v[104:107]\n"
    "s_add_u32 s42, s42, 1\n"
    "s_and_b32 s42, s42, 7\n"
    "ds_read_b64_tr_b16 a[174:175], %[rdw7] offset:512\n"
    "v_mfma_f32_16x16x32_bf16 v[108:111], v[216:219], a[156:159], v[108:111]\n"
    "s_lshl_b32 s43, s42, 7\n"
    "s_add_u32 s40, s43, %[sox]\n"
    "ds_read_b128 v[204:207], %[rdx2] offset:6144\n"
    "v_mfma_f32_16x16x32_bf16 v[124:127], v[220:223], a[156:159], v[124:127]\n"
    "s_lshl_b32 s43, s42, 19\n"
    "s_add_u32 s41, s43, %[sow]\n"
    "ds_read_b128 v[208:211], %[rdx2] offset:8192\n"
    "v_mfma_f32_16x16x32_bf16 v[120:123], v[220:223], a[152:155], v[120:123]\n"
    "s_add_u32 s47, s41, 262144\n"
    "s_add_u32 m0, %[m0x], 49152\n"
    "ds_read_b128 v[212:215], %[rdx2] offset:10240\n"
    "v_mfma_f32_16x16x32_bf16 v[116:119], v[220:223], a[148:151], v[116:119]\n"
    "buffer_load_dwordx4 %[vx0], %[srdx], s40 offen offset:0 lds\n"
    "v_mfma_f32_16x16x32_bf16 v[112:115], v[220:223], a[144:147], v[112:115]\n"
    "buffer_load_dwordx4 %[vx1], %[srdx], s40 offen offset:1024 lds\n"
    "s_waitcnt lgkmcnt(10)\n"
    "v_mfma_f32_16x16x32_bf16 v[0:3], v[192:195], a[160:163], v[0:3]\n"
    "ds_read_b128 v[216:219], %[rdx2] offset:12288\n"
    "s_waitcnt lgkmcnt(9)\n"
    "v_mfma_f32_16x16x32_bf16 v[4:7], v[192:195], a[164:167], v[4:7]\n"
    "ds_read_b64_tr_b16 a[176:177], %[rdw4] offset:4096\n"
    "s_waitcnt lgkmcnt(7)\n"
    "v_mfma_f32_16x16x32_bf16 v[8:11], v[192:195], a[168:171], v[8:11]\n"
    "ds_read_b64_tr_b16 a[178:179], %[rdw4] offset:4608\n"
    "s_waitcnt lgkmcnt(5)\n"
    "v_mfma_f32_16x16x32_bf16 v[12:15], v[192:195], a[172:175], v[12:15]\n"
    "ds_read_b64_tr_b16 a[180:181], %[rdw5] offset:4096\n"
    "v_mfma_f32_16x16x32_bf16 v[28:31], v[196:199], a[172:175], v[28:31]\n"
    "ds_read_b128 v[220:223], %[rdx2] offset:14336\n"
    "v_mfma_f32_16x16x32_bf16 v[24:27], v[196:199], a[168:171], v[24:27]\n"
    "ds_read_b64_tr_b16 a[182:183], %[rdw5] offset:4608\n"
    "v_mfma_f32_16x16x32_bf16 v[20:23], v[196:199], a[164:167], v[20:23]\n"
    "ds_read_b64_tr_b16 a[184:185], %[rdw6] offset:4096\n"
    "v_mfma_f32_16x16x32_bf16 v[16:19], v[196:199], a[160:163], v[16:19]\n"
    "ds_read_b64_tr_b16 a[186:187], %[rdw6] offset:4608\n"
    "v_mfma_f32_16x16x32_bf16 v[32:35], v[200:203], a[160:163], v[32:35]\n"
    "ds_read_b128 v[192:195], %[rdx3] offset:0\n"
    "v_mfma_f32_16x16x32_bf16 v[36:39], v[200:203], a[164:167], v[36:39]\n"
    "ds_read_b64_tr_b16 a[188:189], %[rdw7] offset:4096\n"
    "v_mfma_f32_16x16x32_bf16 v[40:43], v[200:203], a[168:171], v[40:43]\n"
    "ds_read_b64_tr_b16 a[190:191], %[rdw7] offset:4608\n"
    "v_mfma_f32_16x16x32_bf16 v[44:47], v[200:203], a[172:175], v[44:47]\n"
    "buffer_load_dwordx4 %[vx2], %[srdx], s40 offen offset:2048 lds\n"
    "v_mfma_f32_16x16x32_bf16 v[60:63], v[204:207], a[172:175], v[60:63]\n"
    "ds_read_b128 v[196:199], %[rdx3] offset:2048\n"
    "v_mfma_f32_16x16x32_bf16 v[56:59], v[204:207], a[168:171], v[56:59]\n"
    "buffer_load_dwordx4 %[vx3], %[srdx], s40 offen offset:3072 lds\n"
    "v_mfma_f32_16x16x32_bf16 v[52:55], v[204:207], a[164:167], v[52:55]\n"
    "s_add_u32 m0, %[m0w], 49152\n"
    "v_mfma_f32_16x16x32_bf16 v[48:51], v[204:207], a[160:163], v[48:51]\n"
    "buffer_load_dwordx4 %[vw0], %[srdw], s41 offen offset:0 nt lds\n"
    "s_waitcnt lgkmcnt(12)\n"
    "v_mfma_f32_16x16x32_bf16 v[64:67], v[208:211], a[160:163], v[64:67]\n"
    "ds_read_b128 v[200:203], %[rdx3] offset:4096\n"
    "v_mfma_f32_16x16x32_bf16 v[68:71], v[208:211], a[164:167], v[68:71]\n"
    "buffer_load_dwordx4 %[vw1], %[srdw], s41 offen offset:1024 nt lds\n"
    "v_mfma_f32_16x16x32_bf16 v[72:75], v[208:211], a[168:171], v[72:75]\n"
    "buffer_load_dwordx4 %[vw2], %[srdw], s41 offen offset:2048 nt lds\n"
    "v_mfma_f32_16x16x32_bf16 v[76:79], v[208:211], a[172:175], v[76:79]\n"
    "buffer_load_dwordx4 %[vw3], %[srdw], s41 offen offset:3072 nt lds\n"
    "v_mfma_f32_16x16x32_bf16 v[92:95], v[212:215], a[172:175], v[92:95]\n"
    "s_add_u32 m0, %[m0w], 53248\n"
    "ds_read_b128 v[204:207], %[rdx3] offset:6144\n"
    "v_mfma_f32_16x16x32_bf16 v[88:91], v[212:215], a[168:171], v[88:91]\n"
    "buffer_load_dwordx4 %[vw0], %[srdw], s47 offen offset:0 nt lds\n"
    "v_mfma_f32_16x16x32_bf16 v[84:87], v[212:215], a[164:167], v[84:87]\n"
    "buffer_load_dwordx4 %[vw1], %[srdw], s47 offen offset:1024 nt lds\n"
    "v_mfma_f32_16x16x32_bf16 v[80:83], v[212:215], a[160:163], v[80:83]\n"
    "s_waitcnt lgkmcnt(12)\n"
    "v_mfma_f32_16x16x32_bf16 v[96:99], v[216:219], a[160:163], v[96:99]\n"
    "ds_read_b128 v[208:211], %[rdx3] offset:8192\n"
    "v_mfma_f32_16x16x32_bf16 v[100:103], v[216:219], a[164:167], v[100:103]\n"
    "v_mfma_f32_16x16x32_bf16 v[104:107], v[216:219], a[168:171], v[104:107]\n"
    "buffer_load_dwordx4 %[vw2], %[srdw], s47 offen offset:2048 nt lds\n"
    "v_mfma_f32_16x16x32_bf16 v[108:111], v[216:219], a[172:175], v[108:111]\n"
    "s_waitcnt lgkmcnt(9)\n"
    "v_mfma_f32_16x16x32_bf16 v[124:127], v[220:223], a[172:175], v[124:127]\n"
    "ds_read_b128 v[212:215], %[rdx3] offset:10240\n"
    "v_mfma_f32_16x16x32_bf16 v[120:123], v[220:223], a[168:171], v[120:123]\n"
    "v_mfma_f32_16x16x32_bf16 v[116:119], v[220:223], a[164:167], v[116:119]\n"
    "buffer_load_dwordx4 %[vw3], %[srdw], s47 offen offset:3072 nt lds\n"
    "v_mfma_f32_16x16x32_bf16 v[112:115], v[220:223], a[160:163], v[112:115]\n"
    "s_waitcnt lgkmcnt(6)\n"
    "v_mfma_f32_16x16x32_bf16 v[0:3], v[192:195], a[176:179], v[0:3]\n"
    "ds_read_b128 v[216:219], %[rdx3] offset:12288\n"
    "v_mfma_f32_16x16x32_bf16 v[4:7], v[192:195], a[180:183], v[4:7]\n"
    "v_mfma_f32_16x16x32_bf16 v[8:11], v[192:195], a[184:187], v[8:11]\n"
    "s_waitcnt lgkmcnt(5)\n"
    "v_mfma_f32_16x16x32_bf16 v[12:15], v[192:195], a[188:191], v[12:15]\n"
    "v_mfma_f32_16x16x32_bf16 v[28:31], v[196:199], a[188:191], v[28:31]\n"
    "ds_read_b128 v[220:223], %[rdx3] offset:14336\n"
    "v_mfma_f32_16x16x32_bf16 v[24:27], v[196:199], a[184:187], v[24:27]\n"
    "v_mfma_f32_16x16x32_bf16 v[20:23], v[196:199], a[180:183], v[20:23]\n"
    "v_mfma_f32_16x16x32_bf16 v[16:19], v[196:199], a[176:179], v[16:19]\n"
    "s_waitcnt lgkmcnt(4)\n"
    "v_mfma_f32_16x16x32_bf16 v[32:35], v[200:203], a[176:179], v[32:35]\n"
    "v_mfma_f32_16x16x32_bf16 v[36:39], v[200:203], a[180:183], v[36:39]\n"
    "v_mfma_f32_16x16x32_bf16 v[40:43], v[200:203], a[184:187], v[40:43]\n"
    "v_mfma_f32_16x16x32_bf16 v[44:47], v[200:203], a[188:191], v[44:47]\n"
    "v_mfma_f32_16x16x32_bf16 v[60:63], v[204:207], a[188:191], v[60:63]\n"
    "v_mfma_f32_16x16x32_bf16 v[56:59], v[204:207], a[184:187], v[56:59]\n"
    "v_mfma_f32_16x16x32_bf16 v[52:55], v[204:207], a[180:183], v[52:55]\n"
    "v_mfma_f32_16x16x32_bf16 v[48:51], v[204:207], a[176:179], v[48:51]\n"
    "s_waitcnt vmcnt(12) lgkmcnt(0)\n"
    "s_barrier\n"
    "v_mfma_f32_16x16x32_bf16 v[64:67], v[208:211], a[176:179], v[64:67]\n"
    "ds_read_b64_tr_b16 a[192:193], %[rdw0] offset:0\n"
    "v_mfma_f32_16x16x32_bf16 v[68:71], v[208:211], a[180:183], v[68:71]\n"
    "ds_read_b64_tr_b16 a[194:195], %[rdw0] offset:512\n"
    "v_mfma_f32_16x16x32_bf16 v[72:75], v[208:211], a[184:187], v[72:75]\n"
    "ds_read_b128 v[192:195], %[rdx0] offset:0\n"
    "v_mfma_f32_16x16x32_bf16 v[76:79], v[208:211], a[188:191], v[76:79]\n"
    "ds_read_b64_tr_b16 a[196:197], %[rdw1] offset:0\n"
    "v_mfma_f32_16x16x32_bf16 v[92:95], v[212:215], a[188:191], v[92:95]\n"
    "ds_read_b64_tr_b16 a[198:199], %[rdw1] offset:512\n"
    "v_mfma_f32_16x16x32_bf16 v[88:91], v[212:215], a[184:187], v[88:91]\n"
    "ds_read_b128 v[196:199], %[rdx0] offset:2048\n"
    "v_mfma_f32_16x16x32_bf16 v[84:87], v[212:215], a[180:183], v[84:87]\n"
    "ds_read_b64_tr_b16 a[200:201], %[rdw2] offset:0\n"
    "v_mfma_f32_16x16x32_bf16 v[80:83], v[212:215], a[176:179], v[80:83]\n"
    "ds_read_b64_tr_b16 a[202:203], %[rdw2] offset:512\n"
    "v_mfma_f32_16x16x32_bf16 v[96:99], v[216:219], a[176:179], v[96:99]\n"
    "ds_read_b128 v[200:203], %[rdx0] offset:4096\n"
    "v_mfma_f32_16x16x32_bf16 v[100:103], v[216:219], a[180:183], v[100:103]\n"
    "ds_read_b64_tr_b16 a[204:205], %[rdw3] offset:0\n"
    "v_mfma_f32_16x16x32_bf16 v[104:107], v[216:219], a[184:187], v[104:107]\n"
    "ds_read_b64_tr_b16 a[206:207], %[rdw3] offset:512\n"
    "v_mfma_f32_16x16x32_bf16 v[108:111], v[216:219], a[188:191], v[108:111]\n"
    "ds_read_b128 v[204:207], %[rdx0] offset:6144\n"
    "v_mfma_f32_16x16x32_bf16 v[124:127], v[220:223], a[188:191], v[124:127]\n"
    "s_add_u32 s45, s45, 128\n"
    "s_and_b32 s45, s45, 1023\n"
    "ds_read_b128 v[208:211], %[rdx0] offset:8192\n"
    "v_mfma_f32_16x16x32_bf16 v[120:123], v[220:223], a[184:187], v[120:123]\n"
    "s_add_u32 s44, s45, %[sot0]\n"
    "s_add_u32 m0, %[m0t], 98304\n"
    "ds_read_b128 v[212:215], %[rdx0] offset:10240\n"
    "v_mfma_f32_16x16x32_bf16 v[116:119], v[220:223], a[180:183], v[116:119]\n"
    "buffer_load_dwordx4 %[vx0], %[srdx], s44 offen lds\n"
    "v_mfma_f32_16x16x32_bf16 v[112:115], v[220:223], a[176:179], v[112:115]\n"
    "buffer_load_dwordx4 %[vx1], %[srdx], s44 offen offset:1024 lds\n"
    "s_waitcnt lgkmcnt(10)\n"
    "v_mfma_f32_16x16x32_bf16 v[0:3], v[192:195], a[192:195], v[0:3]\n"
    "ds_read_b128 v[216:219], %[rdx0] offset:12288\n"
    "s_waitcnt lgkmcnt(9)\n"
    "v_mfma_f32_16x16x32_bf16 v[4:7], v[192:195], a[196:199], v[4:7]\n"
    "ds_read_b64_tr_b16 a[208:209], %[rdw0] offset:4096\n"
    "s_waitcnt lgkmcnt(7)\n"
    "v_mfma_f32_16x16x32_bf16 v[8:11], v[192:195], a[200:203], v[8:11]\n"
    "ds_read_b64_tr_b16 a[210:211], %[rdw0] offset:4608\n"
    "s_waitcnt lgkmcnt(5)\n"
    "v_mfma_f32_16x16x32_bf16 v[12:15], v[192:195], a[204:207], v[12:15]\n"
    "ds_read_b64_tr_b16 a[212:213], %[rdw1] offset:4096\n"
    "v_mfma_f32_16x16x32_bf16 v[28:31], v[196:199], a[204:207], v[28:31]\n"
    "ds_read_b128 v[220:223], %[rdx0] offset:14336\n"
    "v_mfma_f32_16x16x32_bf16 v[24:27], v[196:199], a[200:203], v[24:27]\n"
    "ds_read_b64_tr_b16 a[214:215], %[rdw1] offset:4608\n"
    "v_mfma_f32_16x16x32_bf16 v[20:23], v[196:199], a[196:199], v[20:23]\n"
    "ds_read_b64_tr_b16 a[216:217], %[rdw2] offset:4096\n"
    "v_mfma_f32_16x16x32_bf16 v[16:19], v[196:199], a[192:195], v[16:19]\n"
    "ds_read_b64_tr_b16 a[218:219], %[rdw2] offset:4608\n"
    "v_mfma_f32_16x16x32_bf16 v[32:35], v[200:203], a[192:195], v[32:35]\n"
    "ds_read_b128 v[192:195], %[rdx1] offset:0\n"
    "v_mfma_f32_16x16x32_bf16 v[36:39], v[200:203], a[196:199], v[36:39]\n"
    "s_add_u32 s45, s45, 128\n"
    "s_and_b32 s45, s45, 1023\n"
    "ds_read_b64_tr_b16 a[220:221], %[rdw3] offset:4096\n"
    "v_mfma_f32_16x16x32_bf16 v[40:43], v[200:203], a[200:203], v[40:43]\n"
    "s_add_u32 s44, s45, %[sot0]\n"
    "s_add_u32 m0, %[m0t], 106496\n"
    "ds_read_b64_tr_b16 a[222:223], %[rdw3] offset:4608\n"
    "v_mfma_f32_16x16x32_bf16 v[44:47], v[200:203], a[204:207], v[44:47]\n"
    "buffer_load_dwordx4 %[vx0], %[srdx], s44 offen lds\n"
    "v_mfma_f32_16x16x32_bf16 v[60:63], v[204:207], a[204:207], v[60:63]\n"
    "ds_read_b128 v[196:199], %[rdx1] offset:2048\n"
    "v_mfma_f32_16x16x32_bf16 v[56:59], v[204:207], a[200:203], v[56:59]\n"
    "buffer_load_dwordx4 %[vx1], %[srdx], s44 offen offset:1024 lds\n"
    "v_mfma_f32_16x16x32_bf16 v[52:55], v[204:207], a[196:199], v[52:55]\n"
    "v_mfma_f32_16x16x32_bf16 v[48:51], v[204:207], a[192:195], v[48:51]\n"
    "s_add_u32 s45, s45, 128\n"
    "s_and_b32 s45, s45, 1023\n"
    "s_waitcnt lgkmcnt(12)\n"
    "v_mfma_f32_16x16x32_bf16 v[64:67], v[208:211], a[192:195], v[64:67]\n"
    "s_add_u32 s44, s45, %[sot0]\n"
    "s_add_u32 m0, %[m0t], 114688\n"
    "ds_read_b128 v[200:203], %[rdx1] offset:4096\n"
    "v_mfma_f32_16x16x32_bf16 v[68:71], v[208:211], a[196:199], v[68:71]\n"
    "buffer_load_dwordx4 %[vx0], %[srdx], s44 offen lds\n"
    "v_mfma_f32_16x16x32_bf16 v[72:75], v[208:211], a[200:203], v[72:75]\n"
    "buffer_load_dwordx4 %[vx1], %[srdx], s44 offen offset:1024 lds\n"
    "v_mfma_f32_16x16x32_bf16 v[76:79], v[208:211], a[204:207], v[76:79]\n"
    "s_add_u32 s45, s45, 128\n"
    "s_and_b32 s45, s45, 1023\n"
    "v_mfma_f32_16x16x32_bf16 v[92:95], v[212:215], a[204:207], v[92:95]\n"
    "s_add_u32 s44, s45, %[sot0]\n"
    "s_add_u32 m0, %[m0t], 122880\n"
    "ds_read_b128 v[204:207], %[rdx1] offset:6144\n"
    "v_mfma_f32_16x16x32_bf16 v[88:91], v[212:215], a[200:203], v[88:91]\n"
    "buffer_load_dwordx4 %[vx0], %[srdx], s44 offen lds\n"
    "v_mfma_f32_16x16x32_bf16 v[84:87], v[212:215], a[196:199], v[84:87]\n"
    "buffer_load_dwordx4 %[vx1], %[srdx], s44 offen offset:1024 lds\n"
    "v_mfma_f32_16x16x32_bf16 v[80:83], v[212:215], a[192:195], v[80:83]\n"
    "s_add_u32 s45, s45, 128\n"
    "s_and_b32 s45, s45, 1023\n"
    "s_waitcnt lgkmcnt(12)\n"
    "v_mfma_f32_16x16x32_bf16 v[96:99], v[216:219], a[192:195], v[96:99]\n"
    "s_add_u32 s44, s45, %[sot0]\n"
    "s_add_u32 m0, %[m0t], 131072\n"
    "ds_read_b128 v[208:211], %[rdx1] offset:8192\n"
    "v_mfma_f32_16x16x32_bf16 v[100:103], v[216:219], a[196:199], v[100:103]\n"
    "buffer_load_dwordx4 %[vx0], %[srdx], s44 offen lds\n"
    "v_mfma_f32_16x16x32_bf16 v[104:107], v[216:219], a[200:203], v[104:107]\n"
    "buffer_load_dwordx4 %[vx1], %[srdx], s44 offen offset:1024 lds\n"
    "v_mfma_f32_16x16x32_bf16 v[108:111], v[216:219], a[204:207], v[108:111]\n"
    "s_add_u32 s45, s45, 128\n"
    "s_and_b32 s45, s45, 1023\n"
    "s_waitcnt lgkmcnt(9)\n"
    "v_mfma_f32_16x16x32_bf16 v[124:127], v[220:223], a[204:207], v[124:127]\n"
    "s_add_u32 s44, s45, %[sot0]\n"
    "s_add_u32 m0, %[m0t], 139264\n"
    "ds_read_b128 v[212:215], %[rdx1] offset:10240\n"
    "v_mfma_f32_16x16x32_bf16 v[120:123], v[220:223], a[200:203], v[120:123]\n"
    "buffer_load_dwordx4 %[vx0], %[srdx], s44 offen lds\n"
    "v_mfma_f32_16x16x32_bf16 v[116:119], v[220:223], a[196:199], v[116:119]\n"
    "buffer_load_dwordx4 %[vx1], %[srdx], s44 offen offset:1024 lds\n"
    "v_mfma_f32_16x16x32_bf16 v[112:115], v[220:223], a[192:195], v[112:115]\n"
    "s_add_u32 s45, s45, 128\n"
    "s_and_b32 s45, s45, 1023\n"
    "s_waitcnt lgkmcnt(6)\n"
    "v_mfma_f32_16x16x32_bf16 v[0:3], v[192:195], a[208:211], v[0:3]\n"
    "s_add_u32 s44, s45, %[sot0]\n"
    "s_add_u32 m0, %[m0t], 147456\n"
    "ds_read_b128 v[216:219], %[rdx1] offset:12288\n"
    "v_mfma_f32_16x16x32_bf16 v[4:7], v[192:195], a[212:215], v[4:7]\n"
    "buffer_load_dwordx4 %[vx0], %[srdx], s44 offen lds\n"
    "v_mfma_f32_16x16x32_bf16 v[8:11], v[192:195], a[216:219], v[8:11]\n"
    "buffer_load_dwordx4 %[vx1], %[srdx], s44 offen offset:1024 lds\n"
    "s_waitcnt lgkmcnt(5)\n"
    "v_mfma_f32_16x16x32_bf16 v[12:15], v[192:195], a[220:223], v[12:15]\n"
    "s_add_u32 s45, s45, 128\n"
    "s_and_b32 s45, s45, 1023\n"
    "v_mfma_f32_16x16x32_bf16 v[28:31], v[196:199], a[220:223], v[28:31]\n"
    "s_add_u32 s44, s45, %[sot0]\n"
    "s_add_u32 m0, %[m0t], 155648\n"
    "ds_read_b128 v[220:223], %[rdx1] offset:14336\n"
    "v_mfma_f32_16x16x32_bf16 v[24:27], v[196:199], a[216:219], v[24:27]\n"
    "buffer_load_dwordx4 %[vx0], %[srdx], s44 offen lds\n"
    "v_mfma_f32_16x16x32_bf16 v[20:23], v[196:199], a[212:215], v[20:23]\n"
    "buffer_load_dwordx4 %[vx1], %[srdx], s44 offen offset:1024 lds\n"
    "v_mfma_f32_16x16x32_bf16 v[16:19], v[196:199], a[208:211], v[16:19]\n"
    "s_waitcnt lgkmcnt(4)\n"
    "v_mfma_f32_16x16x32_bf16 v[32:35], v[200:203], a[208:211], v[32:35]\n"
    "v_mfma_f32_16x16x32_bf16 v[36:39], v[200:203], a[212:215], v[36:39]\n"
    "v_mfma_f32_16x16x32_bf16 v[40:43], v[200:203], a[216:219], v[40:43]\n"
    "v_mfma_f32_16x16x32_bf16 v[44:47], v[200:203], a[220:223], v[44:47]\n"
    "v_mfma_f32_16x16x32_bf16 v[60:63], v[204:207], a[220:223], v[60:63]\n"
    "v_mfma_f32_16x16x32_bf16 v[56:59], v[204:207], a[216:219], v[56:59]\n"
    "v_mfma_f32_16x16x32_bf16 v[52:55], v[204:207], a[212:215], v[52:55]\n"
    "v_mfma_f32_16x16x32_bf16 v[48:51], v[204:207], a[208:211], v[48:51]\n"
    "s_waitcnt vmcnt(16) lgkmcnt(0)\n"
    "s_barrier\n"
    "v_mfma_f32_16x16x32_bf16 v[64:67], v[208:211], a[208:211], v[64:67]\n"
    "ds_read_b64_tr_b16 a[224:225], %[rdw0] offset:49152\n"
    "v_mfma_f32_16x16x32_bf16 v[68:71], v[208:211], a[212:215], v[68:71]\n"
    "ds_read_b64_tr_b16 a[226:227], %[rdw0] offset:49664\n"
    "v_mfma_f32_16x16x32_bf16 v[72:75], v[208:211], a[216:219], v[72:75]\n"
    "ds_read_b128 v[192:195], %[rdx0] offset:49152\n"
    "v_mfma_f32_16x16x32_bf16 v[76:79], v[208:211], a[220:223], v[76:79]\n"
    "ds_read_b64_tr_b16 a[228:229], %[rdw1] offset:49152\n"
    "v_mfma_f32_16x16x32_bf16 v[92:95], v[212:215], a[220:223], v[92:95]\n"
    "ds_read_b64_tr_b16 a[230:231], %[rdw1] offset:49664\n"
    "v_mfma_f32_16x16x32_bf16 v[88:91], v[212:215], a[216:219], v[88:91]\n"
    "ds_read_b128 v[196:199], %[rdx0] offset:51200\n"
    "v_mfma_f32_16x16x32_bf16 v[84:87], v[212:215], a[212:215], v[84:87]\n"
    "ds_read_b64_tr_b16 a[232:233], %[rdw2] offset:49152\n"
    "v_mfma_f32_16x16x32_bf16 v[80:83], v[212:215], a[208:211], v[80:83]\n"
    "ds_read_b64_tr_b16 a[234:235], %[rdw2] offset:49664\n"
    "v_mfma_f32_16x16x32_bf16 v[96:99], v[216:219], a[208:211], v[96:99]\n"
    "ds_read_b128 v[200:203], %[rdx0] offset:53248\n"
    "v_mfma_f32_16x16x32_bf16 v[100:103], v[216:219], a[212:215], v[100:103]\n"
    "ds_read_b64_tr_b16 a[236:237], %[rdw3] offset:49152\n"
    "v_mfma_f32_16x16x32_bf16 v[104:107], v[216:219], a[216:219], v[104:107]\n"
    "ds_read_b64_tr_b16 a[238:239], %[rdw3] offset:49664\n"
    "v_mfma_f32_16x16x32_bf16 v[108:111], v[216:219], a[220:223], v[108:111]\n"
    "ds_read_b128 v[204:207], %[rdx0] offset:55296\n"
    "v_mfma_f32_16x16x32_bf16 v[124:127], v[220:223], a[220:223], v[124:127]\n"
    "s_add_u32 s45, s45, 128\n"
    "s_and_b32 s45, s45, 1023\n"
    "ds_read_b128 v[208:211], %[rdx0] offset:57344\n"
    "v_mfma_f32_16x16x32_bf16 v[120:123], v[220:223], a[216:219], v[120:123]\n"
    "s_add_u32 s44, s45, %[sot1]\n"
    "s_add_u32 m0, %[m0t], 0\n"
    "ds_read_b128 v[212:215], %[rdx0] offset:59392\n"
    "v_mfma_f32_16x16x32_bf16 v[116:119], v[220:223], a[212:215], v[116:119]\n"
    "buffer_load_dwordx4 %[vx0], %[srdx], s44 offen lds\n"
    "v_mfma_f32_16x16x32_bf16 v[112:115], v[220:223], a[208:211], v[112:115]\n"
    "buffer_load_dwordx4 %[vx1], %[srdx], s44 offen offset:1024 lds\n"
    "s_waitcnt lgkmcnt(10)\n"
    "v_mfma_f32_16x16x32_bf16 v[0:3], v[192:195], a[224:227], v[0:3]\n"
    "ds_read_b128 v[216:219], %[rdx0] offset:61440\n"
    "s_waitcnt lgkmcnt(9)\n"
    "v_mfma_f32_16x16x32_bf16 v[4:7], v[192:195], a[228:231], v[4:7]\n"
    "ds_read_b64_tr_b16 a[240:241], %[rdw0] offset:53248\n"
    "s_waitcnt lgkmcnt(7)\n"
    "v_mfma_f32_16x16x32_bf16 v[8:11], v[192:195], a[232:235], v[8:11]\n"
    "ds_read_b64_tr_b16 a[242:243], %[rdw0] offset:53760\n"
    "s_waitcnt lgkmcnt(5)\n"
    "v_mfma_f32_16x16x32_bf16 v[12:15], v[192:195], a[236:239], v[12:15]\n"
    "ds_read_b64_tr_b16 a[244:245], %[rdw1] offset:53248\n"
    "v_mfma_f32_16x16x32_bf16 v[28:31], v[196:199], a[236:239], v[28:31]\n"
    "ds_read_b128 v[220:223], %[rdx0] offset:63488\n"
    "v_mfma_f32_16x16x32_bf16 v[24:27], v[196:199], a[232:235], v[24:27]\n"
    "ds_read_b64_tr_b16 a[246:247], %[rdw1] offset:53760\n"
    "v_mfma_f32_16x16x32_bf16 v[20:23], v[196:199], a[228:231], v[20:23]\n"
    "ds_read_b64_tr_b16 a[248:249], %[rdw2] offset:53248\n"
    "v_mfma_f32_16x16x32_bf16 v[16:19], v[196:199], a[224:227], v[16:19]\n"
    "ds_read_b64_tr_b16 a[250:251], %[rdw2] offset:53760\n"
    "v_mfma_f32_16x16x32_bf16 v[32:35], v[200:203], a[224:227], v[32:35]\n"
    "ds_read_b128 v[192:195], %[rdx1] offset:49152\n"
    "v_mfma_f32_16x16x32_bf16 v[36:39], v[200:203], a[228:231], v[36:39]\n"
    "s_add_u32 s45, s45, 128\n"
    "s_and_b32 s45, s45, 1023\n"
    "ds_read_b64_tr_b16 a[252:253], %[rdw3] offset:53248\n"
    "v_mfma_f32_16x16x32_bf16 v[40:43], v[200:203], a[232:235], v[40:43]\n"
    "s_add_u32 s44, s45, %[sot1]\n"
    "s_add_u32 m0, %[m0t], 8192\n"
    "ds_read_b64_tr_b16 a[254:255], %[rdw3] offset:53760\n"
    "v_mfma_f32_16x16x32_bf16 v[44:47], v[200:203], a[236:239], v[44:47]\n"
    "buffer_load_dwordx4 %[vx0], %[srdx], s44 offen lds\n"
    "v_mfma_f32_16x16x32_bf16 v[60:63], v[204:207], a[236:239], v[60:63]\n"
    "ds_read_b128 v[196:199], %[rdx1] offset:51200\n"
    "v_mfma_f32_16x16x32_bf16 v[56:59], v[204:207], a[232:235], v[56:59]\n"
    "buffer_load_dwordx4 %[vx1], %[srdx], s44 offen offset:1024 lds\n"
    "v_mfma_f32_16x16x32_bf16 v[52:55], v[204:207], a[228:231], v[52:55]\n"
    "v_mfma_f32_16x16x32_bf16 v[48:51], v[204:207], a[224:227], v[48:51]\n"
    "s_add_u32 s45, s45, 128\n"
    "s_and_b32 s45, s45, 1023\n"
    "s_waitcnt lgkmcnt(12)\n"
    "v_mfma_f32_16x16x32_bf16 v[64:67], v[208:211], a[224:227], v[64:67]\n"
    "s_add_u32 s44, s45, %[sot1]\n"
    "s_add_u32 m0, %[m0t], 16384\n"
    "ds_read_b128 v[200:203], %[rdx1] offset:53248\n"
    "v_mfma_f32_16x16x32_bf16 v[68:71], v[208:211], a[228:231], v[68:71]\n"
    "buffer_load_dwordx4 %[vx0], %[srdx], s44 offen lds\n"
    "v_mfma_f32_16x16x32_bf16 v[72:75], v[208:211], a[232:235], v[72:75]\n"
    "buffer_load_dwordx4 %[vx1], %[srdx], s44 offen offset:1024 lds\n"
    "v_mfma_f32_16x16x32_bf16 v[76:79], v[208:211], a[236:239], v[76:79]\n"
    "s_add_u32 s45, s45, 128\n"
    "s_and_b32 s45, s45, 1023\n"
    "v_mfma_f32_16x16x32_bf16 v[92:95], v[212:215], a[236:239], v[92:95]\n"
    "s_add_u32 s44, s45, %[sot1]\n"
    "s_add_u32 m0, %[m0t], 24576\n"
    "ds_read_b128 v[204:207], %[rdx1] offset:55296\n"
    "v_mfma_f32_16x16x32_bf16 v[88:91], v[212:215], a[232:235], v[88:91]\n"
    "buffer_load_dwordx4 %[vx0], %[srdx], s44 offen lds\n"
    "v_mfma_f32_16x16x32_bf16 v[84:87], v[212:215], a[228:231], v[84:87]\n"
    "buffer_load_dwordx4 %[vx1], %[srdx], s44 offen offset:1024 lds\n"
    "v_mfma_f32_16x16x32_bf16 v[80:83], v[212:215], a[224:227], v[80:83]\n"
    "s_add_u32 s45, s45, 128\n"
    "s_and_b32 s45, s45, 1023\n"
    "s_waitcnt lgkmcnt(12)\n"
    "v_mfma_f32_16x16x32_bf16 v[96:99], v[216:219], a[224:227], v[96:99]\n"
    "s_add_u32 s44, s45, %[sot1]\n"
    "s_add_u32 m0, %[m0t], 32768\n"
    "ds_read_b128 v[208:211], %[rdx1] offset:57344\n"
    "v_mfma_f32_16x16x32_bf16 v[100:103], v[216:219], a[228:231], v[100:103]\n"
    "buffer_load_dwordx4 %[vx0], %[srdx], s44 offen lds\n"
    "v_mfma_f32_16x16x32_bf16 v[104:107], v[216:219], a[232:235], v[104:107]\n"
    "buffer_load_dwordx4 %[vx1], %[srdx], s44 offen offset:1024 lds\n"
    "v_mfma_f32_16x16x32_bf16 v[108:111], v[216:219], a[236:239], v[108:111]\n"
    "s_add_u32 s45, s45, 128\n"
    "s_and_b32 s45, s45, 1023\n"
    "s_waitcnt lgkmcnt(9)\n"
    "v_mfma_f32_16x16x32_bf16 v[124:127], v[220:223], a[236:239], v[124:127]\n"
    "s_add_u32 s44, s45, %[sot1]\n"
    "s_add_u32 m0, %[m0t], 40960\n"
    "ds_read_b128 v[212:215], %[rdx1] offset:59392\n"
    "v_mfma_f32_16x16x32_bf16 v[120:123], v[220:223], a[232:235], v[120:123]\n"
    "buffer_load_dwordx4 %[vx0], %[srdx], s44 offen lds\n"
    "v_mfma_f32_16x16x32_bf16 v[116:119], v[220:223], a[228:231], v[116:119]\n"
    "buffer_load_dwordx4 %[vx1], %[srdx], s44 offen offset:1024 lds\n"
    "v_mfma_f32_16x16x32_bf16 v[112:115], v[220:223], a[224:227], v[112:115]\n"
    "s_waitcnt lgkmcnt(6)\n"
    "v_mfma_f32_16x16x32_bf16 v[0:3], v[192:195], a[240:243], v[0:3]\n"
    "ds_read_b128 v[216:219], %[rdx1] offset:61440\n"
    "v_mfma_f32_16x16x32_bf16 v[4:7], v[192:195], a[244:247], v[4:7]\n"
    "v_mfma_f32_16x16x32_bf16 v[8:11], v[192:195], a[248:251], v[8:11]\n"
    "s_waitcnt lgkmcnt(5)\n"
    "v_mfma_f32_16x16x32_bf16 v[12:15], v[192:195], a[252:255], v[12:15]\n"
    "v_mfma_f32_16x16x32_bf16 v[28:31], v[196:199], a[252:255], v[28:31]\n"
    "ds_read_b128 v[220:223], %[rdx1] offset:63488\n"
    "v_mfma_f32_16x16x32_bf16 v[24:27], v[196:199], a[248:251], v[24:27]\n"
    "v_mfma_f32_16x16x32_bf16 v[20:23], v[196:199], a[244:247], v[20:23]\n"
    "v_mfma_f32_16x16x32_bf16 v[16:19], v[196:199], a[240:243], v[16:19]\n"
    "s_waitcnt lgkmcnt(4)\n"
    "v_mfma_f32_16x16x32_bf16 v[32:35], v[200:203], a[240:243], v[32:35]\n"
    "v_mfma_f32_16x16x32_bf16 v[36:39], v[200:203], a[244:247], v[36:39]\n"
    "v_mfma_f32_16x16x32_bf16 v[40:43], v[200:203], a[248:251], v[40:43]\n"
    "v_mfma_f32_16x16x32_bf16 v[44:47], v[200:203], a[252:255], v[44:47]\n"
    "v_mfma_f32_16x16x32_bf16 v[60:63], v[204:207], a[252:255], v[60:63]\n"
    "v_mfma_f32_16x16x32_bf16 v[56:59], v[204:207], a[248:251], v[56:59]\n"
    "v_mfma_f32_16x16x32_bf16 v[52:55], v[204:207], a[244:247], v[52:55]\n"
    "v_mfma_f32_16x16x32_bf16 v[48:51], v[204:207], a[240:243], v[48:51]\n"
    "s_waitcnt vmcnt(12) lgkmcnt(0)\n"
    "s_barrier\n"
    "v_mfma_f32_16x16x32_bf16 v[64:67], v[208:211], a[240:243], v[64:67]\n"
    "ds_read_b128 v[192:195], %[rdx2] offset:0\n"
    "v_mfma_f32_16x16x32_bf16 v[68:71], v[208:211], a[244:247], v[68:71]\n"
    "s_add_u32 s45, s45, 128\n"
    "s_and_b32 s45, s45, 1023\n"
    "v_mfma_f32_16x16x32_bf16 v[72:75], v[208:211], a[248:251], v[72:75]\n"
    "s_add_u32 s44, s45, %[sot1]\n"
    "s_add_u32 m0, %[m0t], 49152\n"
    "ds_read_b128 v[196:199], %[rdx2] offset:2048\n"
    "v_mfma_f32_16x16x32_bf16 v[76:79], v[208:211], a[252:255], v[76:79]\n"
    "buffer_load_dwordx4 %[vx0], %[srdx], s44 offen lds\n"
    "v_mfma_f32_16x16x32_bf16 v[92:95], v[212:215], a[252:255], v[92:95]\n"
    "ds_read_b128 v[200:203], %[rdx2] offset:4096\n"
    "v_mfma_f32_16x16x32_bf16 v[88:91], v[212:215], a[248:251], v[88:91]\n"
    "v_mfma_f32_16x16x32_bf16 v[84:87], v[212:215], a[244:247], v[84:87]\n"
    "ds_read_b128 v[204:207], %[rdx2] offset:6144\n"
    "v_mfma_f32_16x16x32_bf16 v[80:83], v[212:215], a[240:243], v[80:83]\n"
    "buffer_load_dwordx4 %[vx1], %[srdx], s44 offen offset:1024 lds\n"
    "v_mfma_f32_16x16x32_bf16 v[96:99], v[216:219], a[240:243], v[96:99]\n"
    "ds_read_b128 v[208:211], %[rdx3] offset:0\n"
    "v_mfma_f32_16x16x32_bf16 v[100:103], v[216:219], a[244:247], v[100:103]\n"
    "v_mfma_f32_16x16x32_bf16 v[104:107], v[216:219], a[248:251], v[104:107]\n"
    "v_mfma_f32_16x16x32_bf16 v[108:111], v[216:219], a[252:255], v[108:111]\n"
    "s_add_u32 s45, s45, 128\n"
    "s_and_b32 s45, s45, 1023\n"
    "ds_read_b128 v[212:215], %[rdx3] offset:2048\n"
    "v_mfma_f32_16x16x32_bf16 v[124:127], v[220:223], a[252:255], v[124:127]\n"
    "s_add_u32 s44, s45, %[sot1]\n"
    "s_add_u32 m0, %[m0t], 57344\n"
    "v_mfma_f32_16x16x32_bf16 v[120:123], v[220:223], a[248:251], v[120:123]\n"
    "buffer_load_dwordx4 %[vx0], %[srdx], s44 offen lds\n"
    "v_mfma_f32_16x16x32_bf16 v[116:119], v[220:223], a[244:247], v[116:119]\n"
    "v_mfma_f32_16x16x32_bf16 v[112:115], v[220:223], a[240:243], v[112:115]\n"
    "s_waitcnt lgkmcnt(4)\n"
    "v_mfma_f32_16x16x32_bf16 v[128:131], v[192:195], a[0:3], 0\n"
    "ds_read_b128 v[216:219], %[rdx3] offset:4096\n"
    "v_mfma_f32_16x16x32_bf16 v[132:135], v[192:195], a[4:7], 0\n"
    "v_mfma_f32_16x16x32_bf16 v[136:139], v[192:195], a[8:11], 0\n"
    "v_mfma_f32_16x16x32_bf16 v[140:143], v[192:195], a[12:15], 0\n"
    "buffer_load_dwordx4 %[vx1], %[srdx], s44 offen offset:1024 lds\n"
    "v_mfma_f32_16x16x32_bf16 v[156:159], v[196:199], a[12:15], 0\n"
    "ds_read_b128 v[220:223], %[rdx3] offset:6144\n"
    "v_mfma_f32_16x16x32_bf16 v[152:155], v[196:199], a[8:11], 0\n"
    "v_mfma_f32_16x16x32_bf16 v[148:151], v[196:199], a[4:7], 0\n"
    "v_mfma_f32_16x16x32_bf16 v[144:147], v[196:199], a[0:3], 0\n"
    "s_waitcnt lgkmcnt(4)\n"
    "v_mfma_f32_16x16x32_bf16 v[160:163], v[200:203], a[0:3], 0\n"
    "ds_read_b128 v[192:195], %[rdx2] offset:8192\n"
    "v_mfma_f32_16x16x32_bf16 v[164:167], v[200:203], a[4:7], 0\n"
    "v_mfma_f32_16x16x32_bf16 v[168:171], v[200:203], a[8:11], 0\n"
    "v_mfma_f32_16x16x32_bf16 v[172:175], v[200:203], a[12:15], 0\n"
    "v_mfma_f32_16x16x32_bf16 v[188:191], v[204:207], a[12:15], 0\n"
    "ds_read_b128 v[196:199], %[rdx2] offset:10240\n"
    "v_mfma_f32_16x16x32_bf16 v[184:187], v[204:207], a[8:11], 0\n"
    "v_mfma_f32_16x16x32_bf16 v[180:183], v[204:207], a[4:7], 0\n"
    "v_mfma_f32_16x16x32_bf16 v[176:179], v[204:207], a[0:3], 0\n"
    "s_waitcnt lgkmcnt(4)\n"
    "v_cvt_pk_bf16_f32 v0, v0, v1\n"
    "v_mfma_f32_16x16x32_bf16 v[128:131], v[208:211], a[16:19], v[128:131]\n"
    "ds_read_b128 v[200:203], %[rdx2] offset:12288\n"
    "v_mfma_f32_16x16x32_bf16 v[132:135], v[208:211], a[20:23], v[132:135]\n"
    "v_cvt_pk_bf16_f32 v1, v2, v3\n"
    "v_mfma_f32_16x16x32_bf16 v[136:139], v[208:211], a[24:27], v[136:139]\n"
    "v_cvt_pk_bf16_f32 v2, v16, v17\n"
    "v_mfma_f32_16x16x32_bf16 v[140:143], v[208:211], a[28:31], v[140:143]\n"
    "v_mfma_f32_16x16x32_bf16 v[156:159], v[212:215], a[28:31], v[156:159]\n"
    "ds_read_b128 v[204:207], %[rdx2] offset:14336\n"
    "v_mfma_f32_16x16x32_bf16 v[152:155], v[212:215], a[24:27], v[152:155]\n"
    "v_cvt_pk_bf16_f32 v3, v18, v19\n"
    "v_mfma_f32_16x16x32_bf16 v[148:151], v[212:215], a[20:23], v[148:151]\n"
    "v_mfma_f32_16x16x32_bf16 v[144:147], v[212:215], a[16:19], v[144:147]\n"
    "s_add_u32 s46, %[soo], 0\n"
    "buffer_store_dwordx4 v[0:3], %[vo], %[srdo], s46 offen sc1\n"
    "s_waitcnt lgkmcnt(4)\n"
    "v_mfma_f32_16x16x32_bf16 v[160:163], v[216:219], a[16:19], v[160:163]\n"
    "ds_read_b128 v[208:211], %[rdx3] offset:8192\n"
    "v_mfma_f32_16x16x32_bf16 v[164:167], v[216:219], a[20:23], v[164:167]\n"
    "v_mfma_f32_16x16x32_bf16 v[168:171], v[216:219], a[24:27], v[168:171]\n"
    "v_mfma_f32_16x16x32_bf16 v[172:175], v[216:219], a[28:31], v[172:175]\n"
    "v_mfma_f32_16x16x32_bf16 v[188:191], v[220:223], a[28:31], v[188:191]\n"
    "ds_read_b128 v[212:215], %[rdx3] offset:10240\n"
    "v_mfma_f32_16x16x32_bf16 v[184:187], v[220:223], a[24:27], v[184:187]\n"
    "v_mfma_f32_16x16x32_bf16 v[180:183], v[220:223], a[20:23], v[180:183]\n"
    "v_mfma_f32_16x16x32_bf16 v[176:179], v[220:223], a[16:19], v[176:179]\n"
    "s_waitcnt lgkmcnt(4)\n"
    "v_mfma_f32_16x16x32_bf16 v[128:131], v[192:195], a[32:35], v[128:131]\n"
    "ds_read_b128 v[216:219], %[rdx3] offset:12288\n"
    "v_mfma_f32_16x16x32_bf16 v[132:135], v[192:195], a[36:39], v[132:135]\n"
    "v_mfma_f32_16x16x32_bf16 v[136:139], v[192:195], a[40:43], v[136:139]\n"
    "v_mfma_f32_16x16x32_bf16 v[140:143], v[192:195], a[44:47], v[140:143]\n"
    "v_mfma_f32_16x16x32_bf16 v[156:159], v[196:199], a[44:47], v[156:159]\n"
    "ds_read_b128 v[220:223], %[rdx3] offset:14336\n"
    "v_cvt_pk_bf16_f32 v4, v4, v5\n"
    "v_mfma_f32_16x16x32_bf16 v[152:155], v[196:199], a[40:43], v[152:155]\n"
    "v_mfma_f32_16x16x32_bf16 v[148:151], v[196:199], a[36:39], v[148:151]\n"
    "v_cvt_pk_bf16_f32 v5, v6, v7\n"
    "v_mfma_f32_16x16x32_bf16 v[144:147], v[196:199], a[32:35], v[144:147]\n"
    "s_waitcnt lgkmcnt(4)\n"
    "v_cvt_pk_bf16_f32 v6, v20, v21\n"
    "v_mfma_f32_16x16x32_bf16 v[160:163], v[200:203], a[32:35], v[160:163]\n"
    "ds_read_b128 v[192:195], %[rdx2] offset:16384\n"
    "v_mfma_f32_16x16x32_bf16 v[164:167], v[200:203], a[36:39], v[164:167]\n"
    "v_mfma_f32_16x16x32_bf16 v[168:171], v[200:203], a[40:43], v[168:171]\n"
    "v_cvt_pk_bf16_f32 v7, v22, v23\n"
    "v_mfma_f32_16x16x32_bf16 v[172:175], v[200:203], a[44:47], v[172:175]\n"
    "v_mfma_f32_16x16x32_bf16 v[188:191], v[204:207], a[44:47], v[188:191]\n"
    "s_add_u32 s46, %[soo], 1024\n"
    "ds_read_b128 v[196:199], %[rdx2] offset:18432\n"
    "buffer_store_dwordx4 v[4:7], %[vo], %[srdo], s46 offen sc1\n"
    "v_mfma_f32_16x16x32_bf16 v[184:187], v[204:207], a[40:43], v[184:187]\n"
    "v_mfma_f32_16x16x32_bf16 v[180:183], v[204:207], a[36:39], v[180:183]\n"
    "v_mfma_f32_16x16x32_bf16 v[176:179], v[204:207], a[32:35], v[176:179]\n"
    "s_waitcnt lgkmcnt(4)\n"
    "v_mfma_f32_16x16x32_bf16 v[128:131], v[208:211], a[48:51], v[128:131]\n"
    "ds_read_b128 v[200:203], %[rdx2] offset:20480\n"
    "v_mfma_f32_16x16x32_bf16 v[132:135], v[208:211], a[52:55], v[132:135]\n"
    "v_mfma_f32_16x16x32_bf16 v[136:139], v[208:211], a[56:59], v[136:139]\n"
    "v_mfma_f32_16x16x32_bf16 v[140:143], v[208:211], a[60:63], v[140:143]\n"
    "v_mfma_f32_16x16x32_bf16 v[156:159], v[212:215], a[60:63], v[156:159]\n"
    "ds_read_b128 v[204:207], %[rdx2] offset:22528\n"
    "v_mfma_f32_16x16x32_bf16 v[152:155], v[212:215], a[56:59], v[152:155]\n"
    "v_mfma_f32_16x16x32_bf16 v[148:151], v[212:215], a[52:55], v[148:151]\n"
    "v_mfma_f32_16x16x32_bf16 v[144:147], v[212:215], a[48:51], v[144:147]\n"
    "s_waitcnt lgkmcnt(4)\n"
    "v_mfma_f32_16x16x32_bf16 v[160:163], v[216:219], a[48:51], v[160:163]\n"
    "ds_read_b128 v[208:211], %[rdx3] offset:16384\n"
    "v_mfma_f32_16x16x32_bf16 v[164:167], v[216:219], a[52:55], v[164:167]\n"
    "v_cvt_pk_bf16_f32 v8, v8, v9\n"
    "v_mfma_f32_16x16x32_bf16 v[168:171], v[216:219], a[56:59], v[168:171]\n"
    "v_cvt_pk_bf16_f32 v9, v10, v11\n"
    "v_mfma_f32_16x16x32_bf16 v[172:175], v[216:219], a[60:63], v[172:175]\n"
    "v_mfma_f32_16x16x32_bf16 v[188:191], v[220:223], a[60:63], v[188:191]\n"
    "ds_read_b128 v[212:215], %[rdx3] offset:18432\n"
    "v_cvt_pk_bf16_f32 v10, v24, v25\n"
    "v_mfma_f32_16x16x32_bf16 v[184:187], v[220:223], a[56:59], v[184:187]\n"
    "v_mfma_f32_16x16x32_bf16 v[180:183], v[220:223], a[52:55], v[180:183]\n"
    "v_cvt_pk_bf16_f32 v11, v26, v27\n"
    "v_mfma_f32_16x16x32_bf16 v[176:179], v[220:223], a[48:51], v[176:179]\n"
    "s_waitcnt lgkmcnt(4)\n"
    "v_mfma_f32_16x16x32_bf16 v[128:131], v[192:195], a[64:67], v[128:131]\n"
    "ds_read_b128 v[216:219], %[rdx3] offset:20480\n"
    "v_mfma_f32_16x16x32_bf16 v[132:135], v[192:195], a[68:71], v[132:135]\n"
    "s_add_u32 s46, %[soo], 2048\n"
    "buffer_store_dwordx4 v[8:11], %[vo], %[srdo], s46 offen sc1\n"
    "v_mfma_f32_16x16x32_bf16 v[136:139], v[192:195], a[72:75], v[136:139]\n"
    "v_mfma_f32_16x16x32_bf16 v[140:143], v[192:195], a[76:79], v[140:143]\n"
    "v_mfma_f32_16x16x32_bf16 v[156:159], v[196:199], a[76:79], v[156:159]\n"
    "ds_read_b128 v[220:223], %[rdx3] offset:22528\n"
    "v_mfma_f32_16x16x32_bf16 v[152:155], v[196:199], a[72:75], v[152:155]\n"
    "v_mfma_f32_16x16x32_bf16 v[148:151], v[196:199], a[68:71], v[148:151]\n"
    "v_mfma_f32_16x16x32_bf16 v[144:147], v[196:199], a[64:67], v[144:147]\n"
    "s_waitcnt lgkmcnt(4)\n"
    "v_mfma_f32_16x16x32_bf16 v[160:163], v[200:203], a[64:67], v[160:163]\n"
    "ds_read_b128 v[192:195], %[rdx2] offset:24576\n"
    "v_mfma_f32_16x16x32_bf16 v[164:167], v[200:203], a[68:71], v[164:167]\n"
    "v_mfma_f32_16x16x32_bf16 v[168:171], v[200:203], a[72:75], v[168:171]\n"
    "v_mfma_f32_16x16x32_bf16 v[172:175], v[200:203], a[76:79], v[172:175]\n"
    "v_mfma_f32_16x16x32_bf16 v[188:191], v[204:207], a[76:79], v[188:191]\n"
    "ds_read_b128 v[196:199], %[rdx2] offset:26624\n"
    "v_mfma_f32_16x16x32_bf16 v[184:187], v[204:207], a[72:75], v[184:187]\n"
    "v_cvt_pk_bf16_f32 v12, v12, v13\n"
    "v_mfma_f32_16x16x32_bf16 v[180:183], v[204:207], a[68:71], v[180:183]\n"
    "v_cvt_pk_bf16_f32 v13, v14, v15\n"
    "v_mfma_f32_16x16x32_bf16 v[176:179], v[204:207], a[64:67], v[176:179]\n"
    "s_waitcnt lgkmcnt(4)\n"
    "v_mfma_f32_16x16x32_bf16 v[128:131], v[208:211], a[80:83], v[128:131]\n"
    "ds_read_b128 v[200:203], %[rdx2] offset:28672\n"
    "v_cvt_pk_bf16_f32 v14, v28, v29\n"
    "v_mfma_f32_16x16x32_bf16 v[132:135], v[208:211], a[84:87], v[132:135]\n"
    "v_mfma_f32_16x16x32_bf16 v[136:139], v[208:211], a[88:91], v[136:139]\n"
    "v_cvt_pk_bf16_f32 v15, v30, v31\n"
    "v_mfma_f32_16x16x32_bf16 v[140:143], v[208:211], a[92:95], v[140:143]\n"
    "v_mfma_f32_16x16x32_bf16 v[156:159], v[212:215], a[92:95], v[156:159]\n"
    "ds_read_b128 v[204:207], %[rdx2] offset:30720\n"
    "v_mfma_f32_16x16x32_bf16 v[152:155], v[212:215], a[88:91], v[152:155]\n"
    "s_add_u32 s46, %[soo], 3072\n"
    "buffer_store_dwordx4 v[12:15], %[vo], %[srdo], s46 offen sc1\n"
    "v_mfma_f32_16x16x32_bf16 v[148:151], v[212:215], a[84:87], v[148:151]\n"
    "v_mfma_f32_16x16x32_bf16 v[144:147], v[212:215], a[80:83], v[144:147]\n"
    "s_waitcnt lgkmcnt(4)\n"
    "v_mfma_f32_16x16x32_bf16 v[160:163], v[216:219], a[80:83], v[160:163]\n"
    "ds_read_b128 v[208:211], %[rdx3] offset:24576\n"
    "v_mfma_f32_16x16x32_bf16 v[164:167], v[216:219], a[84:87], v[164:167]\n"
    "buffer_load_dword %[vdum], %[vpf], %[srdp], 0 offen ; dummy\n"
    "v_mfma_f32_16x16x32_bf16 v[168:171], v[216:219], a[88:91], v[168:171]\n"
    "v_mfma_f32_16x16x32_bf16 v[172:175], v[216:219], a[92:95], v[172:175]\n"
    "v_mfma_f32_16x16x32_bf16 v[188:191], v[220:223], a[92:95], v[188:191]\n"
    "ds_read_b128 v[212:215], %[rdx3] offset:26624\n"
    "v_mfma_f32_16x16x32_bf16 v[184:187], v[220:223], a[88:91], v[184:187]\n"
    "v_mfma_f32_16x16x32_bf16 v[180:183], v[220:223], a[84:87], v[180:183]\n"
    "v_mfma_f32_16x16x32_bf16 v[176:179], v[220:223], a[80:83], v[176:179]\n"
    "s_waitcnt lgkmcnt(4)\n"
    "v_mfma_f32_16x16x32_bf16 v[128:131], v[192:195], a[96:99], v[128:131]\n"
    "ds_read_b128 v[216:219], %[rdx3] offset:28672\n"
    "v_mfma_f32_16x16x32_bf16 v[132:135], v[192:195], a[100:103], v[132:135]\n"
    "v_mfma_f32_16x16x32_bf16 v[136:139], v[192:195], a[104:107], v[136:139]\n"
    "v_cvt_pk_bf16_f32 v32, v32, v33\n"
    "v_mfma_f32_16x16x32_bf16 v[140:143], v[192:195], a[108:111], v[140:143]\n"
    "v_cvt_pk_bf16_f32 v33, v34, v35\n"
    "v_mfma_f32_16x16x32_bf16 v[156:159], v[196:199], a[108:111], v[156:159]\n"
    "ds_read_b128 v[220:223], %[rdx3] offset:30720\n"
    "v_mfma_f32_16x16x32_bf16 v[152:155], v[196:199], a[104:107], v[152:155]\n"
    "v_cvt_pk_bf16_f32 v34, v48, v49\n"
    "v_mfma_f32_16x16x32_bf16 v[148:151], v[196:199], a[100:103], v[148:151]\n"
    "v_mfma_f32_16x16x32_bf16 v[144:147], v[196:199], a[96:99], v[144:147]\n"
    "s_waitcnt lgkmcnt(4)\n"
    "v_cvt_pk_bf16_f32 v35, v50, v51\n"
    "v_mfma_f32_16x16x32_bf16 v[160:163], v[200:203], a[96:99], v[160:163]\n"
    "ds_read_b128 v[192:195], %[rdx2] offset:32768\n"
    "v_mfma_f32_16x16x32_bf16 v[164:167], v[200:203], a[100:103], v[164:167]\n"
    "v_mfma_f32_16x16x32_bf16 v[168:171], v[200:203], a[104:107], v[168:171]\n"
    "s_add_u32 s46, %[soo], 8192\n"
    "buffer_store_dwordx4 v[32:35], %[vo], %[srdo], s46 offen sc1\n"
    "v_mfma_f32_16x16x32_bf16 v[172:175], v[200:203], a[108:111], v[172:175]\n"
    "v_mfma_f32_16x16x32_bf16 v[188:191], v[204:207], a[108:111], v[188:191]\n"
    "ds_read_b128 v[196:199], %[rdx2] offset:34816\n"
    "v_mfma_f32_16x16x32_bf16 v[184:187], v[204:207], a[104:107], v[184:187]\n"
    "v_mfma_f32_16x16x32_bf16 v[180:183], v[204:207], a[100:103], v[180:183]\n"
    "v_mfma_f32_16x16x32_bf16 v[176:179], v[204:207], a[96:99], v[176:179]\n"
    "s_waitcnt lgkmcnt(4)\n"
    "v_mfma_f32_16x16x32_bf16 v[128:131], v[208:211], a[112:115], v[128:131]\n"
    "ds_read_b128 v[200:203], %[rdx2] offset:36864\n"
    "v_mfma_f32_16x16x32_bf16 v[132:135], v[208:211], a[116:119], v[132:135]\n"
    "v_mfma_f32_16x16x32_bf16 v[136:139], v[208:211], a[120:123], v[136:139]\n"
    "v_mfma_f32_16x16x32_bf16 v[140:143], v[208:211], a[124:127], v[140:143]\n"
    "v_mfma_f32_16x16x32_bf16 v[156:159], v[212:215], a[124:127], v[156:159]\n"
    "ds_read_b128 v[204:207], %[rdx2] offset:38912\n"
    "v_mfma_f32_16x16x32_bf16 v[152:155], v[212:215], a[120:123], v[152:155]\n"
    "v_mfma_f32_16x16x32_bf16 v[148:151], v[212:215], a[116:119], v[148:151]\n"
    "v_mfma_f32_16x16x32_bf16 v[144:147], v[212:215], a[112:115], v[144:147]\n"
    "s_waitcnt lgkmcnt(4)\n"
    "v_cvt_pk_bf16_f32 v36, v36, v37\n"
    "v_mfma_f32_16x16x32_bf16 v[160:163], v[216:219], a[112:115], v[160:163]\n"
    "ds_read_b128 v[208:211], %[rdx3] offset:32768\n"
    "v_mfma_f32_16x16x32_bf16 v[164:167], v[216:219], a[116:119], v[164:167]\n"
    "v_cvt_pk_bf16_f32 v37, v38, v39\n"
    "v_mfma_f32_16x16x32_bf16 v[168:171], v[216:219], a[120:123], v[168:171]\n"
    "v_cvt_pk_bf16_f32 v38, v52, v53\n"
    "v_mfma_f32_16x16x32_bf16 v[172:175], v[216:219], a[124:127], v[172:175]\n"
    "v_mfma_f32_16x16x32_bf16 v[188:191], v[220:223], a[124:127], v[188:191]\n"
    "ds_read_b128 v[212:215], %[rdx3] offset:34816\n"
    "v_mfma_f32_16x16x32_bf16 v[184:187], v[220:223], a[120:123], v[184:187]\n"
    "v_cvt_pk_bf16_f32 v39, v54, v55\n"
    "v_mfma_f32_16x16x32_bf16 v[180:183], v[220:223], a[116:119], v[180:183]\n"
    "v_mfma_f32_16x16x32_bf16 v[176:179], v[220:223], a[112:115], v[176:179]\n"
    "s_add_u32 s46, %[soo], 9216\n"
    "buffer_store_dwordx4 v[36:39], %[vo], %[srdo], s46 offen sc1\n"
    "s_waitcnt lgkmcnt(4)\n"
    "v_mfma_f32_16x16x32_bf16 v[128:131], v[192:195], a[128:131], v[128:131]\n"
    "ds_read_b128 v[216:219], %[rdx3] offset:36864\n"
    "v_mfma_f32_16x16x32_bf16 v[132:135], v[192:195], a[132:135], v[132:135]\n"
    "buffer_load_dword %[vdum], %[vpf], %[srdp], s49 offen ; dummy\n"
    "v_mfma_f32_16x16x32_bf16 v[136:139], v[192:195], a[136:139], v[136:139]\n"
    "v_mfma_f32_16x16x32_bf16 v[140:143], v[192:195], a[140:143], v[140:143]\n"
    "v_mfma_f32_16x16x32_bf16 v[156:159], v[196:199], a[140:143], v[156:159]\n"
    "ds_read_b128 v[220:223], %[rdx3] offset:38912\n"
    "v_mfma_f32_16x16x32_bf16 v[152:155], v[196:199], a[136:139], v[152:155]\n"
    "v_mfma_f32_16x16x32_bf16 v[148:151], v[196:199], a[132:135], v[148:151]\n"
    "v_mfma_f32_16x16x32_bf16 v[144:147], v[196:199], a[128:131], v[144:147]\n"
    "s_waitcnt lgkmcnt(4)\n"
    "v_mfma_f32_16x16x32_bf16 v[160:163], v[200:203], a[128:131], v[160:163]\n"
    "ds_read_b128 v[192:195], %[rdx2] offset:40960\n"
    "v_mfma_f32_16x16x32_bf16 v[164:167], v[200:203], a[132:135], v[164:167]\n"
    "v_mfma_f32_16x16x32_bf16 v[168:171], v[200:203], a[136:139], v[168:171]\n"
    "v_mfma_f32_16x16x32_bf16 v[172:175], v[200:203], a[140:143], v[172:175]\n"
    "v_cvt_pk_bf16_f32 v40, v40, v41\n"
    "v_mfma_f32_16x16x32_bf16 v[188:191], v[204:207], a[140:143], v[188:191]\n"
    "ds_read_b128 v[196:199], %[rdx2] offset:43008\n"
    "v_mfma_f32_16x16x32_bf16 v[184:187], v[204:207], a[136:139], v[184:187]\n"
    "v_cvt_pk_bf16_f32 v41, v42, v43\n"
    "v_mfma_f32_16x16x32_bf16 v[180:183], v[204:207], a[132:135], v[180:183]\n"
    "v_cvt_pk_bf16_f32 v42, v56, v57\n"
    "v_mfma_f32_16x16x32_bf16 v[176:179], v[204:207], a[128:131], v[176:179]\n"
    "s_waitcnt lgkmcnt(4)\n"
    "v_mfma_f32_16x16x32_bf16 v[128:131], v[208:211], a[144:147], v[128:131]\n"
    "ds_read_b128 v[200:203], %[rdx2] offset:45056\n"
    "v_mfma_f32_16x16x32_bf16 v[132:135], v[208:211], a[148:151], v[132:135]\n"
    "v_cvt_pk_bf16_f32 v43, v58, v59\n"
    "v_mfma_f32_16x16x32_bf16 v[136:139], v[208:211], a[152:155], v[136:139]\n"
    "v_mfma_f32_16x16x32_bf16 v[140:143], v[208:211], a[156:159], v[140:143]\n"
    "s_add_u32 s46, %[soo], 10240\n"
    "buffer_store_dwordx4 v[40:43], %[vo], %[srdo], s46 offen sc1\n"
    "v_mfma_f32_16x16x32_bf16 v[156:159], v[212:215], a[156:159], v[156:159]\n"
    "ds_read_b128 v[204:207], %[rdx2] offset:47104\n"
    "v_mfma_f32_16x16x32_bf16 v[152:155], v[212:215], a[152:155], v[152:155]\n"
    "v_mfma_f32_16x16x32_bf16 v[148:151], v[212:215], a[148:151], v[148:151]\n"
    "v_mfma_f32_16x16x32_bf16 v[144:147], v[212:215], a[144:147], v[144:147]\n"
    "s_waitcnt lgkmcnt(4)\n"
    "v_mfma_f32_16x16x32_bf16 v[160:163], v[216:219], a[144:147], v[160:163]\n"
    "ds_read_b128 v[208:211], %[rdx3] offset:40960\n"
    "v_mfma_f32_16x16x32_bf16 v[164:167], v[216:219], a[148:151], v[164:167]\n"
    "v_mfma_f32_16x16x32_bf16 v[168:171], v[216:219], a[152:155], v[168:171]\n"
    "v_mfma_f32_16x16x32_bf16 v[172:175], v[216:219], a[156:159], v[172:175]\n"
    "v_mfma_f32_16x16x32_bf16 v[188:191], v[220:223], a[156:159], v[188:191]\n"
    "ds_read_b128 v[212:215], %[rdx3] offset:43008\n"
    "v_mfma_f32_16x16x32_bf16 v[184:187], v[220:223], a[152:155], v[184:187]\n"
    "v_mfma_f32_16x16x32_bf16 v[180:183], v[220:223], a[148:151], v[180:183]\n"
    "v_mfma_f32_16x16x32_bf16 v[176:179], v[220:223], a[144:147], v[176:179]\n"
    "s_waitcnt lgkmcnt(4)\n"
    "v_mfma_f32_16x16x32_bf16 v[128:131], v[192:195], a[160:163], v[128:131]\n"
    "ds_read_b128 v[216:219], %[rdx3] offset:45056\n"
    "v_cvt_pk_bf16_f32 v44, v44, v45\n"
    "v_mfma_f32_16x16x32_bf16 v[132:135], v[192:195], a[164:167], v[132:135]\n"
    "v_mfma_f32_16x16x32_bf16 v[136:139], v[192:195], a[168:171], v[136:139]\n"
    "v_cvt_pk_bf16_f32 v45, v46, v47\n"
    "v_mfma_f32_16x16x32_bf16 v[140:143], v[192:195], a[172:175], v[140:143]\n"
    "v_cvt_pk_bf16_f32 v46, v60, v61\n"
    "v_mfma_f32_16x16x32_bf16 v[156:159], v[196:199], a[172:175], v[156:159]\n"
    "ds_read_b128 v[220:223], %[rdx3] offset:47104\n"
    "v_mfma_f32_16x16x32_bf16 v[152:155], v[196:199], a[168:171], v[152:155]\n"
    "v_mfma_f32_16x16x32_bf16 v[148:151], v[196:199], a[164:167], v[148:151]\n"
    "v_cvt_pk_bf16_f32 v47, v62, v63\n"
    "v_mfma_f32_16x16x32_bf16 v[144:147], v[196:199], a[160:163], v[144:147]\n"
    "s_waitcnt lgkmcnt(4)\n"
    "v_mfma_f32_16x16x32_bf16 v[160:163], v[200:203], a[160:163], v[160:163]\n"
    "s_add_u32 s46, %[soo], 11264\n"
    "ds_read_b128 v[192:195], %[rdx2] offset:49152\n"
    "buffer_store_dwordx4 v[44:47], %[vo], %[srdo], s46 offen sc1\n"
    "v_mfma_f32_16x16x32_bf16 v[164:167], v[200:203], a[164:167], v[164:167]\n"
    "v_mfma_f32_16x16x32_bf16 v[168:171], v[200:203], a[168:171], v[168:171]\n"
    "v_mfma_f32_16x16x32_bf16 v[172:175], v[200:203], a[172:175], v[172:175]\n"
    "v_mfma_f32_16x16x32_bf16 v[188:191], v[204:207], a[172:175], v[188:191]\n"
    "ds_read_b128 v[196:199], %[rdx2] offset:51200\n"
    "v_mfma_f32_16x16x32_bf16 v[184:187], v[204:207], a[168:171], v[184:187]\n"
    "v_mfma_f32_16x16x32_bf16 v[180:183], v[204:207], a[164:167], v[180:183]\n"
    "v_mfma_f32_16x16x32_bf16 v[176:179], v[204:207], a[160:163], v[176:179]\n"
    "s_waitcnt lgkmcnt(4)\n"
    "v_mfma_f32_16x16x32_bf16 v[128:131], v[208:211], a[176:179], v[128:131]\n"
    "ds_read_b128 v[200:203], %[rdx2] offset:53248\n"
    "v_mfma_f32_16x16x32_bf16 v[132:135], v[208:211], a[180:183], v[132:135]\n"
    "v_mfma_f32_16x16x32_bf16 v[136:139], v[208:211], a[184:187], v[136:139]\n"
    "v_mfma_f32_16x16x32_bf16 v[140:143], v[208:211], a[188:191], v[140:143]\n"
    "v_mfma_f32_16x16x32_bf16 v[156:159], v[212:215], a[188:191], v[156:159]\n"
    "ds_read_b128 v[204:207], %[rdx2] offset:55296\n"
    "v_mfma_f32_16x16x32_bf16 v[152:155], v[212:215], a[184:187], v[152:155]\n"
    "v_cvt_pk_bf16_f32 v64, v64, v65\n"
    "v_mfma_f32_16x16x32_bf16 v[148:151], v[212:215], a[180:183], v[148:151]\n"
    "v_cvt_pk_bf16_f32 v65, v66, v67\n"
    "v_mfma_f32_16x16x32_bf16 v[144:147], v[212:215], a[176:179], v[144:147]\n"
    "s_waitcnt lgkmcnt(4)\n"
    "v_mfma_f32_16x16x32_bf16 v[160:163], v[216:219], a[176:179], v[160:163]\n"
    "ds_read_b128 v[208:211], %[rdx3] offset:49152\n"
    "v_cvt_pk_bf16_f32 v66, v80, v81\n"
    "v_mfma_f32_16x16x32_bf16 v[164:167], v[216:219], a[180:183], v[164:167]\n"
    "v_mfma_f32_16x16x32_bf16 v[168:171], v[216:219], a[184:187], v[168:171]\n"
    "v_cvt_pk_bf16_f32 v67, v82, v83\n"
    "v_mfma_f32_16x16x32_bf16 v[172:175], v[216:219], a[188:191], v[172:175]\n"
    "v_mfma_f32_16x16x32_bf16 v[188:191], v[220:223], a[188:191], v[188:191]\n"
    "ds_read_b128 v[212:215], %[rdx3] offset:51200\n"
    "v_mfma_f32_16x16x32_bf16 v[184:187], v[220:223], a[184:187], v[184:187]\n"
    "s_add_u32 s46, %[soo], 16384\n"
    "buffer_store_dwordx4 v[64:67], %[vo], %[srdo], s46 offen sc1\n"
    "v_mfma_f32_16x16x32_bf16 v[180:183], v[220:223], a[180:183], v[180:183]\n"
    "v_mfma_f32_16x16x32_bf16 v[176:179], v[220:223], a[176:179], v[176:179]\n"
    "s_waitcnt lgkmcnt(4)\n"
    "v_mfma_f32_16x16x32_bf16 v[128:131], v[192:195], a[192:195], v[128:131]\n"
    "ds_read_b128 v[216:219], %[rdx3] offset:53248\n"
    "v_mfma_f32_16x16x32_bf16 v[132:135], v[192:195], a[196:199], v[132:135]\n"
    "v_mfma_f32_16x16x32_bf16 v[136:139], v[192:195], a[200:203], v[136:139]\n"
    "v_mfma_f32_16x16x32_bf16 v[140:143], v[192:195], a[204:207], v[140:143]\n"
    "v_mfma_f32_16x16x32_bf16 v[156:159], v[196:199], a[204:207], v[156:159]\n"
    "ds_read_b128 v[220:223], %[rdx3] offset:55296\n"
    "v_mfma_f32_16x16x32_bf16 v[152:155], v[196:199], a[200:203], v[152:155]\n"
    "v_mfma_f32_16x16x32_bf16 v[148:151], v[196:199], a[196:199], v[148:151]\n"
    "v_mfma_f32_16x16x32_bf16 v[144:147], v[196:199], a[192:195], v[144:147]\n"
    "s_waitcnt lgkmcnt(4)\n"
    "v_mfma_f32_16x16x32_bf16 v[160:163], v[200:203], a[192:195], v[160:163]\n"
    "ds_read_b128 v[192:195], %[rdx2] offset:57344\n"
    "v_mfma_f32_16x16x32_bf16 v[164:167], v[200:203], a[196:199], v[164:167]\n"
    "v_cvt_pk_bf16_f32 v68, v68, v69\n"
    "v_mfma_f32_16x16x32_bf16 v[168:171], v[200:203], a[200:203], v[168:171]\n"
    "v_cvt_pk_bf16_f32 v69, v70, v71\n"
    "v_mfma_f32_16x16x32_bf16 v[172:175], v[200:203], a[204:207], v[172:175]\n"
    "v_mfma_f32_16x16x32_bf16 v[188:191], v[204:207], a[204:207], v[188:191]\n"
    "ds_read_b128 v[196:199], %[rdx2] offset:59392\n"
    "v_cvt_pk_bf16_f32 v70, v84, v85\n"
    "v_mfma_f32_16x16x32_bf16 v[184:187], v[204:207], a[200:203], v[184:187]\n"
    "v_mfma_f32_16x16x32_bf16 v[180:183], v[204:207], a[196:199], v[180:183]\n"
    "v_cvt_pk_bf16_f32 v71, v86, v87\n"
    "v_mfma_f32_16x16x32_bf16 v[176:179], v[204:207], a[192:195], v[176:179]\n"
    "s_waitcnt lgkmcnt(4)\n"
    "v_mfma_f32_16x16x32_bf16 v[128:131], v[208:211], a[208:211], v[128:131]\n"
    "ds_read_b128 v[200:203], %[rdx2] offset:61440\n"
    "v_mfma_f32_16x16x32_bf16 v[132:135], v[208:211], a[212:215], v[132:135]\n"
    "s_add_u32 s46, %[soo], 17408\n"
    "buffer_store_dwordx4 v[68:71], %[vo], %[srdo], s46 offen sc1\n"
    "v_mfma_f32_16x16x32_bf16 v[136:139], v[208:211], a[216:219], v[136:139]\n"
    "v_mfma_f32_16x16x32_bf16 v[140:143], v[208:211], a[220:223], v[140:143]\n"
    "v_mfma_f32_16x16x32_bf16 v[156:159], v[212:215], a[220:223], v[156:159]\n"
    "ds_read_b128 v[204:207], %[rdx2] offset:63488\n"
    "v_mfma_f32_16x16x32_bf16 v[152:155], v[212:215], a[216:219], v[152:155]\n"
    "v_mfma_f32_16x16x32_bf16 v[148:151], v[212:215], a[212:215], v[148:151]\n"
    "v_mfma_f32_16x16x32_bf16 v[144:147], v[212:215], a[208:211], v[144:147]\n"
    "s_waitcnt lgkmcnt(4)\n"
    "v_mfma_f32_16x16x32_bf16 v[160:163], v[216:219], a[208:211], v[160:163]\n"
    "ds_read_b128 v[208:211], %[rdx3] offset:57344\n"
    "v_mfma_f32_16x16x32_bf16 v[164:167], v[216:219], a[212:215], v[164:167]\n"
    "v_mfma_f32_16x16x32_bf16 v[168:171], v[216:219], a[216:219], v[168:171]\n"
    "v_mfma_f32_16x16x32_bf16 v[172:175], v[216:219], a[220:223], v[172:175]\n"
    "v_mfma_f32_16x16x32_bf16 v[188:191], v[220:223], a[220:223], v[188:191]\n"
    "ds_read_b128 v[212:215], %[rdx3] offset:59392\n"
    "v_mfma_f32_16x16x32_bf16 v[184:187], v[220:223], a[216:219], v[184:187]\n"
    "v_mfma_f32_16x16x32_bf16 v[180:183], v[220:223], a[212:215], v[180:183]\n"
    "v_cvt_pk_bf16_f32 v72, v72, v73\n"
    "v_mfma_f32_16x16x32_bf16 v[176:179], v[220:223], a[208:211], v[176:179]\n"
    "s_waitcnt vmcnt(12)\n"
    "s_barrier\n"
    "s_waitcnt lgkmcnt(4)\n"
    "v_mfma_f32_16x16x32_bf16 v[128:131], v[192:195], a[224:227], v[128:131]\n"
    "ds_read_b128 v[216:219], %[rdx3] offset:61440\n"
    "v_cvt_pk_bf16_f32 v73, v74, v75\n"
    "v_mfma_f32_16x16x32_bf16 v[132:135], v[192:195], a[228:231], v[132:135]\n"
    "v_mfma_f32_16x16x32_bf16 v[136:139], v[192:195], a[232:235], v[136:139]\n"
    "v_cvt_pk_bf16_f32 v74, v88, v89\n"
    "v_mfma_f32_16x16x32_bf16 v[140:143], v[192:195], a[236:239], v[140:143]\n"
    "v_cvt_pk_bf16_f32 v75, v90, v91\n"
    "v_mfma_f32_16x16x32_bf16 v[156:159], v[196:199], a[236:239], v[156:159]\n"
    "ds_read_b128 v[220:223], %[rdx3] offset:63488\n"
    "v_mfma_f32_16x16x32_bf16 v[152:155], v[196:199], a[232:235], v[152:155]\n"
    "v_mfma_f32_16x16x32_bf16 v[148:151], v[196:199], a[228:231], v[148:151]\n"
    "s_add_u32 s46, %[soo], 18432\n"
    "buffer_store_dwordx4 v[72:75], %[vo], %[srdo], s46 offen sc1\n"
    "v_mfma_f32_16x16x32_bf16 v[144:147], v[196:199], a[224:227], v[144:147]\n"
    "s_waitcnt lgkmcnt(4)\n"
    "v_mfma_f32_16x16x32_bf16 v[160:163], v[200:203], a[224:227], v[160:163]\n"
    "ds_read_b128 v[192:195], %[rdx0] offset:0\n"
    "v_mfma_f32_16x16x32_bf16 v[164:167], v[200:203], a[228:231], v[164:167]\n"
    "v_mfma_f32_16x16x32_bf16 v[168:171], v[200:203], a[232:235], v[168:171]\n"
    "v_mfma_f32_16x16x32_bf16 v[172:175], v[200:203], a[236:239], v[172:175]\n"
    "v_mfma_f32_16x16x32_bf16 v[188:191], v[204:207], a[236:239], v[188:191]\n"
    "ds_read_b128 v[196:199], %[rdx0] offset:2048\n"
    "v_mfma_f32_16x16x32_bf16 v[184:187], v[204:207], a[232:235], v[184:187]\n"
    "v_mfma_f32_16x16x32_bf16 v[180:183], v[204:207], a[228:231], v[180:183]\n"
    "v_mfma_f32_16x16x32_bf16 v[176:179], v[204:207], a[224:227], v[176:179]\n"
    "s_waitcnt lgkmcnt(4)\n"
    "v_mfma_f32_16x16x32_bf16 v[128:131], v[208:211], a[240:243], v[128:131]\n"
    "ds_read_b128 v[200:203], %[rdx0] offset:4096\n"
    "v_mfma_f32_16x16x32_bf16 v[132:135], v[208:211], a[244:247], v[132:135]\n"
    "v_mfma_f32_16x16x32_bf16 v[136:139], v[208:211], a[248:251], v[136:139]\n"
    "v_mfma_f32_16x16x32_bf16 v[140:143], v[208:211], a[252:255], v[140:143]\n"
    "v_cvt_pk_bf16_f32 v76, v76, v77\n"
    "v_mfma_f32_16x16x32_bf16 v[156:159], v[212:215], a[252:255], v[156:159]\n"
    "ds_read_b128 v[204:207], %[rdx0] offset:6144\n"
    "v_mfma_f32_16x16x32_bf16 v[152:155], v[212:215], a[248:251], v[152:155]\n"
    "v_cvt_pk_bf16_f32 v77, v78, v79\n"
    "v_mfma_f32_16x16x32_bf16 v[148:151], v[212:215], a[244:247], v[148:151]\n"
    "v_cvt_pk_bf16_f32 v78, v92, v93\n"
    "v_mfma_f32_16x16x32_bf16 v[144:147], v[212:215], a[240:243], v[144:147]\n"
    "s_waitcnt lgkmcnt(4)\n"
    "v_mfma_f32_16x16x32_bf16 v[160:163], v[216:219], a[240:243], v[160:163]\n"
    "ds_read_b128 v[208:211], %[rdx1] offset:0\n"
    "v_mfma_f32_16x16x32_bf16 v[164:167], v[216:219], a[244:247], v[164:167]\n"
    "v_cvt_pk_bf16_f32 v79, v94, v95\n"
    "v_mfma_f32_16x16x32_bf16 v[168:171], v[216:219], a[248:251], v[168:171]\n"
    "v_mfma_f32_16x16x32_bf16 v[172:175], v[216:219], a[252:255], v[172:175]\n"
    "s_add_u32 s46, %[soo], 19456\n"
    "buffer_store_dwordx4 v[76:79], %[vo], %[srdo], s46 offen sc1\n"
    "v_mfma_f32_16x16x32_bf16 v[188:191], v[220:223], a[252:255], v[188:191]\n"
    "ds_read_b128 v[212:215], %[rdx1] offset:2048\n"
    "v_mfma_f32_16x16x32_bf16 v[184:187], v[220:223], a[248:251], v[184:187]\n"
    "v_mfma_f32_16x16x32_bf16 v[180:183], v[220:223], a[244:247], v[180:183]\n"
    "v_mfma_f32_16x16x32_bf16 v[176:179], v[220:223], a[240:243], v[176:179]\n"
    "s_waitcnt lgkmcnt(4)\n"
    "v_mfma_f32_16x16x32_bf16 v[0:3], v[192:195], a[0:3], 0\n"
    "ds_read_b128 v[216:219], %[rdx1] offset:4096\n"
    "v_mfma_f32_16x16x32_bf16 v[4:7], v[192:195], a[4:7], 0\n"
    "v_mfma_f32_16x16x32_bf16 v[8:11], v[192:195], a[8:11], 0\n"
    "v_mfma_f32_16x16x32_bf16 v[12:15], v[192:195], a[12:15], 0\n"
    "v_mfma_f32_16x16x32_bf16 v[28:31], v[196:199], a[12:15], 0\n"
    "ds_read_b128 v[220:223], %[rdx1] offset:6144\n"
    "v_mfma_f32_16x16x32_bf16 v[24:27], v[196:199], a[8:11], 0\n"
    "v_mfma_f32_16x16x32_bf16 v[20:23], v[196:199], a[4:7], 0\n"
    "v_mfma_f32_16x16x32_bf16 v[16:19], v[196:199], a[0:3], 0\n"
    "s_waitcnt lgkmcnt(4)\n"
    "v_mfma_f32_16x16x32_bf16 v[32:35], v[200:203], a[0:3], 0\n"
    "ds_read_b128 v[192:195], %[rdx0] offset:8192\n"
    "v_mfma_f32_16x16x32_bf16 v[36:39], v[200:203], a[4:7], 0\n"
    "v_mfma_f32_16x16x32_bf16 v[40:43], v[200:203], a[8:11], 0\n"
    "v_mfma_f32_16x16x32_bf16 v[44:47], v[200:203], a[12:15], 0\n"
    "v_mfma_f32_16x16x32_bf16 v[60:63], v[204:207], a[12:15], 0\n"
    "ds_read_b128 v[196:199], %[rdx0] offset:10240\n"
    "v_mfma_f32_16x16x32_bf16 v[56:59], v[204:207], a[8:11], 0\n"
    "v_cvt_pk_bf16_f32 v96, v96, v97\n"
    "v_mfma_f32_16x16x32_bf16 v[52:55], v[204:207], a[4:7], 0\n"
    "v_cvt_pk_bf16_f32 v97, v98, v99\n"
    "v_mfma_f32_16x16x32_bf16 v[48:51], v[204:207], a[0:3], 0\n"
    "s_waitcnt lgkmcnt(4)\n"
    "v_mfma_f32_16x16x32_bf16 v[0:3], v[208:211], a[16:19], v[0:3]\n"
    "ds_read_b128 v[200:203], %[rdx0] offset:12288\n"
    "v_cvt_pk_bf16_f32 v98, v112, v113\n"
    "v_mfma_f32_16x16x32_bf16 v[4:7], v[208:211], a[20:23], v[4:7]\n"
    "v_mfma_f32_16x16x32_bf16 v[8:11], v[208:211], a[24:27], v[8:11]\n"
    "v_cvt_pk_bf16_f32 v99, v114, v115\n"
    "v_mfma_f32_16x16x32_bf16 v[12:15], v[208:211], a[28:31], v[12:15]\n"
    "v_mfma_f32_16x16x32_bf16 v[28:31], v[212:215], a[28:31], v[28:31]\n"
    "ds_read_b128 v[204:207], %[rdx0] offset:14336\n"
    "v_mfma_f32_16x16x32_bf16 v[24:27], v[212:215], a[24:27], v[24:27]\n"
    "s_add_u32 s46, %[soo], 24576\n"
    "buffer_store_dwordx4 v[96:99], %[vo], %[srdo], s46 offen sc1\n"
    "v_mfma_f32_16x16x32_bf16 v[20:23], v[212:215], a[20:23], v[20:23]\n"
    "v_mfma_f32_16x16x32_bf16 v[16:19], v[212:215], a[16:19], v[16:19]\n"
    "s_waitcnt lgkmcnt(4)\n"
    "v_mfma_f32_16x16x32_bf16 v[32:35], v[216:219], a[16:19], v[32:35]\n"
    "ds_read_b128 v[208:211], %[rdx1] offset:8192\n"
    "v_mfma_f32_16x16x32_bf16 v[36:39], v[216:219], a[20:23], v[36:39]\n"
    "v_mfma_f32_16x16x32_bf16 v[40:43], v[216:219], a[24:27], v[40:43]\n"
    "v_mfma_f32_16x16x32_bf16 v[44:47], v[216:219], a[28:31], v[44:47]\n"
    "v_mfma_f32_16x16x32_bf16 v[60:63], v[220:223], a[28:31], v[60:63]\n"
    "ds_read_b128 v[212:215], %[rdx1] offset:10240\n"
    "v_mfma_f32_16x16x32_bf16 v[56:59], v[220:223], a[24:27], v[56:59]\n"
    "v_mfma_f32_16x16x32_bf16 v[52:55], v[220:223], a[20:23], v[52:55]\n"
    "v_mfma_f32_16x16x32_bf16 v[48:51], v[220:223], a[16:19], v[48:51]\n"
    "s_waitcnt lgkmcnt(4)\n"
    "v_mfma_f32_16x16x32_bf16 v[0:3], v[192:195], a[32:35], v[0:3]\n"
    "ds_read_b128 v[216:219], %[rdx1] offset:12288\n"
    "v_mfma_f32_16x16x32_bf16 v[4:7], v[192:195], a[36:39], v[4:7]\n"
    "v_mfma_f32_16x16x32_bf16 v[8:11], v[192:195], a[40:43], v[8:11]\n"
    "v_cvt_pk_bf16_f32 v100, v100, v101\n"
    "v_mfma_f32_16x16x32_bf16 v[12:15], v[192:195], a[44:47], v[12:15]\n"
    "v_cvt_pk_bf16_f32 v101, v102, v103\n"
    "v_mfma_f32_16x16x32_bf16 v[28:31], v[196:199], a[44:47], v[28:31]\n"
    "ds_read_b128 v[220:223], %[rdx1] offset:14336\n"
    "v_mfma_f32_16x16x32_bf16 v[24:27], v[196:199], a[40:43], v[24:27]\n"
    "v_cvt_pk_bf16_f32 v102, v116, v117\n"
    "v_mfma_f32_16x16x32_bf16 v[20:23], v[196:199], a[36:39], v[20:23]\n"
    "v_mfma_f32_16x16x32_bf16 v[16:19], v[196:199], a[32:35], v[16:19]\n"
    "s_waitcnt lgkmcnt(4)\n"
    "v_cvt_pk_bf16_f32 v103, v118, v119\n"
    "v_mfma_f32_16x16x32_bf16 v[32:35], v[200:203], a[32:35], v[32:35]\n"
    "ds_read_b128 v[192:195], %[rdx0] offset:16384\n"
    "v_mfma_f32_16x16x32_bf16 v[36:39], v[200:203], a[36:39], v[36:39]\n"
    "v_mfma_f32_16x16x32_bf16 v[40:43], v[200:203], a[40:43], v[40:43]\n"
    "s_add_u32 s46, %[soo], 25600\n"
    "buffer_store_dwordx4 v[100:103], %[vo], %[srdo], s46 offen sc1\n"
    "v_mfma_f32_16x16x32_bf16 v[44:47], v[200:203], a[44:47], v[44:47]\n"
    "v_mfma_f32_16x16x32_bf16 v[60:63], v[204:207], a[44:47], v[60:63]\n"
    "ds_read_b128 v[196:199], %[rdx0] offset:18432\n"
    "v_mfma_f32_16x16x32_bf16 v[56:59], v[204:207], a[40:43], v[56:59]\n"
    "v_mfma_f32_16x16x32_bf16 v[52:55], v[204:207], a[36:39], v[52:55]\n"
    "v_mfma_f32_16x16x32_bf16 v[48:51], v[204:207], a[32:35], v[48:51]\n"
    "s_waitcnt lgkmcnt(4)\n"
    "v_mfma_f32_16x16x32_bf16 v[0:3], v[208:211], a[48:51], v[0:3]\n"
    "ds_read_b128 v[200:203], %[rdx0] offset:20480\n"
    "v_mfma_f32_16x16x32_bf16 v[4:7], v[208:211], a[52:55], v[4:7]\n"
    "v_mfma_f32_16x16x32_bf16 v[8:11], v[208:211], a[56:59], v[8:11]\n"
    "v_mfma_f32_16x16x32_bf16 v[12:15], v[208:211], a[60:63], v[12:15]\n"
    "v_mfma_f32_16x16x32_bf16 v[28:31], v[212:215], a[60:63], v[28:31]\n"
    "ds_read_b128 v[204:207], %[rdx0] offset:22528\n"
    "v_mfma_f32_16x16x32_bf16 v[24:27], v[212:215], a[56:59], v[24:27]\n"
    "v_mfma_f32_16x16x32_bf16 v[20:23], v[212:215], a[52:55], v[20:23]\n"
    "v_cvt_pk_bf16_f32 v104, v104, v105\n"
    "v_mfma_f32_16x16x32_bf16 v[16:19], v[212:215], a[48:51], v[16:19]\n"
    "s_waitcnt lgkmcnt(4)\n"
    "v_cvt_pk_bf16_f32 v105, v106, v107\n"
    "v_mfma_f32_16x16x32_bf16 v[32:35], v[216:219], a[48:51], v[32:35]\n"
    "ds_read_b128 v[208:211], %[rdx1] offset:16384\n"
    "v_mfma_f32_16x16x32_bf16 v[36:39], v[216:219], a[52:55], v[36:39]\n"
    "v_cvt_pk_bf16_f32 v106, v120, v121\n"
    "v_mfma_f32_16x16x32_bf16 v[40:43], v[216:219], a[56:59], v[40:43]\n"
    "v_mfma_f32_16x16x32_bf16 v[44:47], v[216:219], a[60:63], v[44:47]\n"
    "v_cvt_pk_bf16_f32 v107, v122, v123\n"
    "v_mfma_f32_16x16x32_bf16 v[60:63], v[220:223], a[60:63], v[60:63]\n"
    "ds_read_b128 v[212:215], %[rdx1] offset:18432\n"
    "v_mfma_f32_16x16x32_bf16 v[56:59], v[220:223], a[56:59], v[56:59]\n"
    "v_mfma_f32_16x16x32_bf16 v[52:55], v[220:223], a[52:55], v[52:55]\n"
    "s_add_u32 s46, %[soo], 26624\n"
    "buffer_store_dwordx4 v[104:107], %[vo], %[srdo], s46 offen sc1\n"
    "v_mfma_f32_16x16x32_bf16 v[48:51], v[220:223], a[48:51], v[48:51]\n"
    "s_waitcnt lgkmcnt(4)\n"
    "v_mfma_f32_16x16x32_bf16 v[0:3], v[192:195], a[64:67], v[0:3]\n"
    "ds_read_b128 v[216:219], %[rdx1] offset:20480\n"
    "v_mfma_f32_16x16x32_bf16 v[4:7], v[192:195], a[68:71], v[4:7]\n"
    "v_mfma_f32_16x16x32_bf16 v[8:11], v[192:195], a[72:75], v[8:11]\n"
    "v_mfma_f32_16x16x32_bf16 v[12:15], v[192:195], a[76:79], v[12:15]\n"
    "v_mfma_f32_16x16x32_bf16 v[28:31], v[196:199], a[76:79], v[28:31]\n"
    "ds_read_b128 v[220:223], %[rdx1] offset:22528\n"
    "v_mfma_f32_16x16x32_bf16 v[24:27], v[196:199], a[72:75], v[24:27]\n"
    "v_mfma_f32_16x16x32_bf16 v[20:23], v[196:199], a[68:71], v[20:23]\n"
    "v_mfma_f32_16x16x32_bf16 v[16:19], v[196:199], a[64:67], v[16:19]\n"
    "s_waitcnt lgkmcnt(4)\n"
    "v_mfma_f32_16x16x32_bf16 v[32:35], v[200:203], a[64:67], v[32:35]\n"
    "ds_read_b128 v[192:195], %[rdx0] offset:24576\n"
    "v_mfma_f32_16x16x32_bf16 v[36:39], v[200:203], a[68:71], v[36:39]\n"
    "v_mfma_f32_16x16x32_bf16 v[40:43], v[200:203], a[72:75], v[40:43]\n"
    "v_mfma_f32_16x16x32_bf16 v[44:47], v[200:203], a[76:79], v[44:47]\n"
    "v_cvt_pk_bf16_f32 v108, v108, v109\n"
    "v_mfma_f32_16x16x32_bf16 v[60:63], v[204:207], a[76:79], v[60:63]\n"
    "ds_read_b128 v[196:199], %[rdx0] offset:26624\n"
    "v_mfma_f32_16x16x32_bf16 v[56:59], v[204:207], a[72:75], v[56:59]\n"
    "v_cvt_pk_bf16_f32 v109, v110, v111\n"
    "v_mfma_f32_16x16x32_bf16 v[52:55], v[204:207], a[68:71], v[52:55]\n"
    "v_cvt_pk_bf16_f32 v110, v124, v125\n"
    "v_mfma_f32_16x16x32_bf16 v[48:51], v[204:207], a[64:67], v[48:51]\n"
    "s_waitcnt lgkmcnt(4)\n"
    "v_mfma_f32_16x16x32_bf16 v[0:3], v[208:211], a[80:83], v[0:3]\n"
    "ds_read_b128 v[200:203], %[rdx0] offset:28672\n"
    "v_mfma_f32_16x16x32_bf16 v[4:7], v[208:211], a[84:87], v[4:7]\n"
    "v_cvt_pk_bf16_f32 v111, v126, v127\n"
    "v_mfma_f32_16x16x32_bf16 v[8:11], v[208:211], a[88:91], v[8:11]\n"
    "v_mfma_f32_16x16x32_bf16 v[12:15], v[208:211], a[92:95], v[12:15]\n"
    "s_add_u32 s46, %[soo], 27648\n"
    "buffer_store_dwordx4 v[108:111], %[vo], %[srdo], s46 offen sc1\n"
    "v_mfma_f32_16x16x32_bf16 v[28:31], v[212:215], a[92:95], v[28:31]\n"
    "ds_read_b128 v[204:207], %[rdx0] offset:30720\n"
    "v_mfma_f32_16x16x32_bf16 v[24:27], v[212:215], a[88:91], v[24:27]\n"
    "v_mfma_f32_16x16x32_bf16 v[20:23], v[212:215], a[84:87], v[20:23]\n"
    "v_mfma_f32_16x16x32_bf16 v[16:19], v[212:215], a[80:83], v[16:19]\n"
    "s_waitcnt lgkmcnt(4)\n"
    "v_mfma_f32_16x16x32_bf16 v[32:35], v[216:219], a[80:83], v[32:35]\n"
    "ds_read_b128 v[208:211], %[rdx1] offset:24576\n"
    "v_mfma_f32_16x16x32_bf16 v[36:39], v[216:219], a[84:87], v[36:39]\n"
    "v_mfma_f32_16x16x32_bf16 v[40:43], v[216:219], a[88:91], v[40:43]\n"
    "v_mfma_f32_16x16x32_bf16 v[44:47], v[216:219], a[92:95], v[44:47]\n"
    "v_mfma_f32_16x16x32_bf16 v[60:63], v[220:223], a[92:95], v[60:63]\n"
    "ds_read_b128 v[212:215], %[rdx1] offset:26624\n"
    "v_mfma_f32_16x16x32_bf16 v[56:59], v[220:223], a[88:91], v[56:59]\n"
    "v_mfma_f32_16x16x32_bf16 v[52:55], v[220:223], a[84:87], v[52:55]\n"
    "v_mfma_f32_16x16x32_bf16 v[48:51], v[220:223], a[80:83], v[48:51]\n"
    "s_waitcnt lgkmcnt(4)\n"
    "v_cvt_pk_bf16_f32 v128, v128, v129\n"
    "v_mfma_f32_16x16x32_bf16 v[0:3], v[192:195], a[96:99], v[0:3]\n"
    "ds_read_b128 v[216:219], %[rdx1] offset:28672\n"
    "v_mfma_f32_16x16x32_bf16 v[4:7], v[192:195], a[100:103], v[4:7]\n"
    "v_cvt_pk_bf16_f32 v129, v130, v131\n"
    "v_mfma_f32_16x16x32_bf16 v[8:11], v[192:195], a[104:107], v[8:11]\n"
    "v_cvt_pk_bf16_f32 v130, v144, v145\n"
    "v_mfma_f32_16x16x32_bf16 v[12:15], v[192:195], a[108:111], v[12:15]\n"
    "v_mfma_f32_16x16x32_bf16 v[28:31], v[196:199], a[108:111], v[28:31]\n"
    "ds_read_b128 v[220:223], %[rdx1] offset:30720\n"
    "v_mfma_f32_16x16x32_bf16 v[24:27], v[196:199], a[104:107], v[24:27]\n"
    "v_cvt_pk_bf16_f32 v131, v146, v147\n"
    "v_mfma_f32_16x16x32_bf16 v[20:23], v[196:199], a[100:103], v[20:23]\n"
    "v_mfma_f32_16x16x32_bf16 v[16:19], v[196:199], a[96:99], v[16:19]\n"
    "s_add_u32 s46, %[soo], 32768\n"
    "buffer_store_dwordx4 v[128:131], %[vo], %[srdo], s46 offen sc1\n"
    "s_waitcnt lgkmcnt(4)\n"
    "v_mfma_f32_16x16x32_bf16 v[32:35], v[200:203], a[96:99], v[32:35]\n"
    "ds_read_b128 v[192:195], %[rdx0] offset:32768\n"
    "v_mfma_f32_16x16x32_bf16 v[36:39], v[200:203], a[100:103], v[36:39]\n"
    "v_mfma_f32_16x16x32_bf16 v[40:43], v[200:203], a[104:107], v[40:43]\n"
    "v_mfma_f32_16x16x32_bf16 v[44:47], v[200:203], a[108:111], v[44:47]\n"
    "v_mfma_f32_16x16x32_bf16 v[60:63], v[204:207], a[108:111], v[60:63]\n"
    "ds_read_b128 v[196:199], %[rdx0] offset:34816\n"
    "v_mfma_f32_16x16x32_bf16 v[56:59], v[204:207], a[104:107], v[56:59]\n"
    "v_mfma_f32_16x16x32_bf16 v[52:55], v[204:207], a[100:103], v[52:55]\n"
    "v_mfma_f32_16x16x32_bf16 v[48:51], v[204:207], a[96:99], v[48:51]\n"
    "s_waitcnt lgkmcnt(4)\n"
    "v_mfma_f32_16x16x32_bf16 v[0:3], v[208:211], a[112:115], v[0:3]\n"
    "ds_read_b128 v[200:203], %[rdx0] offset:36864\n"
    "v_mfma_f32_16x16x32_bf16 v[4:7], v[208:211], a[116:119], v[4:7]\n"
    "v_mfma_f32_16x16x32_bf16 v[8:11], v[208:211], a[120:123], v[8:11]\n"
    "v_mfma_f32_16x16x32_bf16 v[12:15], v[208:211], a[124:127], v[12:15]\n"
    "v_mfma_f32_16x16x32_bf16 v[28:31], v[212:215], a[124:127], v[28:31]\n"
    "ds_read_b128 v[204:207], %[rdx0] offset:38912\n"
    "v_cvt_pk_bf16_f32 v132, v132, v133\n"
    "v_mfma_f32_16x16x32_bf16 v[24:27], v[212:215], a[120:123], v[24:27]\n"
    "v_mfma_f32_16x16x32_bf16 v[20:23], v[212:215], a[116:119], v[20:23]\n"
    "v_cvt_pk_bf16_f32 v133, v134, v135\n"
    "v_mfma_f32_16x16x32_bf16 v[16:19], v[212:215], a[112:115], v[16:19]\n"
    "s_waitcnt lgkmcnt(4)\n"
    "v_cvt_pk_bf16_f32 v134, v148, v149\n"
    "v_mfma_f32_16x16x32_bf16 v[32:35], v[216:219], a[112:115], v[32:35]\n"
    "ds_read_b128 v[208:211], %[rdx1] offset:32768\n"
    "v_mfma_f32_16x16x32_bf16 v[36:39], v[216:219], a[116:119], v[36:39]\n"
    "v_mfma_f32_16x16x32_bf16 v[40:43], v[216:219], a[120:123], v[40:43]\n"
    "v_cvt_pk_bf16_f32 v135, v150, v151\n"
    "v_mfma_f32_16x16x32_bf16 v[44:47], v[216:219], a[124:127], v[44:47]\n"
    "v_mfma_f32_16x16x32_bf16 v[60:63], v[220:223], a[124:127], v[60:63]\n"
    "s_add_u32 s46, %[soo], 33792\n"
    "ds_read_b128 v[212:215], %[rdx1] offset:34816\n"
    "buffer_store_dwordx4 v[132:135], %[vo], %[srdo], s46 offen sc1\n"
    "v_mfma_f32_16x16x32_bf16 v[56:59], v[220:223], a[120:123], v[56:59]\n"
    "v_mfma_f32_16x16x32_bf16 v[52:55], v[220:223], a[116:119], v[52:55]\n"
    "v_mfma_f32_16x16x32_bf16 v[48:51], v[220:223], a[112:115], v[48:51]\n"
    "s_waitcnt lgkmcnt(4)\n"
    "v_mfma_f32_16x16x32_bf16 v[0:3], v[192:195], a[128:131], v[0:3]\n"
    "ds_read_b128 v[216:219], %[rdx1] offset:36864\n"
    "v_mfma_f32_16x16x32_bf16 v[4:7], v[192:195], a[132:135], v[4:7]\n"
    "v_mfma_f32_16x16x32_bf16 v[8:11], v[192:195], a[136:139], v[8:11]\n"
    "v_mfma_f32_16x16x32_bf16 v[12:15], v[192:195], a[140:143], v[12:15]\n"
    "v_mfma_f32_16x16x32_bf16 v[28:31], v[196:199], a[140:143], v[28:31]\n"
    "ds_read_b128 v[220:223], %[rdx1] offset:38912\n"
    "v_mfma_f32_16x16x32_bf16 v[24:27], v[196:199], a[136:139], v[24:27]\n"
    "v_mfma_f32_16x16x32_bf16 v[20:23], v[196:199], a[132:135], v[20:23]\n"
    "v_mfma_f32_16x16x32_bf16 v[16:19], v[196:199], a[128:131], v[16:19]\n"
    "s_waitcnt lgkmcnt(4)\n"
    "v_mfma_f32_16x16x32_bf16 v[32:35], v[200:203], a[128:131], v[32:35]\n"
    "ds_read_b128 v[192:195], %[rdx0] offset:40960\n"
    "v_cvt_pk_bf16_f32 v136, v136, v137\n"
    "v_mfma_f32_16x16x32_bf16 v[36:39], v[200:203], a[132:135], v[36:39]\n"
    "v_mfma_f32_16x16x32_bf16 v[40:43], v[200:203], a[136:139], v[40:43]\n"
    "v_cvt_pk_bf16_f32 v137, v138, v139\n"
    "v_mfma_f32_16x16x32_bf16 v[44:47], v[200:203], a[140:143], v[44:47]\n"
    "v_cvt_pk_bf16_f32 v138, v152, v153\n"
    "v_mfma_f32_16x16x32_bf16 v[60:63], v[204:207], a[140:143], v[60:63]\n"
    "ds_read_b128 v[196:199], %[rdx0] offset:43008\n"
    "v_mfma_f32_16x16x32_bf16 v[56:59], v[204:207], a[136:139], v[56:59]\n"
    "v_mfma_f32_16x16x32_bf16 v[52:55], v[204:207], a[132:135], v[52:55]\n"
    "v_cvt_pk_bf16_f32 v139, v154, v155\n"
    "v_mfma_f32_16x16x32_bf16 v[48:51], v[204:207], a[128:131], v[48:51]\n"
    "s_waitcnt lgkmcnt(4)\n"
    "v_mfma_f32_16x16x32_bf16 v[0:3], v[208:211], a[144:147], v[0:3]\n"
    "s_add_u32 s46, %[soo], 34816\n"
    "ds_read_b128 v[200:203], %[rdx0] offset:45056\n"
    "buffer_store_dwordx4 v[136:139], %[vo], %[srdo], s46 offen sc1\n"
    "v_mfma_f32_16x16x32_bf16 v[4:7], v[208:211], a[148:151], v[4:7]\n"
    "v_mfma_f32_16x16x32_bf16 v[8:11], v[208:211], a[152:155], v[8:11]\n"
    "v_mfma_f32_16x16x32_bf16 v[12:15], v[208:211], a[156:159], v[12:15]\n"
    "v_mfma_f32_16x16x32_bf16 v[28:31], v[212:215], a[156:159], v[28:31]\n"
    "ds_read_b128 v[204:207], %[rdx0] offset:47104\n"
    "v_mfma_f32_16x16x32_bf16 v[24:27], v[212:215], a[152:155], v[24:27]\n"
    "v_mfma_f32_16x16x32_bf16 v[20:23], v[212:215], a[148:151], v[20:23]\n"
    "v_mfma_f32_16x16x32_bf16 v[16:19], v[212:215], a[144:147], v[16:19]\n"
    "s_waitcnt lgkmcnt(4)\n"
    "v_mfma_f32_16x16x32_bf16 v[32:35], v[216:219], a[144:147], v[32:35]\n"
    "ds_read_b128 v[208:211], %[rdx1] offset:40960\n"
    "v_mfma_f32_16x16x32_bf16 v[36:39], v[216:219], a[148:151], v[36:39]\n"
    "v_mfma_f32_16x16x32_bf16 v[40:43], v[216:219], a[152:155], v[40:43]\n"
    "v_mfma_f32_16x16x32_bf16 v[44:47], v[216:219], a[156:159], v[44:47]\n"
    "v_mfma_f32_16x16x32_bf16 v[60:63], v[220:223], a[156:159], v[60:63]\n"
    "ds_read_b128 v[212:215], %[rdx1] offset:43008\n"
    "v_mfma_f32_16x16x32_bf16 v[56:59], v[220:223], a[152:155], v[56:59]\n"
    "v_cvt_pk_bf16_f32 v140, v140, v141\n"
    "v_mfma_f32_16x16x32_bf16 v[52:55], v[220:223], a[148:151], v[52:55]\n"
    "v_cvt_pk_bf16_f32 v141, v142, v143\n"
    "v_mfma_f32_16x16x32_bf16 v[48:51], v[220:223], a[144:147], v[48:51]\n"
    "s_waitcnt lgkmcnt(4)\n"
    "v_mfma_f32_16x16x32_bf16 v[0:3], v[192:195], a[160:163], v[0:3]\n"
    "ds_read_b128 v[216:219], %[rdx1] offset:45056\n"
    "v_cvt_pk_bf16_f32 v142, v156, v157\n"
    "v_mfma_f32_16x16x32_bf16 v[4:7], v[192:195], a[164:167], v[4:7]\n"
    "v_mfma_f32_16x16x32_bf16 v[8:11], v[192:195], a[168:171], v[8:11]\n"
    "v_cvt_pk_bf16_f32 v143, v158, v159\n"
    "v_mfma_f32_16x16x32_bf16 v[12:15], v[192:195], a[172:175], v[12:15]\n"
    "v_mfma_f32_16x16x32_bf16 v[28:31], v[196:199], a[172:175], v[28:31]\n"
    "ds_read_b128 v[220:223], %[rdx1] offset:47104\n"
    "v_mfma_f32_16x16x32_bf16 v[24:27], v[196:199], a[168:171], v[24:27]\n"
    "s_add_u32 s46, %[soo], 35840\n"
    "buffer_store_dwordx4 v[140:143], %[vo], %[srdo], s46 offen sc1\n"
    "v_mfma_f32_16x16x32_bf16 v[20:23], v[196:199], a[164:167], v[20:23]\n"
    "v_mfma_f32_16x16x32_bf16 v[16:19], v[196:199], a[160:163], v[16:19]\n"
    "s_waitcnt lgkmcnt(4)\n"
    "v_mfma_f32_16x16x32_bf16 v[32:35], v[200:203], a[160:163], v[32:35]\n"
    "ds_read_b128 v[192:195], %[rdx0] offset:49152\n"
    "v_mfma_f32_16x16x32_bf16 v[36:39], v[200:203], a[164:167], v[36:39]\n"
    "v_mfma_f32_16x16x32_bf16 v[40:43], v[200:203], a[168:171], v[40:43]\n"
    "v_mfma_f32_16x16x32_bf16 v[44:47], v[200:203], a[172:175], v[44:47]\n"
    "v_mfma_f32_16x16x32_bf16 v[60:63], v[204:207], a[172:175], v[60:63]\n"
    "ds_read_b128 v[196:199], %[rdx0] offset:51200\n"
    "v_mfma_f32_16x16x32_bf16 v[56:59], v[204:207], a[168:171], v[56:59]\n"
    "v_mfma_f32_16x16x32_bf16 v[52:55], v[204:207], a[164:167], v[52:55]\n"
    "v_mfma_f32_16x16x32_bf16 v[48:51], v[204:207], a[160:163], v[48:51]\n"
    "s_waitcnt lgkmcnt(4)\n"
    "v_mfma_f32_16x16x32_bf16 v[0:3], v[208:211], a[176:179], v[0:3]\n"
    "ds_read_b128 v[200:203], %[rdx0] offset:53248\n"
    "v_mfma_f32_16x16x32_bf16 v[4:7], v[208:211], a[180:183], v[4:7]\n"
    "v_cvt_pk_bf16_f32 v160, v160, v161\n"
    "v_mfma_f32_16x16x32_bf16 v[8:11], v[208:211], a[184:187], v[8:11]\n"
    "v_cvt_pk_bf16_f32 v161, v162, v163\n"
    "v_mfma_f32_16x16x32_bf16 v[12:15], v[208:211], a[188:191], v[12:15]\n"
    "v_mfma_f32_16x16x32_bf16 v[28:31], v[212:215], a[188:191], v[28:31]\n"
    "ds_read_b128 v[204:207], %[rdx0] offset:55296\n"
    "v_cvt_pk_bf16_f32 v162, v176, v177\n"
    "v_mfma_f32_16x16x32_bf16 v[24:27], v[212:215], a[184:187], v[24:27]\n"
    "v_mfma_f32_16x16x32_bf16 v[20:23], v[212:215], a[180:183], v[20:23]\n"
    "v_cvt_pk_bf16_f32 v163, v178, v179\n"
    "v_mfma_f32_16x16x32_bf16 v[16:19], v[212:215], a[176:179], v[16:19]\n"
    "s_waitcnt lgkmcnt(4)\n"
    "v_mfma_f32_16x16x32_bf16 v[32:35], v[216:219], a[176:179], v[32:35]\n"
    "ds_read_b128 v[208:211], %[rdx1] offset:49152\n"
    "v_mfma_f32_16x16x32_bf16 v[36:39], v[216:219], a[180:183], v[36:39]\n"
    "s_add_u32 s46, %[soo], 40960\n"
    "buffer_store_dwordx4 v[160:163], %[vo], %[srdo], s46 offen sc1\n"
    "v_mfma_f32_16x16x32_bf16 v[40:43], v[216:219], a[184:187], v[40:43]\n"
    "v_mfma_f32_16x16x32_bf16 v[44:47], v[216:219], a[188:191], v[44:47]\n"
    "v_mfma_f32_16x16x32_bf16 v[60:63], v[220:223], a[188:191], v[60:63]\n"
    "ds_read_b128 v[212:215], %[rdx1] offset:51200\n"
    "v_mfma_f32_16x16x32_bf16 v[56:59], v[220:223], a[184:187], v[56:59]\n"
    "v_mfma_f32_16x16x32_bf16 v[52:55], v[220:223], a[180:183], v[52:55]\n"
    "v_mfma_f32_16x16x32_bf16 v[48:51], v[220:223], a[176:179], v[48:51]\n"
    "s_waitcnt lgkmcnt(4)\n"
    "v_mfma_f32_16x16x32_bf16 v[0:3], v[192:195], a[192:195], v[0:3]\n"
    "ds_read_b128 v[216:219], %[rdx1] offset:53248\n"
    "v_mfma_f32_16x16x32_bf16 v[4:7], v[192:195], a[196:199], v[4:7]\n"
    "v_mfma_f32_16x16x32_bf16 v[8:11], v[192:195], a[200:203], v[8:11]\n"
    "v_mfma_f32_16x16x32_bf16 v[12:15], v[192:195], a[204:207], v[12:15]\n"
    "v_mfma_f32_16x16x32_bf16 v[28:31], v[196:199], a[204:207], v[28:31]\n"
    "ds_read_b128 v[220:223], %[rdx1] offset:55296\n"
    "v_mfma_f32_16x16x32_bf16 v[24:27], v[196:199], a[200:203], v[24:27]\n"
    "v_mfma_f32_16x16x32_bf16 v[20:23], v[196:199], a[196:199], v[20:23]\n"
    "v_cvt_pk_bf16_f32 v164, v164, v165\n"
    "v_mfma_f32_16x16x32_bf16 v[16:19], v[196:199], a[192:195], v[16:19]\n"
    "s_waitcnt lgkmcnt(4)\n"
    "v_cvt_pk_bf16_f32 v165, v166, v167\n"
    "v_mfma_f32_16x16x32_bf16 v[32:35], v[200:203], a[192:195], v[32:35]\n"
    "ds_read_b128 v[192:195], %[rdx0] offset:57344\n"
    "v_mfma_f32_16x16x32_bf16 v[36:39], v[200:203], a[196:199], v[36:39]\n"
    "v_cvt_pk_bf16_f32 v166, v180, v181\n"
    "v_mfma_f32_16x16x32_bf16 v[40:43], v[200:203], a[200:203], v[40:43]\n"
    "v_mfma_f32_16x16x32_bf16 v[44:47], v[200:203], a[204:207], v[44:47]\n"
    "v_cvt_pk_bf16_f32 v167, v182, v183\n"
    "v_mfma_f32_16x16x32_bf16 v[60:63], v[204:207], a[204:207], v[60:63]\n"
    "ds_read_b128 v[196:199], %[rdx0] offset:59392\n"
    "v_mfma_f32_16x16x32_bf16 v[56:59], v[204:207], a[200:203], v[56:59]\n"
    "v_mfma_f32_16x16x32_bf16 v[52:55], v[204:207], a[196:199], v[52:55]\n"
    "s_add_u32 s46, %[soo], 41984\n"
    "buffer_store_dwordx4 v[164:167], %[vo], %[srdo], s46 offen sc1\n"
    "v_mfma_f32_16x16x32_bf16 v[48:51], v[204:207], a[192:195], v[48:51]\n"
    "s_waitcnt lgkmcnt(4)\n"
    "v_mfma_f32_16x16x32_bf16 v[0:3], v[208:211], a[208:211], v[0:3]\n"
    "ds_read_b128 v[200:203], %[rdx0] offset:61440\n"
    "v_mfma_f32_16x16x32_bf16 v[4:7], v[208:211], a[212:215], v[4:7]\n"
    "v_mfma_f32_16x16x32_bf16 v[8:11], v[208:211], a[216:219], v[8:11]\n"
    "v_mfma_f32_16x16x32_bf16 v[12:15], v[208:211], a[220:223], v[12:15]\n"
    "v_mfma_f32_16x16x32_bf16 v[28:31], v[212:215], a[220:223], v[28:31]\n"
    "ds_read_b128 v[204:207], %[rdx0] offset:63488\n"
    "v_mfma_f32_16x16x32_bf16 v[24:27], v[212:215], a[216:219], v[24:27]\n"
    "v_mfma_f32_16x16x32_bf16 v[20:23], v[212:215], a[212:215], v[20:23]\n"
    "v_mfma_f32_16x16x32_bf16 v[16:19], v[212:215], a[208:211], v[16:19]\n"
    "s_waitcnt lgkmcnt(4)\n"
    "v_mfma_f32_16x16x32_bf16 v[32:35], v[216:219], a[208:211], v[32:35]\n"
    "ds_read_b128 v[208:211], %[rdx1] offset:57344\n"
    "v_mfma_f32_16x16x32_bf16 v[36:39], v[216:219], a[212:215], v[36:39]\n"
    "v_mfma_f32_16x16x32_bf16 v[40:43], v[216:219], a[216:219], v[40:43]\n"
    "v_cvt_pk_bf16_f32 v168, v168, v169\n"
    "v_mfma_f32_16x16x32_bf16 v[44:47], v[216:219], a[220:223], v[44:47]\n"
    "v_cvt_pk_bf16_f32 v169, v170, v171\n"
    "v_mfma_f32_16x16x32_bf16 v[60:63], v[220:223], a[220:223], v[60:63]\n"
    "ds_read_b128 v[212:215], %[rdx1] offset:59392\n"
    "v_mfma_f32_16x16x32_bf16 v[56:59], v[220:223], a[216:219], v[56:59]\n"
    "v_cvt_pk_bf16_f32 v170, v184, v185\n"
    "v_mfma_f32_16x16x32_bf16 v[52:55], v[220:223], a[212:215], v[52:55]\n"
    "v_mfma_f32_16x16x32_bf16 v[48:51], v[220:223], a[208:211], v[48:51]\n"
    "s_waitcnt lgkmcnt(4)\n"
    "v_cvt_pk_bf16_f32 v171, v186, v187\n"
    "v_mfma_f32_16x16x32_bf16 v[0:3], v[192:195], a[224:227], v[0:3]\n"
    "ds_read_b128 v[216:219], %[rdx1] offset:61440\n"
    "v_mfma_f32_16x16x32_bf16 v[4:7], v[192:195], a[228:231], v[4:7]\n"
    "v_mfma_f32_16x16x32_bf16 v[8:11], v[192:195], a[232:235], v[8:11]\n"
    "s_add_u32 s46, %[soo], 43008\n"
    "buffer_store_dwordx4 v[168:171], %[vo], %[srdo], s46 offen sc1\n"
    "v_mfma_f32_16x16x32_bf16 v[12:15], v[192:195], a[236:239], v[12:15]\n"
    "v_mfma_f32_16x16x32_bf16 v[28:31], v[196:199], a[236:239], v[28:31]\n"
    "ds_read_b128 v[220:223], %[rdx1] offset:63488\n"
    "v_mfma_f32_16x16x32_bf16 v[24:27], v[196:199], a[232:235], v[24:27]\n"
    "v_mfma_f32_16x16x32_bf16 v[20:23], v[196:199], a[228:231], v[20:23]\n"
    "v_mfma_f32_16x16x32_bf16 v[16:19], v[196:199], a[224:227], v[16:19]\n"
    "s_waitcnt lgkmcnt(4)\n"
    "v_mfma_f32_16x16x32_bf16 v[32:35], v[200:203], a[224:227], v[32:35]\n"
    "v_mfma_f32_16x16x32_bf16 v[36:39], v[200:203], a[228:231], v[36:39]\n"
    "v_mfma_f32_16x16x32_bf16 v[40:43], v[200:203], a[232:235], v[40:43]\n"
    "v_mfma_f32_16x16x32_bf16 v[44:47], v[200:203], a[236:239], v[44:47]\n"
    "v_mfma_f32_16x16x32_bf16 v[60:63], v[204:207], a[236:239], v[60:63]\n"
    "v_mfma_f32_16x16x32_bf16 v[56:59], v[204:207], a[232:235], v[56:59]\n"
    "v_mfma_f32_16x16x32_bf16 v[52:55], v[204:207], a[228:231], v[52:55]\n"
    "v_mfma_f32_16x16x32_bf16 v[48:51], v[204:207], a[224:227], v[48:51]\n"
    "s_waitcnt lgkmcnt(3)\n"
    "v_cvt_pk_bf16_f32 v172, v172, v173\n"
    "v_mfma_f32_16x16x32_bf16 v[0:3], v[208:211], a[240:243], v[0:3]\n"
    "v_cvt_pk_bf16_f32 v173, v174, v175\n"
    "v_mfma_f32_16x16x32_bf16 v[4:7], v[208:211], a[244:247], v[4:7]\n"
    "v_mfma_f32_16x16x32_bf16 v[8:11], v[208:211], a[248:251], v[8:11]\n"
    "v_cvt_pk_bf16_f32 v174, v188, v189\n"
    "v_mfma_f32_16x16x32_bf16 v[12:15], v[208:211], a[252:255], v[12:15]\n"
    "s_waitcnt lgkmcnt(2)\n"
    "v_mfma_f32_16x16x32_bf16 v[28:31], v[212:215], a[252:255], v[28:31]\n"
    "v_cvt_pk_bf16_f32 v175, v190, v191\n"
    "v_mfma_f32_16x16x32_bf16 v[24:27], v[212:215], a[248:251], v[24:27]\n"
    "v_mfma_f32_16x16x32_bf16 v[20:23], v[212:215], a[244:247], v[20:23]\n"
    "v_mfma_f32_16x16x32_bf16 v[16:19], v[212:215], a[240:243], v[16:19]\n"
    "s_add_u32 s46, %[soo], 44032\n"
    "buffer_store_dwordx4 v[172:175], %[vo], %[srdo], s46 offen sc1\n"
    "s_waitcnt lgkmcnt(1)\n"
    "v_mfma_f32_16x16x32_bf16 v[32:35], v[216:219], a[240:243], v[32:35]\n"
    "v_mfma_f32_16x16x32_bf16 v[36:39], v[216:219], a[244:247], v[36:39]\n"
    "v_mfma_f32_16x16x32_bf16 v[40:43], v[216:219], a[248:251], v[40:43]\n"
    "v_mfma_f32_16x16x32_bf16 v[44:47], v[216:219], a[252:255], v[44:47]\n"
    "s_waitcnt lgkmcnt(0)\n"
    "v_mfma_f32_16x16x32_bf16 v[60:63], v[220:223], a[252:255], v[60:63]\n"
    "v_mfma_f32_16x16x32_bf16 v[56:59], v[220:223], a[248:251], v[56:59]\n"
    "v_mfma_f32_16x16x32_bf16 v[52:55], v[220:223], a[244:247], v[52:55]\n"
    "v_mfma_f32_16x16x32_bf16 v[48:51], v[220:223], a[240:243], v[48:51]\n"
    "v_cvt_pk_bf16_f32 v0, v0, v1\n"
    "v_cvt_pk_bf16_f32 v1, v2, v3\n"
    "v_cvt_pk_bf16_f32 v2, v16, v17\n"
    "v_cvt_pk_bf16_f32 v3, v18, v19\n"
    "s_add_u32 s46, %[soo], 49152\n"
    "buffer_store_dwordx4 v[0:3], %[vo], %[srdo], s46 offen sc1\n"
    "v_cvt_pk_bf16_f32 v4, v4, v5\n"
    "v_cvt_pk_bf16_f32 v5, v6, v7\n"
    "v_cvt_pk_bf16_f32 v6, v20, v21\n"
    "v_cvt_pk_bf16_f32 v7, v22, v23\n"
    "s_add_u32 s46, %[soo], 50176\n"
    "buffer_store_dwordx4 v[4:7], %[vo], %[srdo], s46 offen sc1\n"
    "v_cvt_pk_bf16_f32 v8, v8, v9\n"
    "v_cvt_pk_bf16_f32 v9, v10, v11\n"
    "v_cvt_pk_bf16_f32 v10, v24, v25\n"
    "v_cvt_pk_bf16_f32 v11, v26, v27\n"
    "s_add_u32 s46, %[soo], 51200\n"
    "buffer_store_dwordx4 v[8:11], %[vo], %[srdo], s46 offen sc1\n"
    "v_cvt_pk_bf16_f32 v12, v12, v13\n"
    "v_cvt_pk_bf16_f32 v13, v14, v15\n"
    "v_cvt_pk_bf16_f32 v14, v28, v29\n"
    "v_cvt_pk_bf16_f32 v15, v30, v31\n"
    "s_add_u32 s46, %[soo], 52224\n"
    "buffer_store_dwordx4 v[12:15], %[vo], %[srdo], s46 offen sc1\n"
    "v_cvt_pk_bf16_f32 v32, v32, v33\n"
    "v_cvt_pk_bf16_f32 v33, v34, v35\n"
    "v_cvt_pk_bf16_f32 v34, v48, v49\n"
    "v_cvt_pk_bf16_f32 v35, v50, v51\n"
    "s_add_u32 s46, %[soo], 57344\n"
    "buffer_store_dwordx4 v[32:35], %[vo], %[srdo], s46 offen sc1\n"
    "v_cvt_pk_bf16_f32 v36, v36, v37\n"
    "v_cvt_pk_bf16_f32 v37, v38, v39\n"
    "v_cvt_pk_bf16_f32 v38, v52, v53\n"
    "v_cvt_pk_bf16_f32 v39, v54, v55\n"
    "s_add_u32 s46, %[soo], 58368\n"
    "buffer_store_dwordx4 v[36:39], %[vo], %[srdo], s46 offen sc1\n"
    "v_cvt_pk_bf16_f32 v40, v40, v41\n"
    "v_cvt_pk_bf16_f32 v41, v42, v43\n"
    "v_cvt_pk_bf16_f32 v42, v56, v57\n"
    "v_cvt_pk_bf16_f32 v43, v58, v59\n"
    "s_add_u32 s46, %[soo], 59392\n"
    "buffer_store_dwordx4 v[40:43], %[vo], %[srdo], s46 offen sc1\n"
    "v_cvt_pk_bf16_f32 v44, v44, v45\n"
    "v_cvt_pk_bf16_f32 v45, v46, v47\n"
    "v_cvt_pk_bf16_f32 v46, v60, v61\n"
    "v_cvt_pk_bf16_f32 v47, v62, v63\n"
    "s_add_u32 s46, %[soo], 60416\n"
    "buffer_store_dwordx4 v[44:47], %[vo], %[srdo], s46 offen sc1\n"
    "KE90001:\n"
    : [vdum]"=&v"(vdum)
    : [vx0]"v"(vx[0]), [vx1]"v"(vx[1]), [vx2]"v"(vx[2]), [vx3]"v"(vx[3]), [vw0]"v"(vw[0]), [vw1]"v"(vw[1]), [vw2]"v"(vw[2]), [vw3]"v"(vw[3]), [srdx]"s"(srdx), [srdw]"s"(srdw), [m0x]"s"(m0x), [m0w]"s"(m0w), [sox]"s"(sox), [sow]"s"(sow), [rot]"s"(rot), [vo]"v"(vo), [isv]"s"(isv), [wid]"s"(wid), [rdx0]"v"(rdx[0]), [rdx1]"v"(rdx[1]), [rdx2]"v"(rdx[2]), [rdx3]"v"(rdx[3]), [rdw0]"v"(rdw[0]), [rdw1]"v"(rdw[1]), [rdw2]"v"(rdw[2]), [rdw3]"v"(rdw[3]), [rdw4]"v"(rdw[4]), [rdw5]"v"(rdw[5]), [rdw6]"v"(rdw[6]), [rdw7]"v"(rdw[7]), [srdo]"s"(srdo), [soo]"s"(soo), [m0t]"s"(m0t), [sot0]"s"(sot0), [sot1]"s"(sot1), [vpf]"v"(vpf), [srdp]"s"(srdp)
    : "memory", "scc", "vcc", "s40","s41","s42","s43","s44","s45","s46","s47","s48","s49","s50","s51","s52","s53","s54","s55","s56","s57", "v0","v1","v2","v3","v4","v5","v6","v7","v8","v9","v10","v11","v12","v13","v14","v15","v16","v17","v18","v19","v20","v21","v22","v23","v24","v25","v26","v27","v28","v29","v30","v31","v32","v33","v34","v35","v36","v37","v38","v39","v40","v41","v42","v43","v44","v45","v46","v47","v48","v49","v50","v51","v52","v53","v54","v55","v56","v57","v58","v59","v60","v61","v62","v63","v64","v65","v66","v67","v68","v69","v70","v71","v72","v73","v74","v75","v76","v77","v78","v79","v80","v81","v82","v83","v84","v85","v86","v87","v88","v89","v90","v91","v92","v93","v94","v95","v96","v97","v98","v99","v100","v101","v102","v103","v104","v105","v106","v107","v108","v109","v110","v111","v112","v113","v114","v115","v116","v117","v118","v119","v120","v121","v122","v123","v124","v125","v126","v127","v128","v129","v130","v131","v132","v133","v134","v135","v136","v137","v138","v139","v140","v141","v142","v143","v144","v145","v146","v147","v148","v149","v150","v151","v152","v153","v154","v155","v156","v157","v158","v159","v160","v161","v162","v163","v164","v165","v166","v167","v168","v169","v170","v171","v172","v173","v174","v175","v176","v177","v178","v179","v180","v181","v182","v183","v184","v185","v186","v187","v188","v189","v190","v191","v192","v193","v194","v195","v196","v197","v198","v199","v200","v201","v202","v203","v204","v205","v206","v207","v208","v209","v210","v211","v212","v213","v214","v215","v216","v217","v218","v219","v220","v221","v222","v223", "a0","a1","a2","a3","a4","a5","a6","a7","a8","a9","a10","a11","a12","a13","a14","a15","a16","a17","a18","a19","a20","a21","a22","a23","a24","a25","a26","a27","a28","a29","a30","a31","a32","a33","a34","a35","a36","a37","a38","a39","a40","a41","a42","a43","a44","a45","a46","a47","a48","a49","a50","a51","a52","a53","a54","a55","a56","a57","a58","a59","a60","a61","a62","a63","a64","a65","a66","a67","a68","a69","a70","a71","a72","a73","a74","a75","a76","a77","a78","a79","a80","a81","a82","a83","a84","a85","a86","a87","a88","a89","a90","a91","a92","a93","a94","a95","a96","a97","a98","a99","a100","a101","a102","a103","a104","a105","a106","a107","a108","a109","a110","a111","a112","a113","a114","a115","a116","a117","a118","a119","a120","a121","a122","a123","a124","a125","a126","a127","a128","a129","a130","a131","a132","a133","a134","a135","a136","a137","a138","a139","a140","a141","a142","a143","a144","a145","a146","a147","a148","a149","a150","a151","a152","a153","a154","a155","a156","a157","a158","a159","a160","a161","a162","a163","a164","a165","a166","a167","a168","a169","a170","a171","a172","a173","a174","a175","a176","a177","a178","a179","a180","a181","a182","a183","a184","a185","a186","a187","a188","a189","a190","a191","a192","a193","a194","a195","a196","a197","a198","a199","a200","a201","a202","a203","a204","a205","a206","a207","a208","a209","a210","a211","a212","a213","a214","a215","a216","a217","a218","a219","a220","a221","a222","a223","a224","a225","a226","a227","a228","a229","a230","a231","a232","a233","a234","a235","a236","a237","a238","a239","a240","a241","a242","a243","a244","a245","a246","a247","a248","a249","a250","a251","a252","a253","a254","a255");
  STAMP_END(0);
}

DI void attn_true_max(const char* xb, const char* wq, const char* kb, const char* vb, char* cb, char* lds, u32 h, u32 i, u32 wid, u32 lane) {
  float* qs = (float*)lds + wid * 256; float* ps = qs + 128; const float* cst = (const float*)(lds + 155648);
  for (u32 r = wid; r < 256; r += 4) {
    const u32 row = (r < 128 ? (15 - i) * 128 : i * 128 - 128) + r;
    float q0 = 0.f, q1 = 0.f;
    for (u32 m = 0; m < M; m++) {
      const u32 ks = m >> 5, g = (m >> 3) & 3, j = m & 7;
      const float xh = (float)*(const _Float16*)(xb + ((row >> 4) * 18 + ks) * 1024 + ((row & 15) + 16 * g) * 16 + j * 2);
      const float hv = rbf(xh * cst[ks * 64 + g * 16 + j] + cst[ks * 64 + g * 16 + 8 + j]);
      const u32 w = *(const u32*)(wq + (size_t)m * HD * 2 + h * 256 + lane * 4);
      q0 += hv * bflo(w); q1 += hv * bfhi(w);
    }
    qs[2 * lane] = rbf(q0); qs[2 * lane + 1] = rbf(q1);
    __builtin_amdgcn_wave_barrier(); asm volatile("s_waitcnt lgkmcnt(0)" ::: "memory");
    float mx = -__builtin_inff(), l = 0.f, o0 = 0.f, o1 = 0.f;
    for (u32 k0 = 0; k0 <= row; k0 += 64) {
      const u32 key = k0 + lane; float s = 0.f;
      for (u32 c = 0; c < 16; c++) { const u32x4 kv = *(const u32x4*)(kb + (((size_t)h * 128 + (key >> 4)) * 4 + (c >> 2)) * 1024 + ((key & 15) + 16 * (c & 3)) * 16);
#pragma unroll
        for (int e = 0; e < 4; e++) { const u32 d = (c >> 2) * 32 + pi_k(c & 3, 2 * e); s += qs[d] * bflo(kv[e]) + qs[d + 1] * bfhi(kv[e]); } }
      if (key > row) s = -__builtin_inff();
      float t = s;
#pragma unroll
      for (int o = 32; o; o >>= 1) t = fmaxf(t, __shfl_xor(t, o));
      const float mn = fmaxf(mx, t), al = exp2f(mx - mn), p = exp2f(s - mn); mx = mn;
      t = p;
#pragma unroll
      for (int o = 32; o; o >>= 1) t += __shfl_xor(t, o);
      l = l * al + t; o0 *= al; o1 *= al; ps[lane] = p;
      __builtin_amdgcn_wave_barrier(); asm volatile("s_waitcnt lgkmcnt(0)" ::: "memory");
      for (u32 kk = 0; kk < 64; kk++) { const u32 k5 = kk & 31, d = 2 * lane;
        const char* vp = vb + (((size_t)h * 64 + ((k0 + kk) >> 5)) * 8 + (d >> 4)) * 1024 + ((d & 15) + 16 * ((k5 >> 2) & 3)) * 16 + ((k5 & 3) + 4 * (k5 >> 4)) * 2;
        const float pk = ps[kk]; if (k0 + kk <= row) { o0 += pk * bf2f(*(const u16*)vp); o1 += pk * bf2f(*(const u16*)(vp + 16)); } }
      __builtin_amdgcn_wave_barrier();
    }
    const u32 d = 2 * lane, d5 = d & 31;
    *(u32*)(cb + ((size_t)(row >> 4) * 128 + h * 4 + (d >> 5)) * 1024 + ((row & 15) + 16 * ((d5 >> 2) & 3)) * 16 + ((d5 & 3) + 4 * (d5 >> 4)) * 2) = pk2(o0 / l, o1 / l);
  }
}
__global__ __launch_bounds__(256, 1) void k_attn(const char* __restrict__ xb, const float* __restrict__ modp, const char* __restrict__ wq, const char* __restrict__ kb,
                                                 const char* __restrict__ vb, char* __restrict__ cb, const char* __restrict__ wo,
                                                 const char* __restrict__ w1, const char* __restrict__ w2) {
  STAMP_BEGIN();
  asm volatile("" :: "s"(xb), "s"(modp), "s"(wq), "s"(kb), "s"(vb), "s"(cb), "s"(wo), "s"(w1), "s"(w2));
  __shared__ __attribute__((aligned(1024))) char lds[160512];
  const u32 lb = (u32)(uintptr_t)(__attribute__((address_space(3))) char*)lds;
  const u32 j = blockIdx.x >> 3, h = (blockIdx.x & 7) * 4 + (j >> 3), i = j & 7, rot = 0;
  const u32 tid = threadIdx.x, lane = tid & 63, wid = __builtin_amdgcn_readfirstlane(tid >> 6), W = 2 * i + 2;
  const u32 tbl = (15 - i) * 8 + (wid & 1) * 4, tb0 = wid < 2 ? tbl : i * 8 + (wid & 1) * 4;
  const __amdgpu_buffer_rsrc_t srdx = __builtin_amdgcn_make_buffer_rsrc((void*)(xb - 4096), 0, 128 * 18 * 1024 + 4096, 0x00020000), srdw = __builtin_amdgcn_make_buffer_rsrc((void*)(wq - 4096), 0, M * HD * 2 + 4096, 0x00020000);
  u32 vx[4], vw[4], rdw[4];
#pragma unroll
  for (int p = 0; p < 4; p++) { vx[p] = 4096 - (p & 1) * 2048 + p * 18432 + lane * 16; const u32 jj = wid * 4 + p;
    vw[p] = 4096 - p * 1024 + (8 * (jj & 7) + (lane >> 3)) * (HD * 2) + (jj >> 3) * 128 + ((lane & 7) ^ ((((lane >> 4) & 1) | ((p & 1) << 1)) << 1)) * 16; }
  const u32 m0x = lb + wid * 8192, m0w = lb + 32768 + wid * 4096;
  u32 sox = tb0 * 18432 + rot * 2048, sow = h * 256 + rot * 64 * HD * 2, wrap = 9 - rot, wrapr = 9 - rot;
  const __amdgpu_buffer_rsrc_t srdm = __builtin_amdgcn_make_buffer_rsrc((void*)modp, 0, 3 * 3456 * 4, 0x00020000);
  const u32 vmod = tid * 16, vcw = lb + (tid < 144 ? 155648 + (tid >> 1) * 64 + (tid & 1) * 16 : 160256 + 64);
  const __amdgpu_buffer_rsrc_t srdk0 = __builtin_amdgcn_make_buffer_rsrc((void*)(kb + (size_t)h * 524288), 0, 524288, 0x00020000), srdk = __builtin_amdgcn_make_buffer_rsrc((void*)(kb + (size_t)h * 524288 + 8192), 0, 524288 - 8192, 0x00020000),
                                srdv = __builtin_amdgcn_make_buffer_rsrc((void*)(vb + (size_t)h * 524288), 0, 524288, 0x00020000), srdo = __builtin_amdgcn_make_buffer_rsrc((void*)cb, 0, S * HD * 2, 0x00020000);
  { const u32 g = lane >> 4, q = (lane >> 2) & 3;
#pragma unroll
    for (int w = 0; w < 4; w++) rdw[w] = lb + 32768 + g * 1024 + q * 128 + ((w ^ ((q >> 1) | ((g & 1) << 1))) * 32) + (lane & 3) * 8; }
  u32 rdx = lb + wid * 8192 + lane * 16, rdc = lb + 155648 + rot * 512 + (lane >> 4) * 64, rd = lb + lane * 16, vex = lb + 65536 + wid * 16384 + lane * 16;
  u32 so0 = 16384, so1 = 17 * 16384, soo = (tb0 * 128 + h * 4) * 1024;
  const u32 soo2 = (tbl * 128 + h * 4) * 1024, vdma = lane * 16 + wid * 4096, vkx = lane * 16 + wid * 2048, rd16 = lb + 131072 + lane * 16, vo = lane * 16,
            vgc = 4 * (lane >> 4) - (lane & 15), vqex = lb + 32768 + (wid & 1) * 16384 + lane * 16, m0kv = lb + wid * 4096, m0kx = lb + wid * 2048, sex = lb + 131072 + 8192 + wid * 512;
  const u32 gw = blockIdx.x * 4 + wid, vpf = lane < 32 ? lane * 128 : 0x80000000u; u32 spf = (gw < 105 ? gw : gw < 524 ? gw - 105 : gw - 524) * 45056;
  const u32 pfn = gw < 105 ? HD * M * 2 : S * F * 2;
  const __amdgpu_buffer_rsrc_t srdp = __builtin_amdgcn_make_buffer_rsrc((void*)(gw < 105 ? wo : gw < 524 ? w1 : w2), 0, spf + 45056 < pfn ? spf + 45056 : pfn, 0x00020000);
  asm volatile(
    "s_nop 4\n"
    "s_mov_b32 s44, 13824\n"
    "s_mov_b32 s45, 27648\n"
    "s_mov_b32 s42, 0\n"
    "s_mov_b32 s49, 2048\n"
    "s_mov_b32 s51, 524288\n"
    "s_mov_b32 s40, %[m0x]\n"
    "s_mov_b32 s41, %[m0w]\n"
    "buffer_load_dwordx4 v[64:67], %[vmod], %[srdm], 0 offen\n"
    "buffer_load_dwordx4 v[68:71], %[vmod], %[srdm], 0 offen offset:2304\n"
    "buffer_load_dwordx4 v[72:75], %[vmod], %[srdm], s44 offen\n"
    "buffer_load_dwordx4 v[76:79], %[vmod], %[srdm], s44 offen offset:2304\n"
    "buffer_load_dwordx4 v[80:83], %[vmod], %[srdm], s45 offen\n"
    "buffer_load_dwordx4 v[84:87], %[vmod], %[srdm], s45 offen offset:2304\n"
    "s_add_u32 m0, s40, 0\n"
    "s_nop 0\n"
    "buffer_load_dwordx4 %[vx0], %[srdx], %[sox] offen offset:0 lds\n"
    "buffer_load_dwordx4 %[vx0], %[srdx], %[sox] offen offset:1024 lds\n"
    "buffer_load_dwordx4 %[vx1], %[srdx], %[sox] offen offset:2048 lds\n"
    "buffer_load_dwordx4 %[vx1], %[srdx], %[sox] offen offset:3072 lds\n"
    "s_add_u32 m0, s40, 4096\n"
    "s_nop 0\n"
    "buffer_load_dwordx4 %[vx2], %[srdx], %[sox] offen offset:0 lds\n"
    "buffer_load_dwordx4 %[vx2], %[srdx], %[sox] offen offset:1024 lds\n"
    "buffer_load_dwordx4 %[vx3], %[srdx], %[sox] offen offset:2048 lds\n"
    "buffer_load_dwordx4 %[vx3], %[srdx], %[sox] offen offset:3072 lds\n"
    "s_mov_b32 m0, s41\n"
    "s_nop 0\n"
    "buffer_load_dwordx4 %[vw0], %[srdw], %[sow] offen offset:0 lds\n"
    "buffer_load_dwordx4 %[vw1], %[srdw], %[sow] offen offset:1024 lds\n"
    "buffer_load_dwordx4 %[vw2], %[srdw], %[sow] offen offset:2048 lds\n"
    "buffer_load_dwordx4 %[vw3], %[srdw], %[sow] offen offset:3072 lds\n"
    "s_mov_b32 s43, 0\n"
    "s_mov_b32 s47, 4294868992\n"
    "s_mov_b32 s48, 49152\n"
    "s_mov_b32 s54, 4294963200\n"
    "s_mov_b32 s55, 512\n"
    "s_mov_b32 s50, 6\n"
    "v_mov_b32 v200, 0x3f803f80\n"
    "v_accvgpr_write_b32 a176, v200\n"
    "v_accvgpr_write_b32 a177, v200\n"
    "v_accvgpr_write_b32 a178, v200\n"
    "v_accvgpr_write_b32 a179, v200\n"
    "v_mov_b32 v192, 0\n"
    "v_mov_b32 v193, 0\n"
    "v_mov_b32 v194, 0\n"
    "v_mov_b32 v195, 0\n"
    "v_mov_b32 v196, 0\n"
    "v_mov_b32 v197, 0\n"
    "v_mov_b32 v198, 0\n"
    "v_mov_b32 v199, 0\n"
    "v_mov_b32 v200, %[lb]\n"
    "v_add_u32 v200, 160256, v200\n"
    "ds_write_b32 v200, v192\n"
    "s_mov_b32 s46, 0x3e0293ee\n"
    "s_waitcnt vmcnt(12)\n"
    "v_add_f32 v64, v64, v72\n"
    "v_add_f32 v64, v64, v80\n"
    "v_add_f32 v65, v65, v73\n"
    "v_add_f32 v65, v65, v81\n"
    "v_add_f32 v66, v66, v74\n"
    "v_add_f32 v66, v66, v82\n"
    "v_add_f32 v67, v67, v75\n"
    "v_add_f32 v67, v67, v83\n"
    "v_add_f32 v68, v68, v76\n"
    "v_add_f32 v68, v68, v84\n"
    "v_add_f32 v69, v69, v77\n"
    "v_add_f32 v69, v69, v85\n"
    "v_add_f32 v70, v70, v78\n"
    "v_add_f32 v70, v70, v86\n"
    "v_add_f32 v71, v71, v79\n"
    "v_add_f32 v71, v71, v87\n"
    "v_cvt_pk_bf16_f32 v72, v64, v65\n"
    "v_cvt_pk_bf16_f32 v73, v66, v67\n"
    "v_lshlrev_b32 v64, 16, v72\n"
    "v_and_b32 v65, 0xffff0000, v72\n"
    "v_lshlrev_b32 v66, 16, v73\n"
    "v_and_b32 v67, 0xffff0000, v73\n"
    "v_cvt_pk_bf16_f32 v72, v68, v69\n"
    "v_cvt_pk_bf16_f32 v73, v70, v71\n"
    "v_lshlrev_b32 v68, 16, v72\n"
    "v_and_b32 v69, 0xffff0000, v72\n"
    "v_lshlrev_b32 v70, 16, v73\n"
    "v_and_b32 v71, 0xffff0000, v73\n"
    "v_add_f32 v68, 1.0, v68\n"
    "v_add_f32 v69, 1.0, v69\n"
    "v_add_f32 v70, 1.0, v70\n"
    "v_add_f32 v71, 1.0, v71\n"
    "v_cvt_pk_bf16_f32 v72, v68, v69\n"
    "v_cvt_pk_bf16_f32 v73, v70, v71\n"
    "v_lshlrev_b32 v68, 16, v72\n"
    "v_and_b32 v69, 0xffff0000, v72\n"
    "v_lshlrev_b32 v70, 16, v73\n"
    "v_and_b32 v71, 0xffff0000, v73\n"
    "v_mul_f32 v64, s46, v64\n"
    "v_mul_f32 v65, s46, v65\n"
    "v_mul_f32 v66, s46, v66\n"
    "v_mul_f32 v67, s46, v67\n"
    "v_mul_f32 v68, s46, v68\n"
    "v_mul_f32 v69, s46, v69\n"
    "v_mul_f32 v70, s46, v70\n"
    "v_mul_f32 v71, s46, v71\n"
    "ds_write_b128 %[vcw], v[68:71]\n"
    "ds_write_b128 %[vcw], v[64:67] offset:32\n"
    "s_sub_u32 %[wrap], %[wrap], 1\n"
    "s_cmp_eq_u32 %[wrap], 0\n"
    "s_cselect_b32 s44, 4294950912, s49\n"
    "s_cselect_b32 s45, 4290772992, s51\n"
    "s_add_u32 %[sox], %[sox], s44\n"
    "s_add_u32 %[sow], %[sow], s45\n"
    "s_add_u32 s42, s42, 49152\n"
    "s_cmp_eq_u32 s42, 147456\n"
    "s_cselect_b32 s42, 0, s42\n"
    "s_add_u32 s40, s42, %[m0x]\n"
    "s_add_u32 s41, s42, %[m0w]\n"
    "s_add_u32 m0, s40, 0\n"
    "s_nop 0\n"
    "buffer_load_dwordx4 %[vx0], %[srdx], %[sox] offen offset:0 lds\n"
    "buffer_load_dwordx4 %[vx0], %[srdx], %[sox] offen offset:1024 lds\n"
    "buffer_load_dwordx4 %[vx1], %[srdx], %[sox] offen offset:2048 lds\n"
    "buffer_load_dwordx4 %[vx1], %[srdx], %[sox] offen offset:3072 lds\n"
    "s_add_u32 m0, s40, 4096\n"
    "s_nop 0\n"
    "buffer_load_dwordx4 %[vx2], %[srdx], %[sox] offen offset:0 lds\n"
    "buffer_load_dwordx4 %[vx2], %[srdx], %[sox] offen offset:1024 lds\n"
    "buffer_load_dwordx4 %[vx3], %[srdx], %[sox] offen offset:2048 lds\n"
    "buffer_load_dwordx4 %[vx3], %[srdx], %[sox] offen offset:3072 lds\n"
    "s_mov_b32 m0, s41\n"
    "s_nop 0\n"
    "buffer_load_dwordx4 %[vw0], %[srdw], %[sow] offen offset:0 lds\n"
    "buffer_load_dwordx4 %[vw1], %[srdw], %[sow] offen offset:1024 lds\n"
    "buffer_load_dwordx4 %[vw2], %[srdw], %[sow] offen offset:2048 lds\n"
    "buffer_load_dwordx4 %[vw3], %[srdw], %[sow] offen offset:3072 lds\n"
    "s_getpc_b64 s[56:57]\n"
    "CT_%=:\n"
    "s_add_u32 s56, s56, QM_%=-CT_%=\n"
    "s_addc_u32 s57, s57, 0\n"
    "s_mul_i32 s44, %[wid], 8192\n"
    "v_lshl_add_u32 v212, %[vo], 3, s44\n"
    "v_min_u32 v213, END_%=-QM_%=-4, v212\n"
    "global_load_dword v214, v213, s[56:57] ; dummy\n"
    "s_waitcnt vmcnt(13) lgkmcnt(0)\n"
    "s_barrier\n"
    "ds_read_b128 v[0:3], %[rdx] offset:0\n"
    "ds_read_b128 v[4:7], %[rdx] offset:2048\n"
    "ds_read_b128 v[8:11], %[rdx] offset:4096\n"
    "ds_read_b128 v[12:15], %[rdx] offset:6144\n"
    "ds_read_b128 v[32:35], %[rdc] offset:0\n"
    "ds_read_b128 v[36:39], %[rdc] offset:16\n"
    "ds_read_b128 v[40:43], %[rdc] offset:32\n"
    "ds_read_b128 v[44:47], %[rdc] offset:48\n"
    "ds_read_b64_tr_b16 a[180:181], %[rdw0] offset:0\n"
    "ds_read_b64_tr_b16 a[182:183], %[rdw0] offset:512\n"
    "ds_read_b64_tr_b16 a[184:185], %[rdw1] offset:0\n"
    "ds_read_b64_tr_b16 a[186:187], %[rdw1] offset:512\n"
    "ds_read_b64_tr_b16 a[188:189], %[rdw2] offset:0\n"
    "ds_read_b64_tr_b16 a[190:191], %[rdw2] offset:512\n"
    "ds_read_b64_tr_b16 a[192:193], %[rdw3] offset:0\n"
    "s_sub_u32 %[wrap], %[wrap], 1\n"
    "s_cmp_eq_u32 %[wrap], 0\n"
    "s_cselect_b32 s44, 4294950912, s49\n"
    "s_cselect_b32 s45, 4290772992, s51\n"
    "s_add_u32 %[sox], %[sox], s44\n"
    "s_add_u32 %[sow], %[sow], s45\n"
    "s_add_u32 s42, s42, 49152\n"
    "s_cmp_eq_u32 s42, 147456\n"
    "s_cselect_b32 s42, 0, s42\n"
    "s_add_u32 s40, s42, %[m0x]\n"
    "s_add_u32 s41, s42, %[m0w]\n"
    "s_waitcnt lgkmcnt(7)\n"
    "ds_read_b64_tr_b16 a[194:195], %[rdw3] offset:512\n"
    "ds_read_b64_tr_b16 a[196:197], %[rdw0] offset:8192\n"
    "ds_read_b64_tr_b16 a[198:199], %[rdw0] offset:8704\n"
    "ds_read_b64_tr_b16 a[200:201], %[rdw1] offset:8192\n"
    "ds_read_b64_tr_b16 a[202:203], %[rdw1] offset:8704\n"
    "ds_read_b64_tr_b16 a[204:205], %[rdw2] offset:8192\n"
    "ds_read_b64_tr_b16 a[206:207], %[rdw2] offset:8704\n"
    "ds_read_b64_tr_b16 a[208:209], %[rdw3] offset:8192\n"
    "ds_read_b64_tr_b16 a[210:211], %[rdw3] offset:8704\n"
    "v_fma_mix_f32 v200, v0, v32, v40 op_sel:[0,0,0] op_sel_hi:[1,0,0]\n"
    "v_fma_mix_f32 v201, v0, v33, v41 op_sel:[1,0,0] op_sel_hi:[1,0,0]\n"
    "v_cvt_pk_bf16_f32 v16, v200, v201\n"
    "v_fma_mix_f32 v202, v1, v34, v42 op_sel:[0,0,0] op_sel_hi:[1,0,0]\n"
    "v_fma_mix_f32 v203, v1, v35, v43 op_sel:[1,0,0] op_sel_hi:[1,0,0]\n"
    "v_cvt_pk_bf16_f32 v17, v202, v203\n"
    "v_fma_mix_f32 v200, v2, v36, v44 op_sel:[0,0,0] op_sel_hi:[1,0,0]\n"
    "v_fma_mix_f32 v201, v2, v37, v45 op_sel:[1,0,0] op_sel_hi:[1,0,0]\n"
    "v_cvt_pk_bf16_f32 v18, v200, v201\n"
    "v_fma_mix_f32 v202, v3, v38, v46 op_sel:[0,0,0] op_sel_hi:[1,0,0]\n"
    "v_fma_mix_f32 v203, v3, v39, v47 op_sel:[1,0,0] op_sel_hi:[1,0,0]\n"
    "v_cvt_pk_bf16_f32 v19, v202, v203\n"
    "v_fma_mix_f32 v200, v4, v32, v40 op_sel:[0,0,0] op_sel_hi:[1,0,0]\n"
    "v_fma_mix_f32 v201, v4, v33, v41 op_sel:[1,0,0] op_sel_hi:[1,0,0]\n"
    "v_cvt_pk_bf16_f32 v20, v200, v201\n"
    "v_fma_mix_f32 v202, v5, v34, v42 op_sel:[0,0,0] op_sel_hi:[1,0,0]\n"
    "v_fma_mix_f32 v203, v5, v35, v43 op_sel:[1,0,0] op_sel_hi:[1,0,0]\n"
    "v_cvt_pk_bf16_f32 v21, v202, v203\n"
    "v_fma_mix_f32 v200, v6, v36, v44 op_sel:[0,0,0] op_sel_hi:[1,0,0]\n"
    "v_fma_mix_f32 v201, v6, v37, v45 op_sel:[1,0,0] op_sel_hi:[1,0,0]\n"
    "v_cvt_pk_bf16_f32 v22, v200, v201\n"
    "v_fma_mix_f32 v202, v7, v38, v46 op_sel:[0,0,0] op_sel_hi:[1,0,0]\n"
    "v_fma_mix_f32 v203, v7, v39, v47 op_sel:[1,0,0] op_sel_hi:[1,0,0]\n"
    "v_cvt_pk_bf16_f32 v23, v202, v203\n"
    "s_add_u32 m0, s40, 0\n"
    "s_nop 0\n"
    "buffer_load_dwordx4 %[vx0], %[srdx], %[sox] offen offset:0 lds\n"
    "buffer_load_dwordx4 %[vx0], %[srdx], %[sox] offen offset:1024 lds\n"
    "buffer_load_dwordx4 %[vx1], %[srdx], %[sox] offen offset:2048 lds\n"
    "buffer_load_dwordx4 %[vx1], %[srdx], %[sox] offen offset:3072 lds\n"
    "s_add_u32 m0, s40, 4096\n"
    "s_nop 0\n"
    "buffer_load_dwordx4 %[vx2], %[srdx], %[sox] offen offset:0 lds\n"
    "s_waitcnt lgkmcnt(0)\n"
    "v_mfma_f32_16x16x32_bf16 v[64:67], a[180:183], v[16:19], 0\n"
    "ds_read_b128 v[0:3], %[rdx] offset:1024\n"
    "v_fma_mix_f32 v200, v8, v32, v40 op_sel:[0,0,0] op_sel_hi:[1,0,0]\n"
    "v_fma_mix_f32 v201, v8, v33, v41 op_sel:[1,0,0] op_sel_hi:[1,0,0]\n"
    "v_mfma_f32_16x16x32_bf16 v[68:71], a[180:183], v[20:23], 0\n"
    "ds_read_b128 v[4:7], %[rdx] offset:3072\n"
    "v_cvt_pk_bf16_f32 v24, v200, v201\n"
    "v_fma_mix_f32 v202, v9, v34, v42 op_sel:[0,0,0] op_sel_hi:[1,0,0]\n"
    "v_mfma_f32_16x16x32_bf16 v[84:87], a[184:187], v[20:23], 0\n"
    "ds_read_b128 v[48:51], %[rdc] offset:256\n"
    "v_fma_mix_f32 v203, v9, v35, v43 op_sel:[1,0,0] op_sel_hi:[1,0,0]\n"
    "v_mfma_f32_16x16x32_bf16 v[80:83], a[184:187], v[16:19], 0\n"
    "ds_read_b128 v[52:55], %[rdc] offset:272\n"
    "v_cvt_pk_bf16_f32 v25, v202, v203\n"
    "v_fma_mix_f32 v200, v10, v36, v44 op_sel:[0,0,0] op_sel_hi:[1,0,0]\n"
    "v_mfma_f32_16x16x32_bf16 v[96:99], a[188:191], v[16:19], 0\n"
    "ds_read_b128 v[56:59], %[rdc] offset:288\n"
    "v_fma_mix_f32 v201, v10, v37, v45 op_sel:[1,0,0] op_sel_hi:[1,0,0]\n"
    "v_cvt_pk_bf16_f32 v26, v200, v201\n"
    "v_mfma_f32_16x16x32_bf16 v[100:103], a[188:191], v[20:23], 0\n"
    "ds_read_b128 v[60:63], %[rdc] offset:304\n"
    "v_fma_mix_f32 v202, v11, v38, v46 op_sel:[0,0,0] op_sel_hi:[1,0,0]\n"
    "v_mfma_f32_16x16x32_bf16 v[116:119], a[192:195], v[20:23], 0\n"
    "ds_read_b64_tr_b16 a[212:213], %[rdw0] offset:4096\n"
    "v_fma_mix_f32 v203, v11, v39, v47 op_sel:[1,0,0] op_sel_hi:[1,0,0]\n"
    "v_cvt_pk_bf16_f32 v27, v202, v203\n"
    "v_mfma_f32_16x16x32_bf16 v[112:115], a[192:195], v[16:19], 0\n"
    "ds_read_b64_tr_b16 a[214:215], %[rdw0] offset:4608\n"
    "v_fma_mix_f32 v200, v12, v32, v40 op_sel:[0,0,0] op_sel_hi:[1,0,0]\n"
    "v_mfma_f32_16x16x32_bf16 v[128:131], a[196:199], v[16:19], 0\n"
    "ds_read_b64_tr_b16 a[216:217], %[rdw1] offset:4096\n"
    "v_fma_mix_f32 v201, v12, v33, v41 op_sel:[1,0,0] op_sel_hi:[1,0,0]\n"
    "v_cvt_pk_bf16_f32 v28, v200, v201\n"
    "v_mfma_f32_16x16x32_bf16 v[132:135], a[196:199], v[20:23], 0\n"
    "ds_read_b64_tr_b16 a[218:219], %[rdw1] offset:4608\n"
    "v_fma_mix_f32 v202, v13, v34, v42 op_sel:[0,0,0] op_sel_hi:[1,0,0]\n"
    "v_fma_mix_f32 v203, v13, v35, v43 op_sel:[1,0,0] op_sel_hi:[1,0,0]\n"
    "v_mfma_f32_16x16x32_bf16 v[148:151], a[200:203], v[20:23], 0\n"
    "ds_read_b64_tr_b16 a[220:221], %[rdw2] offset:4096\n"
    "v_cvt_pk_bf16_f32 v29, v202, v203\n"
    "v_mfma_f32_16x16x32_bf16 v[144:147], a[200:203], v[16:19], 0\n"
    "ds_read_b64_tr_b16 a[222:223], %[rdw2] offset:4608\n"
    "v_fma_mix_f32 v200, v14, v36, v44 op_sel:[0,0,0] op_sel_hi:[1,0,0]\n"
    "v_fma_mix_f32 v201, v14, v37, v45 op_sel:[1,0,0] op_sel_hi:[1,0,0]\n"
    "v_mfma_f32_16x16x32_bf16 v[160:163], a[204:207], v[16:19], 0\n"
    "ds_read_b64_tr_b16 a[224:225], %[rdw3] offset:4096\n"
    "v_cvt_pk_bf16_f32 v30, v200, v201\n"
    "v_mfma_f32_16x16x32_bf16 v[164:167], a[204:207], v[20:23], 0\n"
    "ds_read_b64_tr_b16 a[226:227], %[rdw3] offset:4608\n"
    "v_fma_mix_f32 v202, v15, v38, v46 op_sel:[0,0,0] op_sel_hi:[1,0,0]\n"
    "v_fma_mix_f32 v203, v15, v39, v47 op_sel:[1,0,0] op_sel_hi:[1,0,0]\n"
    "v_mfma_f32_16x16x32_bf16 v[180:183], a[208:211], v[20:23], 0\n"
    "buffer_load_dwordx4 %[vx2], %[srdx], %[sox] offen offset:1024 lds\n"
    "v_cvt_pk_bf16_f32 v31, v202, v203\n"
    "v_mfma_f32_16x16x32_bf16 v[176:179], a[208:211], v[16:19], 0\n"
    "s_waitcnt lgkmcnt(8)\n"
    "v_mfma_f32_16x16x32_bf16 v[72:75], a[180:183], v[24:27], 0\n"
    "ds_read_b128 v[8:11], %[rdx] offset:5120\n"
    "v_fma_mix_f32 v200, v0, v48, v56 op_sel:[0,0,0] op_sel_hi:[1,0,0]\n"
    "v_fma_mix_f32 v201, v0, v49, v57 op_sel:[1,0,0] op_sel_hi:[1,0,0]\n"
    "v_cvt_pk_bf16_f32 v16, v200, v201\n"
    "v_mfma_f32_16x16x32_bf16 v[76:79], a[180:183], v[28:31], 0\n"
    "ds_read_b128 v[12:15], %[rdx] offset:7168\n"
    "v_fma_mix_f32 v202, v1, v50, v58 op_sel:[0,0,0] op_sel_hi:[1,0,0]\n"
    "v_fma_mix_f32 v203, v1, v51, v59 op_sel:[1,0,0] op_sel_hi:[1,0,0]\n"
    "v_mfma_f32_16x16x32_bf16 v[92:95], a[184:187], v[28:31], 0\n"
    "ds_read_b64_tr_b16 a[228:229], %[rdw0] offset:12288\n"
    "v_cvt_pk_bf16_f32 v17, v202, v203\n"
    "v_fma_mix_f32 v200, v2, v52, v60 op_sel:[0,0,0] op_sel_hi:[1,0,0]\n"
    "v_mfma_f32_16x16x32_bf16 v[88:91], a[184:187], v[24:27], 0\n"
    "ds_read_b64_tr_b16 a[230:231], %[rdw0] offset:12800\n"
    "v_fma_mix_f32 v201, v2, v53, v61 op_sel:[1,0,0] op_sel_hi:[1,0,0]\n"
    "v_cvt_pk_bf16_f32 v18, v200, v201\n"
    "v_mfma_f32_16x16x32_bf16 v[104:107], a[188:191], v[24:27], 0\n"
    "ds_read_b64_tr_b16 a[232:233], %[rdw1] offset:12288\n"
    "v_fma_mix_f32 v202, v3, v54, v62 op_sel:[0,0,0] op_sel_hi:[1,0,0]\n"
    "v_fma_mix_f32 v203, v3, v55, v63 op_sel:[1,0,0] op_sel_hi:[1,0,0]\n"
    "v_mfma_f32_16x16x32_bf16 v[108:111], a[188:191], v[28:31], 0\n"
    "ds_read_b64_tr_b16 a[234:235], %[rdw1] offset:12800\n"
    "v_cvt_pk_bf16_f32 v19, v202, v203\n"
    "v_fma_mix_f32 v200, v4, v48, v56 op_sel:[0,0,0] op_sel_hi:[1,0,0]\n"
    "v_mfma_f32_16x16x32_bf16 v[124:127], a[192:195], v[28:31], 0\n"
    "ds_read_b64_tr_b16 a[236:237], %[rdw2] offset:12288\n"
    "v_fma_mix_f32 v201, v4, v49, v57 op_sel:[1,0,0] op_sel_hi:[1,0,0]\n"
    "v_cvt_pk_bf16_f32 v20, v200, v201\n"
    "v_mfma_f32_16x16x32_bf16 v[120:123], a[192:195], v[24:27], 0\n"
    "ds_read_b64_tr_b16 a[238:239], %[rdw2] offset:12800\n"
    "v_fma_mix_f32 v202, v5, v50, v58 op_sel:[0,0,0] op_sel_hi:[1,0,0]\n"
    "v_fma_mix_f32 v203, v5, v51, v59 op_sel:[1,0,0] op_sel_hi:[1,0,0]\n"
    "v_mfma_f32_16x16x32_bf16 v[136:139], a[196:199], v[24:27], 0\n"
    "ds_read_b64_tr_b16 a[240:241], %[rdw3] offset:12288\n"
    "v_cvt_pk_bf16_f32 v21, v202, v203\n"
    "v_fma_mix_f32 v200, v6, v52, v60 op_sel:[0,0,0] op_sel_hi:[1,0,0]\n"
    "v_mfma_f32_16x16x32_bf16 v[140:143], a[196:199], v[28:31], 0\n"
    "ds_read_b64_tr_b16 a[242:243], %[rdw3] offset:12800\n"
    "v_fma_mix_f32 v201, v6, v53, v61 op_sel:[1,0,0] op_sel_hi:[1,0,0]\n"
    "v_cvt_pk_bf16_f32 v22, v200, v201\n"
    "v_mfma_f32_16x16x32_bf16 v[156:159], a[200:203], v[28:31], 0\n"
    "s_add_u32 s43, s43, 1\n"
    "buffer_load_dwordx4 %[vx3], %[srdx], %[sox] offen offset:2048 lds\n"
    "s_cmp_eq_u32 s43, 3\n"
    "s_cselect_b32 s46, s47, s48\n"
    "v_fma_mix_f32 v202, v7, v54, v62 op_sel:[0,0,0] op_sel_hi:[1,0,0]\n"
    "v_mfma_f32_16x16x32_bf16 v[152:155], a[200:203], v[24:27], 0\n"
    "buffer_load_dwordx4 %[vx3], %[srdx], %[sox] offen offset:3072 lds\n"
    "s_mov_b32 m0, s41\n"
    "s_cselect_b32 s43, 0, s43\n"
    "v_mfma_f32_16x16x32_bf16 v[168:171], a[204:207], v[24:27], 0\n"
    "buffer_load_dwordx4 %[vw0], %[srdw], %[sow] offen offset:0 lds\n"
    "v_add_u32 %[rdx], s46, %[rdx]\n"
    "v_add_u32 %[rdw0], s46, %[rdw0]\n"
    "v_fma_mix_f32 v203, v7, v55, v63 op_sel:[1,0,0] op_sel_hi:[1,0,0]\n"
    "v_mfma_f32_16x16x32_bf16 v[172:175], a[204:207], v[28:31], 0\n"
    "buffer_load_dwordx4 %[vw1], %[srdw], %[sow] offen offset:1024 lds\n"
    "v_add_u32 %[rdw1], s46, %[rdw1]\n"
    "v_add_u32 %[rdw2], s46, %[rdw2]\n"
    "v_cvt_pk_bf16_f32 v23, v202, v203\n"
    "v_mfma_f32_16x16x32_bf16 v[188:191], a[208:211], v[28:31], 0\n"
    "buffer_load_dwordx4 %[vw2], %[srdw], %[sow] offen offset:2048 lds\n"
    "v_add_u32 %[rdw3], s46, %[rdw3]\n"
    "s_sub_u32 %[wrapr], %[wrapr], 1\n"
    "v_mfma_f32_16x16x32_bf16 v[184:187], a[208:211], v[24:27], 0\n"
    "s_cmp_eq_u32 %[wrapr], 0\n"
    "buffer_load_dwordx4 %[vw3], %[srdw], %[sow] offen offset:3072 lds\n"
    "s_cselect_b32 s46, s54, s55\n"
    "v_add_u32 %[rdc], s46, %[rdc]\n"
    "s_waitcnt vmcnt(12) lgkmcnt(0)\n"
    "s_barrier\n"
    "QM_%=:\n"
    "v_mfma_f32_16x16x32_bf16 v[64:67], a[212:215], v[16:19], v[64:67]\n"
    "s_sub_u32 %[wrap], %[wrap], 1\n"
    "ds_read_b128 v[0:3], %[rdx] offset:0\n"
    "v_fma_mix_f32 v200, v8, v48, v56 op_sel:[0,0,0] op_sel_hi:[1,0,0]\n"
    "v_fma_mix_f32 v201, v8, v49, v57 op_sel:[1,0,0] op_sel_hi:[1,0,0]\n"
    "v_mfma_f32_16x16x32_bf16 v[68:71], a[212:215], v[20:23], v[68:71]\n"
    "s_cmp_eq_u32 %[wrap], 0\n"
    "ds_read_b128 v[4:7], %[rdx] offset:2048\n"
    "v_cvt_pk_bf16_f32 v24, v200, v201\n"
    "v_fma_mix_f32 v202, v9, v50, v58 op_sel:[0,0,0] op_sel_hi:[1,0,0]\n"
    "v_mfma_f32_16x16x32_bf16 v[84:87], a[216:219], v[20:23], v[84:87]\n"
    "s_cselect_b32 s44, 4294950912, s49\n"
    "ds_read_b128 v[32:35], %[rdc] offset:0\n"
    "v_fma_mix_f32 v203, v9, v51, v59 op_sel:[1,0,0] op_sel_hi:[1,0,0]\n"
    "v_mfma_f32_16x16x32_bf16 v[80:83], a[216:219], v[16:19], v[80:83]\n"
    "s_cselect_b32 s45, 4290772992, s51\n"
    "ds_read_b128 v[36:39], %[rdc] offset:16\n"
    "v_cvt_pk_bf16_f32 v25, v202, v203\n"
    "v_fma_mix_f32 v200, v10, v52, v60 op_sel:[0,0,0] op_sel_hi:[1,0,0]\n"
    "v_mfma_f32_16x16x32_bf16 v[96:99], a[220:223], v[16:19], v[96:99]\n"
    "s_add_u32 %[sox], %[sox], s44\n"
    "ds_read_b128 v[40:43], %[rdc] offset:32\n"
    "v_fma_mix_f32 v201, v10, v53, v61 op_sel:[1,0,0] op_sel_hi:[1,0,0]\n"
    "v_cvt_pk_bf16_f32 v26, v200, v201\n"
    "v_mfma_f32_16x16x32_bf16 v[100:103], a[220:223], v[20:23], v[100:103]\n"
    "s_add_u32 %[sow], %[sow], s45\n"
    "ds_read_b128 v[44:47], %[rdc] offset:48\n"
    "v_fma_mix_f32 v202, v11, v54, v62 op_sel:[0,0,0] op_sel_hi:[1,0,0]\n"
    "v_mfma_f32_16x16x32_bf16 v[116:119], a[224:227], v[20:23], v[116:119]\n"
    "s_add_u32 s42, s42, 49152\n"
    "ds_read_b64_tr_b16 a[180:181], %[rdw0] offset:0\n"
    "v_fma_mix_f32 v203, v11, v55, v63 op_sel:[1,0,0] op_sel_hi:[1,0,0]\n"
    "v_cvt_pk_bf16_f32 v27, v202, v203\n"
    "v_mfma_f32_16x16x32_bf16 v[112:115], a[224:227], v[16:19], v[112:115]\n"
    "s_cmp_eq_u32 s42, 147456\n"
    "ds_read_b64_tr_b16 a[182:183], %[rdw0] offset:512\n"
    "v_fma_mix_f32 v200, v12, v48, v56 op_sel:[0,0,0] op_sel_hi:[1,0,0]\n"
    "v_mfma_f32_16x16x32_bf16 v[128:131], a[228:231], v[16:19], v[128:131]\n"
    "s_cselect_b32 s42, 0, s42\n"
    "ds_read_b64_tr_b16 a[184:185], %[rdw1] offset:0\n"
    "v_fma_mix_f32 v201, v12, v49, v57 op_sel:[1,0,0] op_sel_hi:[1,0,0]\n"
    "v_cvt_pk_bf16_f32 v28, v200, v201\n"
    "v_mfma_f32_16x16x32_bf16 v[132:135], a[228:231], v[20:23], v[132:135]\n"
    "s_add_u32 s40, s42, %[m0x]\n"
    "ds_read_b64_tr_b16 a[186:187], %[rdw1] offset:512\n"
    "v_fma_mix_f32 v202, v13, v50, v58 op_sel:[0,0,0] op_sel_hi:[1,0,0]\n"
    "v_fma_mix_f32 v203, v13, v51, v59 op_sel:[1,0,0] op_sel_hi:[1,0,0]\n"
    "v_mfma_f32_16x16x32_bf16 v[148:151], a[232:235], v[20:23], v[148:151]\n"
    "s_add_u32 s41, s42, %[m0w]\n"
    "ds_read_b64_tr_b16 a[188:189], %[rdw2] offset:0\n"
    "v_cvt_pk_bf16_f32 v29, v202, v203\n"
    "v_mfma_f32_16x16x32_bf16 v[144:147], a[232:235], v[16:19], v[144:147]\n"
    "ds_read_b64_tr_b16 a[190:191], %[rdw2] offset:512\n"
    "v_fma_mix_f32 v200, v14, v52, v60 op_sel:[0,0,0] op_sel_hi:[1,0,0]\n"
    "v_fma_mix_f32 v201, v14, v53, v61 op_sel:[1,0,0] op_sel_hi:[1,0,0]\n"
    "v_mfma_f32_16x16x32_bf16 v[160:163], a[236:239], v[16:19], v[160:163]\n"
    "ds_read_b64_tr_b16 a[192:193], %[rdw3] offset:0\n"
    "v_cvt_pk_bf16_f32 v30, v200, v201\n"
    "v_mfma_f32_16x16x32_bf16 v[164:167], a[236:239], v[20:23], v[164:167]\n"
    "s_add_u32 m0, s40, 0\n"
    "ds_read_b64_tr_b16 a[194:195], %[rdw3] offset:512\n"
    "v_fma_mix_f32 v202, v15, v54, v62 op_sel:[0,0,0] op_sel_hi:[1,0,0]\n"
    "v_fma_mix_f32 v203, v15, v55, v63 op_sel:[1,0,0] op_sel_hi:[1,0,0]\n"
    "v_mfma_f32_16x16x32_bf16 v[180:183], a[240:243], v[20:23], v[180:183]\n"
    "buffer_load_dwordx4 %[vx0], %[srdx], %[sox] offen offset:0 lds\n"
    "v_cvt_pk_bf16_f32 v31, v202, v203\n"
    "v_mfma_f32_16x16x32_bf16 v[176:179], a[240:243], v[16:19], v[176:179]\n"
    "s_waitcnt lgkmcnt(8)\n"
    "v_mfma_f32_16x16x32_bf16 v[72:75], a[212:215], v[24:27], v[72:75]\n"
    "ds_read_b128 v[8:11], %[rdx] offset:4096\n"
    "v_fma_mix_f32 v200, v0, v32, v40 op_sel:[0,0,0] op_sel_hi:[1,0,0]\n"
    "v_fma_mix_f32 v201, v0, v33, v41 op_sel:[1,0,0] op_sel_hi:[1,0,0]\n"
    "v_mfma_f32_16x16x32_bf16 v[76:79], a[212:215], v[28:31], v[76:79]\n"
    "ds_read_b128 v[12:15], %[rdx] offset:6144\n"
    "v_cvt_pk_bf16_f32 v16, v200, v201\n"
    "v_fma_mix_f32 v202, v1, v34, v42 op_sel:[0,0,0] op_sel_hi:[1,0,0]\n"
    "v_mfma_f32_16x16x32_bf16 v[92:95], a[216:219], v[28:31], v[92:95]\n"
    "ds_read_b64_tr_b16 a[196:197], %[rdw0] offset:8192\n"
    "v_fma_mix_f32 v203, v1, v35, v43 op_sel:[1,0,0] op_sel_hi:[1,0,0]\n"
    "v_mfma_f32_16x16x32_bf16 v[88:91], a[216:219], v[24:27], v[88:91]\n"
    "ds_read_b64_tr_b16 a[198:199], %[rdw0] offset:8704\n"
    "v_cvt_pk_bf16_f32 v17, v202, v203\n"
    "v_fma_mix_f32 v200, v2, v36, v44 op_sel:[0,0,0] op_sel_hi:[1,0,0]\n"
    "v_mfma_f32_16x16x32_bf16 v[104:107], a[220:223], v[24:27], v[104:107]\n"
    "ds_read_b64_tr_b16 a[200:201], %[rdw1] offset:8192\n"
    "v_fma_mix_f32 v201, v2, v37, v45 op_sel:[1,0,0] op_sel_hi:[1,0,0]\n"
    "v_cvt_pk_bf16_f32 v18, v200, v201\n"
    "v_mfma_f32_16x16x32_bf16 v[108:111], a[220:223], v[28:31], v[108:111]\n"
    "ds_read_b64_tr_b16 a[202:203], %[rdw1] offset:8704\n"
    "v_fma_mix_f32 v202, v3, v38, v46 op_sel:[0,0,0] op_sel_hi:[1,0,0]\n"
    "v_mfma_f32_16x16x32_bf16 v[124:127], a[224:227], v[28:31], v[124:127]\n"
    "ds_read_b64_tr_b16 a[204:205], %[rdw2] offset:8192\n"
    "v_fma_mix_f32 v203, v3, v39, v47 op_sel:[1,0,0] op_sel_hi:[1,0,0]\n"
    "v_cvt_pk_bf16_f32 v19, v202, v203\n"
    "v_mfma_f32_16x16x32_bf16 v[120:123], a[224:227], v[24:27], v[120:123]\n"
    "ds_read_b64_tr_b16 a[206:207], %[rdw2] offset:8704\n"
    "v_fma_mix_f32 v200, v4, v32, v40 op_sel:[0,0,0] op_sel_hi:[1,0,0]\n"
    "v_fma_mix_f32 v201, v4, v33, v41 op_sel:[1,0,0] op_sel_hi:[1,0,0]\n"
    "v_mfma_f32_16x16x32_bf16 v[136:139], a[228:231], v[24:27], v[136:139]\n"
    "ds_read_b64_tr_b16 a[208:209], %[rdw3] offset:8192\n"
    "v_cvt_pk_bf16_f32 v20, v200, v201\n"
    "v_mfma_f32_16x16x32_bf16 v[140:143], a[228:231], v[28:31], v[140:143]\n"
    "ds_read_b64_tr_b16 a[210:211], %[rdw3] offset:8704\n"
    "v_fma_mix_f32 v202, v5, v34, v42 op_sel:[0,0,0] op_sel_hi:[1,0,0]\n"
    "v_fma_mix_f32 v203, v5, v35, v43 op_sel:[1,0,0] op_sel_hi:[1,0,0]\n"
    "v_mfma_f32_16x16x32_bf16 v[156:159], a[232:235], v[28:31], v[156:159]\n"
    "buffer_load_dwordx4 %[vx0], %[srdx], %[sox] offen offset:1024 lds\n"
    "v_cvt_pk_bf16_f32 v21, v202, v203\n"
    "v_fma_mix_f32 v200, v6, v36, v44 op_sel:[0,0,0] op_sel_hi:[1,0,0]\n"
    "v_mfma_f32_16x16x32_bf16 v[152:155], a[232:235], v[24:27], v[152:155]\n"
    "buffer_load_dwordx4 %[vx1], %[srdx], %[sox] offen offset:2048 lds\n"
    "v_fma_mix_f32 v201, v6, v37, v45 op_sel:[1,0,0] op_sel_hi:[1,0,0]\n"
    "v_mfma_f32_16x16x32_bf16 v[168:171], a[236:239], v[24:27], v[168:171]\n"
    "buffer_load_dwordx4 %[vx1], %[srdx], %[sox] offen offset:3072 lds\n"
    "s_add_u32 m0, s40, 4096\n"
    "v_cvt_pk_bf16_f32 v22, v200, v201\n"
    "v_mfma_f32_16x16x32_bf16 v[172:175], a[236:239], v[28:31], v[172:175]\n"
    "buffer_load_dwordx4 %[vx2], %[srdx], %[sox] offen offset:0 lds\n"
    "v_fma_mix_f32 v202, v7, v38, v46 op_sel:[0,0,0] op_sel_hi:[1,0,0]\n"
    "v_fma_mix_f32 v203, v7, v39, v47 op_sel:[1,0,0] op_sel_hi:[1,0,0]\n"
    "v_mfma_f32_16x16x32_bf16 v[188:191], a[240:243], v[28:31], v[188:191]\n"
    "buffer_load_dwordx4 %[vx2], %[srdx], %[sox] offen offset:1024 lds\n"
    "v_cvt_pk_bf16_f32 v23, v202, v203\n"
    "v_mfma_f32_16x16x32_bf16 v[184:187], a[240:243], v[24:27], v[184:187]\n"
    "s_waitcnt lgkmcnt(0)\n"
    "v_mfma_f32_16x16x32_bf16 v[64:67], a[180:183], v[16:19], v[64:67]\n"
    "ds_read_b128 v[0:3], %[rdx] offset:1024\n"
    "v_fma_mix_f32 v200, v8, v32, v40 op_sel:[0,0,0] op_sel_hi:[1,0,0]\n"
    "v_fma_mix_f32 v201, v8, v33, v41 op_sel:[1,0,0] op_sel_hi:[1,0,0]\n"
    "v_mfma_f32_16x16x32_bf16 v[68:71], a[180:183], v[20:23], v[68:71]\n"
    "ds_read_b128 v[4:7], %[rdx] offset:3072\n"
    "v_cvt_pk_bf16_f32 v24, v200, v201\n"
    "v_fma_mix_f32 v202, v9, v34, v42 op_sel:[0,0,0] op_sel_hi:[1,0,0]\n"
    "v_mfma_f32_16x16x32_bf16 v[84:87], a[184:187], v[20:23], v[84:87]\n"
    "ds_read_b128 v[48:51], %[rdc] offset:256\n"
    "v_fma_mix_f32 v203, v9, v35, v43 op_sel:[1,0,0] op_sel_hi:[1,0,0]\n"
    "v_mfma_f32_16x16x32_bf16 v[80:83], a[184:187], v[16:19], v[80:83]\n"
    "ds_read_b128 v[52:55], %[rdc] offset:272\n"
    "v_cvt_pk_bf16_f32 v25, v202, v203\n"
    "v_fma_mix_f32 v200, v10, v36, v44 op_sel:[0,0,0] op_sel_hi:[1,0,0]\n"
    "v_mfma_f32_16x16x32_bf16 v[96:99], a[188:191], v[16:19], v[96:99]\n"
    "ds_read_b128 v[56:59], %[rdc] offset:288\n"
    "v_fma_mix_f32 v201, v10, v37, v45 op_sel:[1,0,0] op_sel_hi:[1,0,0]\n"
    "v_cvt_pk_bf16_f32 v26, v200, v201\n"
    "v_mfma_f32_16x16x32_bf16 v[100:103], a[188:191], v[20:23], v[100:103]\n"
    "ds_read_b128 v[60:63], %[rdc] offset:304\n"
    "v_fma_mix_f32 v202, v11, v38, v46 op_sel:[0,0,0] op_sel_hi:[1,0,0]\n"
    "v_mfma_f32_16x16x32_bf16 v[116:119], a[192:195], v[20:23], v[116:119]\n"
    "ds_read_b64_tr_b16 a[212:213], %[rdw0] offset:4096\n"
    "v_fma_mix_f32 v203, v11, v39, v47 op_sel:[1,0,0] op_sel_hi:[1,0,0]\n"
    "v_cvt_pk_bf16_f32 v27, v202, v203\n"
    "v_mfma_f32_16x16x32_bf16 v[112:115], a[192:195], v[16:19], v[112:115]\n"
    "ds_read_b64_tr_b16 a[214:215], %[rdw0] offset:4608\n"
    "v_fma_mix_f32 v200, v12, v32, v40 op_sel:[0,0,0] op_sel_hi:[1,0,0]\n"
    "v_mfma_f32_16x16x32_bf16 v[128:131], a[196:199], v[16:19], v[128:131]\n"
    "ds_read_b64_tr_b16 a[216:217], %[rdw1] offset:4096\n"
    "v_fma_mix_f32 v201, v12, v33, v41 op_sel:[1,0,0] op_sel_hi:[1,0,0]\n"
    "v_cvt_pk_bf16_f32 v28, v200, v201\n"
    "v_mfma_f32_16x16x32_bf16 v[132:135], a[196:199], v[20:23], v[132:135]\n"
    "ds_read_b64_tr_b16 a[218:219], %[rdw1] offset:4608\n"
    "v_fma_mix_f32 v202, v13, v34, v42 op_sel:[0,0,0] op_sel_hi:[1,0,0]\n"
    "v_fma_mix_f32 v203, v13, v35, v43 op_sel:[1,0,0] op_sel_hi:[1,0,0]\n"
    "v_mfma_f32_16x16x32_bf16 v[148:151], a[200:203], v[20:23], v[148:151]\n"
    "ds_read_b64_tr_b16 a[220:221], %[rdw2] offset:4096\n"
    "v_cvt_pk_bf16_f32 v29, v202, v203\n"
    "v_mfma_f32_16x16x32_bf16 v[144:147], a[200:203], v[16:19], v[144:147]\n"
    "ds_read_b64_tr_b16 a[222:223], %[rdw2] offset:4608\n"
    "v_fma_mix_f32 v200, v14, v36, v44 op_sel:[0,0,0] op_sel_hi:[1,0,0]\n"
    "v_fma_mix_f32 v201, v14, v37, v45 op_sel:[1,0,0] op_sel_hi:[1,0,0]\n"
    "v_mfma_f32_16x16x32_bf16 v[160:163], a[204:207], v[16:19], v[160:163]\n"
    "ds_read_b64_tr_b16 a[224:225], %[rdw3] offset:4096\n"
    "v_cvt_pk_bf16_f32 v30, v200, v201\n"
    "v_mfma_f32_16x16x32_bf16 v[164:167], a[204:207], v[20:23], v[164:167]\n"
    "ds_read_b64_tr_b16 a[226:227], %[rdw3] offset:4608\n"
    "v_fma_mix_f32 v202, v15, v38, v46 op_sel:[0,0,0] op_sel_hi:[1,0,0]\n"
    "v_fma_mix_f32 v203, v15, v39, v47 op_sel:[1,0,0] op_sel_hi:[1,0,0]\n"
    "v_mfma_f32_16x16x32_bf16 v[180:183], a[208:211], v[20:23], v[180:183]\n"
    "buffer_load_dwordx4 %[vx3], %[srdx], %[sox] offen offset:2048 lds\n"
    "v_cvt_pk_bf16_f32 v31, v202, v203\n"
    "v_mfma_f32_16x16x32_bf16 v[176:179], a[208:211], v[16:19], v[176:179]\n"
    "s_waitcnt lgkmcnt(8)\n"
    "v_mfma_f32_16x16x32_bf16 v[72:75], a[180:183], v[24:27], v[72:75]\n"
    "ds_read_b128 v[8:11], %[rdx] offset:5120\n"
    "v_fma_mix_f32 v200, v0, v48, v56 op_sel:[0,0,0] op_sel_hi:[1,0,0]\n"
    "v_fma_mix_f32 v201, v0, v49, v57 op_sel:[1,0,0] op_sel_hi:[1,0,0]\n"
    "v_cvt_pk_bf16_f32 v16, v200, v201\n"
    "v_mfma_f32_16x16x32_bf16 v[76:79], a[180:183], v[28:31], v[76:79]\n"
    "ds_read_b128 v[12:15], %[rdx] offset:7168\n"
    "v_fma_mix_f32 v202, v1, v50, v58 op_sel:[0,0,0] op_sel_hi:[1,0,0]\n"
    "v_fma_mix_f32 v203, v1, v51, v59 op_sel:[1,0,0] op_sel_hi:[1,0,0]\n"
    "v_mfma_f32_16x16x32_bf16 v[92:95], a[184:187], v[28:31], v[92:95]\n"
    "ds_read_b64_tr_b16 a[228:229], %[rdw0] offset:12288\n"
    "v_cvt_pk_bf16_f32 v17, v202, v203\n"
    "v_fma_mix_f32 v200, v2, v52, v60 op_sel:[0,0,0] op_sel_hi:[1,0,0]\n"
    "v_mfma_f32_16x16x32_bf16 v[88:91], a[184:187], v[24:27], v[88:91]\n"
    "ds_read_b64_tr_b16 a[230:231], %[rdw0] offset:12800\n"
    "v_fma_mix_f32 v201, v2, v53, v61 op_sel:[1,0,0] op_sel_hi:[1,0,0]\n"
    "v_cvt_pk_bf16_f32 v18, v200, v201\n"
    "v_mfma_f32_16x16x32_bf16 v[104:107], a[188:191], v[24:27], v[104:107]\n"
    "ds_read_b64_tr_b16 a[232:233], %[rdw1] offset:12288\n"
    "v_fma_mix_f32 v202, v3, v54, v62 op_sel:[0,0,0] op_sel_hi:[1,0,0]\n"
    "v_fma_mix_f32 v203, v3, v55, v63 op_sel:[1,0,0] op_sel_hi:[1,0,0]\n"
    "v_mfma_f32_16x16x32_bf16 v[108:111], a[188:191], v[28:31], v[108:111]\n"
    "ds_read_b64_tr_b16 a[234:235], %[rdw1] offset:12800\n"
    "v_cvt_pk_bf16_f32 v19, v202, v203\n"
    "v_fma_mix_f32 v200, v4, v48, v56 op_sel:[0,0,0] op_sel_hi:[1,0,0]\n"
    "v_mfma_f32_16x16x32_bf16 v[124:127], a[192:195], v[28:31], v[124:127]\n"
    "ds_read_b64_tr_b16 a[236:237], %[rdw2] offset:12288\n"
    "v_fma_mix_f32 v201, v4, v49, v57 op_sel:[1,0,0] op_sel_hi:[1,0,0]\n"
    "v_cvt_pk_bf16_f32 v20, v200, v201\n"
    "v_mfma_f32_16x16x32_bf16 v[120:123], a[192:195], v[24:27], v[120:123]\n"
    "ds_read_b64_tr_b16 a[238:239], %[rdw2] offset:12800\n"
    "v_fma_mix_f32 v202, v5, v50, v58 op_sel:[0,0,0] op_sel_hi:[1,0,0]\n"
    "v_fma_mix_f32 v203, v5, v51, v59 op_sel:[1,0,0] op_sel_hi:[1,0,0]\n"
    "v_mfma_f32_16x16x32_bf16 v[136:139], a[196:199], v[24:27], v[136:139]\n"
    "ds_read_b64_tr_b16 a[240:241], %[rdw3] offset:12288\n"
    "v_cvt_pk_bf16_f32 v21, v202, v203\n"
    "v_fma_mix_f32 v200, v6, v52, v60 op_sel:[0,0,0] op_sel_hi:[1,0,0]\n"
    "v_fma_mix_f32 v201, v6, v53, v61 op_sel:[1,0,0] op_sel_hi:[1,0,0]\n"
    "v_mfma_f32_16x16x32_bf16 v[140:143], a[196:199], v[28:31], v[140:143]\n"
    "ds_read_b64_tr_b16 a[242:243], %[rdw3] offset:12800\n"
    "v_cvt_pk_bf16_f32 v22, v200, v201\n"
    "v_fma_mix_f32 v202, v7, v54, v62 op_sel:[0,0,0] op_sel_hi:[1,0,0]\n"
    "v_mfma_f32_16x16x32_bf16 v[156:159], a[200:203], v[28:31], v[156:159]\n"
    "buffer_load_dwordx4 %[vx3], %[srdx], %[sox] offen offset:3072 lds\n"
    "s_mov_b32 m0, s41\n"
    "s_add_u32 s43, s43, 1\n"
    "s_cmp_eq_u32 s43, 3\n"
    "v_mfma_f32_16x16x32_bf16 v[152:155], a[200:203], v[24:27], v[152:155]\n"
    "s_cselect_b32 s46, s47, s48\n"
    "buffer_load_dwordx4 %[vw0], %[srdw], %[sow] offen offset:0 lds\n"
    "s_cselect_b32 s43, 0, s43\n"
    "v_add_u32 %[rdx], s46, %[rdx]\n"
    "v_add_u32 %[rdw0], s46, %[rdw0]\n"
    "v_mfma_f32_16x16x32_bf16 v[168:171], a[204:207], v[24:27], v[168:171]\n"
    "buffer_load_dwordx4 %[vw1], %[srdw], %[sow] offen offset:1024 lds\n"
    "v_add_u32 %[rdw1], s46, %[rdw1]\n"
    "v_fma_mix_f32 v203, v7, v55, v63 op_sel:[1,0,0] op_sel_hi:[1,0,0]\n"
    "v_mfma_f32_16x16x32_bf16 v[172:175], a[204:207], v[28:31], v[172:175]\n"
    "buffer_load_dwordx4 %[vw2], %[srdw], %[sow] offen offset:2048 lds\n"
    "v_add_u32 %[rdw2], s46, %[rdw2]\n"
    "v_cvt_pk_bf16_f32 v23, v202, v203\n"
    "v_mfma_f32_16x16x32_bf16 v[188:191], a[208:211], v[28:31], v[188:191]\n"
    "buffer_load_dwordx4 %[vw3], %[srdw], %[sow] offen offset:3072 lds\n"
    "v_add_u32 %[rdw3], s46, %[rdw3]\n"
    "s_sub_u32 %[wrapr], %[wrapr], 1\n"
    "s_cmp_eq_u32 %[wrapr], 0\n"
    "v_mfma_f32_16x16x32_bf16 v[184:187], a[208:211], v[24:27], v[184:187]\n"
    "s_cselect_b32 s46, s54, s55\n"
    "v_add_u32 %[rdc], s46, %[rdc]\n"
    "s_sub_u32 s50, s50, 1\n"
    "s_cmp_lg_u32 s50, 0\n"
    "s_waitcnt vmcnt(12) lgkmcnt(0)\n"
    "s_barrier\n"
    "s_cbranch_scc1 QM_%=\n"
    "v_mfma_f32_16x16x32_bf16 v[64:67], a[212:215], v[16:19], v[64:67]\n"
    "ds_read_b128 v[0:3], %[rdx] offset:0\n"
    "v_fma_mix_f32 v200, v8, v48, v56 op_sel:[0,0,0] op_sel_hi:[1,0,0]\n"
    "v_fma_mix_f32 v201, v8, v49, v57 op_sel:[1,0,0] op_sel_hi:[1,0,0]\n"
    "v_mfma_f32_16x16x32_bf16 v[68:71], a[212:215], v[20:23], v[68:71]\n"
    "ds_read_b128 v[4:7], %[rdx] offset:2048\n"
    "v_cvt_pk_bf16_f32 v24, v200, v201\n"
    "v_fma_mix_f32 v202, v9, v50, v58 op_sel:[0,0,0] op_sel_hi:[1,0,0]\n"
    "v_mfma_f32_16x16x32_bf16 v[84:87], a[216:219], v[20:23], v[84:87]\n"
    "ds_read_b128 v[32:35], %[rdc] offset:0\n"
    "v_fma_mix_f32 v203, v9, v51, v59 op_sel:[1,0,0] op_sel_hi:[1,0,0]\n"
    "v_mfma_f32_16x16x32_bf16 v[80:83], a[216:219], v[16:19], v[80:83]\n"
    "ds_read_b128 v[36:39], %[rdc] offset:16\n"
    "v_cvt_pk_bf16_f32 v25, v202, v203\n"
    "v_fma_mix_f32 v200, v10, v52, v60 op_sel:[0,0,0] op_sel_hi:[1,0,0]\n"
    "v_mfma_f32_16x16x32_bf16 v[96:99], a[220:223], v[16:19], v[96:99]\n"
    "ds_read_b128 v[40:43], %[rdc] offset:32\n"
    "v_fma_mix_f32 v201, v10, v53, v61 op_sel:[1,0,0] op_sel_hi:[1,0,0]\n"
    "v_cvt_pk_bf16_f32 v26, v200, v201\n"
    "v_mfma_f32_16x16x32_bf16 v[100:103], a[220:223], v[20:23], v[100:103]\n"
    "ds_read_b128 v[44:47], %[rdc] offset:48\n"
    "v_fma_mix_f32 v202, v11, v54, v62 op_sel:[0,0,0] op_sel_hi:[1,0,0]\n"
    "v_mfma_f32_16x16x32_bf16 v[116:119], a[224:227], v[20:23], v[116:119]\n"
    "ds_read_b64_tr_b16 a[180:181], %[rdw0] offset:0\n"
    "v_fma_mix_f32 v203, v11, v55, v63 op_sel:[1,0,0] op_sel_hi:[1,0,0]\n"
    "v_cvt_pk_bf16_f32 v27, v202, v203\n"
    "v_mfma_f32_16x16x32_bf16 v[112:115], a[224:227], v[16:19], v[112:115]\n"
    "ds_read_b64_tr_b16 a[182:183], %[rdw0] offset:512\n"
    "v_fma_mix_f32 v200, v12, v48, v56 op_sel:[0,0,0] op_sel_hi:[1,0,0]\n"
    "v_mfma_f32_16x16x32_bf16 v[128:131], a[228:231], v[16:19], v[128:131]\n"
    "ds_read_b64_tr_b16 a[184:185], %[rdw1] offset:0\n"
    "v_fma_mix_f32 v201, v12, v49, v57 op_sel:[1,0,0] op_sel_hi:[1,0,0]\n"
    "v_cvt_pk_bf16_f32 v28, v200, v201\n"
    "v_mfma_f32_16x16x32_bf16 v[132:135], a[228:231], v[20:23], v[132:135]\n"
    "ds_read_b64_tr_b16 a[186:187], %[rdw1] offset:512\n"
    "v_fma_mix_f32 v202, v13, v50, v58 op_sel:[0,0,0] op_sel_hi:[1,0,0]\n"
    "v_fma_mix_f32 v203, v13, v51, v59 op_sel:[1,0,0] op_sel_hi:[1,0,0]\n"
    "v_mfma_f32_16x16x32_bf16 v[148:151], a[232:235], v[20:23], v[148:151]\n"
    "ds_read_b64_tr_b16 a[188:189], %[rdw2] offset:0\n"
    "v_cvt_pk_bf16_f32 v29, v202, v203\n"
    "v_mfma_f32_16x16x32_bf16 v[144:147], a[232:235], v[16:19], v[144:147]\n"
    "ds_read_b64_tr_b16 a[190:191], %[rdw2] offset:512\n"
    "v_fma_mix_f32 v200, v14, v52, v60 op_sel:[0,0,0] op_sel_hi:[1,0,0]\n"
    "v_fma_mix_f32 v201, v14, v53, v61 op_sel:[1,0,0] op_sel_hi:[1,0,0]\n"
    "v_mfma_f32_16x16x32_bf16 v[160:163], a[236:239], v[16:19], v[160:163]\n"
    "ds_read_b64_tr_b16 a[192:193], %[rdw3] offset:0\n"
    "v_cvt_pk_bf16_f32 v30, v200, v201\n"
    "v_mfma_f32_16x16x32_bf16 v[164:167], a[236:239], v[20:23], v[164:167]\n"
    "s_add_u32 m0, %[m0kx], 147456\n"
    "ds_read_b64_tr_b16 a[194:195], %[rdw3] offset:512\n"
    "v_fma_mix_f32 v202, v15, v54, v62 op_sel:[0,0,0] op_sel_hi:[1,0,0]\n"
    "v_fma_mix_f32 v203, v15, v55, v63 op_sel:[1,0,0] op_sel_hi:[1,0,0]\n"
    "v_mfma_f32_16x16x32_bf16 v[180:183], a[240:243], v[20:23], v[180:183]\n"
    "buffer_load_dwordx4 %[vkx], %[srdk0], 0 offen lds\n"
    "v_cvt_pk_bf16_f32 v31, v202, v203\n"
    "v_mfma_f32_16x16x32_bf16 v[176:179], a[240:243], v[16:19], v[176:179]\n"
    "s_waitcnt lgkmcnt(8)\n"
    "v_mfma_f32_16x16x32_bf16 v[72:75], a[212:215], v[24:27], v[72:75]\n"
    "ds_read_b128 v[8:11], %[rdx] offset:4096\n"
    "v_fma_mix_f32 v200, v0, v32, v40 op_sel:[0,0,0] op_sel_hi:[1,0,0]\n"
    "v_fma_mix_f32 v201, v0, v33, v41 op_sel:[1,0,0] op_sel_hi:[1,0,0]\n"
    "v_mfma_f32_16x16x32_bf16 v[76:79], a[212:215], v[28:31], v[76:79]\n"
    "ds_read_b128 v[12:15], %[rdx] offset:6144\n"
    "v_cvt_pk_bf16_f32 v16, v200, v201\n"
    "v_fma_mix_f32 v202, v1, v34, v42 op_sel:[0,0,0] op_sel_hi:[1,0,0]\n"
    "v_mfma_f32_16x16x32_bf16 v[92:95], a[216:219], v[28:31], v[92:95]\n"
    "ds_read_b64_tr_b16 a[196:197], %[rdw0] offset:8192\n"
    "v_fma_mix_f32 v203, v1, v35, v43 op_sel:[1,0,0] op_sel_hi:[1,0,0]\n"
    "v_mfma_f32_16x16x32_bf16 v[88:91], a[216:219], v[24:27], v[88:91]\n"
    "ds_read_b64_tr_b16 a[198:199], %[rdw0] offset:8704\n"
    "v_cvt_pk_bf16_f32 v17, v202, v203\n"
    "v_fma_mix_f32 v200, v2, v36, v44 op_sel:[0,0,0] op_sel_hi:[1,0,0]\n"
    "v_mfma_f32_16x16x32_bf16 v[104:107], a[220:223], v[24:27], v[104:107]\n"
    "ds_read_b64_tr_b16 a[200:201], %[rdw1] offset:8192\n"
    "v_fma_mix_f32 v201, v2, v37, v45 op_sel:[1,0,0] op_sel_hi:[1,0,0]\n"
    "v_cvt_pk_bf16_f32 v18, v200, v201\n"
    "v_mfma_f32_16x16x32_bf16 v[108:111], a[220:223], v[28:31], v[108:111]\n"
    "ds_read_b64_tr_b16 a[202:203], %[rdw1] offset:8704\n"
    "v_fma_mix_f32 v202, v3, v38, v46 op_sel:[0,0,0] op_sel_hi:[1,0,0]\n"
    "v_mfma_f32_16x16x32_bf16 v[124:127], a[224:227], v[28:31], v[124:127]\n"
    "ds_read_b64_tr_b16 a[204:205], %[rdw2] offset:8192\n"
    "v_fma_mix_f32 v203, v3, v39, v47 op_sel:[1,0,0] op_sel_hi:[1,0,0]\n"
    "v_cvt_pk_bf16_f32 v19, v202, v203\n"
    "v_mfma_f32_16x16x32_bf16 v[120:123], a[224:227], v[24:27], v[120:123]\n"
    "ds_read_b64_tr_b16 a[206:207], %[rdw2] offset:8704\n"
    "v_fma_mix_f32 v200, v4, v32, v40 op_sel:[0,0,0] op_sel_hi:[1,0,0]\n"
    "v_mfma_f32_16x16x32_bf16 v[136:139], a[228:231], v[24:27], v[136:139]\n"
    "ds_read_b64_tr_b16 a[208:209], %[rdw3] offset:8192\n"
    "v_fma_mix_f32 v201, v4, v33, v41 op_sel:[1,0,0] op_sel_hi:[1,0,0]\n"
    "v_cvt_pk_bf16_f32 v20, v200, v201\n"
    "v_mfma_f32_16x16x32_bf16 v[140:143], a[228:231], v[28:31], v[140:143]\n"
    "ds_read_b64_tr_b16 a[210:211], %[rdw3] offset:8704\n"
    "v_fma_mix_f32 v202, v5, v34, v42 op_sel:[0,0,0] op_sel_hi:[1,0,0]\n"
    "v_fma_mix_f32 v203, v5, v35, v43 op_sel:[1,0,0] op_sel_hi:[1,0,0]\n"
    "v_mfma_f32_16x16x32_bf16 v[156:159], a[232:235], v[28:31], v[156:159]\n"
    "buffer_load_dwordx4 %[vkx], %[srdk0], 0 offen offset:1024 lds\n"
    "s_add_u32 m0, %[m0kv], 0\n"
    "v_mfma_f32_16x16x32_bf16 v[152:155], a[232:235], v[24:27], v[152:155]\n"
    "buffer_load_dwordx4 %[vdma], %[srdk], 0 offen offset:0 lds\n"
    "v_cvt_pk_bf16_f32 v21, v202, v203\n"
    "v_fma_mix_f32 v200, v6, v36, v44 op_sel:[0,0,0] op_sel_hi:[1,0,0]\n"
    "v_mfma_f32_16x16x32_bf16 v[168:171], a[236:239], v[24:27], v[168:171]\n"
    "buffer_load_dwordx4 %[vdma], %[srdk], 0 offen offset:1024 lds\n"
    "v_fma_mix_f32 v201, v6, v37, v45 op_sel:[1,0,0] op_sel_hi:[1,0,0]\n"
    "v_cvt_pk_bf16_f32 v22, v200, v201\n"
    "v_mfma_f32_16x16x32_bf16 v[172:175], a[236:239], v[28:31], v[172:175]\n"
    "buffer_load_dwordx4 %[vdma], %[srdk], 0 offen offset:2048 lds\n"
    "v_fma_mix_f32 v202, v7, v38, v46 op_sel:[0,0,0] op_sel_hi:[1,0,0]\n"
    "v_mfma_f32_16x16x32_bf16 v[188:191], a[240:243], v[28:31], v[188:191]\n"
    "v_fma_mix_f32 v203, v7, v39, v47 op_sel:[1,0,0] op_sel_hi:[1,0,0]\n"
    "v_cvt_pk_bf16_f32 v23, v202, v203\n"
    "v_mfma_f32_16x16x32_bf16 v[184:187], a[240:243], v[24:27], v[184:187]\n"
    "s_waitcnt lgkmcnt(0)\n"
    "v_mfma_f32_16x16x32_bf16 v[64:67], a[180:183], v[16:19], v[64:67]\n"
    "ds_read_b128 v[0:3], %[rdx] offset:1024\n"
    "v_fma_mix_f32 v200, v8, v32, v40 op_sel:[0,0,0] op_sel_hi:[1,0,0]\n"
    "v_fma_mix_f32 v201, v8, v33, v41 op_sel:[1,0,0] op_sel_hi:[1,0,0]\n"
    "v_mfma_f32_16x16x32_bf16 v[68:71], a[180:183], v[20:23], v[68:71]\n"
    "ds_read_b128 v[4:7], %[rdx] offset:3072\n"
    "v_cvt_pk_bf16_f32 v24, v200, v201\n"
    "v_fma_mix_f32 v202, v9, v34, v42 op_sel:[0,0,0] op_sel_hi:[1,0,0]\n"
    "v_mfma_f32_16x16x32_bf16 v[84:87], a[184:187], v[20:23], v[84:87]\n"
    "ds_read_b128 v[48:51], %[rdc] offset:256\n"
    "v_fma_mix_f32 v203, v9, v35, v43 op_sel:[1,0,0] op_sel_hi:[1,0,0]\n"
    "v_mfma_f32_16x16x32_bf16 v[80:83], a[184:187], v[16:19], v[80:83]\n"
    "ds_read_b128 v[52:55], %[rdc] offset:272\n"
    "v_cvt_pk_bf16_f32 v25, v202, v203\n"
    "v_fma_mix_f32 v200, v10, v36, v44 op_sel:[0,0,0] op_sel_hi:[1,0,0]\n"
    "v_mfma_f32_16x16x32_bf16 v[96:99], a[188:191], v[16:19], v[96:99]\n"
    "ds_read_b128 v[56:59], %[rdc] offset:288\n"
    "v_fma_mix_f32 v201, v10, v37, v45 op_sel:[1,0,0] op_sel_hi:[1,0,0]\n"
    "v_cvt_pk_bf16_f32 v26, v200, v201\n"
    "v_mfma_f32_16x16x32_bf16 v[100:103], a[188:191], v[20:23], v[100:103]\n"
    "ds_read_b128 v[60:63], %[rdc] offset:304\n"
    "v_fma_mix_f32 v202, v11, v38, v46 op_sel:[0,0,0] op_sel_hi:[1,0,0]\n"
    "v_mfma_f32_16x16x32_bf16 v[116:119], a[192:195], v[20:23], v[116:119]\n"
    "ds_read_b64_tr_b16 a[212:213], %[rdw0] offset:4096\n"
    "v_fma_mix_f32 v203, v11, v39, v47 op_sel:[1,0,0] op_sel_hi:[1,0,0]\n"
    "v_cvt_pk_bf16_f32 v27, v202, v203\n"
    "v_mfma_f32_16x16x32_bf16 v[112:115], a[192:195], v[16:19], v[112:115]\n"
    "ds_read_b64_tr_b16 a[214:215], %[rdw0] offset:4608\n"
    "v_fma_mix_f32 v200, v12, v32, v40 op_sel:[0,0,0] op_sel_hi:[1,0,0]\n"
    "v_mfma_f32_16x16x32_bf16 v[128:131], a[196:199], v[16:19], v[128:131]\n"
    "ds_read_b64_tr_b16 a[216:217], %[rdw1] offset:4096\n"
    "v_fma_mix_f32 v201, v12, v33, v41 op_sel:[1,0,0] op_sel_hi:[1,0,0]\n"
    "v_cvt_pk_bf16_f32 v28, v200, v201\n"
    "v_mfma_f32_16x16x32_bf16 v[132:135], a[196:199], v[20:23], v[132:135]\n"
    "ds_read_b64_tr_b16 a[218:219], %[rdw1] offset:4608\n"
    "v_fma_mix_f32 v202, v13, v34, v42 op_sel:[0,0,0] op_sel_hi:[1,0,0]\n"
    "v_fma_mix_f32 v203, v13, v35, v43 op_sel:[1,0,0] op_sel_hi:[1,0,0]\n"
    "v_mfma_f32_16x16x32_bf16 v[148:151], a[200:203], v[20:23], v[148:151]\n"
    "ds_read_b64_tr_b16 a[220:221], %[rdw2] offset:4096\n"
    "v_cvt_pk_bf16_f32 v29, v202, v203\n"
    "v_mfma_f32_16x16x32_bf16 v[144:147], a[200:203], v[16:19], v[144:147]\n"
    "ds_read_b64_tr_b16 a[222:223], %[rdw2] offset:4608\n"
    "v_fma_mix_f32 v200, v14, v36, v44 op_sel:[0,0,0] op_sel_hi:[1,0,0]\n"
    "v_fma_mix_f32 v201, v14, v37, v45 op_sel:[1,0,0] op_sel_hi:[1,0,0]\n"
    "v_mfma_f32_16x16x32_bf16 v[160:163], a[204:207], v[16:19], v[160:163]\n"
    "ds_read_b64_tr_b16 a[224:225], %[rdw3] offset:4096\n"
    "v_cvt_pk_bf16_f32 v30, v200, v201\n"
    "v_mfma_f32_16x16x32_bf16 v[164:167], a[204:207], v[20:23], v[164:167]\n"
    "ds_read_b64_tr_b16 a[226:227], %[rdw3] offset:4608\n"
    "v_fma_mix_f32 v202, v15, v38, v46 op_sel:[0,0,0] op_sel_hi:[1,0,0]\n"
    "v_fma_mix_f32 v203, v15, v39, v47 op_sel:[1,0,0] op_sel_hi:[1,0,0]\n"
    "v_mfma_f32_16x16x32_bf16 v[180:183], a[208:211], v[20:23], v[180:183]\n"
    "buffer_load_dwordx4 %[vdma], %[srdk], 0 offen offset:3072 lds\n"
    "v_cvt_pk_bf16_f32 v31, v202, v203\n"
    "v_mfma_f32_16x16x32_bf16 v[176:179], a[208:211], v[16:19], v[176:179]\n"
    "s_waitcnt lgkmcnt(8)\n"
    "v_mfma_f32_16x16x32_bf16 v[72:75], a[180:183], v[24:27], v[72:75]\n"
    "ds_read_b128 v[8:11], %[rdx] offset:5120\n"
    "v_fma_mix_f32 v200, v0, v48, v56 op_sel:[0,0,0] op_sel_hi:[1,0,0]\n"
    "v_fma_mix_f32 v201, v0, v49, v57 op_sel:[1,0,0] op_sel_hi:[1,0,0]\n"
    "v_mfma_f32_16x16x32_bf16 v[76:79], a[180:183], v[28:31], v[76:79]\n"
    "ds_read_b128 v[12:15], %[rdx] offset:7168\n"
    "v_cvt_pk_bf16_f32 v16, v200, v201\n"
    "v_fma_mix_f32 v202, v1, v50, v58 op_sel:[0,0,0] op_sel_hi:[1,0,0]\n"
    "v_mfma_f32_16x16x32_bf16 v[92:95], a[184:187], v[28:31], v[92:95]\n"
    "ds_read_b64_tr_b16 a[228:229], %[rdw0] offset:12288\n"
    "v_fma_mix_f32 v203, v1, v51, v59 op_sel:[1,0,0] op_sel_hi:[1,0,0]\n"
    "v_cvt_pk_bf16_f32 v17, v202, v203\n"
    "v_mfma_f32_16x16x32_bf16 v[88:91], a[184:187], v[24:27], v[88:91]\n"
    "ds_read_b64_tr_b16 a[230:231], %[rdw0] offset:12800\n"
    "v_fma_mix_f32 v200, v2, v52, v60 op_sel:[0,0,0] op_sel_hi:[1,0,0]\n"
    "v_fma_mix_f32 v201, v2, v53, v61 op_sel:[1,0,0] op_sel_hi:[1,0,0]\n"
    "v_mfma_f32_16x16x32_bf16 v[104:107], a[188:191], v[24:27], v[104:107]\n"
    "ds_read_b64_tr_b16 a[232:233], %[rdw1] offset:12288\n"
    "v_cvt_pk_bf16_f32 v18, v200, v201\n"
    "v_fma_mix_f32 v202, v3, v54, v62 op_sel:[0,0,0] op_sel_hi:[1,0,0]\n"
    "v_mfma_f32_16x16x32_bf16 v[108:111], a[188:191], v[28:31], v[108:111]\n"
    "ds_read_b64_tr_b16 a[234:235], %[rdw1] offset:12800\n"
    "v_fma_mix_f32 v203, v3, v55, v63 op_sel:[1,0,0] op_sel_hi:[1,0,0]\n"
    "v_cvt_pk_bf16_f32 v19, v202, v203\n"
    "v_mfma_f32_16x16x32_bf16 v[124:127], a[192:195], v[28:31], v[124:127]\n"
    "ds_read_b64_tr_b16 a[236:237], %[rdw2] offset:12288\n"
    "v_fma_mix_f32 v200, v4, v48, v56 op_sel:[0,0,0] op_sel_hi:[1,0,0]\n"
    "v_fma_mix_f32 v201, v4, v49, v57 op_sel:[1,0,0] op_sel_hi:[1,0,0]\n"
    "v_mfma_f32_16x16x32_bf16 v[120:123], a[192:195], v[24:27], v[120:123]\n"
    "ds_read_b64_tr_b16 a[238:239], %[rdw2] offset:12800\n"
    "v_cvt_pk_bf16_f32 v20, v200, v201\n"
    "v_fma_mix_f32 v202, v5, v50, v58 op_sel:[0,0,0] op_sel_hi:[1,0,0]\n"
    "v_mfma_f32_16x16x32_bf16 v[136:139], a[196:199], v[24:27], v[136:139]\n"
    "ds_read_b64_tr_b16 a[240:241], %[rdw3] offset:12288\n"
    "v_fma_mix_f32 v203, v5, v51, v59 op_sel:[1,0,0] op_sel_hi:[1,0,0]\n"
    "v_cvt_pk_bf16_f32 v21, v202, v203\n"
    "v_mfma_f32_16x16x32_bf16 v[140:143], a[196:199], v[28:31], v[140:143]\n"
    "s_add_u32 m0, %[m0kv], 16384\n"
    "ds_read_b64_tr_b16 a[242:243], %[rdw3] offset:12800\n"
    "v_fma_mix_f32 v200, v6, v52, v60 op_sel:[0,0,0] op_sel_hi:[1,0,0]\n"
    "v_fma_mix_f32 v201, v6, v53, v61 op_sel:[1,0,0] op_sel_hi:[1,0,0]\n"
    "v_mfma_f32_16x16x32_bf16 v[156:159], a[200:203], v[28:31], v[156:159]\n"
    "s_add_u32 s43, s43, 1\n"
    "buffer_load_dwordx4 %[vdma], %[srdv], 0 offen offset:0 lds\n"
    "s_cmp_eq_u32 s43, 3\n"
    "s_cselect_b32 s46, s47, s48\n"
    "v_cvt_pk_bf16_f32 v22, v200, v201\n"
    "v_mfma_f32_16x16x32_bf16 v[152:155], a[200:203], v[24:27], v[152:155]\n"
    "s_cselect_b32 s43, 0, s43\n"
    "buffer_load_dwordx4 %[vdma], %[srdv], 0 offen offset:1024 lds\n"
    "v_add_u32 %[rdx], s46, %[rdx]\n"
    "v_add_u32 %[rdw0], s46, %[rdw0]\n"
    "v_mfma_f32_16x16x32_bf16 v[168:171], a[204:207], v[24:27], v[168:171]\n"
    "buffer_load_dwordx4 %[vdma], %[srdv], 0 offen offset:2048 lds\n"
    "v_add_u32 %[rdw1], s46, %[rdw1]\n"
    "v_fma_mix_f32 v202, v7, v54, v62 op_sel:[0,0,0] op_sel_hi:[1,0,0]\n"
    "v_fma_mix_f32 v203, v7, v55, v63 op_sel:[1,0,0] op_sel_hi:[1,0,0]\n"
    "v_mfma_f32_16x16x32_bf16 v[172:175], a[204:207], v[28:31], v[172:175]\n"
    "buffer_load_dwordx4 %[vdma], %[srdv], 0 offen offset:3072 lds\n"
    "v_add_u32 %[rdw2], s46, %[rdw2]\n"
    "v_cvt_pk_bf16_f32 v23, v202, v203\n"
    "v_mfma_f32_16x16x32_bf16 v[188:191], a[208:211], v[28:31], v[188:191]\n"
    "v_add_u32 %[rdw3], s46, %[rdw3]\n"
    "s_sub_u32 %[wrapr], %[wrapr], 1\n"
    "s_cmp_eq_u32 %[wrapr], 0\n"
    "v_mfma_f32_16x16x32_bf16 v[184:187], a[208:211], v[24:27], v[184:187]\n"
    "s_cselect_b32 s46, s54, s55\n"
    "v_add_u32 %[rdc], s46, %[rdc]\n"
    "s_waitcnt vmcnt(10) lgkmcnt(0)\n"
    "s_barrier\n"
    "v_mfma_f32_16x16x32_bf16 v[64:67], a[212:215], v[16:19], v[64:67]\n"
    "ds_read_b128 v[0:3], %[rdx] offset:0\n"
    "v_fma_mix_f32 v200, v8, v48, v56 op_sel:[0,0,0] op_sel_hi:[1,0,0]\n"
    "v_fma_mix_f32 v201, v8, v49, v57 op_sel:[1,0,0] op_sel_hi:[1,0,0]\n"
    "v_mfma_f32_16x16x32_bf16 v[68:71], a[212:215], v[20:23], v[68:71]\n"
    "ds_read_b128 v[4:7], %[rdx] offset:2048\n"
    "v_cvt_pk_bf16_f32 v24, v200, v201\n"
    "v_fma_mix_f32 v202, v9, v50, v58 op_sel:[0,0,0] op_sel_hi:[1,0,0]\n"
    "v_mfma_f32_16x16x32_bf16 v[84:87], a[216:219], v[20:23], v[84:87]\n"
    "ds_read_b128 v[32:35], %[rdc] offset:0\n"
    "v_fma_mix_f32 v203, v9, v51, v59 op_sel:[1,0,0] op_sel_hi:[1,0,0]\n"
    "v_mfma_f32_16x16x32_bf16 v[80:83], a[216:219], v[16:19], v[80:83]\n"
    "ds_read_b128 v[36:39], %[rdc] offset:16\n"
    "v_cvt_pk_bf16_f32 v25, v202, v203\n"
    "v_fma_mix_f32 v200, v10, v52, v60 op_sel:[0,0,0] op_sel_hi:[1,0,0]\n"
    "v_mfma_f32_16x16x32_bf16 v[96:99], a[220:223], v[16:19], v[96:99]\n"
    "ds_read_b128 v[40:43], %[rdc] offset:32\n"
    "v_fma_mix_f32 v201, v10, v53, v61 op_sel:[1,0,0] op_sel_hi:[1,0,0]\n"
    "v_mfma_f32_16x16x32_bf16 v[100:103], a[220:223], v[20:23], v[100:103]\n"
    "ds_read_b128 v[44:47], %[rdc] offset:48\n"
    "v_cvt_pk_bf16_f32 v26, v200, v201\n"
    "v_fma_mix_f32 v202, v11, v54, v62 op_sel:[0,0,0] op_sel_hi:[1,0,0]\n"
    "v_mfma_f32_16x16x32_bf16 v[116:119], a[224:227], v[20:23], v[116:119]\n"
    "ds_read_b64_tr_b16 a[180:181], %[rdw0] offset:0\n"
    "v_fma_mix_f32 v203, v11, v55, v63 op_sel:[1,0,0] op_sel_hi:[1,0,0]\n"
    "v_mfma_f32_16x16x32_bf16 v[112:115], a[224:227], v[16:19], v[112:115]\n"
    "ds_read_b64_tr_b16 a[182:183], %[rdw0] offset:512\n"
    "v_cvt_pk_bf16_f32 v27, v202, v203\n"
    "v_fma_mix_f32 v200, v12, v48, v56 op_sel:[0,0,0] op_sel_hi:[1,0,0]\n"
    "v_mfma_f32_16x16x32_bf16 v[128:131], a[228:231], v[16:19], v[128:131]\n"
    "ds_read_b64_tr_b16 a[184:185], %[rdw1] offset:0\n"
    "v_fma_mix_f32 v201, v12, v49, v57 op_sel:[1,0,0] op_sel_hi:[1,0,0]\n"
    "v_mfma_f32_16x16x32_bf16 v[132:135], a[228:231], v[20:23], v[132:135]\n"
    "ds_read_b64_tr_b16 a[186:187], %[rdw1] offset:512\n"
    "v_cvt_pk_bf16_f32 v28, v200, v201\n"
    "v_fma_mix_f32 v202, v13, v50, v58 op_sel:[0,0,0] op_sel_hi:[1,0,0]\n"
    "v_mfma_f32_16x16x32_bf16 v[148:151], a[232:235], v[20:23], v[148:151]\n"
    "ds_read_b64_tr_b16 a[188:189], %[rdw2] offset:0\n"
    "v_fma_mix_f32 v203, v13, v51, v59 op_sel:[1,0,0] op_sel_hi:[1,0,0]\n"
    "v_cvt_pk_bf16_f32 v29, v202, v203\n"
    "v_mfma_f32_16x16x32_bf16 v[144:147], a[232:235], v[16:19], v[144:147]\n"
    "ds_read_b64_tr_b16 a[190:191], %[rdw2] offset:512\n"
    "v_fma_mix_f32 v200, v14, v52, v60 op_sel:[0,0,0] op_sel_hi:[1,0,0]\n"
    "v_mfma_f32_16x16x32_bf16 v[160:163], a[236:239], v[16:19], v[160:163]\n"
    "ds_read_b64_tr_b16 a[192:193], %[rdw3] offset:0\n"
    "v_fma_mix_f32 v201, v14, v53, v61 op_sel:[1,0,0] op_sel_hi:[1,0,0]\n"
    "v_cvt_pk_bf16_f32 v30, v200, v201\n"
    "v_mfma_f32_16x16x32_bf16 v[164:167], a[236:239], v[20:23], v[164:167]\n"
    "ds_read_b64_tr_b16 a[194:195], %[rdw3] offset:512\n"
    "v_fma_mix_f32 v202, v15, v54, v62 op_sel:[0,0,0] op_sel_hi:[1,0,0]\n"
    "v_mfma_f32_16x16x32_bf16 v[180:183], a[240:243], v[20:23], v[180:183]\n"
    "v_fma_mix_f32 v203, v15, v55, v63 op_sel:[1,0,0] op_sel_hi:[1,0,0]\n"
    "v_cvt_pk_bf16_f32 v31, v202, v203\n"
    "v_mfma_f32_16x16x32_bf16 v[176:179], a[240:243], v[16:19], v[176:179]\n"
    "s_waitcnt lgkmcnt(8)\n"
    "v_mfma_f32_16x16x32_bf16 v[72:75], a[212:215], v[24:27], v[72:75]\n"
    "ds_read_b128 v[8:11], %[rdx] offset:4096\n"
    "v_fma_mix_f32 v200, v0, v32, v40 op_sel:[0,0,0] op_sel_hi:[1,0,0]\n"
    "v_fma_mix_f32 v201, v0, v33, v41 op_sel:[1,0,0] op_sel_hi:[1,0,0]\n"
    "v_mfma_f32_16x16x32_bf16 v[76:79], a[212:215], v[28:31], v[76:79]\n"
    "ds_read_b128 v[12:15], %[rdx] offset:6144\n"
    "v_cvt_pk_bf16_f32 v16, v200, v201\n"
    "v_mfma_f32_16x16x32_bf16 v[92:95], a[216:219], v[28:31], v[92:95]\n"
    "ds_read_b64_tr_b16 a[196:197], %[rdw0] offset:8192\n"
    "v_fma_mix_f32 v202, v1, v34, v42 op_sel:[0,0,0] op_sel_hi:[1,0,0]\n"
    "v_fma_mix_f32 v203, v1, v35, v43 op_sel:[1,0,0] op_sel_hi:[1,0,0]\n"
    "v_mfma_f32_16x16x32_bf16 v[88:91], a[216:219], v[24:27], v[88:91]\n"
    "ds_read_b64_tr_b16 a[198:199], %[rdw0] offset:8704\n"
    "v_cvt_pk_bf16_f32 v17, v202, v203\n"
    "v_mfma_f32_16x16x32_bf16 v[104:107], a[220:223], v[24:27], v[104:107]\n"
    "ds_read_b64_tr_b16 a[200:201], %[rdw1] offset:8192\n"
    "v_fma_mix_f32 v200, v2, v36, v44 op_sel:[0,0,0] op_sel_hi:[1,0,0]\n"
    "v_mfma_f32_16x16x32_bf16 v[108:111], a[220:223], v[28:31], v[108:111]\n"
    "ds_read_b64_tr_b16 a[202:203], %[rdw1] offset:8704\n"
    "v_fma_mix_f32 v201, v2, v37, v45 op_sel:[1,0,0] op_sel_hi:[1,0,0]\n"
    "v_cvt_pk_bf16_f32 v18, v200, v201\n"
    "v_mfma_f32_16x16x32_bf16 v[124:127], a[224:227], v[28:31], v[124:127]\n"
    "ds_read_b64_tr_b16 a[204:205], %[rdw2] offset:8192\n"
    "v_fma_mix_f32 v202, v3, v38, v46 op_sel:[0,0,0] op_sel_hi:[1,0,0]\n"
    "v_mfma_f32_16x16x32_bf16 v[120:123], a[224:227], v[24:27], v[120:123]\n"
    "ds_read_b64_tr_b16 a[206:207], %[rdw2] offset:8704\n"
    "v_fma_mix_f32 v203, v3, v39, v47 op_sel:[1,0,0] op_sel_hi:[1,0,0]\n"
    "v_mfma_f32_16x16x32_bf16 v[136:139], a[228:231], v[24:27], v[136:139]\n"
    "ds_read_b64_tr_b16 a[208:209], %[rdw3] offset:8192\n"
    "v_cvt_pk_bf16_f32 v19, v202, v203\n"
    "v_fma_mix_f32 v200, v4, v32, v40 op_sel:[0,0,0] op_sel_hi:[1,0,0]\n"
    "v_mfma_f32_16x16x32_bf16 v[140:143], a[228:231], v[28:31], v[140:143]\n"
    "ds_read_b64_tr_b16 a[210:211], %[rdw3] offset:8704\n"
    "v_fma_mix_f32 v201, v4, v33, v41 op_sel:[1,0,0] op_sel_hi:[1,0,0]\n"
    "v_mfma_f32_16x16x32_bf16 v[156:159], a[232:235], v[28:31], v[156:159]\n"
    "v_cvt_pk_bf16_f32 v20, v200, v201\n"
    "v_fma_mix_f32 v202, v5, v34, v42 op_sel:[0,0,0] op_sel_hi:[1,0,0]\n"
    "v_mfma_f32_16x16x32_bf16 v[152:155], a[232:235], v[24:27], v[152:155]\n"
    "v_fma_mix_f32 v203, v5, v35, v43 op_sel:[1,0,0] op_sel_hi:[1,0,0]\n"
    "v_cvt_pk_bf16_f32 v21, v202, v203\n"
    "v_mfma_f32_16x16x32_bf16 v[168:171], a[236:239], v[24:27], v[168:171]\n"
    "v_fma_mix_f32 v200, v6, v36, v44 op_sel:[0,0,0] op_sel_hi:[1,0,0]\n"
    "v_fma_mix_f32 v201, v6, v37, v45 op_sel:[1,0,0] op_sel_hi:[1,0,0]\n"
    "v_mfma_f32_16x16x32_bf16 v[172:175], a[236:239], v[28:31], v[172:175]\n"
    "v_cvt_pk_bf16_f32 v22, v200, v201\n"
    "v_fma_mix_f32 v202, v7, v38, v46 op_sel:[0,0,0] op_sel_hi:[1,0,0]\n"
    "v_mfma_f32_16x16x32_bf16 v[188:191], a[240:243], v[28:31], v[188:191]\n"
    "v_fma_mix_f32 v203, v7, v39, v47 op_sel:[1,0,0] op_sel_hi:[1,0,0]\n"
    "v_cvt_pk_bf16_f32 v23, v202, v203\n"
    "v_mfma_f32_16x16x32_bf16 v[184:187], a[240:243], v[24:27], v[184:187]\n"
    "s_waitcnt lgkmcnt(0)\n"
    "v_mfma_f32_16x16x32_bf16 v[64:67], a[180:183], v[16:19], v[64:67]\n"
    "ds_read_b128 v[0:3], %[rdx] offset:1024\n"
    "v_fma_mix_f32 v200, v8, v32, v40 op_sel:[0,0,0] op_sel_hi:[1,0,0]\n"
    "v_fma_mix_f32 v201, v8, v33, v41 op_sel:[1,0,0] op_sel_hi:[1,0,0]\n"
    "v_mfma_f32_16x16x32_bf16 v[68:71], a[180:183], v[20:23], v[68:71]\n"
    "ds_read_b128 v[4:7], %[rdx] offset:3072\n"
    "v_cvt_pk_bf16_f32 v24, v200, v201\n"
    "v_fma_mix_f32 v202, v9, v34, v42 op_sel:[0,0,0] op_sel_hi:[1,0,0]\n"
    "v_mfma_f32_16x16x32_bf16 v[84:87], a[184:187], v[20:23], v[84:87]\n"
    "ds_read_b128 v[48:51], %[rdc] offset:256\n"
    "v_fma_mix_f32 v203, v9, v35, v43 op_sel:[1,0,0] op_sel_hi:[1,0,0]\n"
    "v_mfma_f32_16x16x32_bf16 v[80:83], a[184:187], v[16:19], v[80:83]\n"
    "ds_read_b128 v[52:55], %[rdc] offset:272\n"
    "v_cvt_pk_bf16_f32 v25, v202, v203\n"
    "v_fma_mix_f32 v200, v10, v36, v44 op_sel:[0,0,0] op_sel_hi:[1,0,0]\n"
    "v_mfma_f32_16x16x32_bf16 v[96:99], a[188:191], v[16:19], v[96:99]\n"
    "ds_read_b128 v[56:59], %[rdc] offset:288\n"
    "v_fma_mix_f32 v201, v10, v37, v45 op_sel:[1,0,0] op_sel_hi:[1,0,0]\n"
    "v_mfma_f32_16x16x32_bf16 v[100:103], a[188:191], v[20:23], v[100:103]\n"
    "ds_read_b128 v[60:63], %[rdc] offset:304\n"
    "v_cvt_pk_bf16_f32 v26, v200, v201\n"
    "v_fma_mix_f32 v202, v11, v38, v46 op_sel:[0,0,0] op_sel_hi:[1,0,0]\n"
    "v_mfma_f32_16x16x32_bf16 v[116:119], a[192:195], v[20:23], v[116:119]\n"
    "ds_read_b64_tr_b16 a[212:213], %[rdw0] offset:4096\n"
    "v_fma_mix_f32 v203, v11, v39, v47 op_sel:[1,0,0] op_sel_hi:[1,0,0]\n"
    "v_mfma_f32_16x16x32_bf16 v[112:115], a[192:195], v[16:19], v[112:115]\n"
    "ds_read_b64_tr_b16 a[214:215], %[rdw0] offset:4608\n"
    "v_cvt_pk_bf16_f32 v27, v202, v203\n"
    "v_fma_mix_f32 v200, v12, v32, v40 op_sel:[0,0,0] op_sel_hi:[1,0,0]\n"
    "v_mfma_f32_16x16x32_bf16 v[128:131], a[196:199], v[16:19], v[128:131]\n"
    "ds_read_b64_tr_b16 a[216:217], %[rdw1] offset:4096\n"
    "v_fma_mix_f32 v201, v12, v33, v41 op_sel:[1,0,0] op_sel_hi:[1,0,0]\n"
    "v_mfma_f32_16x16x32_bf16 v[132:135], a[196:199], v[20:23], v[132:135]\n"
    "ds_read_b64_tr_b16 a[218:219], %[rdw1] offset:4608\n"
    "v_cvt_pk_bf16_f32 v28, v200, v201\n"
    "v_fma_mix_f32 v202, v13, v34, v42 op_sel:[0,0,0] op_sel_hi:[1,0,0]\n"
    "v_mfma_f32_16x16x32_bf16 v[148:151], a[200:203], v[20:23], v[148:151]\n"
    "ds_read_b64_tr_b16 a[220:221], %[rdw2] offset:4096\n"
    "v_fma_mix_f32 v203, v13, v35, v43 op_sel:[1,0,0] op_sel_hi:[1,0,0]\n"
    "v_cvt_pk_bf16_f32 v29, v202, v203\n"
    "v_mfma_f32_16x16x32_bf16 v[144:147], a[200:203], v[16:19], v[144:147]\n"
    "ds_read_b64_tr_b16 a[222:223], %[rdw2] offset:4608\n"
    "v_fma_mix_f32 v200, v14, v36, v44 op_sel:[0,0,0] op_sel_hi:[1,0,0]\n"
    "v_mfma_f32_16x16x32_bf16 v[160:163], a[204:207], v[16:19], v[160:163]\n"
    "ds_read_b64_tr_b16 a[224:225], %[rdw3] offset:4096\n"
    "v_fma_mix_f32 v201, v14, v37, v45 op_sel:[1,0,0] op_sel_hi:[1,0,0]\n"
    "v_cvt_pk_bf16_f32 v30, v200, v201\n"
    "v_mfma_f32_16x16x32_bf16 v[164:167], a[204:207], v[20:23], v[164:167]\n"
    "ds_read_b64_tr_b16 a[226:227], %[rdw3] offset:4608\n"
    "v_fma_mix_f32 v202, v15, v38, v46 op_sel:[0,0,0] op_sel_hi:[1,0,0]\n"
    "v_mfma_f32_16x16x32_bf16 v[180:183], a[208:211], v[20:23], v[180:183]\n"
    "v_fma_mix_f32 v203, v15, v39, v47 op_sel:[1,0,0] op_sel_hi:[1,0,0]\n"
    "v_cvt_pk_bf16_f32 v31, v202, v203\n"
    "v_mfma_f32_16x16x32_bf16 v[176:179], a[208:211], v[16:19], v[176:179]\n"
    "s_waitcnt lgkmcnt(8)\n"
    "v_mfma_f32_16x16x32_bf16 v[72:75], a[180:183], v[24:27], v[72:75]\n"
    "ds_read_b128 v[8:11], %[rdx] offset:5120\n"
    "v_fma_mix_f32 v200, v0, v48, v56 op_sel:[0,0,0] op_sel_hi:[1,0,0]\n"
    "v_fma_mix_f32 v201, v0, v49, v57 op_sel:[1,0,0] op_sel_hi:[1,0,0]\n"
    "v_mfma_f32_16x16x32_bf16 v[76:79], a[180:183], v[28:31], v[76:79]\n"
    "ds_read_b128 v[12:15], %[rdx] offset:7168\n"
    "v_cvt_pk_bf16_f32 v16, v200, v201\n"
    "v_fma_mix_f32 v202, v1, v50, v58 op_sel:[0,0,0] op_sel_hi:[1,0,0]\n"
    "v_mfma_f32_16x16x32_bf16 v[92:95], a[184:187], v[28:31], v[92:95]\n"
    "ds_read_b64_tr_b16 a[228:229], %[rdw0] offset:12288\n"
    "v_fma_mix_f32 v203, v1, v51, v59 op_sel:[1,0,0] op_sel_hi:[1,0,0]\n"
    "v_cvt_pk_bf16_f32 v17, v202, v203\n"
    "v_mfma_f32_16x16x32_bf16 v[88:91], a[184:187], v[24:27], v[88:91]\n"
    "ds_read_b64_tr_b16 a[230:231], %[rdw0] offset:12800\n"
    "v_fma_mix_f32 v200, v2, v52, v60 op_sel:[0,0,0] op_sel_hi:[1,0,0]\n"
    "v_fma_mix_f32 v201, v2, v53, v61 op_sel:[1,0,0] op_sel_hi:[1,0,0]\n"
    "v_mfma_f32_16x16x32_bf16 v[104:107], a[188:191], v[24:27], v[104:107]\n"
    "ds_read_b64_tr_b16 a[232:233], %[rdw1] offset:12288\n"
    "v_cvt_pk_bf16_f32 v18, v200, v201\n"
    "v_fma_mix_f32 v202, v3, v54, v62 op_sel:[0,0,0] op_sel_hi:[1,0,0]\n"
    "v_mfma_f32_16x16x32_bf16 v[108:111], a[188:191], v[28:31], v[108:111]\n"
    "ds_read_b64_tr_b16 a[234:235], %[rdw1] offset:12800\n"
    "v_fma_mix_f32 v203, v3, v55, v63 op_sel:[1,0,0] op_sel_hi:[1,0,0]\n"
    "v_cvt_pk_bf16_f32 v19, v202, v203\n"
    "v_mfma_f32_16x16x32_bf16 v[124:127], a[192:195], v[28:31], v[124:127]\n"
    "ds_read_b64_tr_b16 a[236:237], %[rdw2] offset:12288\n"
    "v_fma_mix_f32 v200, v4, v48, v56 op_sel:[0,0,0] op_sel_hi:[1,0,0]\n"
    "v_fma_mix_f32 v201, v4, v49, v57 op_sel:[1,0,0] op_sel_hi:[1,0,0]\n"
    "v_mfma_f32_16x16x32_bf16 v[120:123], a[192:195], v[24:27], v[120:123]\n"
    "ds_read_b64_tr_b16 a[238:239], %[rdw2] offset:12800\n"
    "v_cvt_pk_bf16_f32 v20, v200, v201\n"
    "v_fma_mix_f32 v202, v5, v50, v58 op_sel:[0,0,0] op_sel_hi:[1,0,0]\n"
    "v_mfma_f32_16x16x32_bf16 v[136:139], a[196:199], v[24:27], v[136:139]\n"
    "ds_read_b64_tr_b16 a[240:241], %[rdw3] offset:12288\n"
    "v_fma_mix_f32 v203, v5, v51, v59 op_sel:[1,0,0] op_sel_hi:[1,0,0]\n"
    "v_cvt_pk_bf16_f32 v21, v202, v203\n"
    "v_mfma_f32_16x16x32_bf16 v[140:143], a[196:199], v[28:31], v[140:143]\n"
    "ds_read_b64_tr_b16 a[242:243], %[rdw3] offset:12800\n"
    "v_fma_mix_f32 v200, v6, v52, v60 op_sel:[0,0,0] op_sel_hi:[1,0,0]\n"
    "v_fma_mix_f32 v201, v6, v53, v61 op_sel:[1,0,0] op_sel_hi:[1,0,0]\n"
    "v_mfma_f32_16x16x32_bf16 v[156:159], a[200:203], v[28:31], v[156:159]\n"
    "s_add_u32 s43, s43, 1\n"
    "s_cmp_eq_u32 s43, 3\n"
    "s_cselect_b32 s46, s47, s48\n"
    "v_cvt_pk_bf16_f32 v22, v200, v201\n"
    "v_mfma_f32_16x16x32_bf16 v[152:155], a[200:203], v[24:27], v[152:155]\n"
    "s_cselect_b32 s43, 0, s43\n"
    "v_add_u32 %[rdx], s46, %[rdx]\n"
    "v_add_u32 %[rdw0], s46, %[rdw0]\n"
    "v_fma_mix_f32 v202, v7, v54, v62 op_sel:[0,0,0] op_sel_hi:[1,0,0]\n"
    "v_mfma_f32_16x16x32_bf16 v[168:171], a[204:207], v[24:27], v[168:171]\n"
    "v_add_u32 %[rdw1], s46, %[rdw1]\n"
    "v_add_u32 %[rdw2], s46, %[rdw2]\n"
    "v_fma_mix_f32 v203, v7, v55, v63 op_sel:[1,0,0] op_sel_hi:[1,0,0]\n"
    "v_mfma_f32_16x16x32_bf16 v[172:175], a[204:207], v[28:31], v[172:175]\n"
    "v_add_u32 %[rdw3], s46, %[rdw3]\n"
    "s_sub_u32 %[wrapr], %[wrapr], 1\n"
    "v_cvt_pk_bf16_f32 v23, v202, v203\n"
    "v_mfma_f32_16x16x32_bf16 v[188:191], a[208:211], v[28:31], v[188:191]\n"
    "s_cmp_eq_u32 %[wrapr], 0\n"
    "s_cselect_b32 s46, s54, s55\n"
    "v_add_u32 %[rdc], s46, %[rdc]\n"
    "v_mfma_f32_16x16x32_bf16 v[184:187], a[208:211], v[24:27], v[184:187]\n"
    "s_waitcnt lgkmcnt(0)\n"
    "v_mfma_f32_16x16x32_bf16 v[64:67], a[212:215], v[16:19], v[64:67]\n"
    "v_fma_mix_f32 v200, v8, v48, v56 op_sel:[0,0,0] op_sel_hi:[1,0,0]\n"
    "v_fma_mix_f32 v201, v8, v49, v57 op_sel:[1,0,0] op_sel_hi:[1,0,0]\n"
    "v_mfma_f32_16x16x32_bf16 v[68:71], a[212:215], v[20:23], v[68:71]\n"
    "v_cvt_pk_bf16_f32 v24, v200, v201\n"
    "v_fma_mix_f32 v202, v9, v50, v58 op_sel:[0,0,0] op_sel_hi:[1,0,0]\n"
    "v_mfma_f32_16x16x32_bf16 v[84:87], a[216:219], v[20:23], v[84:87]\n"
    "v_fma_mix_f32 v203, v9, v51, v59 op_sel:[1,0,0] op_sel_hi:[1,0,0]\n"
    "v_cvt_pk_bf16_f32 v25, v202, v203\n"
    "v_mfma_f32_16x16x32_bf16 v[80:83], a[216:219], v[16:19], v[80:83]\n"
    "v_fma_mix_f32 v200, v10, v52, v60 op_sel:[0,0,0] op_sel_hi:[1,0,0]\n"
    "v_mfma_f32_16x16x32_bf16 v[96:99], a[220:223], v[16:19], v[96:99]\n"
    "v_fma_mix_f32 v201, v10, v53, v61 op_sel:[1,0,0] op_sel_hi:[1,0,0]\n"
    "v_cvt_pk_bf16_f32 v26, v200, v201\n"
    "v_mfma_f32_16x16x32_bf16 v[100:103], a[220:223], v[20:23], v[100:103]\n"
    "v_fma_mix_f32 v202, v11, v54, v62 op_sel:[0,0,0] op_sel_hi:[1,0,0]\n"
    "v_fma_mix_f32 v203, v11, v55, v63 op_sel:[1,0,0] op_sel_hi:[1,0,0]\n"
    "v_mfma_f32_16x16x32_bf16 v[116:119], a[224:227], v[20:23], v[116:119]\n"
    "v_cvt_pk_bf16_f32 v27, v202, v203\n"
    "v_fma_mix_f32 v200, v12, v48, v56 op_sel:[0,0,0] op_sel_hi:[1,0,0]\n"
    "v_mfma_f32_16x16x32_bf16 v[112:115], a[224:227], v[16:19], v[112:115]\n"
    "v_fma_mix_f32 v201, v12, v49, v57 op_sel:[1,0,0] op_sel_hi:[1,0,0]\n"
    "v_mfma_f32_16x16x32_bf16 v[128:131], a[228:231], v[16:19], v[128:131]\n"
    "v_cvt_pk_bf16_f32 v28, v200, v201\n"
    "v_fma_mix_f32 v202, v13, v50, v58 op_sel:[0,0,0] op_sel_hi:[1,0,0]\n"
    "v_mfma_f32_16x16x32_bf16 v[132:135], a[228:231], v[20:23], v[132:135]\n"
    "v_fma_mix_f32 v203, v13, v51, v59 op_sel:[1,0,0] op_sel_hi:[1,0,0]\n"
    "v_cvt_pk_bf16_f32 v29, v202, v203\n"
    "v_mfma_f32_16x16x32_bf16 v[148:151], a[232:235], v[20:23], v[148:151]\n"
    "v_fma_mix_f32 v200, v14, v52, v60 op_sel:[0,0,0] op_sel_hi:[1,0,0]\n"
    "v_mfma_f32_16x16x32_bf16 v[144:147], a[232:235], v[16:19], v[144:147]\n"
    "v_fma_mix_f32 v201, v14, v53, v61 op_sel:[1,0,0] op_sel_hi:[1,0,0]\n"
    "v_cvt_pk_bf16_f32 v30, v200, v201\n"
    "v_mfma_f32_16x16x32_bf16 v[160:163], a[236:239], v[16:19], v[160:163]\n"
    "v_fma_mix_f32 v202, v15, v54, v62 op_sel:[0,0,0] op_sel_hi:[1,0,0]\n"
    "v_fma_mix_f32 v203, v15, v55, v63 op_sel:[1,0,0] op_sel_hi:[1,0,0]\n"
    "v_mfma_f32_16x16x32_bf16 v[164:167], a[236:239], v[20:23], v[164:167]\n"
    "v_cvt_pk_bf16_f32 v31, v202, v203\n"
    "v_mfma_f32_16x16x32_bf16 v[180:183], a[240:243], v[20:23], v[180:183]\n"
    "v_mfma_f32_16x16x32_bf16 v[176:179], a[240:243], v[16:19], v[176:179]\n"
    "v_mfma_f32_16x16x32_bf16 v[72:75], a[212:215], v[24:27], v[72:75]\n"
    "v_mfma_f32_16x16x32_bf16 v[76:79], a[212:215], v[28:31], v[76:79]\n"
    "v_mfma_f32_16x16x32_bf16 v[92:95], a[216:219], v[28:31], v[92:95]\n"
    "v_cvt_pk_bf16_f32 v64, v64, v65\n"
    "v_cvt_pk_bf16_f32 v65, v66, v67\n"
    "v_mfma_f32_16x16x32_bf16 v[88:91], a[216:219], v[24:27], v[88:91]\n"
    "v_cvt_pk_bf16_f32 v66, v80, v81\n"
    "v_cvt_pk_bf16_f32 v67, v82, v83\n"
    "v_mfma_f32_16x16x32_bf16 v[104:107], a[220:223], v[24:27], v[104:107]\n"
    "v_accvgpr_write_b32 a0, v64\n"
    "v_accvgpr_write_b32 a1, v65\n"
    "v_mfma_f32_16x16x32_bf16 v[108:111], a[220:223], v[28:31], v[108:111]\n"
    "v_accvgpr_write_b32 a2, v66\n"
    "v_accvgpr_write_b32 a3, v67\n"
    "v_mfma_f32_16x16x32_bf16 v[124:127], a[224:227], v[28:31], v[124:127]\n"
    "v_cvt_pk_bf16_f32 v96, v96, v97\n"
    "v_cvt_pk_bf16_f32 v97, v98, v99\n"
    "v_mfma_f32_16x16x32_bf16 v[120:123], a[224:227], v[24:27], v[120:123]\n"
    "v_cvt_pk_bf16_f32 v98, v112, v113\n"
    "v_cvt_pk_bf16_f32 v99, v114, v115\n"
    "v_mfma_f32_16x16x32_bf16 v[136:139], a[228:231], v[24:27], v[136:139]\n"
    "v_accvgpr_write_b32 a16, v96\n"
    "v_accvgpr_write_b32 a17, v97\n"
    "v_mfma_f32_16x16x32_bf16 v[140:143], a[228:231], v[28:31], v[140:143]\n"
    "v_accvgpr_write_b32 a18, v98\n"
    "v_accvgpr_write_b32 a19, v99\n"
    "v_mfma_f32_16x16x32_bf16 v[156:159], a[232:235], v[28:31], v[156:159]\n"
    "v_cvt_pk_bf16_f32 v128, v128, v129\n"
    "v_cvt_pk_bf16_f32 v129, v130, v131\n"
    "v_mfma_f32_16x16x32_bf16 v[152:155], a[232:235], v[24:27], v[152:155]\n"
    "v_cvt_pk_bf16_f32 v130, v144, v145\n"
    "v_cvt_pk_bf16_f32 v131, v146, v147\n"
    "v_mfma_f32_16x16x32_bf16 v[168:171], a[236:239], v[24:27], v[168:171]\n"
    "v_accvgpr_write_b32 a32, v128\n"
    "v_accvgpr_write_b32 a33, v129\n"
    "v_mfma_f32_16x16x32_bf16 v[172:175], a[236:239], v[28:31], v[172:175]\n"
    "v_accvgpr_write_b32 a34, v130\n"
    "v_accvgpr_write_b32 a35, v131\n"
    "v_mfma_f32_16x16x32_bf16 v[188:191], a[240:243], v[28:31], v[188:191]\n"
    "v_cvt_pk_bf16_f32 v160, v160, v161\n"
    "v_cvt_pk_bf16_f32 v161, v162, v163\n"
    "v_mfma_f32_16x16x32_bf16 v[184:187], a[240:243], v[24:27], v[184:187]\n"
    "v_cvt_pk_bf16_f32 v162, v176, v177\n"
    "v_cvt_pk_bf16_f32 v163, v178, v179\n"
    "v_accvgpr_write_b32 a48, v160\n"
    "v_accvgpr_write_b32 a49, v161\n"
    "v_accvgpr_write_b32 a50, v162\n"
    "v_accvgpr_write_b32 a51, v163\n"
    "v_cvt_pk_bf16_f32 v68, v68, v69\n"
    "v_cvt_pk_bf16_f32 v69, v70, v71\n"
    "v_cvt_pk_bf16_f32 v70, v84, v85\n"
    "v_cvt_pk_bf16_f32 v71, v86, v87\n"
    "v_accvgpr_write_b32 a4, v68\n"
    "v_accvgpr_write_b32 a5, v69\n"
    "v_accvgpr_write_b32 a6, v70\n"
    "v_accvgpr_write_b32 a7, v71\n"
    "v_cvt_pk_bf16_f32 v100, v100, v101\n"
    "v_cvt_pk_bf16_f32 v101, v102, v103\n"
    "v_cvt_pk_bf16_f32 v102, v116, v117\n"
    "v_cvt_pk_bf16_f32 v103, v118, v119\n"
    "v_accvgpr_write_b32 a20, v100\n"
    "v_accvgpr_write_b32 a21, v101\n"
    "v_accvgpr_write_b32 a22, v102\n"
    "v_accvgpr_write_b32 a23, v103\n"
    "v_cvt_pk_bf16_f32 v132, v132, v133\n"
    "v_cvt_pk_bf16_f32 v133, v134, v135\n"
    "v_cvt_pk_bf16_f32 v134, v148, v149\n"
    "v_cvt_pk_bf16_f32 v135, v150, v151\n"
    "v_accvgpr_write_b32 a36, v132\n"
    "v_accvgpr_write_b32 a37, v133\n"
    "v_accvgpr_write_b32 a38, v134\n"
    "v_accvgpr_write_b32 a39, v135\n"
    "v_cvt_pk_bf16_f32 v164, v164, v165\n"
    "v_cvt_pk_bf16_f32 v165, v166, v167\n"
    "v_cvt_pk_bf16_f32 v166, v180, v181\n"
    "v_cvt_pk_bf16_f32 v167, v182, v183\n"
    "v_accvgpr_write_b32 a52, v164\n"
    "v_accvgpr_write_b32 a53, v165\n"
    "v_accvgpr_write_b32 a54, v166\n"
    "v_accvgpr_write_b32 a55, v167\n"
    "v_cvt_pk_bf16_f32 v72, v72, v73\n"
    "v_cvt_pk_bf16_f32 v73, v74, v75\n"
    "v_cvt_pk_bf16_f32 v74, v88, v89\n"
    "v_cvt_pk_bf16_f32 v75, v90, v91\n"
    "v_accvgpr_write_b32 a8, v72\n"
    "v_accvgpr_write_b32 a9, v73\n"
    "v_accvgpr_write_b32 a10, v74\n"
    "v_accvgpr_write_b32 a11, v75\n"
    "v_cvt_pk_bf16_f32 v104, v104, v105\n"
    "v_cvt_pk_bf16_f32 v105, v106, v107\n"
    "v_cvt_pk_bf16_f32 v106, v120, v121\n"
    "v_cvt_pk_bf16_f32 v107, v122, v123\n"
    "v_accvgpr_write_b32 a24, v104\n"
    "v_accvgpr_write_b32 a25, v105\n"
    "v_accvgpr_write_b32 a26, v106\n"
    "v_accvgpr_write_b32 a27, v107\n"
    "v_cvt_pk_bf16_f32 v136, v136, v137\n"
    "v_cvt_pk_bf16_f32 v137, v138, v139\n"
    "v_cvt_pk_bf16_f32 v138, v152, v153\n"
    "v_cvt_pk_bf16_f32 v139, v154, v155\n"
    "v_accvgpr_write_b32 a40, v136\n"
    "v_accvgpr_write_b32 a41, v137\n"
    "v_accvgpr_write_b32 a42, v138\n"
    "v_accvgpr_write_b32 a43, v139\n"
    "v_cvt_pk_bf16_f32 v168, v168, v169\n"
    "v_cvt_pk_bf16_f32 v169, v170, v171\n"
    "v_cvt_pk_bf16_f32 v170, v184, v185\n"
    "v_cvt_pk_bf16_f32 v171, v186, v187\n"
    "v_accvgpr_write_b32 a56, v168\n"
    "v_accvgpr_write_b32 a57, v169\n"
    "v_accvgpr_write_b32 a58, v170\n"
    "v_accvgpr_write_b32 a59, v171\n"
    "v_cvt_pk_bf16_f32 v76, v76, v77\n"
    "v_cvt_pk_bf16_f32 v77, v78, v79\n"
    "v_cvt_pk_bf16_f32 v78, v92, v93\n"
    "v_cvt_pk_bf16_f32 v79, v94, v95\n"
    "v_accvgpr_write_b32 a12, v76\n"
    "v_accvgpr_write_b32 a13, v77\n"
    "v_accvgpr_write_b32 a14, v78\n"
    "v_accvgpr_write_b32 a15, v79\n"
    "v_cvt_pk_bf16_f32 v108, v108, v109\n"
    "v_cvt_pk_bf16_f32 v109, v110, v111\n"
    "v_cvt_pk_bf16_f32 v110, v124, v125\n"
    "v_cvt_pk_bf16_f32 v111, v126, v127\n"
    "v_accvgpr_write_b32 a28, v108\n"
    "v_accvgpr_write_b32 a29, v109\n"
    "v_accvgpr_write_b32 a30, v110\n"
    "v_accvgpr_write_b32 a31, v111\n"
    "v_cvt_pk_bf16_f32 v140, v140, v141\n"
    "v_cvt_pk_bf16_f32 v141, v142, v143\n"
    "v_cvt_pk_bf16_f32 v142, v156, v157\n"
    "v_cvt_pk_bf16_f32 v143, v158, v159\n"
    "v_accvgpr_write_b32 a44, v140\n"
    "v_accvgpr_write_b32 a45, v141\n"
    "v_accvgpr_write_b32 a46, v142\n"
    "v_accvgpr_write_b32 a47, v143\n"
    "v_cvt_pk_bf16_f32 v172, v172, v173\n"
    "v_cvt_pk_bf16_f32 v173, v174, v175\n"
    "v_cvt_pk_bf16_f32 v174, v188, v189\n"
    "v_cvt_pk_bf16_f32 v175, v190, v191\n"
    "v_accvgpr_write_b32 a60, v172\n"
    "v_accvgpr_write_b32 a61, v173\n"
    "v_accvgpr_write_b32 a62, v174\n"
    "v_accvgpr_write_b32 a63, v175\n"
    "s_cmp_ge_u32 %[wid], 2\n"
    "s_cbranch_scc1 RB_%=\n"
    "ds_write_b128 %[vqex], v[64:67] offset:0\n"
    "ds_write_b128 %[vqex], v[68:71] offset:1024\n"
    "ds_write_b128 %[vqex], v[72:75] offset:2048\n"
    "ds_write_b128 %[vqex], v[76:79] offset:3072\n"
    "ds_write_b128 %[vqex], v[96:99] offset:4096\n"
    "ds_write_b128 %[vqex], v[100:103] offset:5120\n"
    "ds_write_b128 %[vqex], v[104:107] offset:6144\n"
    "ds_write_b128 %[vqex], v[108:111] offset:7168\n"
    "ds_write_b128 %[vqex], v[128:131] offset:8192\n"
    "ds_write_b128 %[vqex], v[132:135] offset:9216\n"
    "ds_write_b128 %[vqex], v[136:139] offset:10240\n"
    "ds_write_b128 %[vqex], v[140:143] offset:11264\n"
    "ds_write_b128 %[vqex], v[160:163] offset:12288\n"
    "ds_write_b128 %[vqex], v[164:167] offset:13312\n"
    "ds_write_b128 %[vqex], v[168:171] offset:14336\n"
    "ds_write_b128 %[vqex], v[172:175] offset:15360\n"
    "s_waitcnt vmcnt(0) lgkmcnt(0)\n"
    "s_barrier\n"
    "s_add_u32 m0, %[m0kx], 131072\n"
    "s_mov_b32 s44, 278528\n"
    "buffer_load_dwordx4 %[vkx], %[srdk0], s44 offen lds\n"
    "buffer_load_dwordx4 %[vkx], %[srdk0], s44 offen offset:1024 lds\n"
    "s_add_u32 s40, %[m0kv], 65536\n"
    "s_getpc_b64 s[52:53]\n"
    "H1_%=:\n"
    "s_add_u32 s52, s52, R1_%=-H1_%=\n"
    "s_addc_u32 s53, s53, 0\n"
    "s_branch F1_%=\n"
    "R1_%=:\n"
    "s_sub_u32 s50, %[W], 2\n"
    "s_cmp_eq_u32 s50, 0\n"
    "s_cbranch_scc1 R2_%=\n"
    "s_getpc_b64 s[52:53]\n"
    "H2_%=:\n"
    "s_add_u32 s52, s52, R2_%=-H2_%=\n"
    "s_addc_u32 s53, s53, 0\n"
    "s_branch V1_%=\n"
    "R2_%=:\n"
    "s_cmp_eq_u32 %[W], 16\n"
    "s_cselect_b32 s44, %[wid], 1\n"
    "s_cmp_eq_u32 s44, 0\n"
    "s_cbranch_scc1 A7_%=\n"
    "s_sub_u32 s50, 17, %[W]\n"
    "s_cmp_eq_u32 s50, 0\n"
    "s_cbranch_scc1 R3_%=\n"
    "s_getpc_b64 s[52:53]\n"
    "H3_%=:\n"
    "s_add_u32 s52, s52, R3_%=-H3_%=\n"
    "s_addc_u32 s53, s53, 0\n"
    "s_branch V2_%=\n"
    "R3_%=:\n"
    "s_getpc_b64 s[52:53]\n"
    "H4_%=:\n"
    "s_add_u32 s52, s52, R4_%=-H4_%=\n"
    "s_addc_u32 s53, s53, 0\n"
    "s_branch V6_%=\n"
    "R4_%=:\n"
    "v_mfma_f32_16x16x32_bf16 v[128:131], a[160:163], v[32:35], v[128:131]\n"
    "s_mov_b32 s47, 0x2b800000\n"
    "v_mov_b32 v210, 0x53800000\n"
    "v_mfma_f32_16x16x32_bf16 v[132:135], a[160:163], v[36:39], v[132:135]\n"
    "v_mfma_f32_16x16x32_bf16 v[136:139], a[160:163], v[40:43], v[136:139]\n"
    "ds_write_b128 %[vex], v[72:75] offset:0\n"
    "v_mfma_f32_16x16x32_bf16 v[140:143], a[160:163], v[44:47], v[140:143]\n"
    "v_mfma_f32_16x16x32_bf16 v[156:159], a[164:167], v[44:47], v[156:159]\n"
    "ds_write_b128 %[vex], v[76:79] offset:1024\n"
    "v_mov_b32 v211, 0\n"
    "v_mfma_f32_16x16x32_bf16 v[152:155], a[164:167], v[40:43], v[152:155]\n"
    "v_mfma_f32_16x16x32_bf16 v[148:151], a[164:167], v[36:39], v[148:151]\n"
    "ds_write_b128 %[vex], v[88:91] offset:2048\n"
    "v_mfma_f32_16x16x32_bf16 v[144:147], a[164:167], v[32:35], v[144:147]\n"
    "v_mfma_f32_16x16x32_bf16 v[160:163], a[168:171], v[32:35], v[160:163]\n"
    "ds_write_b128 %[vex], v[92:95] offset:3072\n"
    "v_mfma_f32_16x16x32_bf16 v[164:167], a[168:171], v[36:39], v[164:167]\n"
    "v_lshrrev_b32 v206, 1, %[vo]\n"
    "v_mfma_f32_16x16x32_bf16 v[168:171], a[168:171], v[40:43], v[168:171]\n"
    "ds_write_b128 %[vex], v[104:107] offset:4096\n"
    "v_mfma_f32_16x16x32_bf16 v[172:175], a[168:171], v[44:47], v[172:175]\n"
    "v_mfma_f32_16x16x32_bf16 v[188:191], a[172:175], v[44:47], v[188:191]\n"
    "ds_write_b128 %[vex], v[108:111] offset:5120\n"
    "v_mfma_f32_16x16x32_bf16 v[184:187], a[172:175], v[40:43], v[184:187]\n"
    "v_mfma_f32_16x16x32_bf16 v[180:183], a[172:175], v[36:39], v[180:183]\n"
    "ds_write_b128 %[vex], v[120:123] offset:6144\n"
    "v_mfma_f32_16x16x32_bf16 v[176:179], a[172:175], v[32:35], v[176:179]\n"
    "v_add_u32 v206, %[sex], v206\n"
    "v_mfma_f32_16x16x32_bf16 a[128:131], a[176:179], v[32:35], a[128:131]\n"
    "ds_write_b128 %[vex], v[124:127] offset:7168\n"
    "v_mfma_f32_16x16x32_bf16 a[132:135], a[176:179], v[36:39], a[132:135]\n"
    "v_mfma_f32_16x16x32_bf16 a[136:139], a[176:179], v[40:43], a[136:139]\n"
    "v_mfma_f32_16x16x32_bf16 a[140:143], a[176:179], v[44:47], a[140:143]\n"
    "ds_write_b128 %[vex], v[136:139] offset:8192\n"
    "ds_write_b128 %[vex], v[140:143] offset:9216\n"
    "ds_write_b128 %[vex], v[152:155] offset:10240\n"
    "ds_write_b128 %[vex], v[156:159] offset:11264\n"
    "ds_write_b128 %[vex], v[168:171] offset:12288\n"
    "ds_write_b128 %[vex], v[172:175] offset:13312\n"
    "ds_write_b128 %[vex], v[184:187] offset:14336\n"
    "ds_write_b128 %[vex], v[188:191] offset:15360\n"
    "v_accvgpr_read_b32 v200, a136\n"
    "v_accvgpr_read_b32 v201, a140\n"
    "v_accvgpr_read_b32 v202, a128\n"
    "v_accvgpr_read_b32 v204, a132\n"
    "ds_write_b64 v206, v[200:201]\n"
    "v_xor_b32 %[vex], 32768, %[vex]\n"
    "v_xor_b32 v206, 1024, v206\n"
    "s_waitcnt lgkmcnt(0)\n"
    "s_barrier\n"
    "s_branch CA_%=\n"
    "A7_%=:\n"
    "v_cmp_lt_i32 vcc, 0, %[vgc]\n"
    "v_mov_b32 v196, 0xff800000\n"
    "s_nop 0\n"
    "v_cndmask_b32 v192, 0, v196, vcc\n"
    "v_cmp_lt_i32 vcc, -1, %[vgc]\n"
    "v_mov_b32 v197, 0xff800000\n"
    "s_nop 0\n"
    "v_cndmask_b32 v193, 0, v197, vcc\n"
    "v_cmp_lt_i32 vcc, -2, %[vgc]\n"
    "v_mov_b32 v198, 0xff800000\n"
    "s_nop 0\n"
    "v_cndmask_b32 v194, 0, v198, vcc\n"
    "v_cmp_lt_i32 vcc, -3, %[vgc]\n"
    "v_mov_b32 v199, 0xff800000\n"
    "s_nop 0\n"
    "v_cndmask_b32 v195, 0, v199, vcc\n"
    "s_mov_b32 s50, 1\n"
    "s_getpc_b64 s[52:53]\n"
    "H5_%=:\n"
    "s_add_u32 s52, s52, R5_%=-H5_%=\n"
    "s_addc_u32 s53, s53, 0\n"
    "s_branch V2_%=\n"
    "R5_%=:\n"
    "s_getpc_b64 s[52:53]\n"
    "H6_%=:\n"
    "s_add_u32 s52, s52, R6_%=-H6_%=\n"
    "s_addc_u32 s53, s53, 0\n"
    "s_branch V5_%=\n"
    "R6_%=:\n"
    "v_mfma_f32_16x16x32_bf16 v[136:139], a[160:163], v[40:43], v[136:139]\n"
    "s_mov_b32 s47, 0x2b800000\n"
    "v_mov_b32 v210, 0x53800000\n"
    "v_mfma_f32_16x16x32_bf16 v[140:143], a[160:163], v[44:47], v[140:143]\n"
    "v_mov_b32 v211, 0\n"
    "v_mfma_f32_16x16x32_bf16 v[156:159], a[164:167], v[44:47], v[156:159]\n"
    "ds_write_b128 %[vex], v[72:75] offset:0\n"
    "v_mfma_f32_16x16x32_bf16 v[152:155], a[164:167], v[40:43], v[152:155]\n"
    "ds_write_b128 %[vex], v[76:79] offset:1024\n"
    "v_lshrrev_b32 v206, 1, %[vo]\n"
    "v_mfma_f32_16x16x32_bf16 v[168:171], a[168:171], v[40:43], v[168:171]\n"
    "ds_write_b128 %[vex], v[88:91] offset:2048\n"
    "v_mfma_f32_16x16x32_bf16 v[172:175], a[168:171], v[44:47], v[172:175]\n"
    "ds_write_b128 %[vex], v[92:95] offset:3072\n"
    "v_mfma_f32_16x16x32_bf16 v[188:191], a[172:175], v[44:47], v[188:191]\n"
    "ds_write_b128 %[vex], v[104:107] offset:4096\n"
    "v_add_u32 v206, %[sex], v206\n"
    "v_mfma_f32_16x16x32_bf16 v[184:187], a[172:175], v[40:43], v[184:187]\n"
    "ds_write_b128 %[vex], v[108:111] offset:5120\n"
    "v_mfma_f32_16x16x32_bf16 a[136:139], a[176:179], v[40:43], a[136:139]\n"
    "ds_write_b128 %[vex], v[120:123] offset:6144\n"
    "v_mfma_f32_16x16x32_bf16 a[140:143], a[176:179], v[44:47], a[140:143]\n"
    "ds_write_b128 %[vex], v[124:127] offset:7168\n"
    "ds_write_b128 %[vex], v[136:139] offset:8192\n"
    "ds_write_b128 %[vex], v[140:143] offset:9216\n"
    "ds_write_b128 %[vex], v[152:155] offset:10240\n"
    "ds_write_b128 %[vex], v[156:159] offset:11264\n"
    "ds_write_b128 %[vex], v[168:171] offset:12288\n"
    "ds_write_b128 %[vex], v[172:175] offset:13312\n"
    "ds_write_b128 %[vex], v[184:187] offset:14336\n"
    "ds_write_b128 %[vex], v[188:191] offset:15360\n"
    "v_accvgpr_read_b32 v200, a136\n"
    "v_accvgpr_read_b32 v201, a140\n"
    "v_accvgpr_read_b32 v202, a128\n"
    "v_accvgpr_read_b32 v204, a132\n"
    "ds_write_b64 v206, v[200:201]\n"
    "v_xor_b32 %[vex], 32768, %[vex]\n"
    "v_xor_b32 v206, 1024, v206\n"
    "s_waitcnt lgkmcnt(0)\n"
    "s_barrier\n"
    "CA_%=:\n"
    "ds_read_b64 v[200:201], v206\n"
    "ds_read_b128 v[0:3], %[vex] offset:0\n"
    "ds_read_b128 v[8:11], %[vex] offset:2048\n"
    "ds_read_b128 v[16:19], %[vex] offset:4096\n"
    "ds_read_b128 v[24:27], %[vex] offset:6144\n"
    "ds_read_b128 v[32:35], %[vex] offset:8192\n"
    "ds_read_b128 v[40:43], %[vex] offset:10240\n"
    "ds_read_b128 v[48:51], %[vex] offset:12288\n"
    "ds_read_b128 v[56:59], %[vex] offset:14336\n"
    "ds_read_b128 v[4:7], %[vex] offset:1024\n"
    "ds_read_b128 v[12:15], %[vex] offset:3072\n"
    "ds_read_b128 v[20:23], %[vex] offset:5120\n"
    "ds_read_b128 v[28:31], %[vex] offset:7168\n"
    "ds_read_b128 v[36:39], %[vex] offset:9216\n"
    "ds_read_b128 v[44:47], %[vex] offset:11264\n"
    "s_waitcnt lgkmcnt(14)\n"
    "v_add_f32 v202, v202, v200\n"
    "v_add_f32 v204, v204, v201\n"
    "ds_read_b128 v[52:55], %[vex] offset:13312\n"
    "ds_read_b128 v[60:63], %[vex] offset:15360\n"
    "v_med3_f32 v208, v202, s47, v210\n"
    "v_cmp_neq_f32 vcc, v208, v202\n"
    "v_rcp_f32 v202, v202\n"
    "s_nop 0\n"
    "v_cndmask_b32 v208, 0, 1, vcc\n"
    "v_or_b32 v211, v211, v208\n"
    "v_med3_f32 v208, v204, s47, v210\n"
    "v_cmp_neq_f32 vcc, v208, v204\n"
    "v_rcp_f32 v204, v204\n"
    "s_nop 0\n"
    "v_cndmask_b32 v208, 0, 1, vcc\n"
    "v_or_b32 v211, v211, v208\n"
    "s_waitcnt lgkmcnt(8)\n"
    "v_pk_add_f32 v[64:65], v[64:65], v[0:1]\n"
    "v_pk_add_f32 v[66:67], v[66:67], v[2:3]\n"
    "v_pk_add_f32 v[80:81], v[80:81], v[8:9]\n"
    "v_pk_add_f32 v[82:83], v[82:83], v[10:11]\n"
    "v_pk_add_f32 v[96:97], v[96:97], v[16:17]\n"
    "v_pk_add_f32 v[98:99], v[98:99], v[18:19]\n"
    "v_pk_add_f32 v[112:113], v[112:113], v[24:25]\n"
    "v_pk_add_f32 v[114:115], v[114:115], v[26:27]\n"
    "v_pk_add_f32 v[128:129], v[128:129], v[32:33]\n"
    "v_pk_add_f32 v[130:131], v[130:131], v[34:35]\n"
    "v_pk_add_f32 v[144:145], v[144:145], v[40:41]\n"
    "v_pk_add_f32 v[146:147], v[146:147], v[42:43]\n"
    "v_pk_add_f32 v[160:161], v[160:161], v[48:49]\n"
    "v_pk_add_f32 v[162:163], v[162:163], v[50:51]\n"
    "v_pk_add_f32 v[176:177], v[176:177], v[56:57]\n"
    "v_pk_add_f32 v[178:179], v[178:179], v[58:59]\n"
    "s_add_u32 s44, %[soo], 0\n"
    "v_pk_mul_f32 v[64:65], v[64:65], v[202:203] op_sel_hi:[1,0]\n"
    "v_pk_mul_f32 v[66:67], v[66:67], v[202:203] op_sel_hi:[1,0]\n"
    "v_pk_mul_f32 v[80:81], v[80:81], v[202:203] op_sel_hi:[1,0]\n"
    "v_pk_mul_f32 v[82:83], v[82:83], v[202:203] op_sel_hi:[1,0]\n"
    "v_cvt_pk_bf16_f32 v64, v64, v65\n"
    "v_cvt_pk_bf16_f32 v65, v66, v67\n"
    "v_cvt_pk_bf16_f32 v66, v80, v81\n"
    "v_cvt_pk_bf16_f32 v67, v82, v83\n"
    "buffer_store_dwordx4 v[64:67], %[vo], %[srdo], s44 offen offset:0 sc1\n"
    "v_pk_mul_f32 v[96:97], v[96:97], v[202:203] op_sel_hi:[1,0]\n"
    "v_pk_mul_f32 v[98:99], v[98:99], v[202:203] op_sel_hi:[1,0]\n"
    "v_pk_mul_f32 v[112:113], v[112:113], v[202:203] op_sel_hi:[1,0]\n"
    "v_pk_mul_f32 v[114:115], v[114:115], v[202:203] op_sel_hi:[1,0]\n"
    "v_cvt_pk_bf16_f32 v96, v96, v97\n"
    "v_cvt_pk_bf16_f32 v97, v98, v99\n"
    "v_cvt_pk_bf16_f32 v98, v112, v113\n"
    "v_cvt_pk_bf16_f32 v99, v114, v115\n"
    "buffer_store_dwordx4 v[96:99], %[vo], %[srdo], s44 offen offset:1024 sc1\n"
    "v_pk_mul_f32 v[128:129], v[128:129], v[202:203] op_sel_hi:[1,0]\n"
    "v_pk_mul_f32 v[130:131], v[130:131], v[202:203] op_sel_hi:[1,0]\n"
    "v_pk_mul_f32 v[144:145], v[144:145], v[202:203] op_sel_hi:[1,0]\n"
    "v_pk_mul_f32 v[146:147], v[146:147], v[202:203] op_sel_hi:[1,0]\n"
    "v_cvt_pk_bf16_f32 v128, v128, v129\n"
    "v_cvt_pk_bf16_f32 v129, v130, v131\n"
    "v_cvt_pk_bf16_f32 v130, v144, v145\n"
    "v_cvt_pk_bf16_f32 v131, v146, v147\n"
    "buffer_store_dwordx4 v[128:131], %[vo], %[srdo], s44 offen offset:2048 sc1\n"
    "v_pk_mul_f32 v[160:161], v[160:161], v[202:203] op_sel_hi:[1,0]\n"
    "v_pk_mul_f32 v[162:163], v[162:163], v[202:203] op_sel_hi:[1,0]\n"
    "v_pk_mul_f32 v[176:177], v[176:177], v[202:203] op_sel_hi:[1,0]\n"
    "v_pk_mul_f32 v[178:179], v[178:179], v[202:203] op_sel_hi:[1,0]\n"
    "v_cvt_pk_bf16_f32 v160, v160, v161\n"
    "v_cvt_pk_bf16_f32 v161, v162, v163\n"
    "v_cvt_pk_bf16_f32 v162, v176, v177\n"
    "v_cvt_pk_bf16_f32 v163, v178, v179\n"
    "buffer_store_dwordx4 v[160:163], %[vo], %[srdo], s44 offen offset:3072 sc1\n"
    "s_waitcnt lgkmcnt(0)\n"
    "v_pk_add_f32 v[68:69], v[68:69], v[4:5]\n"
    "v_pk_add_f32 v[70:71], v[70:71], v[6:7]\n"
    "v_pk_add_f32 v[84:85], v[84:85], v[12:13]\n"
    "v_pk_add_f32 v[86:87], v[86:87], v[14:15]\n"
    "v_pk_add_f32 v[100:101], v[100:101], v[20:21]\n"
    "v_pk_add_f32 v[102:103], v[102:103], v[22:23]\n"
    "v_pk_add_f32 v[116:117], v[116:117], v[28:29]\n"
    "v_pk_add_f32 v[118:119], v[118:119], v[30:31]\n"
    "v_pk_add_f32 v[132:133], v[132:133], v[36:37]\n"
    "v_pk_add_f32 v[134:135], v[134:135], v[38:39]\n"
    "v_pk_add_f32 v[148:149], v[148:149], v[44:45]\n"
    "v_pk_add_f32 v[150:151], v[150:151], v[46:47]\n"
    "v_pk_add_f32 v[164:165], v[164:165], v[52:53]\n"
    "v_pk_add_f32 v[166:167], v[166:167], v[54:55]\n"
    "v_pk_add_f32 v[180:181], v[180:181], v[60:61]\n"
    "v_pk_add_f32 v[182:183], v[182:183], v[62:63]\n"
    "s_add_u32 s44, %[soo], 131072\n"
    "v_pk_mul_f32 v[68:69], v[68:69], v[204:205] op_sel_hi:[1,0]\n"
    "v_pk_mul_f32 v[70:71], v[70:71], v[204:205] op_sel_hi:[1,0]\n"
    "v_pk_mul_f32 v[84:85], v[84:85], v[204:205] op_sel_hi:[1,0]\n"
    "v_pk_mul_f32 v[86:87], v[86:87], v[204:205] op_sel_hi:[1,0]\n"
    "v_cvt_pk_bf16_f32 v68, v68, v69\n"
    "v_cvt_pk_bf16_f32 v69, v70, v71\n"
    "v_cvt_pk_bf16_f32 v70, v84, v85\n"
    "v_cvt_pk_bf16_f32 v71, v86, v87\n"
    "buffer_store_dwordx4 v[68:71], %[vo], %[srdo], s44 offen offset:0 sc1\n"
    "v_pk_mul_f32 v[100:101], v[100:101], v[204:205] op_sel_hi:[1,0]\n"
    "v_pk_mul_f32 v[102:103], v[102:103], v[204:205] op_sel_hi:[1,0]\n"
    "v_pk_mul_f32 v[116:117], v[116:117], v[204:205] op_sel_hi:[1,0]\n"
    "v_pk_mul_f32 v[118:119], v[118:119], v[204:205] op_sel_hi:[1,0]\n"
    "v_cvt_pk_bf16_f32 v100, v100, v101\n"
    "v_cvt_pk_bf16_f32 v101, v102, v103\n"
    "v_cvt_pk_bf16_f32 v102, v116, v117\n"
    "v_cvt_pk_bf16_f32 v103, v118, v119\n"
    "buffer_store_dwordx4 v[100:103], %[vo], %[srdo], s44 offen offset:1024 sc1\n"
    "v_pk_mul_f32 v[132:133], v[132:133], v[204:205] op_sel_hi:[1,0]\n"
    "v_pk_mul_f32 v[134:135], v[134:135], v[204:205] op_sel_hi:[1,0]\n"
    "v_pk_mul_f32 v[148:149], v[148:149], v[204:205] op_sel_hi:[1,0]\n"
    "v_pk_mul_f32 v[150:151], v[150:151], v[204:205] op_sel_hi:[1,0]\n"
    "v_cvt_pk_bf16_f32 v132, v132, v133\n"
    "v_cvt_pk_bf16_f32 v133, v134, v135\n"
    "v_cvt_pk_bf16_f32 v134, v148, v149\n"
    "v_cvt_pk_bf16_f32 v135, v150, v151\n"
    "buffer_store_dwordx4 v[132:135], %[vo], %[srdo], s44 offen offset:2048 sc1\n"
    "v_pk_mul_f32 v[164:165], v[164:165], v[204:205] op_sel_hi:[1,0]\n"
    "v_pk_mul_f32 v[166:167], v[166:167], v[204:205] op_sel_hi:[1,0]\n"
    "v_pk_mul_f32 v[180:181], v[180:181], v[204:205] op_sel_hi:[1,0]\n"
    "v_pk_mul_f32 v[182:183], v[182:183], v[204:205] op_sel_hi:[1,0]\n"
    "v_cvt_pk_bf16_f32 v164, v164, v165\n"
    "v_cvt_pk_bf16_f32 v165, v166, v167\n"
    "v_cvt_pk_bf16_f32 v166, v180, v181\n"
    "v_cvt_pk_bf16_f32 v167, v182, v183\n"
    "buffer_store_dwordx4 v[164:167], %[vo], %[srdo], s44 offen offset:3072 sc1\n"
    "v_mov_b32 v208, %[lb]\n"
    "v_add_u32 v208, 160256, v208\n"
    "ds_or_b32 v208, v211\n"
    "s_branch END_%=\n"
    "RB_%=:\n"
    "s_waitcnt vmcnt(0) lgkmcnt(0)\n"
    "s_barrier\n"
    "ds_read_b128 a[64:67], %[vqex] offset:0\n"
    "ds_read_b128 a[68:71], %[vqex] offset:1024\n"
    "ds_read_b128 a[72:75], %[vqex] offset:2048\n"
    "ds_read_b128 a[76:79], %[vqex] offset:3072\n"
    "ds_read_b128 a[80:83], %[vqex] offset:4096\n"
    "ds_read_b128 a[84:87], %[vqex] offset:5120\n"
    "ds_read_b128 a[88:91], %[vqex] offset:6144\n"
    "ds_read_b128 a[92:95], %[vqex] offset:7168\n"
    "ds_read_b128 a[96:99], %[vqex] offset:8192\n"
    "ds_read_b128 a[100:103], %[vqex] offset:9216\n"
    "ds_read_b128 a[104:107], %[vqex] offset:10240\n"
    "ds_read_b128 a[108:111], %[vqex] offset:11264\n"
    "ds_read_b128 a[112:115], %[vqex] offset:12288\n"
    "ds_read_b128 a[116:119], %[vqex] offset:13312\n"
    "ds_read_b128 a[120:123], %[vqex] offset:14336\n"
    "ds_read_b128 a[124:127], %[vqex] offset:15360\n"
    "s_add_u32 m0, %[m0kx], 131072\n"
    "s_mov_b32 s44, 278528\n"
    "buffer_load_dwordx4 %[vkx], %[srdk0], s44 offen lds\n"
    "buffer_load_dwordx4 %[vkx], %[srdk0], s44 offen offset:1024 lds\n"
    "s_add_u32 s40, %[m0kv], 65536\n"
    "s_cmp_eq_u32 %[wid], 3\n"
    "s_cbranch_scc0 ROLE2_%=\n"
    "s_cmp_eq_u32 %[W], 2\n"
    "s_cbranch_scc1 B1a_%=\n"
    "s_getpc_b64 s[52:53]\n"
    "H7_%=:\n"
    "s_add_u32 s52, s52, R7_%=-H7_%=\n"
    "s_addc_u32 s53, s53, 0\n"
    "s_branch F1_%=\n"
    "R7_%=:\n"
    "s_sub_u32 s50, %[W], 3\n"
    "s_cmp_eq_u32 s50, 0\n"
    "s_cbranch_scc1 R8_%=\n"
    "s_getpc_b64 s[52:53]\n"
    "H8_%=:\n"
    "s_add_u32 s52, s52, R8_%=-H8_%=\n"
    "s_addc_u32 s53, s53, 0\n"
    "s_branch V1_%=\n"
    "R8_%=:\n"
    "v_cmp_lt_i32 vcc, 0, %[vgc]\n"
    "v_mov_b32 v196, 0xff800000\n"
    "s_nop 0\n"
    "v_cndmask_b32 v192, 0, v196, vcc\n"
    "v_cmp_lt_i32 vcc, -1, %[vgc]\n"
    "v_mov_b32 v197, 0xff800000\n"
    "s_nop 0\n"
    "v_cndmask_b32 v193, 0, v197, vcc\n"
    "v_cmp_lt_i32 vcc, -2, %[vgc]\n"
    "v_mov_b32 v198, 0xff800000\n"
    "s_nop 0\n"
    "v_cndmask_b32 v194, 0, v198, vcc\n"
    "v_cmp_lt_i32 vcc, -3, %[vgc]\n"
    "v_mov_b32 v199, 0xff800000\n"
    "s_nop 0\n"
    "v_cndmask_b32 v195, 0, v199, vcc\n"
    "s_mov_b32 s50, 1\n"
    "s_getpc_b64 s[52:53]\n"
    "H9_%=:\n"
    "s_add_u32 s52, s52, R9_%=-H9_%=\n"
    "s_addc_u32 s53, s53, 0\n"
    "s_branch V1_%=\n"
    "R9_%=:\n"
    "s_branch B1b_%=\n"
    "B1a_%=:\n"
    "v_cmp_lt_i32 vcc, 0, %[vgc]\n"
    "v_mov_b32 v196, 0xff800000\n"
    "s_nop 0\n"
    "v_cndmask_b32 v192, 0, v196, vcc\n"
    "v_cmp_lt_i32 vcc, -1, %[vgc]\n"
    "v_mov_b32 v197, 0xff800000\n"
    "s_nop 0\n"
    "v_cndmask_b32 v193, 0, v197, vcc\n"
    "v_cmp_lt_i32 vcc, -2, %[vgc]\n"
    "v_mov_b32 v198, 0xff800000\n"
    "s_nop 0\n"
    "v_cndmask_b32 v194, 0, v198, vcc\n"
    "v_cmp_lt_i32 vcc, -3, %[vgc]\n"
    "v_mov_b32 v199, 0xff800000\n"
    "s_nop 0\n"
    "v_cndmask_b32 v195, 0, v199, vcc\n"
    "s_getpc_b64 s[52:53]\n"
    "H10_%=:\n"
    "s_add_u32 s52, s52, R10_%=-H10_%=\n"
    "s_addc_u32 s53, s53, 0\n"
    "s_branch F1_%=\n"
    "R10_%=:\n"
    "B1b_%=:\n"
    "s_getpc_b64 s[52:53]\n"
    "H11_%=:\n"
    "s_add_u32 s52, s52, R11_%=-H11_%=\n"
    "s_addc_u32 s53, s53, 0\n"
    "s_branch V4_%=\n"
    "R11_%=:\n"
    "s_getpc_b64 s[52:53]\n"
    "H12_%=:\n"
    "s_add_u32 s52, s52, R12_%=-H12_%=\n"
    "s_addc_u32 s53, s53, 0\n"
    "s_branch ES_%=\n"
    "R12_%=:\n"
    "s_waitcnt vmcnt(16) lgkmcnt(0)\n"
    "s_barrier\n"
    "s_cmp_eq_u32 %[W], 16\n"
    "s_cbranch_scc1 B7_%=\n"
    "v_mov_b32 v192, 0\n"
    "v_mov_b32 v193, 0\n"
    "v_mov_b32 v194, 0\n"
    "v_mov_b32 v195, 0\n"
    "v_mov_b32 v196, 0\n"
    "v_mov_b32 v197, 0\n"
    "v_mov_b32 v198, 0\n"
    "v_mov_b32 v199, 0\n"
    "s_getpc_b64 s[52:53]\n"
    "H13_%=:\n"
    "s_add_u32 s52, s52, R13_%=-H13_%=\n"
    "s_addc_u32 s53, s53, 0\n"
    "s_branch F2_%=\n"
    "R13_%=:\n"
    "s_sub_u32 s50, 14, %[W]\n"
    "s_cmp_eq_u32 s50, 0\n"
    "s_cbranch_scc1 R14_%=\n"
    "s_getpc_b64 s[52:53]\n"
    "H14_%=:\n"
    "s_add_u32 s52, s52, R14_%=-H14_%=\n"
    "s_addc_u32 s53, s53, 0\n"
    "s_branch V2_%=\n"
    "R14_%=:\n"
    "v_cmp_lt_i32 vcc, 0, %[vgc]\n"
    "v_mov_b32 v196, 0xff800000\n"
    "s_nop 0\n"
    "v_cndmask_b32 v192, 0, v196, vcc\n"
    "v_cmp_lt_i32 vcc, -1, %[vgc]\n"
    "v_mov_b32 v197, 0xff800000\n"
    "s_nop 0\n"
    "v_cndmask_b32 v193, 0, v197, vcc\n"
    "v_cmp_lt_i32 vcc, -2, %[vgc]\n"
    "v_mov_b32 v198, 0xff800000\n"
    "s_nop 0\n"
    "v_cndmask_b32 v194, 0, v198, vcc\n"
    "v_cmp_lt_i32 vcc, -3, %[vgc]\n"
    "v_mov_b32 v199, 0xff800000\n"
    "s_nop 0\n"
    "v_cndmask_b32 v195, 0, v199, vcc\n"
    "s_mov_b32 s50, 1\n"
    "s_getpc_b64 s[52:53]\n"
    "H15_%=:\n"
    "s_add_u32 s52, s52, R15_%=-H15_%=\n"
    "s_addc_u32 s53, s53, 0\n"
    "s_branch V2_%=\n"
    "R15_%=:\n"
    "s_branch B8_%=\n"
    "B7_%=:\n"
    "s_getpc_b64 s[52:53]\n"
    "H16_%=:\n"
    "s_add_u32 s52, s52, R16_%=-H16_%=\n"
    "s_addc_u32 s53, s53, 0\n"
    "s_branch SB_%=\n"
    "R16_%=:\n"
    "B8_%=:\n"
    "s_getpc_b64 s[52:53]\n"
    "H17_%=:\n"
    "s_add_u32 s52, s52, R17_%=-H17_%=\n"
    "s_addc_u32 s53, s53, 0\n"
    "s_branch V5_%=\n"
    "R17_%=:\n"
    "v_mfma_f32_16x16x32_bf16 v[136:139], a[160:163], v[40:43], v[136:139]\n"
    "s_mov_b32 s47, 0x2b800000\n"
    "v_mov_b32 v210, 0x53800000\n"
    "v_mfma_f32_16x16x32_bf16 v[140:143], a[160:163], v[44:47], v[140:143]\n"
    "v_mov_b32 v211, 0\n"
    "v_mfma_f32_16x16x32_bf16 v[156:159], a[164:167], v[44:47], v[156:159]\n"
    "ds_write_b128 %[vex], v[64:67] offset:0\n"
    "v_mfma_f32_16x16x32_bf16 v[152:155], a[164:167], v[40:43], v[152:155]\n"
    "ds_write_b128 %[vex], v[68:71] offset:1024\n"
    "v_lshrrev_b32 v206, 1, %[vo]\n"
    "v_mfma_f32_16x16x32_bf16 v[168:171], a[168:171], v[40:43], v[168:171]\n"
    "ds_write_b128 %[vex], v[80:83] offset:2048\n"
    "v_mfma_f32_16x16x32_bf16 v[172:175], a[168:171], v[44:47], v[172:175]\n"
    "ds_write_b128 %[vex], v[84:87] offset:3072\n"
    "v_mfma_f32_16x16x32_bf16 v[188:191], a[172:175], v[44:47], v[188:191]\n"
    "ds_write_b128 %[vex], v[96:99] offset:4096\n"
    "v_add_u32 v206, %[sex], v206\n"
    "v_mfma_f32_16x16x32_bf16 v[184:187], a[172:175], v[40:43], v[184:187]\n"
    "ds_write_b128 %[vex], v[100:103] offset:5120\n"
    "v_mfma_f32_16x16x32_bf16 a[136:139], a[176:179], v[40:43], a[136:139]\n"
    "ds_write_b128 %[vex], v[112:115] offset:6144\n"
    "v_mfma_f32_16x16x32_bf16 a[140:143], a[176:179], v[44:47], a[140:143]\n"
    "ds_write_b128 %[vex], v[116:119] offset:7168\n"
    "ds_write_b128 %[vex], v[128:131] offset:8192\n"
    "ds_write_b128 %[vex], v[132:135] offset:9216\n"
    "ds_write_b128 %[vex], v[144:147] offset:10240\n"
    "ds_write_b128 %[vex], v[148:151] offset:11264\n"
    "ds_write_b128 %[vex], v[160:163] offset:12288\n"
    "ds_write_b128 %[vex], v[164:167] offset:13312\n"
    "ds_write_b128 %[vex], v[176:179] offset:14336\n"
    "ds_write_b128 %[vex], v[180:183] offset:15360\n"
    "v_accvgpr_read_b32 v200, a128\n"
    "v_accvgpr_read_b32 v201, a132\n"
    "v_accvgpr_read_b32 v202, a136\n"
    "v_accvgpr_read_b32 v204, a140\n"
    "ds_write_b64 v206, v[200:201]\n"
    "v_xor_b32 %[vex], 32768, %[vex]\n"
    "v_xor_b32 v206, 1024, v206\n"
    "s_waitcnt lgkmcnt(0)\n"
    "s_barrier\n"
    "s_branch CB_%=\n"
    "ROLE2_%=:\n"
    "s_cmp_eq_u32 %[W], 2\n"
    "s_cbranch_scc1 D0_%=\n"
    "s_getpc_b64 s[52:53]\n"
    "H18_%=:\n"
    "s_add_u32 s52, s52, R18_%=-H18_%=\n"
    "s_addc_u32 s53, s53, 0\n"
    "s_branch F1_%=\n"
    "R18_%=:\n"
    "s_sub_u32 s50, %[W], 4\n"
    "s_cmp_eq_u32 s50, 0\n"
    "s_cbranch_scc1 R19_%=\n"
    "s_getpc_b64 s[52:53]\n"
    "H19_%=:\n"
    "s_add_u32 s52, s52, R19_%=-H19_%=\n"
    "s_addc_u32 s53, s53, 0\n"
    "s_branch V1_%=\n"
    "R19_%=:\n"
    "v_cmp_lt_i32 vcc, 0, %[vgc]\n"
    "v_mov_b32 v196, 0xff800000\n"
    "s_nop 0\n"
    "v_cndmask_b32 v192, 0, v196, vcc\n"
    "v_cmp_lt_i32 vcc, -1, %[vgc]\n"
    "v_mov_b32 v197, 0xff800000\n"
    "s_nop 0\n"
    "v_cndmask_b32 v193, 0, v197, vcc\n"
    "v_cmp_lt_i32 vcc, -2, %[vgc]\n"
    "v_mov_b32 v198, 0xff800000\n"
    "s_nop 0\n"
    "v_cndmask_b32 v194, 0, v198, vcc\n"
    "v_cmp_lt_i32 vcc, -3, %[vgc]\n"
    "v_mov_b32 v199, 0xff800000\n"
    "s_nop 0\n"
    "v_cndmask_b32 v195, 0, v199, vcc\n"
    "s_mov_b32 s50, 1\n"
    "s_getpc_b64 s[52:53]\n"
    "H20_%=:\n"
    "s_add_u32 s52, s52, R20_%=-H20_%=\n"
    "s_addc_u32 s53, s53, 0\n"
    "s_branch V1_%=\n"
    "R20_%=:\n"
    "s_branch D1_%=\n"
    "D0_%=:\n"
    "v_cmp_lt_i32 vcc, 0, %[vgc]\n"
    "v_mov_b32 v196, 0xff800000\n"
    "s_nop 0\n"
    "v_cndmask_b32 v192, 0, v196, vcc\n"
    "v_cmp_lt_i32 vcc, -1, %[vgc]\n"
    "v_mov_b32 v197, 0xff800000\n"
    "s_nop 0\n"
    "v_cndmask_b32 v193, 0, v197, vcc\n"
    "v_cmp_lt_i32 vcc, -2, %[vgc]\n"
    "v_mov_b32 v198, 0xff800000\n"
    "s_nop 0\n"
    "v_cndmask_b32 v194, 0, v198, vcc\n"
    "v_cmp_lt_i32 vcc, -3, %[vgc]\n"
    "v_mov_b32 v199, 0xff800000\n"
    "s_nop 0\n"
    "v_cndmask_b32 v195, 0, v199, vcc\n"
    "s_getpc_b64 s[52:53]\n"
    "H21_%=:\n"
    "s_add_u32 s52, s52, R21_%=-H21_%=\n"
    "s_addc_u32 s53, s53, 0\n"
    "s_branch SA_%=\n"
    "R21_%=:\n"
    "D1_%=:\n"
    "s_getpc_b64 s[52:53]\n"
    "H22_%=:\n"
    "s_add_u32 s52, s52, R22_%=-H22_%=\n"
    "s_addc_u32 s53, s53, 0\n"
    "s_branch V3_%=\n"
    "R22_%=:\n"
    "s_add_u32 m0, s40, 0\n"
    "s_nop 0\n"
    "buffer_load_dwordx4 %[vdma], %[srdk], %[so0] offen offset:0 lds\n"
    "buffer_load_dwordx4 %[vdma], %[srdk], %[so0] offen offset:1024 lds\n"
    "buffer_load_dwordx4 %[vdma], %[srdk], %[so0] offen offset:2048 lds\n"
    "buffer_load_dwordx4 %[vdma], %[srdk], %[so0] offen offset:3072 lds\n"
    "s_add_u32 m0, s40, 16384\n"
    "s_nop 0\n"
    "buffer_load_dwordx4 %[vdma], %[srdv], %[so0] offen offset:0 lds\n"
    "buffer_load_dwordx4 %[vdma], %[srdv], %[so0] offen offset:1024 lds\n"
    "buffer_load_dwordx4 %[vdma], %[srdv], %[so0] offen offset:2048 lds\n"
    "buffer_load_dwordx4 %[vdma], %[srdv], %[so0] offen offset:3072 lds\n"
    "s_add_u32 m0, s40, 32768\n"
    "s_nop 0\n"
    "buffer_load_dwordx4 %[vdma], %[srdk], %[so1] offen offset:0 lds\n"
    "buffer_load_dwordx4 %[vdma], %[srdk], %[so1] offen offset:1024 lds\n"
    "buffer_load_dwordx4 %[vdma], %[srdk], %[so1] offen offset:2048 lds\n"
    "buffer_load_dwordx4 %[vdma], %[srdk], %[so1] offen offset:3072 lds\n"
    "s_add_u32 m0, s40, 49152\n"
    "s_nop 0\n"
    "buffer_load_dwordx4 %[vdma], %[srdv], %[so1] offen offset:0 lds\n"
    "buffer_load_dwordx4 %[vdma], %[srdv], %[so1] offen offset:1024 lds\n"
    "buffer_load_dwordx4 %[vdma], %[srdv], %[so1] offen offset:2048 lds\n"
    "buffer_load_dwordx4 %[vdma], %[srdv], %[so1] offen offset:3072 lds\n"
    "s_add_u32 %[so0], %[so0], 16384\n"
    "s_xor_b32 s40, s40, 65536\n"
    "s_add_u32 %[so1], %[so1], 16384\n"
    "s_getpc_b64 s[52:53]\n"
    "H23_%=:\n"
    "s_add_u32 s52, s52, R23_%=-H23_%=\n"
    "s_addc_u32 s53, s53, 0\n"
    "s_branch ES_%=\n"
    "R23_%=:\n"
    "v_xor_b32 %[rd], 65536, %[rd]\n"
    "s_waitcnt vmcnt(16) lgkmcnt(0)\n"
    "s_barrier\n"
    "s_cmp_eq_u32 %[W], 16\n"
    "s_cbranch_scc1 D7_%=\n"
    "v_mov_b32 v192, 0\n"
    "v_mov_b32 v193, 0\n"
    "v_mov_b32 v194, 0\n"
    "v_mov_b32 v195, 0\n"
    "v_mov_b32 v196, 0\n"
    "v_mov_b32 v197, 0\n"
    "v_mov_b32 v198, 0\n"
    "v_mov_b32 v199, 0\n"
    "s_cmp_eq_u32 %[W], 14\n"
    "s_cbranch_scc1 D3a_%=\n"
    "s_getpc_b64 s[52:53]\n"
    "H24_%=:\n"
    "s_add_u32 s52, s52, R24_%=-H24_%=\n"
    "s_addc_u32 s53, s53, 0\n"
    "s_branch F2_%=\n"
    "R24_%=:\n"
    "s_sub_u32 s50, 13, %[W]\n"
    "s_cmp_eq_u32 s50, 0\n"
    "s_cbranch_scc1 R25_%=\n"
    "s_getpc_b64 s[52:53]\n"
    "H25_%=:\n"
    "s_add_u32 s52, s52, R25_%=-H25_%=\n"
    "s_addc_u32 s53, s53, 0\n"
    "s_branch V2_%=\n"
    "R25_%=:\n"
    "v_cmp_lt_i32 vcc, 0, %[vgc]\n"
    "v_mov_b32 v196, 0xff800000\n"
    "s_nop 0\n"
    "v_cndmask_b32 v192, 0, v196, vcc\n"
    "v_cmp_lt_i32 vcc, -1, %[vgc]\n"
    "v_mov_b32 v197, 0xff800000\n"
    "s_nop 0\n"
    "v_cndmask_b32 v193, 0, v197, vcc\n"
    "v_cmp_lt_i32 vcc, -2, %[vgc]\n"
    "v_mov_b32 v198, 0xff800000\n"
    "s_nop 0\n"
    "v_cndmask_b32 v194, 0, v198, vcc\n"
    "v_cmp_lt_i32 vcc, -3, %[vgc]\n"
    "v_mov_b32 v199, 0xff800000\n"
    "s_nop 0\n"
    "v_cndmask_b32 v195, 0, v199, vcc\n"
    "s_mov_b32 s50, 1\n"
    "s_getpc_b64 s[52:53]\n"
    "H26_%=:\n"
    "s_add_u32 s52, s52, R26_%=-H26_%=\n"
    "s_addc_u32 s53, s53, 0\n"
    "s_branch V2_%=\n"
    "R26_%=:\n"
    "s_branch D3b_%=\n"
    "D3a_%=:\n"
    "v_cmp_lt_i32 vcc, 0, %[vgc]\n"
    "v_mov_b32 v196, 0xff800000\n"
    "s_nop 0\n"
    "v_cndmask_b32 v192, 0, v196, vcc\n"
    "v_cmp_lt_i32 vcc, -1, %[vgc]\n"
    "v_mov_b32 v197, 0xff800000\n"
    "s_nop 0\n"
    "v_cndmask_b32 v193, 0, v197, vcc\n"
    "v_cmp_lt_i32 vcc, -2, %[vgc]\n"
    "v_mov_b32 v198, 0xff800000\n"
    "s_nop 0\n"
    "v_cndmask_b32 v194, 0, v198, vcc\n"
    "v_cmp_lt_i32 vcc, -3, %[vgc]\n"
    "v_mov_b32 v199, 0xff800000\n"
    "s_nop 0\n"
    "v_cndmask_b32 v195, 0, v199, vcc\n"
    "s_getpc_b64 s[52:53]\n"
    "H27_%=:\n"
    "s_add_u32 s52, s52, R27_%=-H27_%=\n"
    "s_addc_u32 s53, s53, 0\n"
    "s_branch F2_%=\n"
    "R27_%=:\n"
    "D3b_%=:\n"
    "s_getpc_b64 s[52:53]\n"
    "H28_%=:\n"
    "s_add_u32 s52, s52, R28_%=-H28_%=\n"
    "s_addc_u32 s53, s53, 0\n"
    "s_branch V4_%=\n"
    "R28_%=:\n"
    "v_mfma_f32_16x16x32_bf16 v[136:139], a[160:163], v[40:43], v[136:139]\n"
    "v_mfma_f32_16x16x32_bf16 v[140:143], a[160:163], v[44:47], v[140:143]\n"
    "v_mfma_f32_16x16x32_bf16 v[156:159], a[164:167], v[44:47], v[156:159]\n"
    "v_mfma_f32_16x16x32_bf16 v[152:155], a[164:167], v[40:43], v[152:155]\n"
    "v_mfma_f32_16x16x32_bf16 v[168:171], a[168:171], v[40:43], v[168:171]\n"
    "v_mfma_f32_16x16x32_bf16 v[172:175], a[168:171], v[44:47], v[172:175]\n"
    "v_mfma_f32_16x16x32_bf16 v[188:191], a[172:175], v[44:47], v[188:191]\n"
    "v_mfma_f32_16x16x32_bf16 v[184:187], a[172:175], v[40:43], v[184:187]\n"
    "v_mfma_f32_16x16x32_bf16 a[136:139], a[176:179], v[40:43], a[136:139]\n"
    "v_mfma_f32_16x16x32_bf16 a[140:143], a[176:179], v[44:47], a[140:143]\n"
    "s_waitcnt vmcnt(0) lgkmcnt(0)\n"
    "s_barrier\n"
    "s_branch D8_%=\n"
    "D7_%=:\n"
    "v_mov_b32 v64, 0\n"
    "v_mov_b32 v65, 0\n"
    "v_mov_b32 v66, 0\n"
    "v_mov_b32 v67, 0\n"
    "v_mov_b32 v68, 0\n"
    "v_mov_b32 v69, 0\n"
    "v_mov_b32 v70, 0\n"
    "v_mov_b32 v71, 0\n"
    "v_mov_b32 v72, 0\n"
    "v_mov_b32 v73, 0\n"
    "v_mov_b32 v74, 0\n"
    "v_mov_b32 v75, 0\n"
    "v_mov_b32 v76, 0\n"
    "v_mov_b32 v77, 0\n"
    "v_mov_b32 v78, 0\n"
    "v_mov_b32 v79, 0\n"
    "v_mov_b32 v80, 0\n"
    "v_mov_b32 v81, 0\n"
    "v_mov_b32 v82, 0\n"
    "v_mov_b32 v83, 0\n"
    "v_mov_b32 v84, 0\n"
    "v_mov_b32 v85, 0\n"
    "v_mov_b32 v86, 0\n"
    "v_mov_b32 v87, 0\n"
    "v_mov_b32 v88, 0\n"
    "v_mov_b32 v89, 0\n"
    "v_mov_b32 v90, 0\n"
    "v_mov_b32 v91, 0\n"
    "v_mov_b32 v92, 0\n"
    "v_mov_b32 v93, 0\n"
    "v_mov_b32 v94, 0\n"
    "v_mov_b32 v95, 0\n"
    "v_mov_b32 v96, 0\n"
    "v_mov_b32 v97, 0\n"
    "v_mov_b32 v98, 0\n"
    "v_mov_b32 v99, 0\n"
    "v_mov_b32 v100, 0\n"
    "v_mov_b32 v101, 0\n"
    "v_mov_b32 v102, 0\n"
    "v_mov_b32 v103, 0\n"
    "v_mov_b32 v104, 0\n"
    "v_mov_b32 v105, 0\n"
    "v_mov_b32 v106, 0\n"
    "v_mov_b32 v107, 0\n"
    "v_mov_b32 v108, 0\n"
    "v_mov_b32 v109, 0\n"
    "v_mov_b32 v110, 0\n"
    "v_mov_b32 v111, 0\n"
    "v_mov_b32 v112, 0\n"
    "v_mov_b32 v113, 0\n"
    "v_mov_b32 v114, 0\n"
    "v_mov_b32 v115, 0\n"
    "v_mov_b32 v116, 0\n"
    "v_mov_b32 v117, 0\n"
    "v_mov_b32 v118, 0\n"
    "v_mov_b32 v119, 0\n"
    "v_mov_b32 v120, 0\n"
    "v_mov_b32 v121, 0\n"
    "v_mov_b32 v122, 0\n"
    "v_mov_b32 v123, 0\n"
    "v_mov_b32 v124, 0\n"
    "v_mov_b32 v125, 0\n"
    "v_mov_b32 v126, 0\n"
    "v_mov_b32 v127, 0\n"
    "v_mov_b32 v128, 0\n"
    "v_mov_b32 v129, 0\n"
    "v_mov_b32 v130, 0\n"
    "v_mov_b32 v131, 0\n"
    "v_mov_b32 v132, 0\n"
    "v_mov_b32 v133, 0\n"
    "v_mov_b32 v134, 0\n"
    "v_mov_b32 v135, 0\n"
    "v_mov_b32 v136, 0\n"
    "v_mov_b32 v137, 0\n"
    "v_mov_b32 v138, 0\n"
    "v_mov_b32 v139, 0\n"
    "v_mov_b32 v140, 0\n"
    "v_mov_b32 v141, 0\n"
    "v_mov_b32 v142, 0\n"
    "v_mov_b32 v143, 0\n"
    "v_mov_b32 v144, 0\n"
    "v_mov_b32 v145, 0\n"
    "v_mov_b32 v146, 0\n"
    "v_mov_b32 v147, 0\n"
    "v_mov_b32 v148, 0\n"
    "v_mov_b32 v149, 0\n"
    "v_mov_b32 v150, 0\n"
    "v_mov_b32 v151, 0\n"
    "v_mov_b32 v152, 0\n"
    "v_mov_b32 v153, 0\n"
    "v_mov_b32 v154, 0\n"
    "v_mov_b32 v155, 0\n"
    "v_mov_b32 v156, 0\n"
    "v_mov_b32 v157, 0\n"
    "v_mov_b32 v158, 0\n"
    "v_mov_b32 v159, 0\n"
    "v_mov_b32 v160, 0\n"
    "v_mov_b32 v161, 0\n"
    "v_mov_b32 v162, 0\n"
    "v_mov_b32 v163, 0\n"
    "v_mov_b32 v164, 0\n"
    "v_mov_b32 v165, 0\n"
    "v_mov_b32 v166, 0\n"
    "v_mov_b32 v167, 0\n"
    "v_mov_b32 v168, 0\n"
    "v_mov_b32 v169, 0\n"
    "v_mov_b32 v170, 0\n"
    "v_mov_b32 v171, 0\n"
    "v_mov_b32 v172, 0\n"
    "v_mov_b32 v173, 0\n"
    "v_mov_b32 v174, 0\n"
    "v_mov_b32 v175, 0\n"
    "v_mov_b32 v176, 0\n"
    "v_mov_b32 v177, 0\n"
    "v_mov_b32 v178, 0\n"
    "v_mov_b32 v179, 0\n"
    "v_mov_b32 v180, 0\n"
    "v_mov_b32 v181, 0\n"
    "v_mov_b32 v182, 0\n"
    "v_mov_b32 v183, 0\n"
    "v_mov_b32 v184, 0\n"
    "v_mov_b32 v185, 0\n"
    "v_mov_b32 v186, 0\n"
    "v_mov_b32 v187, 0\n"
    "v_mov_b32 v188, 0\n"
    "v_mov_b32 v189, 0\n"
    "v_mov_b32 v190, 0\n"
    "v_mov_b32 v191, 0\n"
    "v_accvgpr_write_b32 a128, 0\n"
    "v_accvgpr_write_b32 a129, 0\n"
    "v_accvgpr_write_b32 a130, 0\n"
    "v_accvgpr_write_b32 a131, 0\n"
    "v_accvgpr_write_b32 a132, 0\n"
    "v_accvgpr_write_b32 a133, 0\n"
    "v_accvgpr_write_b32 a134, 0\n"
    "v_accvgpr_write_b32 a135, 0\n"
    "v_accvgpr_write_b32 a136, 0\n"
    "v_accvgpr_write_b32 a137, 0\n"
    "v_accvgpr_write_b32 a138, 0\n"
    "v_accvgpr_write_b32 a139, 0\n"
    "v_accvgpr_write_b32 a140, 0\n"
    "v_accvgpr_write_b32 a141, 0\n"
    "v_accvgpr_write_b32 a142, 0\n"
    "v_accvgpr_write_b32 a143, 0\n"
    "D8_%=:\n"
    "s_nop 7\n"
    "s_mov_b32 s47, 0x2b800000\n"
    "v_mov_b32 v210, 0x53800000\n"
    "v_mov_b32 v211, 0\n"
    "v_lshrrev_b32 v206, 1, %[vo]\n"
    "v_add_u32 v206, %[sex], v206\n"
    "ds_write_b128 %[vex], v[64:67] offset:0\n"
    "ds_write_b128 %[vex], v[68:71] offset:1024\n"
    "ds_write_b128 %[vex], v[80:83] offset:2048\n"
    "ds_write_b128 %[vex], v[84:87] offset:3072\n"
    "ds_write_b128 %[vex], v[96:99] offset:4096\n"
    "ds_write_b128 %[vex], v[100:103] offset:5120\n"
    "ds_write_b128 %[vex], v[112:115] offset:6144\n"
    "ds_write_b128 %[vex], v[116:119] offset:7168\n"
    "ds_write_b128 %[vex], v[128:131] offset:8192\n"
    "ds_write_b128 %[vex], v[132:135] offset:9216\n"
    "ds_write_b128 %[vex], v[144:147] offset:10240\n"
    "ds_write_b128 %[vex], v[148:151] offset:11264\n"
    "ds_write_b128 %[vex], v[160:163] offset:12288\n"
    "ds_write_b128 %[vex], v[164:167] offset:13312\n"
    "ds_write_b128 %[vex], v[176:179] offset:14336\n"
    "ds_write_b128 %[vex], v[180:183] offset:15360\n"
    "v_accvgpr_read_b32 v200, a128\n"
    "v_accvgpr_read_b32 v201, a132\n"
    "v_accvgpr_read_b32 v202, a136\n"
    "v_accvgpr_read_b32 v204, a140\n"
    "ds_write_b64 v206, v[200:201]\n"
    "v_xor_b32 %[vex], 32768, %[vex]\n"
    "v_xor_b32 v206, 1024, v206\n"
    "s_waitcnt lgkmcnt(0)\n"
    "s_barrier\n"
    "CB_%=:\n"
    "ds_read_b64 v[200:201], v206\n"
    "ds_read_b128 v[0:3], %[vex] offset:0\n"
    "ds_read_b128 v[8:11], %[vex] offset:2048\n"
    "ds_read_b128 v[16:19], %[vex] offset:4096\n"
    "ds_read_b128 v[24:27], %[vex] offset:6144\n"
    "ds_read_b128 v[32:35], %[vex] offset:8192\n"
    "ds_read_b128 v[40:43], %[vex] offset:10240\n"
    "ds_read_b128 v[48:51], %[vex] offset:12288\n"
    "ds_read_b128 v[56:59], %[vex] offset:14336\n"
    "ds_read_b128 v[4:7], %[vex] offset:1024\n"
    "ds_read_b128 v[12:15], %[vex] offset:3072\n"
    "ds_read_b128 v[20:23], %[vex] offset:5120\n"
    "ds_read_b128 v[28:31], %[vex] offset:7168\n"
    "ds_read_b128 v[36:39], %[vex] offset:9216\n"
    "ds_read_b128 v[44:47], %[vex] offset:11264\n"
    "s_waitcnt lgkmcnt(14)\n"
    "v_add_f32 v202, v202, v200\n"
    "v_add_f32 v204, v204, v201\n"
    "ds_read_b128 v[52:55], %[vex] offset:13312\n"
    "ds_read_b128 v[60:63], %[vex] offset:15360\n"
    "v_med3_f32 v208, v202, s47, v210\n"
    "v_cmp_neq_f32 vcc, v208, v202\n"
    "v_rcp_f32 v202, v202\n"
    "s_nop 0\n"
    "v_cndmask_b32 v208, 0, 1, vcc\n"
    "v_or_b32 v211, v211, v208\n"
    "v_med3_f32 v208, v204, s47, v210\n"
    "v_cmp_neq_f32 vcc, v208, v204\n"
    "v_rcp_f32 v204, v204\n"
    "s_nop 0\n"
    "v_cndmask_b32 v208, 0, 1, vcc\n"
    "v_or_b32 v211, v211, v208\n"
    "s_waitcnt lgkmcnt(8)\n"
    "v_pk_add_f32 v[72:73], v[72:73], v[0:1]\n"
    "v_pk_add_f32 v[74:75], v[74:75], v[2:3]\n"
    "v_pk_add_f32 v[88:89], v[88:89], v[8:9]\n"
    "v_pk_add_f32 v[90:91], v[90:91], v[10:11]\n"
    "v_pk_add_f32 v[104:105], v[104:105], v[16:17]\n"
    "v_pk_add_f32 v[106:107], v[106:107], v[18:19]\n"
    "v_pk_add_f32 v[120:121], v[120:121], v[24:25]\n"
    "v_pk_add_f32 v[122:123], v[122:123], v[26:27]\n"
    "v_pk_add_f32 v[136:137], v[136:137], v[32:33]\n"
    "v_pk_add_f32 v[138:139], v[138:139], v[34:35]\n"
    "v_pk_add_f32 v[152:153], v[152:153], v[40:41]\n"
    "v_pk_add_f32 v[154:155], v[154:155], v[42:43]\n"
    "v_pk_add_f32 v[168:169], v[168:169], v[48:49]\n"
    "v_pk_add_f32 v[170:171], v[170:171], v[50:51]\n"
    "v_pk_add_f32 v[184:185], v[184:185], v[56:57]\n"
    "v_pk_add_f32 v[186:187], v[186:187], v[58:59]\n"
    "s_add_u32 s44, %[soo], 262144\n"
    "v_pk_mul_f32 v[72:73], v[72:73], v[202:203] op_sel_hi:[1,0]\n"
    "v_pk_mul_f32 v[74:75], v[74:75], v[202:203] op_sel_hi:[1,0]\n"
    "v_pk_mul_f32 v[88:89], v[88:89], v[202:203] op_sel_hi:[1,0]\n"
    "v_pk_mul_f32 v[90:91], v[90:91], v[202:203] op_sel_hi:[1,0]\n"
    "v_cvt_pk_bf16_f32 v72, v72, v73\n"
    "v_cvt_pk_bf16_f32 v73, v74, v75\n"
    "v_cvt_pk_bf16_f32 v74, v88, v89\n"
    "v_cvt_pk_bf16_f32 v75, v90, v91\n"
    "buffer_store_dwordx4 v[72:75], %[vo], %[srdo], s44 offen offset:0 sc1\n"
    "v_pk_mul_f32 v[104:105], v[104:105], v[202:203] op_sel_hi:[1,0]\n"
    "v_pk_mul_f32 v[106:107], v[106:107], v[202:203] op_sel_hi:[1,0]\n"
    "v_pk_mul_f32 v[120:121], v[120:121], v[202:203] op_sel_hi:[1,0]\n"
    "v_pk_mul_f32 v[122:123], v[122:123], v[202:203] op_sel_hi:[1,0]\n"
    "v_cvt_pk_bf16_f32 v104, v104, v105\n"
    "v_cvt_pk_bf16_f32 v105, v106, v107\n"
    "v_cvt_pk_bf16_f32 v106, v120, v121\n"
    "v_cvt_pk_bf16_f32 v107, v122, v123\n"
    "buffer_store_dwordx4 v[104:107], %[vo], %[srdo], s44 offen offset:1024 sc1\n"
    "v_pk_mul_f32 v[136:137], v[136:137], v[202:203] op_sel_hi:[1,0]\n"
    "v_pk_mul_f32 v[138:139], v[138:139], v[202:203] op_sel_hi:[1,0]\n"
    "v_pk_mul_f32 v[152:153], v[152:153], v[202:203] op_sel_hi:[1,0]\n"
    "v_pk_mul_f32 v[154:155], v[154:155], v[202:203] op_sel_hi:[1,0]\n"
    "v_cvt_pk_bf16_f32 v136, v136, v137\n"
    "v_cvt_pk_bf16_f32 v137, v138, v139\n"
    "v_cvt_pk_bf16_f32 v138, v152, v153\n"
    "v_cvt_pk_bf16_f32 v139, v154, v155\n"
    "buffer_store_dwordx4 v[136:139], %[vo], %[srdo], s44 offen offset:2048 sc1\n"
    "v_pk_mul_f32 v[168:169], v[168:169], v[202:203] op_sel_hi:[1,0]\n"
    "v_pk_mul_f32 v[170:171], v[170:171], v[202:203] op_sel_hi:[1,0]\n"
    "v_pk_mul_f32 v[184:185], v[184:185], v[202:203] op_sel_hi:[1,0]\n"
    "v_pk_mul_f32 v[186:187], v[186:187], v[202:203] op_sel_hi:[1,0]\n"
    "v_cvt_pk_bf16_f32 v168, v168, v169\n"
    "v_cvt_pk_bf16_f32 v169, v170, v171\n"
    "v_cvt_pk_bf16_f32 v170, v184, v185\n"
    "v_cvt_pk_bf16_f32 v171, v186, v187\n"
    "buffer_store_dwordx4 v[168:171], %[vo], %[srdo], s44 offen offset:3072 sc1\n"
    "s_waitcnt lgkmcnt(0)\n"
    "v_pk_add_f32 v[76:77], v[76:77], v[4:5]\n"
    "v_pk_add_f32 v[78:79], v[78:79], v[6:7]\n"
    "v_pk_add_f32 v[92:93], v[92:93], v[12:13]\n"
    "v_pk_add_f32 v[94:95], v[94:95], v[14:15]\n"
    "v_pk_add_f32 v[108:109], v[108:109], v[20:21]\n"
    "v_pk_add_f32 v[110:111], v[110:111], v[22:23]\n"
    "v_pk_add_f32 v[124:125], v[124:125], v[28:29]\n"
    "v_pk_add_f32 v[126:127], v[126:127], v[30:31]\n"
    "v_pk_add_f32 v[140:141], v[140:141], v[36:37]\n"
    "v_pk_add_f32 v[142:143], v[142:143], v[38:39]\n"
    "v_pk_add_f32 v[156:157], v[156:157], v[44:45]\n"
    "v_pk_add_f32 v[158:159], v[158:159], v[46:47]\n"
    "v_pk_add_f32 v[172:173], v[172:173], v[52:53]\n"
    "v_pk_add_f32 v[174:175], v[174:175], v[54:55]\n"
    "v_pk_add_f32 v[188:189], v[188:189], v[60:61]\n"
    "v_pk_add_f32 v[190:191], v[190:191], v[62:63]\n"
    "s_add_u32 s44, %[soo], 393216\n"
    "v_pk_mul_f32 v[76:77], v[76:77], v[204:205] op_sel_hi:[1,0]\n"
    "v_pk_mul_f32 v[78:79], v[78:79], v[204:205] op_sel_hi:[1,0]\n"
    "v_pk_mul_f32 v[92:93], v[92:93], v[204:205] op_sel_hi:[1,0]\n"
    "v_pk_mul_f32 v[94:95], v[94:95], v[204:205] op_sel_hi:[1,0]\n"
    "v_cvt_pk_bf16_f32 v76, v76, v77\n"
    "v_cvt_pk_bf16_f32 v77, v78, v79\n"
    "v_cvt_pk_bf16_f32 v78, v92, v93\n"
    "v_cvt_pk_bf16_f32 v79, v94, v95\n"
    "buffer_store_dwordx4 v[76:79], %[vo], %[srdo], s44 offen offset:0 sc1\n"
    "v_pk_mul_f32 v[108:109], v[108:109], v[204:205] op_sel_hi:[1,0]\n"
    "v_pk_mul_f32 v[110:111], v[110:111], v[204:205] op_sel_hi:[1,0]\n"
    "v_pk_mul_f32 v[124:125], v[124:125], v[204:205] op_sel_hi:[1,0]\n"
    "v_pk_mul_f32 v[126:127], v[126:127], v[204:205] op_sel_hi:[1,0]\n"
    "v_cvt_pk_bf16_f32 v108, v108, v109\n"
    "v_cvt_pk_bf16_f32 v109, v110, v111\n"
    "v_cvt_pk_bf16_f32 v110, v124, v125\n"
    "v_cvt_pk_bf16_f32 v111, v126, v127\n"
    "buffer_store_dwordx4 v[108:111], %[vo], %[srdo], s44 offen offset:1024 sc1\n"
    "v_pk_mul_f32 v[140:141], v[140:141], v[204:205] op_sel_hi:[1,0]\n"
    "v_pk_mul_f32 v[142:143], v[142:143], v[204:205] op_sel_hi:[1,0]\n"
    "v_pk_mul_f32 v[156:157], v[156:157], v[204:205] op_sel_hi:[1,0]\n"
    "v_pk_mul_f32 v[158:159], v[158:159], v[204:205] op_sel_hi:[1,0]\n"
    "v_cvt_pk_bf16_f32 v140, v140, v141\n"
    "v_cvt_pk_bf16_f32 v141, v142, v143\n"
    "v_cvt_pk_bf16_f32 v142, v156, v157\n"
    "v_cvt_pk_bf16_f32 v143, v158, v159\n"
    "buffer_store_dwordx4 v[140:143], %[vo], %[srdo], s44 offen offset:2048 sc1\n"
    "v_pk_mul_f32 v[172:173], v[172:173], v[204:205] op_sel_hi:[1,0]\n"
    "v_pk_mul_f32 v[174:175], v[174:175], v[204:205] op_sel_hi:[1,0]\n"
    "v_pk_mul_f32 v[188:189], v[188:189], v[204:205] op_sel_hi:[1,0]\n"
    "v_pk_mul_f32 v[190:191], v[190:191], v[204:205] op_sel_hi:[1,0]\n"
    "v_cvt_pk_bf16_f32 v172, v172, v173\n"
    "v_cvt_pk_bf16_f32 v173, v174, v175\n"
    "v_cvt_pk_bf16_f32 v174, v188, v189\n"
    "v_cvt_pk_bf16_f32 v175, v190, v191\n"
    "buffer_store_dwordx4 v[172:175], %[vo], %[srdo], s44 offen offset:3072 sc1\n"
    "v_mov_b32 v208, %[lb]\n"
    "v_add_u32 v208, 160256, v208\n"
    "ds_or_b32 v208, v211\n"
    "s_branch END_%=\n"
    ".p2align 6\n"
    "V1_%=:\n"
    "v_mfma_f32_16x16x32_bf16 v[128:131], a[160:163], v[32:35], v[128:131]\n"
    "ds_read_b128 a[144:147], %[rd] offset:0\n"
    "v_mfma_f32_16x16x32_bf16 v[132:135], a[160:163], v[36:39], v[132:135]\n"
    "ds_read_b128 a[148:151], %[rd] offset:1024\n"
    "v_mfma_f32_16x16x32_bf16 v[136:139], a[160:163], v[40:43], v[136:139]\n"
    "ds_read_b128 a[152:155], %[rd] offset:2048\n"
    "v_mfma_f32_16x16x32_bf16 v[140:143], a[160:163], v[44:47], v[140:143]\n"
    "s_add_u32 m0, s40, 0\n"
    "v_exp_f32 v12, v12\n"
    "v_mfma_f32_16x16x32_bf16 v[156:159], a[164:167], v[44:47], v[156:159]\n"
    "ds_read_b128 a[156:159], %[rd] offset:3072\n"
    "buffer_load_dwordx4 %[vdma], %[srdk], %[so0] offen offset:0 lds\n"
    "v_mfma_f32_16x16x32_bf16 v[152:155], a[164:167], v[40:43], v[152:155]\n"
    "v_exp_f32 v13, v13\n"
    "v_mfma_f32_16x16x32_bf16 v[148:151], a[164:167], v[36:39], v[148:151]\n"
    "v_exp_f32 v14, v14\n"
    "v_mfma_f32_16x16x32_bf16 v[144:147], a[164:167], v[32:35], v[144:147]\n"
    "v_exp_f32 v15, v15\n"
    "v_mfma_f32_16x16x32_bf16 v[160:163], a[168:171], v[32:35], v[160:163]\n"
    "ds_read_b128 a[160:163], %[rd] offset:4096\n"
    "v_mfma_f32_16x16x32_bf16 v[164:167], a[168:171], v[36:39], v[164:167]\n"
    "v_exp_f32 v16, v16\n"
    "v_mfma_f32_16x16x32_bf16 v[168:171], a[168:171], v[40:43], v[168:171]\n"
    "v_exp_f32 v17, v17\n"
    "v_mfma_f32_16x16x32_bf16 v[172:175], a[168:171], v[44:47], v[172:175]\n"
    "v_cvt_pk_bf16_f32 v0, v0, v1\n"
    "v_mfma_f32_16x16x32_bf16 v[188:191], a[172:175], v[44:47], v[188:191]\n"
    "ds_read_b128 a[164:167], %[rd] offset:5120\n"
    "v_mfma_f32_16x16x32_bf16 v[184:187], a[172:175], v[40:43], v[184:187]\n"
    "v_cvt_pk_bf16_f32 v1, v2, v3\n"
    "v_mfma_f32_16x16x32_bf16 v[180:183], a[172:175], v[36:39], v[180:183]\n"
    "buffer_load_dwordx4 %[vdma], %[srdk], %[so0] offen offset:1024 lds\n"
    "v_mfma_f32_16x16x32_bf16 v[176:179], a[172:175], v[32:35], v[176:179]\n"
    "v_exp_f32 v18, v18\n"
    "v_mfma_f32_16x16x32_bf16 a[128:131], a[176:179], v[32:35], a[128:131]\n"
    "v_exp_f32 v19, v19\n"
    "v_mfma_f32_16x16x32_bf16 a[132:135], a[176:179], v[36:39], a[132:135]\n"
    "v_exp_f32 v20, v20\n"
    "v_mfma_f32_16x16x32_bf16 a[136:139], a[176:179], v[40:43], a[136:139]\n"
    "v_exp_f32 v21, v21\n"
    "v_mfma_f32_16x16x32_bf16 a[140:143], a[176:179], v[44:47], a[140:143]\n"
    "s_waitcnt lgkmcnt(2)\n"
    "v_mfma_f32_16x16x32_bf16 v[32:35], a[144:147], a[0:3], 0\n"
    "ds_read_b128 a[168:171], %[rd] offset:6144\n"
    "v_cvt_pk_bf16_f32 v4, v4, v5\n"
    "v_mfma_f32_16x16x32_bf16 v[36:39], a[144:147], a[4:7], 0\n"
    "v_cvt_pk_bf16_f32 v5, v6, v7\n"
    "v_mfma_f32_16x16x32_bf16 v[40:43], a[144:147], a[8:11], 0\n"
    "v_exp_f32 v22, v22\n"
    "v_mfma_f32_16x16x32_bf16 v[44:47], a[144:147], a[12:15], 0\n"
    "v_exp_f32 v23, v23\n"
    "v_mfma_f32_16x16x32_bf16 v[44:47], a[148:151], a[28:31], v[44:47]\n"
    "ds_read_b128 a[172:175], %[rd] offset:7168\n"
    "buffer_load_dwordx4 %[vdma], %[srdk], %[so0] offen offset:2048 lds\n"
    "v_mfma_f32_16x16x32_bf16 v[40:43], a[148:151], a[24:27], v[40:43]\n"
    "v_mfma_f32_16x16x32_bf16 v[36:39], a[148:151], a[20:23], v[36:39]\n"
    "v_cvt_pk_bf16_f32 v2, v16, v17\n"
    "v_cvt_pk_bf16_f32 v3, v18, v19\n"
    "v_mfma_f32_16x16x32_bf16 v[32:35], a[148:151], a[16:19], v[32:35]\n"
    "v_exp_f32 v24, v24\n"
    "v_mfma_f32_16x16x32_bf16 v[32:35], a[152:155], a[32:35], v[32:35]\n"
    "ds_read_b128 a[144:147], %[rd] offset:16384\n"
    "v_mfma_f32_16x16x32_bf16 v[36:39], a[152:155], a[36:39], v[36:39]\n"
    "v_exp_f32 v25, v25\n"
    "v_mfma_f32_16x16x32_bf16 v[40:43], a[152:155], a[40:43], v[40:43]\n"
    "v_mfma_f32_16x16x32_bf16 v[44:47], a[152:155], a[44:47], v[44:47]\n"
    "v_cvt_pk_bf16_f32 v8, v8, v9\n"
    "v_cvt_pk_bf16_f32 v9, v10, v11\n"
    "v_mfma_f32_16x16x32_bf16 v[44:47], a[156:159], a[60:63], v[44:47]\n"
    "ds_read_b128 a[148:151], %[rd] offset:17408\n"
    "v_mfma_f32_16x16x32_bf16 v[40:43], a[156:159], a[56:59], v[40:43]\n"
    "v_exp_f32 v26, v26\n"
    "v_mfma_f32_16x16x32_bf16 v[36:39], a[156:159], a[52:55], v[36:39]\n"
    "v_exp_f32 v27, v27\n"
    "v_mfma_f32_16x16x32_bf16 v[32:35], a[156:159], a[48:51], v[32:35]\n"
    "s_waitcnt lgkmcnt(2)\n"
    "v_mfma_f32_16x16x32_bf16 v[48:51], a[160:163], a[0:3], 0\n"
    "ds_read_b128 a[152:155], %[rd] offset:18432\n"
    "buffer_load_dwordx4 %[vdma], %[srdk], %[so0] offen offset:3072 lds\n"
    "v_mfma_f32_16x16x32_bf16 v[52:55], a[160:163], a[4:7], 0\n"
    "v_cvt_pk_bf16_f32 v6, v20, v21\n"
    "v_cvt_pk_bf16_f32 v7, v22, v23\n"
    "v_mfma_f32_16x16x32_bf16 v[56:59], a[160:163], a[8:11], 0\n"
    "v_exp_f32 v28, v28\n"
    "v_mfma_f32_16x16x32_bf16 v[60:63], a[160:163], a[12:15], 0\n"
    "v_mfma_f32_16x16x32_bf16 v[60:63], a[164:167], a[28:31], v[60:63]\n"
    "ds_read_b128 a[156:159], %[rd] offset:19456\n"
    "v_mfma_f32_16x16x32_bf16 v[56:59], a[164:167], a[24:27], v[56:59]\n"
    "v_exp_f32 v29, v29\n"
    "v_mfma_f32_16x16x32_bf16 v[52:55], a[164:167], a[20:23], v[52:55]\n"
    "v_cvt_pk_bf16_f32 v12, v12, v13\n"
    "v_cvt_pk_bf16_f32 v13, v14, v15\n"
    "v_mfma_f32_16x16x32_bf16 v[48:51], a[164:167], a[16:19], v[48:51]\n"
    "s_add_u32 m0, s40, 16384\n"
    "v_exp_f32 v30, v30\n"
    "v_mfma_f32_16x16x32_bf16 v[48:51], a[168:171], a[32:35], v[48:51]\n"
    "ds_read_b128 a[160:163], %[rd] offset:20480\n"
    "buffer_load_dwordx4 %[vdma], %[srdv], %[so0] offen offset:0 lds\n"
    "v_mfma_f32_16x16x32_bf16 v[52:55], a[168:171], a[36:39], v[52:55]\n"
    "v_mfma_f32_16x16x32_bf16 v[56:59], a[168:171], a[40:43], v[56:59]\n"
    "v_exp_f32 v31, v31\n"
    "v_mfma_f32_16x16x32_bf16 v[60:63], a[168:171], a[44:47], v[60:63]\n"
    "v_cvt_pk_bf16_f32 v10, v24, v25\n"
    "v_mfma_f32_16x16x32_bf16 v[60:63], a[172:175], a[60:63], v[60:63]\n"
    "ds_read_b128 a[164:167], %[rd] offset:21504\n"
    "v_cvt_pk_bf16_f32 v11, v26, v27\n"
    "v_mfma_f32_16x16x32_bf16 v[56:59], a[172:175], a[56:59], v[56:59]\n"
    "v_cvt_pk_bf16_f32 v14, v28, v29\n"
    "v_mfma_f32_16x16x32_bf16 v[52:55], a[172:175], a[52:55], v[52:55]\n"
    "v_cvt_pk_bf16_f32 v15, v30, v31\n"
    "v_mfma_f32_16x16x32_bf16 v[48:51], a[172:175], a[48:51], v[48:51]\n"
    "s_waitcnt lgkmcnt(2)\n"
    "v_mfma_f32_16x16x32_bf16 v[64:67], a[144:147], v[0:3], v[64:67]\n"
    "ds_read_b128 a[168:171], %[rd] offset:22528\n"
    "v_mfma_f32_16x16x32_bf16 v[68:71], a[144:147], v[4:7], v[68:71]\n"
    "v_exp_f32 v32, v32\n"
    "v_mfma_f32_16x16x32_bf16 v[72:75], a[144:147], v[8:11], v[72:75]\n"
    "v_exp_f32 v33, v33\n"
    "v_mfma_f32_16x16x32_bf16 v[76:79], a[144:147], v[12:15], v[76:79]\n"
    "v_exp_f32 v34, v34\n"
    "v_mfma_f32_16x16x32_bf16 v[92:95], a[148:151], v[12:15], v[92:95]\n"
    "ds_read_b128 a[172:175], %[rd] offset:23552\n"
    "buffer_load_dwordx4 %[vdma], %[srdv], %[so0] offen offset:1024 lds\n"
    "v_mfma_f32_16x16x32_bf16 v[88:91], a[148:151], v[8:11], v[88:91]\n"
    "v_exp_f32 v35, v35\n"
    "v_mfma_f32_16x16x32_bf16 v[84:87], a[148:151], v[4:7], v[84:87]\n"
    "v_mfma_f32_16x16x32_bf16 v[80:83], a[148:151], v[0:3], v[80:83]\n"
    "v_exp_f32 v36, v36\n"
    "v_mfma_f32_16x16x32_bf16 v[96:99], a[152:155], v[0:3], v[96:99]\n"
    "ds_read_b128 a[144:147], %[rd] offset:8192\n"
    "v_mfma_f32_16x16x32_bf16 v[100:103], a[152:155], v[4:7], v[100:103]\n"
    "v_exp_f32 v37, v37\n"
    "v_mfma_f32_16x16x32_bf16 v[104:107], a[152:155], v[8:11], v[104:107]\n"
    "v_exp_f32 v38, v38\n"
    "v_mfma_f32_16x16x32_bf16 v[108:111], a[152:155], v[12:15], v[108:111]\n"
    "v_exp_f32 v39, v39\n"
    "v_mfma_f32_16x16x32_bf16 v[124:127], a[156:159], v[12:15], v[124:127]\n"
    "ds_read_b128 a[148:151], %[rd] offset:9216\n"
    "buffer_load_dwordx4 %[vdma], %[srdv], %[so0] offen offset:2048 lds\n"
    "v_mfma_f32_16x16x32_bf16 v[120:123], a[156:159], v[8:11], v[120:123]\n"
    "v_mfma_f32_16x16x32_bf16 v[116:119], a[156:159], v[4:7], v[116:119]\n"
    "v_exp_f32 v40, v40\n"
    "v_mfma_f32_16x16x32_bf16 v[112:115], a[156:159], v[0:3], v[112:115]\n"
    "s_waitcnt lgkmcnt(2)\n"
    "v_exp_f32 v41, v41\n"
    "v_mfma_f32_16x16x32_bf16 v[128:131], a[160:163], v[0:3], v[128:131]\n"
    "ds_read_b128 a[152:155], %[rd] offset:10240\n"
    "v_mfma_f32_16x16x32_bf16 v[132:135], a[160:163], v[4:7], v[132:135]\n"
    "v_exp_f32 v42, v42\n"
    "v_mfma_f32_16x16x32_bf16 v[136:139], a[160:163], v[8:11], v[136:139]\n"
    "v_mfma_f32_16x16x32_bf16 v[140:143], a[160:163], v[12:15], v[140:143]\n"
    "v_exp_f32 v43, v43\n"
    "v_mfma_f32_16x16x32_bf16 v[156:159], a[164:167], v[12:15], v[156:159]\n"
    "ds_read_b128 a[156:159], %[rd] offset:11264\n"
    "v_mfma_f32_16x16x32_bf16 v[152:155], a[164:167], v[8:11], v[152:155]\n"
    "v_exp_f32 v44, v44\n"
    "v_mfma_f32_16x16x32_bf16 v[148:151], a[164:167], v[4:7], v[148:151]\n"
    "v_exp_f32 v45, v45\n"
    "v_mfma_f32_16x16x32_bf16 v[144:147], a[164:167], v[0:3], v[144:147]\n"
    "v_exp_f32 v46, v46\n"
    "v_mfma_f32_16x16x32_bf16 v[160:163], a[168:171], v[0:3], v[160:163]\n"
    "ds_read_b128 a[160:163], %[rd] offset:12288\n"
    "buffer_load_dwordx4 %[vdma], %[srdv], %[so0] offen offset:3072 lds\n"
    "v_mfma_f32_16x16x32_bf16 v[164:167], a[168:171], v[4:7], v[164:167]\n"
    "v_mfma_f32_16x16x32_bf16 v[168:171], a[168:171], v[8:11], v[168:171]\n"
    "buffer_load_dword v215, %[vpf], %[srdp], %[spf] offen ; dummy\n"
    "v_mfma_f32_16x16x32_bf16 v[172:175], a[168:171], v[12:15], v[172:175]\n"
    "v_exp_f32 v47, v47\n"
    "v_mfma_f32_16x16x32_bf16 v[188:191], a[172:175], v[12:15], v[188:191]\n"
    "ds_read_b128 a[164:167], %[rd] offset:13312\n"
    "v_mfma_f32_16x16x32_bf16 v[184:187], a[172:175], v[8:11], v[184:187]\n"
    "v_exp_f32 v48, v48\n"
    "v_mfma_f32_16x16x32_bf16 v[180:183], a[172:175], v[4:7], v[180:183]\n"
    "v_exp_f32 v49, v49\n"
    "v_mfma_f32_16x16x32_bf16 v[176:179], a[172:175], v[0:3], v[176:179]\n"
    "v_cvt_pk_bf16_f32 v32, v32, v33\n"
    "v_mfma_f32_16x16x32_bf16 a[128:131], a[176:179], v[0:3], a[128:131]\n"
    "v_cvt_pk_bf16_f32 v33, v34, v35\n"
    "v_mfma_f32_16x16x32_bf16 a[132:135], a[176:179], v[4:7], a[132:135]\n"
    "v_exp_f32 v50, v50\n"
    "v_mfma_f32_16x16x32_bf16 a[136:139], a[176:179], v[8:11], a[136:139]\n"
    "v_exp_f32 v51, v51\n"
    "v_mfma_f32_16x16x32_bf16 a[140:143], a[176:179], v[12:15], a[140:143]\n"
    "s_waitcnt lgkmcnt(2)\n"
    "v_exp_f32 v52, v52\n"
    "v_mfma_f32_16x16x32_bf16 v[0:3], a[144:147], a[0:3], v[192:195]\n"
    "ds_read_b128 a[168:171], %[rd] offset:14336\n"
    "v_mfma_f32_16x16x32_bf16 v[4:7], a[144:147], a[4:7], 0\n"
    "v_exp_f32 v53, v53\n"
    "v_mfma_f32_16x16x32_bf16 v[8:11], a[144:147], a[8:11], 0\n"
    "v_mfma_f32_16x16x32_bf16 v[12:15], a[144:147], a[12:15], 0\n"
    "v_cvt_pk_bf16_f32 v36, v36, v37\n"
    "v_cvt_pk_bf16_f32 v37, v38, v39\n"
    "v_mfma_f32_16x16x32_bf16 v[12:15], a[148:151], a[28:31], v[12:15]\n"
    "ds_read_b128 a[172:175], %[rd] offset:15360\n"
    "v_mfma_f32_16x16x32_bf16 v[8:11], a[148:151], a[24:27], v[8:11]\n"
    "v_exp_f32 v54, v54\n"
    "v_mfma_f32_16x16x32_bf16 v[4:7], a[148:151], a[20:23], v[4:7]\n"
    "v_exp_f32 v55, v55\n"
    "v_mfma_f32_16x16x32_bf16 v[0:3], a[148:151], a[16:19], v[0:3]\n"
    "v_mfma_f32_16x16x32_bf16 v[0:3], a[152:155], a[32:35], v[0:3]\n"
    "ds_read_b128 a[144:147], %[rd] offset:24576\n"
    "v_cvt_pk_bf16_f32 v34, v48, v49\n"
    "v_mfma_f32_16x16x32_bf16 v[4:7], a[152:155], a[36:39], v[4:7]\n"
    "v_cvt_pk_bf16_f32 v35, v50, v51\n"
    "v_mfma_f32_16x16x32_bf16 v[8:11], a[152:155], a[40:43], v[8:11]\n"
    "v_exp_f32 v56, v56\n"
    "v_mfma_f32_16x16x32_bf16 v[12:15], a[152:155], a[44:47], v[12:15]\n"
    "v_exp_f32 v57, v57\n"
    "v_mfma_f32_16x16x32_bf16 v[12:15], a[156:159], a[60:63], v[12:15]\n"
    "ds_read_b128 a[148:151], %[rd] offset:25600\n"
    "v_mfma_f32_16x16x32_bf16 v[8:11], a[156:159], a[56:59], v[8:11]\n"
    "v_cvt_pk_bf16_f32 v40, v40, v41\n"
    "v_mfma_f32_16x16x32_bf16 v[4:7], a[156:159], a[52:55], v[4:7]\n"
    "v_cvt_pk_bf16_f32 v41, v42, v43\n"
    "v_mfma_f32_16x16x32_bf16 v[0:3], a[156:159], a[48:51], v[0:3]\n"
    "s_waitcnt lgkmcnt(2)\n"
    "v_exp_f32 v58, v58\n"
    "v_mfma_f32_16x16x32_bf16 v[16:19], a[160:163], a[0:3], v[196:199]\n"
    "ds_read_b128 a[152:155], %[rd] offset:26624\n"
    "v_mfma_f32_16x16x32_bf16 v[20:23], a[160:163], a[4:7], v[192:195]\n"
    "v_exp_f32 v59, v59\n"
    "v_mfma_f32_16x16x32_bf16 v[24:27], a[160:163], a[8:11], 0\n"
    "v_cvt_pk_bf16_f32 v38, v52, v53\n"
    "v_cvt_pk_bf16_f32 v39, v54, v55\n"
    "v_mfma_f32_16x16x32_bf16 v[28:31], a[160:163], a[12:15], 0\n"
    "v_exp_f32 v60, v60\n"
    "v_mfma_f32_16x16x32_bf16 v[28:31], a[164:167], a[28:31], v[28:31]\n"
    "ds_read_b128 a[156:159], %[rd] offset:27648\n"
    "v_mfma_f32_16x16x32_bf16 v[24:27], a[164:167], a[24:27], v[24:27]\n"
    "v_exp_f32 v61, v61\n"
    "v_mfma_f32_16x16x32_bf16 v[20:23], a[164:167], a[20:23], v[20:23]\n"
    "v_cvt_pk_bf16_f32 v44, v44, v45\n"
    "v_mfma_f32_16x16x32_bf16 v[16:19], a[164:167], a[16:19], v[16:19]\n"
    "v_cvt_pk_bf16_f32 v45, v46, v47\n"
    "v_mfma_f32_16x16x32_bf16 v[16:19], a[168:171], a[32:35], v[16:19]\n"
    "ds_read_b128 a[160:163], %[rd] offset:28672\n"
    "v_mfma_f32_16x16x32_bf16 v[20:23], a[168:171], a[36:39], v[20:23]\n"
    "v_exp_f32 v62, v62\n"
    "v_mfma_f32_16x16x32_bf16 v[24:27], a[168:171], a[40:43], v[24:27]\n"
    "v_exp_f32 v63, v63\n"
    "v_mfma_f32_16x16x32_bf16 v[28:31], a[168:171], a[44:47], v[28:31]\n"
    "v_cvt_pk_bf16_f32 v42, v56, v57\n"
    "v_mfma_f32_16x16x32_bf16 v[28:31], a[172:175], a[60:63], v[28:31]\n"
    "ds_read_b128 a[164:167], %[rd] offset:29696\n"
    "v_cvt_pk_bf16_f32 v43, v58, v59\n"
    "v_mfma_f32_16x16x32_bf16 v[24:27], a[172:175], a[56:59], v[24:27]\n"
    "v_cvt_pk_bf16_f32 v46, v60, v61\n"
    "v_mfma_f32_16x16x32_bf16 v[20:23], a[172:175], a[52:55], v[20:23]\n"
    "v_cvt_pk_bf16_f32 v47, v62, v63\n"
    "v_mfma_f32_16x16x32_bf16 v[16:19], a[172:175], a[48:51], v[16:19]\n"
    "s_waitcnt lgkmcnt(2)\n"
    "v_mfma_f32_16x16x32_bf16 v[64:67], a[144:147], v[32:35], v[64:67]\n"
    "ds_read_b128 a[168:171], %[rd] offset:30720\n"
    "v_mfma_f32_16x16x32_bf16 v[68:71], a[144:147], v[36:39], v[68:71]\n"
    "v_mfma_f32_16x16x32_bf16 v[72:75], a[144:147], v[40:43], v[72:75]\n"
    "v_exp_f32 v0, v0\n"
    "v_mfma_f32_16x16x32_bf16 v[76:79], a[144:147], v[44:47], v[76:79]\n"
    "v_exp_f32 v1, v1\n"
    "v_mfma_f32_16x16x32_bf16 v[92:95], a[148:151], v[44:47], v[92:95]\n"
    "ds_read_b128 a[172:175], %[rd] offset:31744\n"
    "v_mfma_f32_16x16x32_bf16 v[88:91], a[148:151], v[40:43], v[88:91]\n"
    "v_xor_b32 %[rd], 65536, %[rd]\n"
    "v_mfma_f32_16x16x32_bf16 v[84:87], a[148:151], v[36:39], v[84:87]\n"
    "s_add_u32 %[so0], %[so0], 16384\n"
    "v_exp_f32 v2, v2\n"
    "v_mfma_f32_16x16x32_bf16 v[80:83], a[148:151], v[32:35], v[80:83]\n"
    "s_xor_b32 s40, s40, 65536\n"
    "v_exp_f32 v3, v3\n"
    "v_mfma_f32_16x16x32_bf16 v[96:99], a[152:155], v[32:35], v[96:99]\n"
    "s_add_u32 %[spf], %[spf], 4096\n"
    "v_exp_f32 v4, v4\n"
    "v_mfma_f32_16x16x32_bf16 v[100:103], a[152:155], v[36:39], v[100:103]\n"
    "s_sub_u32 s50, s50, 1\n"
    "v_exp_f32 v5, v5\n"
    "v_mfma_f32_16x16x32_bf16 v[104:107], a[152:155], v[40:43], v[104:107]\n"
    "s_cmp_lg_u32 s50, 0\n"
    "v_exp_f32 v6, v6\n"
    "v_mfma_f32_16x16x32_bf16 v[108:111], a[152:155], v[44:47], v[108:111]\n"
    "v_exp_f32 v7, v7\n"
    "v_mfma_f32_16x16x32_bf16 v[124:127], a[156:159], v[44:47], v[124:127]\n"
    "v_exp_f32 v8, v8\n"
    "v_mfma_f32_16x16x32_bf16 v[120:123], a[156:159], v[40:43], v[120:123]\n"
    "v_exp_f32 v9, v9\n"
    "v_mfma_f32_16x16x32_bf16 v[116:119], a[156:159], v[36:39], v[116:119]\n"
    "v_exp_f32 v10, v10\n"
    "v_mfma_f32_16x16x32_bf16 v[112:115], a[156:159], v[32:35], v[112:115]\n"
    "v_exp_f32 v11, v11\n"
    "s_waitcnt vmcnt(1) lgkmcnt(0)\n"
    "s_barrier\n"
    "s_cbranch_scc1 V1_%=\n"
    "s_setpc_b64 s[52:53]\n"
    ".p2align 6\n"
    "V2_%=:\n"
    "v_mfma_f32_16x16x32_bf16 v[128:131], a[160:163], v[32:35], v[128:131]\n"
    "ds_read_b128 a[144:147], %[rd] offset:0\n"
    "v_mfma_f32_16x16x32_bf16 v[132:135], a[160:163], v[36:39], v[132:135]\n"
    "ds_read_b128 a[148:151], %[rd] offset:1024\n"
    "v_mfma_f32_16x16x32_bf16 v[136:139], a[160:163], v[40:43], v[136:139]\n"
    "ds_read_b128 a[152:155], %[rd] offset:2048\n"
    "v_mfma_f32_16x16x32_bf16 v[140:143], a[160:163], v[44:47], v[140:143]\n"
    "s_add_u32 m0, s40, 0\n"
    "v_exp_f32 v12, v12\n"
    "v_mfma_f32_16x16x32_bf16 v[156:159], a[164:167], v[44:47], v[156:159]\n"
    "ds_read_b128 a[156:159], %[rd] offset:3072\n"
    "buffer_load_dwordx4 %[vdma], %[srdk], %[so0] offen offset:0 lds\n"
    "v_mfma_f32_16x16x32_bf16 v[152:155], a[164:167], v[40:43], v[152:155]\n"
    "v_exp_f32 v13, v13\n"
    "v_mfma_f32_16x16x32_bf16 v[148:151], a[164:167], v[36:39], v[148:151]\n"
    "v_exp_f32 v14, v14\n"
    "v_mfma_f32_16x16x32_bf16 v[144:147], a[164:167], v[32:35], v[144:147]\n"
    "v_exp_f32 v15, v15\n"
    "v_mfma_f32_16x16x32_bf16 v[160:163], a[168:171], v[32:35], v[160:163]\n"
    "ds_read_b128 a[160:163], %[rd] offset:4096\n"
    "v_mfma_f32_16x16x32_bf16 v[164:167], a[168:171], v[36:39], v[164:167]\n"
    "buffer_load_dwordx4 %[vdma], %[srdk], %[so0] offen offset:1024 lds\n"
    "v_mfma_f32_16x16x32_bf16 v[168:171], a[168:171], v[40:43], v[168:171]\n"
    "v_exp_f32 v16, v16\n"
    "v_mfma_f32_16x16x32_bf16 v[172:175], a[168:171], v[44:47], v[172:175]\n"
    "v_exp_f32 v17, v17\n"
    "v_mfma_f32_16x16x32_bf16 v[188:191], a[172:175], v[44:47], v[188:191]\n"
    "ds_read_b128 a[164:167], %[rd] offset:5120\n"
    "v_cvt_pk_bf16_f32 v0, v0, v1\n"
    "v_mfma_f32_16x16x32_bf16 v[184:187], a[172:175], v[40:43], v[184:187]\n"
    "v_cvt_pk_bf16_f32 v1, v2, v3\n"
    "v_mfma_f32_16x16x32_bf16 v[180:183], a[172:175], v[36:39], v[180:183]\n"
    "buffer_load_dwordx4 %[vdma], %[srdk], %[so0] offen offset:2048 lds\n"
    "v_mfma_f32_16x16x32_bf16 v[176:179], a[172:175], v[32:35], v[176:179]\n"
    "v_exp_f32 v18, v18\n"
    "v_mfma_f32_16x16x32_bf16 a[128:131], a[176:179], v[32:35], a[128:131]\n"
    "v_exp_f32 v19, v19\n"
    "v_mfma_f32_16x16x32_bf16 a[132:135], a[176:179], v[36:39], a[132:135]\n"
    "v_exp_f32 v20, v20\n"
    "v_mfma_f32_16x16x32_bf16 a[136:139], a[176:179], v[40:43], a[136:139]\n"
    "v_exp_f32 v21, v21\n"
    "v_mfma_f32_16x16x32_bf16 a[140:143], a[176:179], v[44:47], a[140:143]\n"
    "s_waitcnt lgkmcnt(2)\n"
    "v_cvt_pk_bf16_f32 v4, v4, v5\n"
    "v_mfma_f32_16x16x32_bf16 v[32:35], a[144:147], a[0:3], 0\n"
    "ds_read_b128 a[168:171], %[rd] offset:6144\n"
    "buffer_load_dwordx4 %[vdma], %[srdk], %[so0] offen offset:3072 lds\n"
    "v_mfma_f32_16x16x32_bf16 v[36:39], a[144:147], a[4:7], 0\n"
    "v_cvt_pk_bf16_f32 v5, v6, v7\n"
    "v_mfma_f32_16x16x32_bf16 v[40:43], a[144:147], a[8:11], 0\n"
    "v_exp_f32 v22, v22\n"
    "v_mfma_f32_16x16x32_bf16 v[44:47], a[144:147], a[12:15], 0\n"
    "s_add_u32 m0, s40, 16384\n"
    "v_exp_f32 v23, v23\n"
    "v_mfma_f32_16x16x32_bf16 v[44:47], a[148:151], a[28:31], v[44:47]\n"
    "ds_read_b128 a[172:175], %[rd] offset:7168\n"
    "buffer_load_dwordx4 %[vdma], %[srdv], %[so0] offen offset:0 lds\n"
    "v_mfma_f32_16x16x32_bf16 v[40:43], a[148:151], a[24:27], v[40:43]\n"
    "v_cvt_pk_bf16_f32 v2, v16, v17\n"
    "v_mfma_f32_16x16x32_bf16 v[36:39], a[148:151], a[20:23], v[36:39]\n"
    "v_cvt_pk_bf16_f32 v3, v18, v19\n"
    "v_mfma_f32_16x16x32_bf16 v[32:35], a[148:151], a[16:19], v[32:35]\n"
    "v_exp_f32 v24, v24\n"
    "v_mfma_f32_16x16x32_bf16 v[32:35], a[152:155], a[32:35], v[32:35]\n"
    "ds_read_b128 a[144:147], %[rd] offset:16384\n"
    "v_mfma_f32_16x16x32_bf16 v[36:39], a[152:155], a[36:39], v[36:39]\n"
    "buffer_load_dwordx4 %[vdma], %[srdv], %[so0] offen offset:1024 lds\n"
    "v_mfma_f32_16x16x32_bf16 v[40:43], a[152:155], a[40:43], v[40:43]\n"
    "v_exp_f32 v25, v25\n"
    "v_mfma_f32_16x16x32_bf16 v[44:47], a[152:155], a[44:47], v[44:47]\n"
    "v_cvt_pk_bf16_f32 v8, v8, v9\n"
    "v_cvt_pk_bf16_f32 v9, v10, v11\n"
    "v_mfma_f32_16x16x32_bf16 v[44:47], a[156:159], a[60:63], v[44:47]\n"
    "ds_read_b128 a[148:151], %[rd] offset:17408\n"
    "v_mfma_f32_16x16x32_bf16 v[40:43], a[156:159], a[56:59], v[40:43]\n"
    "v_exp_f32 v26, v26\n"
    "v_mfma_f32_16x16x32_bf16 v[36:39], a[156:159], a[52:55], v[36:39]\n"
    "v_exp_f32 v27, v27\n"
    "v_mfma_f32_16x16x32_bf16 v[32:35], a[156:159], a[48:51], v[32:35]\n"
    "s_waitcnt lgkmcnt(2)\n"
    "v_cvt_pk_bf16_f32 v6, v20, v21\n"
    "v_mfma_f32_16x16x32_bf16 v[48:51], a[160:163], a[0:3], 0\n"
    "ds_read_b128 a[152:155], %[rd] offset:18432\n"
    "buffer_load_dwordx4 %[vdma], %[srdv], %[so0] offen offset:2048 lds\n"
    "v_mfma_f32_16x16x32_bf16 v[52:55], a[160:163], a[4:7], 0\n"
    "v_cvt_pk_bf16_f32 v7, v22, v23\n"
    "v_mfma_f32_16x16x32_bf16 v[56:59], a[160:163], a[8:11], 0\n"
    "v_exp_f32 v28, v28\n"
    "v_mfma_f32_16x16x32_bf16 v[60:63], a[160:163], a[12:15], 0\n"
    "v_exp_f32 v29, v29\n"
    "v_mfma_f32_16x16x32_bf16 v[60:63], a[164:167], a[28:31], v[60:63]\n"
    "ds_read_b128 a[156:159], %[rd] offset:19456\n"
    "buffer_load_dwordx4 %[vdma], %[srdv], %[so0] offen offset:3072 lds\n"
    "v_mfma_f32_16x16x32_bf16 v[56:59], a[164:167], a[24:27], v[56:59]\n"
    "v_mfma_f32_16x16x32_bf16 v[52:55], a[164:167], a[20:23], v[52:55]\n"
    "v_cvt_pk_bf16_f32 v12, v12, v13\n"
    "v_cvt_pk_bf16_f32 v13, v14, v15\n"
    "v_mfma_f32_16x16x32_bf16 v[48:51], a[164:167], a[16:19], v[48:51]\n"
    "s_add_u32 m0, s40, 32768\n"
    "v_exp_f32 v30, v30\n"
    "v_mfma_f32_16x16x32_bf16 v[48:51], a[168:171], a[32:35], v[48:51]\n"
    "ds_read_b128 a[160:163], %[rd] offset:20480\n"
    "buffer_load_dwordx4 %[vdma], %[srdk], %[so1] offen offset:0 lds\n"
    "v_mfma_f32_16x16x32_bf16 v[52:55], a[168:171], a[36:39], v[52:55]\n"
    "v_exp_f32 v31, v31\n"
    "v_mfma_f32_16x16x32_bf16 v[56:59], a[168:171], a[40:43], v[56:59]\n"
    "v_mfma_f32_16x16x32_bf16 v[60:63], a[168:171], a[44:47], v[60:63]\n"
    "v_cvt_pk_bf16_f32 v10, v24, v25\n"
    "v_cvt_pk_bf16_f32 v11, v26, v27\n"
    "v_mfma_f32_16x16x32_bf16 v[60:63], a[172:175], a[60:63], v[60:63]\n"
    "ds_read_b128 a[164:167], %[rd] offset:21504\n"
    "v_mfma_f32_16x16x32_bf16 v[56:59], a[172:175], a[56:59], v[56:59]\n"
    "buffer_load_dwordx4 %[vdma], %[srdk], %[so1] offen offset:1024 lds\n"
    "v_cvt_pk_bf16_f32 v14, v28, v29\n"
    "v_mfma_f32_16x16x32_bf16 v[52:55], a[172:175], a[52:55], v[52:55]\n"
    "v_cvt_pk_bf16_f32 v15, v30, v31\n"
    "v_mfma_f32_16x16x32_bf16 v[48:51], a[172:175], a[48:51], v[48:51]\n"
    "s_waitcnt lgkmcnt(2)\n"
    "v_mfma_f32_16x16x32_bf16 v[64:67], a[144:147], v[0:3], v[64:67]\n"
    "ds_read_b128 a[168:171], %[rd] offset:22528\n"
    "v_mfma_f32_16x16x32_bf16 v[68:71], a[144:147], v[4:7], v[68:71]\n"
    "v_exp_f32 v32, v32\n"
    "v_mfma_f32_16x16x32_bf16 v[72:75], a[144:147], v[8:11], v[72:75]\n"
    "v_exp_f32 v33, v33\n"
    "v_mfma_f32_16x16x32_bf16 v[76:79], a[144:147], v[12:15], v[76:79]\n"
    "v_exp_f32 v34, v34\n"
    "v_mfma_f32_16x16x32_bf16 v[92:95], a[148:151], v[12:15], v[92:95]\n"
    "ds_read_b128 a[172:175], %[rd] offset:23552\n"
    "buffer_load_dwordx4 %[vdma], %[srdk], %[so1] offen offset:2048 lds\n"
    "v_mfma_f32_16x16x32_bf16 v[88:91], a[148:151], v[8:11], v[88:91]\n"
    "v_exp_f32 v35, v35\n"
    "v_mfma_f32_16x16x32_bf16 v[84:87], a[148:151], v[4:7], v[84:87]\n"
    "v_mfma_f32_16x16x32_bf16 v[80:83], a[148:151], v[0:3], v[80:83]\n"
    "v_exp_f32 v36, v36\n"
    "v_mfma_f32_16x16x32_bf16 v[96:99], a[152:155], v[0:3], v[96:99]\n"
    "ds_read_b128 a[144:147], %[rd] offset:8192\n"
    "buffer_load_dwordx4 %[vdma], %[srdk], %[so1] offen offset:3072 lds\n"
    "v_mfma_f32_16x16x32_bf16 v[100:103], a[152:155], v[4:7], v[100:103]\n"
    "v_exp_f32 v37, v37\n"
    "v_mfma_f32_16x16x32_bf16 v[104:107], a[152:155], v[8:11], v[104:107]\n"
    "v_exp_f32 v38, v38\n"
    "v_mfma_f32_16x16x32_bf16 v[108:111], a[152:155], v[12:15], v[108:111]\n"
    "s_add_u32 m0, s40, 49152\n"
    "v_mfma_f32_16x16x32_bf16 v[124:127], a[156:159], v[12:15], v[124:127]\n"
    "ds_read_b128 a[148:151], %[rd] offset:9216\n"
    "buffer_load_dwordx4 %[vdma], %[srdv], %[so1] offen offset:0 lds\n"
    "v_mfma_f32_16x16x32_bf16 v[120:123], a[156:159], v[8:11], v[120:123]\n"
    "v_exp_f32 v39, v39\n"
    "v_mfma_f32_16x16x32_bf16 v[116:119], a[156:159], v[4:7], v[116:119]\n"
    "v_exp_f32 v40, v40\n"
    "v_mfma_f32_16x16x32_bf16 v[112:115], a[156:159], v[0:3], v[112:115]\n"
    "s_waitcnt lgkmcnt(2)\n"
    "v_exp_f32 v41, v41\n"
    "v_mfma_f32_16x16x32_bf16 v[128:131], a[160:163], v[0:3], v[128:131]\n"
    "ds_read_b128 a[152:155], %[rd] offset:10240\n"
    "v_mfma_f32_16x16x32_bf16 v[132:135], a[160:163], v[4:7], v[132:135]\n"
    "buffer_load_dwordx4 %[vdma], %[srdv], %[so1] offen offset:1024 lds\n"
    "v_mfma_f32_16x16x32_bf16 v[136:139], a[160:163], v[8:11], v[136:139]\n"
    "v_exp_f32 v42, v42\n"
    "v_mfma_f32_16x16x32_bf16 v[140:143], a[160:163], v[12:15], v[140:143]\n"
    "v_exp_f32 v43, v43\n"
    "v_mfma_f32_16x16x32_bf16 v[156:159], a[164:167], v[12:15], v[156:159]\n"
    "ds_read_b128 a[156:159], %[rd] offset:11264\n"
    "v_mfma_f32_16x16x32_bf16 v[152:155], a[164:167], v[8:11], v[152:155]\n"
    "v_exp_f32 v44, v44\n"
    "v_mfma_f32_16x16x32_bf16 v[148:151], a[164:167], v[4:7], v[148:151]\n"
    "v_mfma_f32_16x16x32_bf16 v[144:147], a[164:167], v[0:3], v[144:147]\n"
    "v_exp_f32 v45, v45\n"
    "v_mfma_f32_16x16x32_bf16 v[160:163], a[168:171], v[0:3], v[160:163]\n"
    "ds_read_b128 a[160:163], %[rd] offset:12288\n"
    "buffer_load_dwordx4 %[vdma], %[srdv], %[so1] offen offset:2048 lds\n"
    "v_mfma_f32_16x16x32_bf16 v[164:167], a[168:171], v[4:7], v[164:167]\n"
    "v_exp_f32 v46, v46\n"
    "v_mfma_f32_16x16x32_bf16 v[168:171], a[168:171], v[8:11], v[168:171]\n"
    "v_exp_f32 v47, v47\n"
    "v_mfma_f32_16x16x32_bf16 v[172:175], a[168:171], v[12:15], v[172:175]\n"
    "v_mfma_f32_16x16x32_bf16 v[188:191], a[172:175], v[12:15], v[188:191]\n"
    "ds_read_b128 a[164:167], %[rd] offset:13312\n"
    "buffer_load_dwordx4 %[vdma], %[srdv], %[so1] offen offset:3072 lds\n"
    "v_mfma_f32_16x16x32_bf16 v[184:187], a[172:175], v[8:11], v[184:187]\n"
    "v_exp_f32 v48, v48\n"
    "v_mfma_f32_16x16x32_bf16 v[180:183], a[172:175], v[4:7], v[180:183]\n"
    "buffer_load_dword v215, %[vpf], %[srdp], %[spf] offen ; dummy\n"
    "v_mfma_f32_16x16x32_bf16 v[176:179], a[172:175], v[0:3], v[176:179]\n"
    "v_exp_f32 v49, v49\n"
    "v_mfma_f32_16x16x32_bf16 a[128:131], a[176:179], v[0:3], a[128:131]\n"
    "v_cvt_pk_bf16_f32 v32, v32, v33\n"
    "v_mfma_f32_16x16x32_bf16 a[132:135], a[176:179], v[4:7], a[132:135]\n"
    "v_cvt_pk_bf16_f32 v33, v34, v35\n"
    "v_mfma_f32_16x16x32_bf16 a[136:139], a[176:179], v[8:11], a[136:139]\n"
    "v_exp_f32 v50, v50\n"
    "v_mfma_f32_16x16x32_bf16 a[140:143], a[176:179], v[12:15], a[140:143]\n"
    "s_waitcnt lgkmcnt(2)\n"
    "v_exp_f32 v51, v51\n"
    "v_mfma_f32_16x16x32_bf16 v[0:3], a[144:147], a[0:3], v[192:195]\n"
    "ds_read_b128 a[168:171], %[rd] offset:14336\n"
    "v_mfma_f32_16x16x32_bf16 v[4:7], a[144:147], a[4:7], 0\n"
    "v_exp_f32 v52, v52\n"
    "v_mfma_f32_16x16x32_bf16 v[8:11], a[144:147], a[8:11], 0\n"
    "v_exp_f32 v53, v53\n"
    "v_mfma_f32_16x16x32_bf16 v[12:15], a[144:147], a[12:15], 0\n"
    "v_cvt_pk_bf16_f32 v36, v36, v37\n"
    "v_mfma_f32_16x16x32_bf16 v[12:15], a[148:151], a[28:31], v[12:15]\n"
    "ds_read_b128 a[172:175], %[rd] offset:15360\n"
    "v_mfma_f32_16x16x32_bf16 v[8:11], a[148:151], a[24:27], v[8:11]\n"
    "v_cvt_pk_bf16_f32 v37, v38, v39\n"
    "v_mfma_f32_16x16x32_bf16 v[4:7], a[148:151], a[20:23], v[4:7]\n"
    "v_exp_f32 v54, v54\n"
    "v_mfma_f32_16x16x32_bf16 v[0:3], a[148:151], a[16:19], v[0:3]\n"
    "v_exp_f32 v55, v55\n"
    "v_mfma_f32_16x16x32_bf16 v[0:3], a[152:155], a[32:35], v[0:3]\n"
    "ds_read_b128 a[144:147], %[rd] offset:24576\n"
    "v_mfma_f32_16x16x32_bf16 v[4:7], a[152:155], a[36:39], v[4:7]\n"
    "v_cvt_pk_bf16_f32 v34, v48, v49\n"
    "v_cvt_pk_bf16_f32 v35, v50, v51\n"
    "v_mfma_f32_16x16x32_bf16 v[8:11], a[152:155], a[40:43], v[8:11]\n"
    "v_exp_f32 v56, v56\n"
    "v_mfma_f32_16x16x32_bf16 v[12:15], a[152:155], a[44:47], v[12:15]\n"
    "v_exp_f32 v57, v57\n"
    "v_mfma_f32_16x16x32_bf16 v[12:15], a[156:159], a[60:63], v[12:15]\n"
    "ds_read_b128 a[148:151], %[rd] offset:25600\n"
    "v_mfma_f32_16x16x32_bf16 v[8:11], a[156:159], a[56:59], v[8:11]\n"
    "v_cvt_pk_bf16_f32 v40, v40, v41\n"
    "v_mfma_f32_16x16x32_bf16 v[4:7], a[156:159], a[52:55], v[4:7]\n"
    "v_cvt_pk_bf16_f32 v41, v42, v43\n"
    "v_mfma_f32_16x16x32_bf16 v[0:3], a[156:159], a[48:51], v[0:3]\n"
    "s_waitcnt lgkmcnt(2)\n"
    "v_exp_f32 v58, v58\n"
    "v_mfma_f32_16x16x32_bf16 v[16:19], a[160:163], a[0:3], v[196:199]\n"
    "ds_read_b128 a[152:155], %[rd] offset:26624\n"
    "v_mfma_f32_16x16x32_bf16 v[20:23], a[160:163], a[4:7], v[192:195]\n"
    "v_exp_f32 v59, v59\n"
    "v_mfma_f32_16x16x32_bf16 v[24:27], a[160:163], a[8:11], 0\n"
    "v_cvt_pk_bf16_f32 v38, v52, v53\n"
    "v_mfma_f32_16x16x32_bf16 v[28:31], a[160:163], a[12:15], 0\n"
    "v_cvt_pk_bf16_f32 v39, v54, v55\n"
    "v_mfma_f32_16x16x32_bf16 v[28:31], a[164:167], a[28:31], v[28:31]\n"
    "ds_read_b128 a[156:159], %[rd] offset:27648\n"
    "v_mfma_f32_16x16x32_bf16 v[24:27], a[164:167], a[24:27], v[24:27]\n"
    "v_exp_f32 v60, v60\n"
    "v_mfma_f32_16x16x32_bf16 v[20:23], a[164:167], a[20:23], v[20:23]\n"
    "v_exp_f32 v61, v61\n"
    "v_mfma_f32_16x16x32_bf16 v[16:19], a[164:167], a[16:19], v[16:19]\n"
    "v_cvt_pk_bf16_f32 v44, v44, v45\n"
    "v_cvt_pk_bf16_f32 v45, v46, v47\n"
    "v_mfma_f32_16x16x32_bf16 v[16:19], a[168:171], a[32:35], v[16:19]\n"
    "ds_read_b128 a[160:163], %[rd] offset:28672\n"
    "v_mfma_f32_16x16x32_bf16 v[20:23], a[168:171], a[36:39], v[20:23]\n"
    "v_exp_f32 v62, v62\n"
    "v_mfma_f32_16x16x32_bf16 v[24:27], a[168:171], a[40:43], v[24:27]\n"
    "v_exp_f32 v63, v63\n"
    "v_mfma_f32_16x16x32_bf16 v[28:31], a[168:171], a[44:47], v[28:31]\n"
    "v_cvt_pk_bf16_f32 v42, v56, v57\n"
    "v_mfma_f32_16x16x32_bf16 v[28:31], a[172:175], a[60:63], v[28:31]\n"
    "ds_read_b128 a[164:167], %[rd] offset:29696\n"
    "v_cvt_pk_bf16_f32 v43, v58, v59\n"
    "v_mfma_f32_16x16x32_bf16 v[24:27], a[172:175], a[56:59], v[24:27]\n"
    "v_cvt_pk_bf16_f32 v46, v60, v61\n"
    "v_mfma_f32_16x16x32_bf16 v[20:23], a[172:175], a[52:55], v[20:23]\n"
    "v_cvt_pk_bf16_f32 v47, v62, v63\n"
    "v_mfma_f32_16x16x32_bf16 v[16:19], a[172:175], a[48:51], v[16:19]\n"
    "s_waitcnt lgkmcnt(2)\n"
    "v_mfma_f32_16x16x32_bf16 v[64:67], a[144:147], v[32:35], v[64:67]\n"
    "ds_read_b128 a[168:171], %[rd] offset:30720\n"
    "v_mfma_f32_16x16x32_bf16 v[68:71], a[144:147], v[36:39], v[68:71]\n"
    "v_mfma_f32_16x16x32_bf16 v[72:75], a[144:147], v[40:43], v[72:75]\n"
    "v_exp_f32 v0, v0\n"
    "v_mfma_f32_16x16x32_bf16 v[76:79], a[144:147], v[44:47], v[76:79]\n"
    "v_exp_f32 v1, v1\n"
    "v_mfma_f32_16x16x32_bf16 v[92:95], a[148:151], v[44:47], v[92:95]\n"
    "ds_read_b128 a[172:175], %[rd] offset:31744\n"
    "v_mfma_f32_16x16x32_bf16 v[88:91], a[148:151], v[40:43], v[88:91]\n"
    "v_xor_b32 %[rd], 65536, %[rd]\n"
    "v_mfma_f32_16x16x32_bf16 v[84:87], a[148:151], v[36:39], v[84:87]\n"
    "s_add_u32 %[so0], %[so0], 16384\n"
    "v_exp_f32 v2, v2\n"
    "v_mfma_f32_16x16x32_bf16 v[80:83], a[148:151], v[32:35], v[80:83]\n"
    "s_xor_b32 s40, s40, 65536\n"
    "v_exp_f32 v3, v3\n"
    "v_mfma_f32_16x16x32_bf16 v[96:99], a[152:155], v[32:35], v[96:99]\n"
    "s_add_u32 %[so1], %[so1], 16384\n"
    "v_exp_f32 v4, v4\n"
    "v_mfma_f32_16x16x32_bf16 v[100:103], a[152:155], v[36:39], v[100:103]\n"
    "s_add_u32 %[spf], %[spf], 4096\n"
    "v_exp_f32 v5, v5\n"
    "v_mfma_f32_16x16x32_bf16 v[104:107], a[152:155], v[40:43], v[104:107]\n"
    "s_sub_u32 s50, s50, 1\n"
    "v_exp_f32 v6, v6\n"
    "v_mfma_f32_16x16x32_bf16 v[108:111], a[152:155], v[44:47], v[108:111]\n"
    "s_cmp_lg_u32 s50, 0\n"
    "v_exp_f32 v7, v7\n"
    "v_mfma_f32_16x16x32_bf16 v[124:127], a[156:159], v[44:47], v[124:127]\n"
    "v_exp_f32 v8, v8\n"
    "v_mfma_f32_16x16x32_bf16 v[120:123], a[156:159], v[40:43], v[120:123]\n"
    "v_exp_f32 v9, v9\n"
    "v_mfma_f32_16x16x32_bf16 v[116:119], a[156:159], v[36:39], v[116:119]\n"
    "v_exp_f32 v10, v10\n"
    "v_mfma_f32_16x16x32_bf16 v[112:115], a[156:159], v[32:35], v[112:115]\n"
    "v_exp_f32 v11, v11\n"
    "s_waitcnt vmcnt(1) lgkmcnt(0)\n"
    "s_barrier\n"
    "s_cbranch_scc1 V2_%=\n"
    "s_setpc_b64 s[52:53]\n"
    ".p2align 6\n"
    "V3_%=:\n"
    "v_mfma_f32_16x16x32_bf16 v[128:131], a[160:163], v[32:35], v[128:131]\n"
    "ds_read_b128 a[144:147], %[rd] offset:0\n"
    "v_exp_f32 v12, v12\n"
    "v_mfma_f32_16x16x32_bf16 v[132:135], a[160:163], v[36:39], v[132:135]\n"
    "ds_read_b128 a[148:151], %[rd] offset:1024\n"
    "v_exp_f32 v13, v13\n"
    "v_mfma_f32_16x16x32_bf16 v[136:139], a[160:163], v[40:43], v[136:139]\n"
    "ds_read_b128 a[152:155], %[rd] offset:2048\n"
    "v_mfma_f32_16x16x32_bf16 v[140:143], a[160:163], v[44:47], v[140:143]\n"
    "s_add_u32 m0, s40, 0\n"
    "v_exp_f32 v14, v14\n"
    "v_mfma_f32_16x16x32_bf16 v[156:159], a[164:167], v[44:47], v[156:159]\n"
    "ds_read_b128 a[156:159], %[rd] offset:3072\n"
    "buffer_load_dwordx4 %[vdma], %[srdk], %[so0] offen offset:0 lds\n"
    "v_mfma_f32_16x16x32_bf16 v[152:155], a[164:167], v[40:43], v[152:155]\n"
    "v_exp_f32 v15, v15\n"
    "v_mfma_f32_16x16x32_bf16 v[148:151], a[164:167], v[36:39], v[148:151]\n"
    "v_exp_f32 v16, v16\n"
    "v_mfma_f32_16x16x32_bf16 v[144:147], a[164:167], v[32:35], v[144:147]\n"
    "v_exp_f32 v17, v17\n"
    "v_cvt_pk_bf16_f32 v0, v0, v1\n"
    "v_mfma_f32_16x16x32_bf16 v[160:163], a[168:171], v[32:35], v[160:163]\n"
    "ds_read_b128 a[160:163], %[rd] offset:4096\n"
    "v_cvt_pk_bf16_f32 v1, v2, v3\n"
    "v_mfma_f32_16x16x32_bf16 v[164:167], a[168:171], v[36:39], v[164:167]\n"
    "v_exp_f32 v18, v18\n"
    "v_mfma_f32_16x16x32_bf16 v[168:171], a[168:171], v[40:43], v[168:171]\n"
    "v_exp_f32 v19, v19\n"
    "v_mfma_f32_16x16x32_bf16 v[172:175], a[168:171], v[44:47], v[172:175]\n"
    "v_exp_f32 v20, v20\n"
    "v_mfma_f32_16x16x32_bf16 v[188:191], a[172:175], v[44:47], v[188:191]\n"
    "ds_read_b128 a[164:167], %[rd] offset:5120\n"
    "buffer_load_dwordx4 %[vdma], %[srdk], %[so0] offen offset:1024 lds\n"
    "v_mfma_f32_16x16x32_bf16 v[184:187], a[172:175], v[40:43], v[184:187]\n"
    "v_exp_f32 v21, v21\n"
    "v_mfma_f32_16x16x32_bf16 v[180:183], a[172:175], v[36:39], v[180:183]\n"
    "v_cvt_pk_bf16_f32 v4, v4, v5\n"
    "v_cvt_pk_bf16_f32 v5, v6, v7\n"
    "v_mfma_f32_16x16x32_bf16 v[176:179], a[172:175], v[32:35], v[176:179]\n"
    "v_exp_f32 v22, v22\n"
    "v_mfma_f32_16x16x32_bf16 a[128:131], a[176:179], v[32:35], a[128:131]\n"
    "v_exp_f32 v23, v23\n"
    "v_mfma_f32_16x16x32_bf16 a[132:135], a[176:179], v[36:39], a[132:135]\n"
    "v_cvt_pk_bf16_f32 v2, v16, v17\n"
    "v_cvt_pk_bf16_f32 v3, v18, v19\n"
    "v_mfma_f32_16x16x32_bf16 a[136:139], a[176:179], v[40:43], a[136:139]\n"
    "v_exp_f32 v24, v24\n"
    "v_mfma_f32_16x16x32_bf16 a[140:143], a[176:179], v[44:47], a[140:143]\n"
    "s_waitcnt lgkmcnt(2)\n"
    "v_exp_f32 v25, v25\n"
    "v_mfma_f32_16x16x32_bf16 v[40:43], a[144:147], a[8:11], v[192:195]\n"
    "ds_read_b128 a[168:171], %[rd] offset:6144\n"
    "buffer_load_dwordx4 %[vdma], %[srdk], %[so0] offen offset:2048 lds\n"
    "v_cvt_pk_bf16_f32 v8, v8, v9\n"
    "v_mfma_f32_16x16x32_bf16 v[44:47], a[144:147], a[12:15], 0\n"
    "v_cvt_pk_bf16_f32 v9, v10, v11\n"
    "v_exp_f32 v26, v26\n"
    "v_mfma_f32_16x16x32_bf16 v[44:47], a[148:151], a[28:31], v[44:47]\n"
    "ds_read_b128 a[172:175], %[rd] offset:7168\n"
    "v_mfma_f32_16x16x32_bf16 v[40:43], a[148:151], a[24:27], v[40:43]\n"
    "v_exp_f32 v27, v27\n"
    "v_mfma_f32_16x16x32_bf16 v[40:43], a[152:155], a[40:43], v[40:43]\n"
    "ds_read_b128 a[144:147], %[rd] offset:16384\n"
    "v_cvt_pk_bf16_f32 v6, v20, v21\n"
    "v_mfma_f32_16x16x32_bf16 v[44:47], a[152:155], a[44:47], v[44:47]\n"
    "v_cvt_pk_bf16_f32 v7, v22, v23\n"
    "v_exp_f32 v28, v28\n"
    "v_mfma_f32_16x16x32_bf16 v[44:47], a[156:159], a[60:63], v[44:47]\n"
    "ds_read_b128 a[148:151], %[rd] offset:17408\n"
    "v_mfma_f32_16x16x32_bf16 v[40:43], a[156:159], a[56:59], v[40:43]\n"
    "s_waitcnt lgkmcnt(2)\n"
    "v_exp_f32 v29, v29\n"
    "v_mfma_f32_16x16x32_bf16 v[56:59], a[160:163], a[8:11], v[196:199]\n"
    "ds_read_b128 a[152:155], %[rd] offset:18432\n"
    "buffer_load_dwordx4 %[vdma], %[srdk], %[so0] offen offset:3072 lds\n"
    "v_cvt_pk_bf16_f32 v12, v12, v13\n"
    "v_mfma_f32_16x16x32_bf16 v[60:63], a[160:163], a[12:15], v[192:195]\n"
    "v_cvt_pk_bf16_f32 v13, v14, v15\n"
    "v_exp_f32 v30, v30\n"
    "v_mfma_f32_16x16x32_bf16 v[60:63], a[164:167], a[28:31], v[60:63]\n"
    "ds_read_b128 a[156:159], %[rd] offset:19456\n"
    "v_mfma_f32_16x16x32_bf16 v[56:59], a[164:167], a[24:27], v[56:59]\n"
    "v_exp_f32 v31, v31\n"
    "v_mfma_f32_16x16x32_bf16 v[56:59], a[168:171], a[40:43], v[56:59]\n"
    "ds_read_b128 a[160:163], %[rd] offset:20480\n"
    "v_cvt_pk_bf16_f32 v10, v24, v25\n"
    "v_mfma_f32_16x16x32_bf16 v[60:63], a[168:171], a[44:47], v[60:63]\n"
    "v_cvt_pk_bf16_f32 v11, v26, v27\n"
    "v_cvt_pk_bf16_f32 v14, v28, v29\n"
    "v_mfma_f32_16x16x32_bf16 v[60:63], a[172:175], a[60:63], v[60:63]\n"
    "s_add_u32 m0, s40, 16384\n"
    "ds_read_b128 a[164:167], %[rd] offset:21504\n"
    "v_cvt_pk_bf16_f32 v15, v30, v31\n"
    "v_mfma_f32_16x16x32_bf16 v[56:59], a[172:175], a[56:59], v[56:59]\n"
    "s_waitcnt lgkmcnt(2)\n"
    "v_mfma_f32_16x16x32_bf16 v[64:67], a[144:147], v[0:3], v[64:67]\n"
    "ds_read_b128 a[168:171], %[rd] offset:22528\n"
    "buffer_load_dwordx4 %[vdma], %[srdv], %[so0] offen offset:0 lds\n"
    "v_mfma_f32_16x16x32_bf16 v[68:71], a[144:147], v[4:7], v[68:71]\n"
    "v_exp_f32 v40, v40\n"
    "v_mfma_f32_16x16x32_bf16 v[72:75], a[144:147], v[8:11], v[72:75]\n"
    "v_exp_f32 v41, v41\n"
    "v_mfma_f32_16x16x32_bf16 v[76:79], a[144:147], v[12:15], v[76:79]\n"
    "v_exp_f32 v42, v42\n"
    "v_mfma_f32_16x16x32_bf16 v[92:95], a[148:151], v[12:15], v[92:95]\n"
    "ds_read_b128 a[172:175], %[rd] offset:23552\n"
    "v_mfma_f32_16x16x32_bf16 v[88:91], a[148:151], v[8:11], v[88:91]\n"
    "v_exp_f32 v43, v43\n"
    "v_mfma_f32_16x16x32_bf16 v[84:87], a[148:151], v[4:7], v[84:87]\n"
    "v_mfma_f32_16x16x32_bf16 v[80:83], a[148:151], v[0:3], v[80:83]\n"
    "v_exp_f32 v44, v44\n"
    "v_mfma_f32_16x16x32_bf16 v[96:99], a[152:155], v[0:3], v[96:99]\n"
    "ds_read_b128 a[144:147], %[rd] offset:24576\n"
    "buffer_load_dwordx4 %[vdma], %[srdv], %[so0] offen offset:1024 lds\n"
    "v_mfma_f32_16x16x32_bf16 v[100:103], a[152:155], v[4:7], v[100:103]\n"
    "v_exp_f32 v45, v45\n"
    "v_mfma_f32_16x16x32_bf16 v[104:107], a[152:155], v[8:11], v[104:107]\n"
    "v_mfma_f32_16x16x32_bf16 v[108:111], a[152:155], v[12:15], v[108:111]\n"
    "v_exp_f32 v46, v46\n"
    "v_mfma_f32_16x16x32_bf16 v[124:127], a[156:159], v[12:15], v[124:127]\n"
    "ds_read_b128 a[148:151], %[rd] offset:25600\n"
    "v_mfma_f32_16x16x32_bf16 v[120:123], a[156:159], v[8:11], v[120:123]\n"
    "v_exp_f32 v47, v47\n"
    "v_mfma_f32_16x16x32_bf16 v[116:119], a[156:159], v[4:7], v[116:119]\n"
    "v_exp_f32 v56, v56\n"
    "v_mfma_f32_16x16x32_bf16 v[112:115], a[156:159], v[0:3], v[112:115]\n"
    "s_waitcnt lgkmcnt(2)\n"
    "v_mfma_f32_16x16x32_bf16 v[128:131], a[160:163], v[0:3], v[128:131]\n"
    "ds_read_b128 a[152:155], %[rd] offset:26624\n"
    "buffer_load_dwordx4 %[vdma], %[srdv], %[so0] offen offset:2048 lds\n"
    "v_mfma_f32_16x16x32_bf16 v[132:135], a[160:163], v[4:7], v[132:135]\n"
    "v_exp_f32 v57, v57\n"
    "v_mfma_f32_16x16x32_bf16 v[136:139], a[160:163], v[8:11], v[136:139]\n"
    "v_cvt_pk_bf16_f32 v40, v40, v41\n"
    "v_mfma_f32_16x16x32_bf16 v[140:143], a[160:163], v[12:15], v[140:143]\n"
    "v_cvt_pk_bf16_f32 v41, v42, v43\n"
    "v_mfma_f32_16x16x32_bf16 v[156:159], a[164:167], v[12:15], v[156:159]\n"
    "ds_read_b128 a[156:159], %[rd] offset:27648\n"
    "v_mfma_f32_16x16x32_bf16 v[152:155], a[164:167], v[8:11], v[152:155]\n"
    "v_exp_f32 v58, v58\n"
    "v_mfma_f32_16x16x32_bf16 v[148:151], a[164:167], v[4:7], v[148:151]\n"
    "v_exp_f32 v59, v59\n"
    "v_mfma_f32_16x16x32_bf16 v[144:147], a[164:167], v[0:3], v[144:147]\n"
    "v_exp_f32 v60, v60\n"
    "v_mfma_f32_16x16x32_bf16 v[160:163], a[168:171], v[0:3], v[160:163]\n"
    "ds_read_b128 a[160:163], %[rd] offset:28672\n"
    "buffer_load_dwordx4 %[vdma], %[srdv], %[so0] offen offset:3072 lds\n"
    "v_mfma_f32_16x16x32_bf16 v[164:167], a[168:171], v[4:7], v[164:167]\n"
    "v_exp_f32 v61, v61\n"
    "v_mfma_f32_16x16x32_bf16 v[168:171], a[168:171], v[8:11], v[168:171]\n"
    "v_mfma_f32_16x16x32_bf16 v[172:175], a[168:171], v[12:15], v[172:175]\n"
    "v_cvt_pk_bf16_f32 v44, v44, v45\n"
    "v_cvt_pk_bf16_f32 v45, v46, v47\n"
    "v_mfma_f32_16x16x32_bf16 v[188:191], a[172:175], v[12:15], v[188:191]\n"
    "ds_read_b128 a[164:167], %[rd] offset:29696\n"
    "v_mfma_f32_16x16x32_bf16 v[184:187], a[172:175], v[8:11], v[184:187]\n"
    "v_exp_f32 v62, v62\n"
    "v_mfma_f32_16x16x32_bf16 v[180:183], a[172:175], v[4:7], v[180:183]\n"
    "v_exp_f32 v63, v63\n"
    "v_mfma_f32_16x16x32_bf16 v[176:179], a[172:175], v[0:3], v[176:179]\n"
    "v_mfma_f32_16x16x32_bf16 a[128:131], a[176:179], v[0:3], a[128:131]\n"
    "v_cvt_pk_bf16_f32 v42, v56, v57\n"
    "v_cvt_pk_bf16_f32 v43, v58, v59\n"
    "v_mfma_f32_16x16x32_bf16 a[132:135], a[176:179], v[4:7], a[132:135]\n"
    "v_cvt_pk_bf16_f32 v46, v60, v61\n"
    "v_mfma_f32_16x16x32_bf16 a[136:139], a[176:179], v[8:11], a[136:139]\n"
    "v_cvt_pk_bf16_f32 v47, v62, v63\n"
    "v_mfma_f32_16x16x32_bf16 a[140:143], a[176:179], v[12:15], a[140:143]\n"
    "s_waitcnt lgkmcnt(2)\n"
    "v_mfma_f32_16x16x32_bf16 v[72:75], a[144:147], v[40:43], v[72:75]\n"
    "ds_read_b128 a[168:171], %[rd] offset:30720\n"
    "v_mfma_f32_16x16x32_bf16 v[76:79], a[144:147], v[44:47], v[76:79]\n"
    "v_mfma_f32_16x16x32_bf16 v[92:95], a[148:151], v[44:47], v[92:95]\n"
    "ds_read_b128 a[172:175], %[rd] offset:31744\n"
    "v_mfma_f32_16x16x32_bf16 v[88:91], a[148:151], v[40:43], v[88:91]\n"
    "v_xor_b32 %[rd], 65536, %[rd]\n"
    "v_mfma_f32_16x16x32_bf16 v[104:107], a[152:155], v[40:43], v[104:107]\n"
    "s_add_u32 %[so0], %[so0], 16384\n"
    "v_mfma_f32_16x16x32_bf16 v[108:111], a[152:155], v[44:47], v[108:111]\n"
    "s_xor_b32 s40, s40, 65536\n"
    "v_mfma_f32_16x16x32_bf16 v[124:127], a[156:159], v[44:47], v[124:127]\n"
    "v_mfma_f32_16x16x32_bf16 v[120:123], a[156:159], v[40:43], v[120:123]\n"
    "s_waitcnt vmcnt(0) lgkmcnt(0)\n"
    "s_barrier\n"
    "s_setpc_b64 s[52:53]\n"
    ".p2align 6\n"
    "V4_%=:\n"
    "v_mfma_f32_16x16x32_bf16 v[128:131], a[160:163], v[32:35], v[128:131]\n"
    "ds_read_b128 a[144:147], %[rd] offset:0\n"
    "v_exp_f32 v12, v12\n"
    "v_mfma_f32_16x16x32_bf16 v[132:135], a[160:163], v[36:39], v[132:135]\n"
    "ds_read_b128 a[148:151], %[rd] offset:1024\n"
    "v_exp_f32 v13, v13\n"
    "v_mfma_f32_16x16x32_bf16 v[136:139], a[160:163], v[40:43], v[136:139]\n"
    "ds_read_b128 a[152:155], %[rd] offset:2048\n"
    "v_exp_f32 v14, v14\n"
    "v_mfma_f32_16x16x32_bf16 v[140:143], a[160:163], v[44:47], v[140:143]\n"
    "s_add_u32 m0, s40, 0\n"
    "v_exp_f32 v15, v15\n"
    "v_mfma_f32_16x16x32_bf16 v[156:159], a[164:167], v[44:47], v[156:159]\n"
    "ds_read_b128 a[156:159], %[rd] offset:3072\n"
    "buffer_load_dwordx4 %[vdma], %[srdk], %[so0] offen offset:0 lds\n"
    "v_mfma_f32_16x16x32_bf16 v[152:155], a[164:167], v[40:43], v[152:155]\n"
    "v_exp_f32 v16, v16\n"
    "v_mfma_f32_16x16x32_bf16 v[148:151], a[164:167], v[36:39], v[148:151]\n"
    "v_exp_f32 v17, v17\n"
    "v_mfma_f32_16x16x32_bf16 v[144:147], a[164:167], v[32:35], v[144:147]\n"
    "v_cvt_pk_bf16_f32 v0, v0, v1\n"
    "v_mfma_f32_16x16x32_bf16 v[160:163], a[168:171], v[32:35], v[160:163]\n"
    "ds_read_b128 a[160:163], %[rd] offset:4096\n"
    "buffer_load_dwordx4 %[vdma], %[srdk], %[so0] offen offset:1024 lds\n"
    "v_cvt_pk_bf16_f32 v1, v2, v3\n"
    "v_mfma_f32_16x16x32_bf16 v[164:167], a[168:171], v[36:39], v[164:167]\n"
    "v_exp_f32 v18, v18\n"
    "v_mfma_f32_16x16x32_bf16 v[168:171], a[168:171], v[40:43], v[168:171]\n"
    "v_exp_f32 v19, v19\n"
    "v_mfma_f32_16x16x32_bf16 v[172:175], a[168:171], v[44:47], v[172:175]\n"
    "v_exp_f32 v20, v20\n"
    "v_mfma_f32_16x16x32_bf16 v[188:191], a[172:175], v[44:47], v[188:191]\n"
    "ds_read_b128 a[164:167], %[rd] offset:5120\n"
    "buffer_load_dwordx4 %[vdma], %[srdk], %[so0] offen offset:2048 lds\n"
    "v_mfma_f32_16x16x32_bf16 v[184:187], a[172:175], v[40:43], v[184:187]\n"
    "v_exp_f32 v21, v21\n"
    "v_cvt_pk_bf16_f32 v4, v4, v5\n"
    "v_mfma_f32_16x16x32_bf16 v[180:183], a[172:175], v[36:39], v[180:183]\n"
    "v_cvt_pk_bf16_f32 v5, v6, v7\n"
    "v_exp_f32 v22, v22\n"
    "v_mfma_f32_16x16x32_bf16 v[176:179], a[172:175], v[32:35], v[176:179]\n"
    "v_exp_f32 v23, v23\n"
    "v_mfma_f32_16x16x32_bf16 a[128:131], a[176:179], v[32:35], a[128:131]\n"
    "buffer_load_dwordx4 %[vdma], %[srdk], %[so0] offen offset:3072 lds\n"
    "v_mfma_f32_16x16x32_bf16 a[132:135], a[176:179], v[36:39], a[132:135]\n"
    "v_cvt_pk_bf16_f32 v2, v16, v17\n"
    "v_cvt_pk_bf16_f32 v3, v18, v19\n"
    "v_mfma_f32_16x16x32_bf16 a[136:139], a[176:179], v[40:43], a[136:139]\n"
    "s_add_u32 m0, s40, 16384\n"
    "v_exp_f32 v24, v24\n"
    "v_mfma_f32_16x16x32_bf16 a[140:143], a[176:179], v[44:47], a[140:143]\n"
    "s_waitcnt lgkmcnt(2)\n"
    "v_exp_f32 v25, v25\n"
    "v_mfma_f32_16x16x32_bf16 v[40:43], a[144:147], a[8:11], v[192:195]\n"
    "ds_read_b128 a[168:171], %[rd] offset:6144\n"
    "buffer_load_dwordx4 %[vdma], %[srdv], %[so0] offen offset:0 lds\n"
    "v_cvt_pk_bf16_f32 v8, v8, v9\n"
    "v_mfma_f32_16x16x32_bf16 v[44:47], a[144:147], a[12:15], 0\n"
    "v_cvt_pk_bf16_f32 v9, v10, v11\n"
    "v_exp_f32 v26, v26\n"
    "v_mfma_f32_16x16x32_bf16 v[44:47], a[148:151], a[28:31], v[44:47]\n"
    "ds_read_b128 a[172:175], %[rd] offset:7168\n"
    "v_mfma_f32_16x16x32_bf16 v[40:43], a[148:151], a[24:27], v[40:43]\n"
    "v_exp_f32 v27, v27\n"
    "v_mfma_f32_16x16x32_bf16 v[40:43], a[152:155], a[40:43], v[40:43]\n"
    "ds_read_b128 a[144:147], %[rd] offset:16384\n"
    "buffer_load_dwordx4 %[vdma], %[srdv], %[so0] offen offset:1024 lds\n"
    "v_cvt_pk_bf16_f32 v6, v20, v21\n"
    "v_mfma_f32_16x16x32_bf16 v[44:47], a[152:155], a[44:47], v[44:47]\n"
    "v_cvt_pk_bf16_f32 v7, v22, v23\n"
    "v_exp_f32 v28, v28\n"
    "v_mfma_f32_16x16x32_bf16 v[44:47], a[156:159], a[60:63], v[44:47]\n"
    "ds_read_b128 a[148:151], %[rd] offset:17408\n"
    "v_mfma_f32_16x16x32_bf16 v[40:43], a[156:159], a[56:59], v[40:43]\n"
    "s_waitcnt lgkmcnt(2)\n"
    "v_exp_f32 v29, v29\n"
    "v_mfma_f32_16x16x32_bf16 v[56:59], a[160:163], a[8:11], v[196:199]\n"
    "ds_read_b128 a[152:155], %[rd] offset:18432\n"
    "buffer_load_dwordx4 %[vdma], %[srdv], %[so0] offen offset:2048 lds\n"
    "v_cvt_pk_bf16_f32 v12, v12, v13\n"
    "v_mfma_f32_16x16x32_bf16 v[60:63], a[160:163], a[12:15], v[192:195]\n"
    "v_cvt_pk_bf16_f32 v13, v14, v15\n"
    "v_exp_f32 v30, v30\n"
    "v_mfma_f32_16x16x32_bf16 v[60:63], a[164:167], a[28:31], v[60:63]\n"
    "ds_read_b128 a[156:159], %[rd] offset:19456\n"
    "v_mfma_f32_16x16x32_bf16 v[56:59], a[164:167], a[24:27], v[56:59]\n"
    "v_exp_f32 v31, v31\n"
    "v_mfma_f32_16x16x32_bf16 v[56:59], a[168:171], a[40:43], v[56:59]\n"
    "ds_read_b128 a[160:163], %[rd] offset:20480\n"
    "buffer_load_dwordx4 %[vdma], %[srdv], %[so0] offen offset:3072 lds\n"
    "v_cvt_pk_bf16_f32 v10, v24, v25\n"
    "v_mfma_f32_16x16x32_bf16 v[60:63], a[168:171], a[44:47], v[60:63]\n"
    "v_cvt_pk_bf16_f32 v11, v26, v27\n"
    "v_cvt_pk_bf16_f32 v14, v28, v29\n"
    "v_mfma_f32_16x16x32_bf16 v[60:63], a[172:175], a[60:63], v[60:63]\n"
    "s_add_u32 m0, s40, 32768\n"
    "ds_read_b128 a[164:167], %[rd] offset:21504\n"
    "v_cvt_pk_bf16_f32 v15, v30, v31\n"
    "v_mfma_f32_16x16x32_bf16 v[56:59], a[172:175], a[56:59], v[56:59]\n"
    "s_waitcnt lgkmcnt(2)\n"
    "v_mfma_f32_16x16x32_bf16 v[64:67], a[144:147], v[0:3], v[64:67]\n"
    "ds_read_b128 a[168:171], %[rd] offset:22528\n"
    "buffer_load_dwordx4 %[vdma], %[srdk], %[so1] offen offset:0 lds\n"
    "v_mfma_f32_16x16x32_bf16 v[68:71], a[144:147], v[4:7], v[68:71]\n"
    "v_exp_f32 v40, v40\n"
    "v_mfma_f32_16x16x32_bf16 v[72:75], a[144:147], v[8:11], v[72:75]\n"
    "v_exp_f32 v41, v41\n"
    "v_mfma_f32_16x16x32_bf16 v[76:79], a[144:147], v[12:15], v[76:79]\n"
    "v_exp_f32 v42, v42\n"
    "v_mfma_f32_16x16x32_bf16 v[92:95], a[148:151], v[12:15], v[92:95]\n"
    "ds_read_b128 a[172:175], %[rd] offset:23552\n"
    "buffer_load_dwordx4 %[vdma], %[srdk], %[so1] offen offset:1024 lds\n"
    "v_mfma_f32_16x16x32_bf16 v[88:91], a[148:151], v[8:11], v[88:91]\n"
    "v_mfma_f32_16x16x32_bf16 v[84:87], a[148:151], v[4:7], v[84:87]\n"
    "v_exp_f32 v43, v43\n"
    "v_mfma_f32_16x16x32_bf16 v[80:83], a[148:151], v[0:3], v[80:83]\n"
    "v_exp_f32 v44, v44\n"
    "v_mfma_f32_16x16x32_bf16 v[96:99], a[152:155], v[0:3], v[96:99]\n"
    "ds_read_b128 a[144:147], %[rd] offset:24576\n"
    "buffer_load_dwordx4 %[vdma], %[srdk], %[so1] offen offset:2048 lds\n"
    "v_mfma_f32_16x16x32_bf16 v[100:103], a[152:155], v[4:7], v[100:103]\n"
    "v_exp_f32 v45, v45\n"
    "v_mfma_f32_16x16x32_bf16 v[104:107], a[152:155], v[8:11], v[104:107]\n"
    "v_mfma_f32_16x16x32_bf16 v[108:111], a[152:155], v[12:15], v[108:111]\n"
    "v_exp_f32 v46, v46\n"
    "v_mfma_f32_16x16x32_bf16 v[124:127], a[156:159], v[12:15], v[124:127]\n"
    "ds_read_b128 a[148:151], %[rd] offset:25600\n"
    "buffer_load_dwordx4 %[vdma], %[srdk], %[so1] offen offset:3072 lds\n"
    "v_mfma_f32_16x16x32_bf16 v[120:123], a[156:159], v[8:11], v[120:123]\n"
    "v_exp_f32 v47, v47\n"
    "v_mfma_f32_16x16x32_bf16 v[116:119], a[156:159], v[4:7], v[116:119]\n"
    "s_add_u32 m0, s40, 49152\n"
    "v_exp_f32 v56, v56\n"
    "v_mfma_f32_16x16x32_bf16 v[112:115], a[156:159], v[0:3], v[112:115]\n"
    "s_waitcnt lgkmcnt(2)\n"
    "v_mfma_f32_16x16x32_bf16 v[128:131], a[160:163], v[0:3], v[128:131]\n"
    "ds_read_b128 a[152:155], %[rd] offset:26624\n"
    "buffer_load_dwordx4 %[vdma], %[srdv], %[so1] offen offset:0 lds\n"
    "v_mfma_f32_16x16x32_bf16 v[132:135], a[160:163], v[4:7], v[132:135]\n"
    "v_exp_f32 v57, v57\n"
    "v_mfma_f32_16x16x32_bf16 v[136:139], a[160:163], v[8:11], v[136:139]\n"
    "v_cvt_pk_bf16_f32 v40, v40, v41\n"
    "v_mfma_f32_16x16x32_bf16 v[140:143], a[160:163], v[12:15], v[140:143]\n"
    "v_cvt_pk_bf16_f32 v41, v42, v43\n"
    "v_mfma_f32_16x16x32_bf16 v[156:159], a[164:167], v[12:15], v[156:159]\n"
    "ds_read_b128 a[156:159], %[rd] offset:27648\n"
    "buffer_load_dwordx4 %[vdma], %[srdv], %[so1] offen offset:1024 lds\n"
    "v_mfma_f32_16x16x32_bf16 v[152:155], a[164:167], v[8:11], v[152:155]\n"
    "v_exp_f32 v58, v58\n"
    "v_mfma_f32_16x16x32_bf16 v[148:151], a[164:167], v[4:7], v[148:151]\n"
    "v_exp_f32 v59, v59\n"
    "v_mfma_f32_16x16x32_bf16 v[144:147], a[164:167], v[0:3], v[144:147]\n"
    "v_exp_f32 v60, v60\n"
    "v_mfma_f32_16x16x32_bf16 v[160:163], a[168:171], v[0:3], v[160:163]\n"
    "ds_read_b128 a[160:163], %[rd] offset:28672\n"
    "buffer_load_dwordx4 %[vdma], %[srdv], %[so1] offen offset:2048 lds\n"
    "v_mfma_f32_16x16x32_bf16 v[164:167], a[168:171], v[4:7], v[164:167]\n"
    "v_exp_f32 v61, v61\n"
    "v_mfma_f32_16x16x32_bf16 v[168:171], a[168:171], v[8:11], v[168:171]\n"
    "v_mfma_f32_16x16x32_bf16 v[172:175], a[168:171], v[12:15], v[172:175]\n"
    "v_cvt_pk_bf16_f32 v44, v44, v45\n"
    "v_cvt_pk_bf16_f32 v45, v46, v47\n"
    "v_mfma_f32_16x16x32_bf16 v[188:191], a[172:175], v[12:15], v[188:191]\n"
    "ds_read_b128 a[164:167], %[rd] offset:29696\n"
    "buffer_load_dwordx4 %[vdma], %[srdv], %[so1] offen offset:3072 lds\n"
    "v_mfma_f32_16x16x32_bf16 v[184:187], a[172:175], v[8:11], v[184:187]\n"
    "v_exp_f32 v62, v62\n"
    "v_mfma_f32_16x16x32_bf16 v[180:183], a[172:175], v[4:7], v[180:183]\n"
    "v_exp_f32 v63, v63\n"
    "v_mfma_f32_16x16x32_bf16 v[176:179], a[172:175], v[0:3], v[176:179]\n"
    "v_mfma_f32_16x16x32_bf16 a[128:131], a[176:179], v[0:3], a[128:131]\n"
    "v_cvt_pk_bf16_f32 v42, v56, v57\n"
    "v_cvt_pk_bf16_f32 v43, v58, v59\n"
    "v_mfma_f32_16x16x32_bf16 a[132:135], a[176:179], v[4:7], a[132:135]\n"
    "v_cvt_pk_bf16_f32 v46, v60, v61\n"
    "v_mfma_f32_16x16x32_bf16 a[136:139], a[176:179], v[8:11], a[136:139]\n"
    "v_cvt_pk_bf16_f32 v47, v62, v63\n"
    "v_mfma_f32_16x16x32_bf16 a[140:143], a[176:179], v[12:15], a[140:143]\n"
    "s_waitcnt lgkmcnt(2)\n"
    "v_mfma_f32_16x16x32_bf16 v[72:75], a[144:147], v[40:43], v[72:75]\n"
    "ds_read_b128 a[168:171], %[rd] offset:30720\n"
    "v_mfma_f32_16x16x32_bf16 v[76:79], a[144:147], v[44:47], v[76:79]\n"
    "v_mfma_f32_16x16x32_bf16 v[92:95], a[148:151], v[44:47], v[92:95]\n"
    "ds_read_b128 a[172:175], %[rd] offset:31744\n"
    "v_mfma_f32_16x16x32_bf16 v[88:91], a[148:151], v[40:43], v[88:91]\n"
    "v_xor_b32 %[rd], 65536, %[rd]\n"
    "v_mfma_f32_16x16x32_bf16 v[104:107], a[152:155], v[40:43], v[104:107]\n"
    "s_add_u32 %[so0], %[so0], 16384\n"
    "v_mfma_f32_16x16x32_bf16 v[108:111], a[152:155], v[44:47], v[108:111]\n"
    "s_xor_b32 s40, s40, 65536\n"
    "v_mfma_f32_16x16x32_bf16 v[124:127], a[156:159], v[44:47], v[124:127]\n"
    "s_add_u32 %[so1], %[so1], 16384\n"
    "v_mfma_f32_16x16x32_bf16 v[120:123], a[156:159], v[40:43], v[120:123]\n"
    "s_waitcnt lgkmcnt(0)\n"
    "s_setpc_b64 s[52:53]\n"
    ".p2align 6\n"
    "V5_%=:\n"
    "v_mfma_f32_16x16x32_bf16 v[128:131], a[160:163], v[32:35], v[128:131]\n"
    "ds_read_b128 a[144:147], %[rd] offset:0\n"
    "v_exp_f32 v12, v12\n"
    "v_mfma_f32_16x16x32_bf16 v[132:135], a[160:163], v[36:39], v[132:135]\n"
    "ds_read_b128 a[148:151], %[rd] offset:1024\n"
    "v_exp_f32 v13, v13\n"
    "v_mfma_f32_16x16x32_bf16 v[136:139], a[160:163], v[40:43], v[136:139]\n"
    "ds_read_b128 a[152:155], %[rd] offset:2048\n"
    "v_mfma_f32_16x16x32_bf16 v[140:143], a[160:163], v[44:47], v[140:143]\n"
    "v_exp_f32 v14, v14\n"
    "v_mfma_f32_16x16x32_bf16 v[156:159], a[164:167], v[44:47], v[156:159]\n"
    "ds_read_b128 a[156:159], %[rd] offset:3072\n"
    "v_exp_f32 v15, v15\n"
    "v_mfma_f32_16x16x32_bf16 v[152:155], a[164:167], v[40:43], v[152:155]\n"
    "v_exp_f32 v16, v16\n"
    "v_mfma_f32_16x16x32_bf16 v[148:151], a[164:167], v[36:39], v[148:151]\n"
    "v_exp_f32 v17, v17\n"
    "v_mfma_f32_16x16x32_bf16 v[144:147], a[164:167], v[32:35], v[144:147]\n"
    "v_cvt_pk_bf16_f32 v0, v0, v1\n"
    "v_mfma_f32_16x16x32_bf16 v[160:163], a[168:171], v[32:35], v[160:163]\n"
    "ds_read_b128 a[160:163], %[rd] offset:4096\n"
    "v_cvt_pk_bf16_f32 v1, v2, v3\n"
    "v_mfma_f32_16x16x32_bf16 v[164:167], a[168:171], v[36:39], v[164:167]\n"
    "v_exp_f32 v18, v18\n"
    "v_mfma_f32_16x16x32_bf16 v[168:171], a[168:171], v[40:43], v[168:171]\n"
    "v_exp_f32 v19, v19\n"
    "v_mfma_f32_16x16x32_bf16 v[172:175], a[168:171], v[44:47], v[172:175]\n"
    "v_exp_f32 v20, v20\n"
    "v_mfma_f32_16x16x32_bf16 v[188:191], a[172:175], v[44:47], v[188:191]\n"
    "ds_read_b128 a[164:167], %[rd] offset:5120\n"
    "v_exp_f32 v21, v21\n"
    "v_mfma_f32_16x16x32_bf16 v[184:187], a[172:175], v[40:43], v[184:187]\n"
    "v_cvt_pk_bf16_f32 v4, v4, v5\n"
    "v_mfma_f32_16x16x32_bf16 v[180:183], a[172:175], v[36:39], v[180:183]\n"
    "v_cvt_pk_bf16_f32 v5, v6, v7\n"
    "v_exp_f32 v22, v22\n"
    "v_mfma_f32_16x16x32_bf16 v[176:179], a[172:175], v[32:35], v[176:179]\n"
    "v_mfma_f32_16x16x32_bf16 a[128:131], a[176:179], v[32:35], a[128:131]\n"
    "v_exp_f32 v23, v23\n"
    "v_mfma_f32_16x16x32_bf16 a[132:135], a[176:179], v[36:39], a[132:135]\n"
    "v_cvt_pk_bf16_f32 v2, v16, v17\n"
    "v_cvt_pk_bf16_f32 v3, v18, v19\n"
    "v_mfma_f32_16x16x32_bf16 a[136:139], a[176:179], v[40:43], a[136:139]\n"
    "v_exp_f32 v24, v24\n"
    "v_mfma_f32_16x16x32_bf16 a[140:143], a[176:179], v[44:47], a[140:143]\n"
    "s_waitcnt lgkmcnt(2)\n"
    "v_exp_f32 v25, v25\n"
    "v_mfma_f32_16x16x32_bf16 v[40:43], a[144:147], a[8:11], v[192:195]\n"
    "ds_read_b128 a[168:171], %[rd] offset:6144\n"
    "v_cvt_pk_bf16_f32 v8, v8, v9\n"
    "v_mfma_f32_16x16x32_bf16 v[44:47], a[144:147], a[12:15], 0\n"
    "v_cvt_pk_bf16_f32 v9, v10, v11\n"
    "v_exp_f32 v26, v26\n"
    "v_mfma_f32_16x16x32_bf16 v[44:47], a[148:151], a[28:31], v[44:47]\n"
    "ds_read_b128 a[172:175], %[rd] offset:7168\n"
    "v_mfma_f32_16x16x32_bf16 v[40:43], a[148:151], a[24:27], v[40:43]\n"
    "v_exp_f32 v27, v27\n"
    "v_mfma_f32_16x16x32_bf16 v[40:43], a[152:155], a[40:43], v[40:43]\n"
    "ds_read_b128 a[144:147], %[rd] offset:16384\n"
    "v_cvt_pk_bf16_f32 v6, v20, v21\n"
    "v_mfma_f32_16x16x32_bf16 v[44:47], a[152:155], a[44:47], v[44:47]\n"
    "v_cvt_pk_bf16_f32 v7, v22, v23\n"
    "v_exp_f32 v28, v28\n"
    "v_mfma_f32_16x16x32_bf16 v[44:47], a[156:159], a[60:63], v[44:47]\n"
    "ds_read_b128 a[148:151], %[rd] offset:17408\n"
    "v_mfma_f32_16x16x32_bf16 v[40:43], a[156:159], a[56:59], v[40:43]\n"
    "s_waitcnt lgkmcnt(2)\n"
    "v_exp_f32 v29, v29\n"
    "v_mfma_f32_16x16x32_bf16 v[56:59], a[160:163], a[8:11], v[196:199]\n"
    "ds_read_b128 a[152:155], %[rd] offset:18432\n"
    "v_cvt_pk_bf16_f32 v12, v12, v13\n"
    "v_mfma_f32_16x16x32_bf16 v[60:63], a[160:163], a[12:15], v[192:195]\n"
    "v_cvt_pk_bf16_f32 v13, v14, v15\n"
    "v_exp_f32 v30, v30\n"
    "v_mfma_f32_16x16x32_bf16 v[60:63], a[164:167], a[28:31], v[60:63]\n"
    "ds_read_b128 a[156:159], %[rd] offset:19456\n"
    "v_mfma_f32_16x16x32_bf16 v[56:59], a[164:167], a[24:27], v[56:59]\n"
    "v_exp_f32 v31, v31\n"
    "v_mfma_f32_16x16x32_bf16 v[56:59], a[168:171], a[40:43], v[56:59]\n"
    "ds_read_b128 a[160:163], %[rd] offset:20480\n"
    "v_cvt_pk_bf16_f32 v10, v24, v25\n"
    "v_mfma_f32_16x16x32_bf16 v[60:63], a[168:171], a[44:47], v[60:63]\n"
    "v_cvt_pk_bf16_f32 v11, v26, v27\n"
    "v_cvt_pk_bf16_f32 v14, v28, v29\n"
    "v_mfma_f32_16x16x32_bf16 v[60:63], a[172:175], a[60:63], v[60:63]\n"
    "ds_read_b128 a[164:167], %[rd] offset:21504\n"
    "v_cvt_pk_bf16_f32 v15, v30, v31\n"
    "v_mfma_f32_16x16x32_bf16 v[56:59], a[172:175], a[56:59], v[56:59]\n"
    "s_waitcnt lgkmcnt(2)\n"
    "v_mfma_f32_16x16x32_bf16 v[64:67], a[144:147], v[0:3], v[64:67]\n"
    "ds_read_b128 a[168:171], %[rd] offset:22528\n"
    "v_mfma_f32_16x16x32_bf16 v[68:71], a[144:147], v[4:7], v[68:71]\n"
    "v_exp_f32 v40, v40\n"
    "v_mfma_f32_16x16x32_bf16 v[72:75], a[144:147], v[8:11], v[72:75]\n"
    "v_exp_f32 v41, v41\n"
    "v_mfma_f32_16x16x32_bf16 v[76:79], a[144:147], v[12:15], v[76:79]\n"
    "v_exp_f32 v42, v42\n"
    "v_mfma_f32_16x16x32_bf16 v[92:95], a[148:151], v[12:15], v[92:95]\n"
    "ds_read_b128 a[172:175], %[rd] offset:23552\n"
    "v_mfma_f32_16x16x32_bf16 v[88:91], a[148:151], v[8:11], v[88:91]\n"
    "v_exp_f32 v43, v43\n"
    "v_mfma_f32_16x16x32_bf16 v[84:87], a[148:151], v[4:7], v[84:87]\n"
    "v_mfma_f32_16x16x32_bf16 v[80:83], a[148:151], v[0:3], v[80:83]\n"
    "v_exp_f32 v44, v44\n"
    "v_mfma_f32_16x16x32_bf16 v[96:99], a[152:155], v[0:3], v[96:99]\n"
    "ds_read_b128 a[144:147], %[rd] offset:24576\n"
    "v_mfma_f32_16x16x32_bf16 v[100:103], a[152:155], v[4:7], v[100:103]\n"
    "v_exp_f32 v45, v45\n"
    "v_mfma_f32_16x16x32_bf16 v[104:107], a[152:155], v[8:11], v[104:107]\n"
    "v_exp_f32 v46, v46\n"
    "v_mfma_f32_16x16x32_bf16 v[108:111], a[152:155], v[12:15], v[108:111]\n"
    "v_mfma_f32_16x16x32_bf16 v[124:127], a[156:159], v[12:15], v[124:127]\n"
    "ds_read_b128 a[148:151], %[rd] offset:25600\n"
    "v_mfma_f32_16x16x32_bf16 v[120:123], a[156:159], v[8:11], v[120:123]\n"
    "v_exp_f32 v47, v47\n"
    "v_mfma_f32_16x16x32_bf16 v[116:119], a[156:159], v[4:7], v[116:119]\n"
    "v_exp_f32 v56, v56\n"
    "v_mfma_f32_16x16x32_bf16 v[112:115], a[156:159], v[0:3], v[112:115]\n"
    "s_waitcnt lgkmcnt(2)\n"
    "v_exp_f32 v57, v57\n"
    "v_mfma_f32_16x16x32_bf16 v[128:131], a[160:163], v[0:3], v[128:131]\n"
    "ds_read_b128 a[152:155], %[rd] offset:26624\n"
    "v_mfma_f32_16x16x32_bf16 v[132:135], a[160:163], v[4:7], v[132:135]\n"
    "v_cvt_pk_bf16_f32 v40, v40, v41\n"
    "v_mfma_f32_16x16x32_bf16 v[136:139], a[160:163], v[8:11], v[136:139]\n"
    "v_cvt_pk_bf16_f32 v41, v42, v43\n"
    "v_mfma_f32_16x16x32_bf16 v[140:143], a[160:163], v[12:15], v[140:143]\n"
    "v_exp_f32 v58, v58\n"
    "v_mfma_f32_16x16x32_bf16 v[156:159], a[164:167], v[12:15], v[156:159]\n"
    "ds_read_b128 a[156:159], %[rd] offset:27648\n"
    "v_mfma_f32_16x16x32_bf16 v[152:155], a[164:167], v[8:11], v[152:155]\n"
    "v_exp_f32 v59, v59\n"
    "v_mfma_f32_16x16x32_bf16 v[148:151], a[164:167], v[4:7], v[148:151]\n"
    "v_mfma_f32_16x16x32_bf16 v[144:147], a[164:167], v[0:3], v[144:147]\n"
    "v_exp_f32 v60, v60\n"
    "v_mfma_f32_16x16x32_bf16 v[160:163], a[168:171], v[0:3], v[160:163]\n"
    "ds_read_b128 a[160:163], %[rd] offset:28672\n"
    "v_mfma_f32_16x16x32_bf16 v[164:167], a[168:171], v[4:7], v[164:167]\n"
    "v_exp_f32 v61, v61\n"
    "v_mfma_f32_16x16x32_bf16 v[168:171], a[168:171], v[8:11], v[168:171]\n"
    "v_cvt_pk_bf16_f32 v44, v44, v45\n"
    "v_mfma_f32_16x16x32_bf16 v[172:175], a[168:171], v[12:15], v[172:175]\n"
    "v_cvt_pk_bf16_f32 v45, v46, v47\n"
    "v_mfma_f32_16x16x32_bf16 v[188:191], a[172:175], v[12:15], v[188:191]\n"
    "ds_read_b128 a[164:167], %[rd] offset:29696\n"
    "v_mfma_f32_16x16x32_bf16 v[184:187], a[172:175], v[8:11], v[184:187]\n"
    "v_exp_f32 v62, v62\n"
    "v_mfma_f32_16x16x32_bf16 v[180:183], a[172:175], v[4:7], v[180:183]\n"
    "v_exp_f32 v63, v63\n"
    "v_mfma_f32_16x16x32_bf16 v[176:179], a[172:175], v[0:3], v[176:179]\n"
    "v_cvt_pk_bf16_f32 v42, v56, v57\n"
    "v_mfma_f32_16x16x32_bf16 a[128:131], a[176:179], v[0:3], a[128:131]\n"
    "v_cvt_pk_bf16_f32 v43, v58, v59\n"
    "v_mfma_f32_16x16x32_bf16 a[132:135], a[176:179], v[4:7], a[132:135]\n"
    "v_cvt_pk_bf16_f32 v46, v60, v61\n"
    "v_mfma_f32_16x16x32_bf16 a[136:139], a[176:179], v[8:11], a[136:139]\n"
    "v_cvt_pk_bf16_f32 v47, v62, v63\n"
    "v_mfma_f32_16x16x32_bf16 a[140:143], a[176:179], v[12:15], a[140:143]\n"
    "s_waitcnt lgkmcnt(2)\n"
    "v_mfma_f32_16x16x32_bf16 v[72:75], a[144:147], v[40:43], v[72:75]\n"
    "ds_read_b128 a[168:171], %[rd] offset:30720\n"
    "v_mfma_f32_16x16x32_bf16 v[76:79], a[144:147], v[44:47], v[76:79]\n"
    "v_mfma_f32_16x16x32_bf16 v[92:95], a[148:151], v[44:47], v[92:95]\n"
    "ds_read_b128 a[172:175], %[rd] offset:31744\n"
    "v_mfma_f32_16x16x32_bf16 v[88:91], a[148:151], v[40:43], v[88:91]\n"
    "v_xor_b32 %[rd], 65536, %[rd]\n"
    "v_mfma_f32_16x16x32_bf16 v[104:107], a[152:155], v[40:43], v[104:107]\n"
    "v_mfma_f32_16x16x32_bf16 v[108:111], a[152:155], v[44:47], v[108:111]\n"
    "v_mfma_f32_16x16x32_bf16 v[124:127], a[156:159], v[44:47], v[124:127]\n"
    "v_mfma_f32_16x16x32_bf16 v[120:123], a[156:159], v[40:43], v[120:123]\n"
    "s_waitcnt lgkmcnt(0)\n"
    "s_setpc_b64 s[52:53]\n"
    ".p2align 6\n"
    "V6_%=:\n"
    "v_mfma_f32_16x16x32_bf16 v[128:131], a[160:163], v[32:35], v[128:131]\n"
    "ds_read_b128 a[144:147], %[rd] offset:0\n"
    "v_mfma_f32_16x16x32_bf16 v[132:135], a[160:163], v[36:39], v[132:135]\n"
    "ds_read_b128 a[148:151], %[rd] offset:1024\n"
    "v_mfma_f32_16x16x32_bf16 v[136:139], a[160:163], v[40:43], v[136:139]\n"
    "ds_read_b128 a[152:155], %[rd] offset:2048\n"
    "v_mfma_f32_16x16x32_bf16 v[140:143], a[160:163], v[44:47], v[140:143]\n"
    "v_exp_f32 v12, v12\n"
    "v_mfma_f32_16x16x32_bf16 v[156:159], a[164:167], v[44:47], v[156:159]\n"
    "ds_read_b128 a[156:159], %[rd] offset:3072\n"
    "v_mfma_f32_16x16x32_bf16 v[152:155], a[164:167], v[40:43], v[152:155]\n"
    "v_exp_f32 v13, v13\n"
    "v_mfma_f32_16x16x32_bf16 v[148:151], a[164:167], v[36:39], v[148:151]\n"
    "v_exp_f32 v14, v14\n"
    "v_mfma_f32_16x16x32_bf16 v[144:147], a[164:167], v[32:35], v[144:147]\n"
    "v_exp_f32 v15, v15\n"
    "v_mfma_f32_16x16x32_bf16 v[160:163], a[168:171], v[32:35], v[160:163]\n"
    "ds_read_b128 a[160:163], %[rd] offset:4096\n"
    "v_mfma_f32_16x16x32_bf16 v[164:167], a[168:171], v[36:39], v[164:167]\n"
    "v_exp_f32 v16, v16\n"
    "v_mfma_f32_16x16x32_bf16 v[168:171], a[168:171], v[40:43], v[168:171]\n"
    "v_exp_f32 v17, v17\n"
    "v_mfma_f32_16x16x32_bf16 v[172:175], a[168:171], v[44:47], v[172:175]\n"
    "v_cvt_pk_bf16_f32 v0, v0, v1\n"
    "v_mfma_f32_16x16x32_bf16 v[188:191], a[172:175], v[44:47], v[188:191]\n"
    "ds_read_b128 a[164:167], %[rd] offset:5120\n"
    "v_mfma_f32_16x16x32_bf16 v[184:187], a[172:175], v[40:43], v[184:187]\n"
    "v_cvt_pk_bf16_f32 v1, v2, v3\n"
    "v_mfma_f32_16x16x32_bf16 v[180:183], a[172:175], v[36:39], v[180:183]\n"
    "v_exp_f32 v18, v18\n"
    "v_mfma_f32_16x16x32_bf16 v[176:179], a[172:175], v[32:35], v[176:179]\n"
    "v_exp_f32 v19, v19\n"
    "v_mfma_f32_16x16x32_bf16 a[128:131], a[176:179], v[32:35], a[128:131]\n"
    "v_mfma_f32_16x16x32_bf16 a[132:135], a[176:179], v[36:39], a[132:135]\n"
    "v_exp_f32 v20, v20\n"
    "v_mfma_f32_16x16x32_bf16 a[136:139], a[176:179], v[40:43], a[136:139]\n"
    "v_exp_f32 v21, v21\n"
    "v_mfma_f32_16x16x32_bf16 a[140:143], a[176:179], v[44:47], a[140:143]\n"
    "s_waitcnt lgkmcnt(2)\n"
    "v_mfma_f32_16x16x32_bf16 v[32:35], a[144:147], a[0:3], 0\n"
    "ds_read_b128 a[168:171], %[rd] offset:6144\n"
    "v_cvt_pk_bf16_f32 v4, v4, v5\n"
    "v_mfma_f32_16x16x32_bf16 v[36:39], a[144:147], a[4:7], 0\n"
    "v_cvt_pk_bf16_f32 v5, v6, v7\n"
    "v_mfma_f32_16x16x32_bf16 v[40:43], a[144:147], a[8:11], 0\n"
    "v_exp_f32 v22, v22\n"
    "v_mfma_f32_16x16x32_bf16 v[44:47], a[144:147], a[12:15], 0\n"
    "v_exp_f32 v23, v23\n"
    "v_mfma_f32_16x16x32_bf16 v[44:47], a[148:151], a[28:31], v[44:47]\n"
    "ds_read_b128 a[172:175], %[rd] offset:7168\n"
    "v_mfma_f32_16x16x32_bf16 v[40:43], a[148:151], a[24:27], v[40:43]\n"
    "v_cvt_pk_bf16_f32 v2, v16, v17\n"
    "v_mfma_f32_16x16x32_bf16 v[36:39], a[148:151], a[20:23], v[36:39]\n"
    "v_cvt_pk_bf16_f32 v3, v18, v19\n"
    "v_mfma_f32_16x16x32_bf16 v[32:35], a[148:151], a[16:19], v[32:35]\n"
    "v_exp_f32 v24, v24\n"
    "v_mfma_f32_16x16x32_bf16 v[32:35], a[152:155], a[32:35], v[32:35]\n"
    "ds_read_b128 a[144:147], %[rd] offset:16384\n"
    "v_mfma_f32_16x16x32_bf16 v[36:39], a[152:155], a[36:39], v[36:39]\n"
    "v_exp_f32 v25, v25\n"
    "v_mfma_f32_16x16x32_bf16 v[40:43], a[152:155], a[40:43], v[40:43]\n"
    "v_mfma_f32_16x16x32_bf16 v[44:47], a[152:155], a[44:47], v[44:47]\n"
    "v_cvt_pk_bf16_f32 v8, v8, v9\n"
    "v_cvt_pk_bf16_f32 v9, v10, v11\n"
    "v_mfma_f32_16x16x32_bf16 v[44:47], a[156:159], a[60:63], v[44:47]\n"
    "ds_read_b128 a[148:151], %[rd] offset:17408\n"
    "v_mfma_f32_16x16x32_bf16 v[40:43], a[156:159], a[56:59], v[40:43]\n"
    "v_exp_f32 v26, v26\n"
    "v_mfma_f32_16x16x32_bf16 v[36:39], a[156:159], a[52:55], v[36:39]\n"
    "v_exp_f32 v27, v27\n"
    "v_mfma_f32_16x16x32_bf16 v[32:35], a[156:159], a[48:51], v[32:35]\n"
    "s_waitcnt lgkmcnt(2)\n"
    "v_mfma_f32_16x16x32_bf16 v[48:51], a[160:163], a[0:3], 0\n"
    "ds_read_b128 a[152:155], %[rd] offset:18432\n"
    "v_cvt_pk_bf16_f32 v6, v20, v21\n"
    "v_mfma_f32_16x16x32_bf16 v[52:55], a[160:163], a[4:7], 0\n"
    "v_cvt_pk_bf16_f32 v7, v22, v23\n"
    "v_mfma_f32_16x16x32_bf16 v[56:59], a[160:163], a[8:11], 0\n"
    "v_exp_f32 v28, v28\n"
    "v_mfma_f32_16x16x32_bf16 v[60:63], a[160:163], a[12:15], 0\n"
    "v_mfma_f32_16x16x32_bf16 v[60:63], a[164:167], a[28:31], v[60:63]\n"
    "ds_read_b128 a[156:159], %[rd] offset:19456\n"
    "v_mfma_f32_16x16x32_bf16 v[56:59], a[164:167], a[24:27], v[56:59]\n"
    "v_exp_f32 v29, v29\n"
    "v_mfma_f32_16x16x32_bf16 v[52:55], a[164:167], a[20:23], v[52:55]\n"
    "v_cvt_pk_bf16_f32 v12, v12, v13\n"
    "v_cvt_pk_bf16_f32 v13, v14, v15\n"
    "v_mfma_f32_16x16x32_bf16 v[48:51], a[164:167], a[16:19], v[48:51]\n"
    "v_exp_f32 v30, v30\n"
    "v_mfma_f32_16x16x32_bf16 v[48:51], a[168:171], a[32:35], v[48:51]\n"
    "ds_read_b128 a[160:163], %[rd] offset:20480\n"
    "v_mfma_f32_16x16x32_bf16 v[52:55], a[168:171], a[36:39], v[52:55]\n"
    "v_exp_f32 v31, v31\n"
    "v_mfma_f32_16x16x32_bf16 v[56:59], a[168:171], a[40:43], v[56:59]\n"
    "v_mfma_f32_16x16x32_bf16 v[60:63], a[168:171], a[44:47], v[60:63]\n"
    "v_cvt_pk_bf16_f32 v10, v24, v25\n"
    "v_mfma_f32_16x16x32_bf16 v[60:63], a[172:175], a[60:63], v[60:63]\n"
    "ds_read_b128 a[164:167], %[rd] offset:21504\n"
    "v_cvt_pk_bf16_f32 v11, v26, v27\n"
    "v_mfma_f32_16x16x32_bf16 v[56:59], a[172:175], a[56:59], v[56:59]\n"
    "v_cvt_pk_bf16_f32 v14, v28, v29\n"
    "v_mfma_f32_16x16x32_bf16 v[52:55], a[172:175], a[52:55], v[52:55]\n"
    "v_cvt_pk_bf16_f32 v15, v30, v31\n"
    "v_mfma_f32_16x16x32_bf16 v[48:51], a[172:175], a[48:51], v[48:51]\n"
    "s_waitcnt lgkmcnt(2)\n"
    "v_mfma_f32_16x16x32_bf16 v[64:67], a[144:147], v[0:3], v[64:67]\n"
    "ds_read_b128 a[168:171], %[rd] offset:22528\n"
    "v_exp_f32 v32, v32\n"
    "v_mfma_f32_16x16x32_bf16 v[68:71], a[144:147], v[4:7], v[68:71]\n"
    "v_exp_f32 v33, v33\n"
    "v_mfma_f32_16x16x32_bf16 v[72:75], a[144:147], v[8:11], v[72:75]\n"
    "v_exp_f32 v34, v34\n"
    "v_mfma_f32_16x16x32_bf16 v[76:79], a[144:147], v[12:15], v[76:79]\n"
    "v_exp_f32 v35, v35\n"
    "v_mfma_f32_16x16x32_bf16 v[92:95], a[148:151], v[12:15], v[92:95]\n"
    "ds_read_b128 a[172:175], %[rd] offset:23552\n"
    "v_exp_f32 v36, v36\n"
    "v_mfma_f32_16x16x32_bf16 v[88:91], a[148:151], v[8:11], v[88:91]\n"
    "v_exp_f32 v37, v37\n"
    "v_mfma_f32_16x16x32_bf16 v[84:87], a[148:151], v[4:7], v[84:87]\n"
    "v_exp_f32 v38, v38\n"
    "v_mfma_f32_16x16x32_bf16 v[80:83], a[148:151], v[0:3], v[80:83]\n"
    "v_exp_f32 v39, v39\n"
    "v_mfma_f32_16x16x32_bf16 v[96:99], a[152:155], v[0:3], v[96:99]\n"
    "ds_read_b128 a[144:147], %[rd] offset:24576\n"
    "v_exp_f32 v40, v40\n"
    "v_mfma_f32_16x16x32_bf16 v[100:103], a[152:155], v[4:7], v[100:103]\n"
    "v_exp_f32 v41, v41\n"
    "v_mfma_f32_16x16x32_bf16 v[104:107], a[152:155], v[8:11], v[104:107]\n"
    "v_exp_f32 v42, v42\n"
    "v_mfma_f32_16x16x32_bf16 v[108:111], a[152:155], v[12:15], v[108:111]\n"
    "v_exp_f32 v43, v43\n"
    "v_mfma_f32_16x16x32_bf16 v[124:127], a[156:159], v[12:15], v[124:127]\n"
    "ds_read_b128 a[148:151], %[rd] offset:25600\n"
    "v_exp_f32 v44, v44\n"
    "v_mfma_f32_16x16x32_bf16 v[120:123], a[156:159], v[8:11], v[120:123]\n"
    "v_exp_f32 v45, v45\n"
    "v_mfma_f32_16x16x32_bf16 v[116:119], a[156:159], v[4:7], v[116:119]\n"
    "v_exp_f32 v46, v46\n"
    "v_mfma_f32_16x16x32_bf16 v[112:115], a[156:159], v[0:3], v[112:115]\n"
    "s_waitcnt lgkmcnt(2)\n"
    "v_exp_f32 v47, v47\n"
    "v_mfma_f32_16x16x32_bf16 v[128:131], a[160:163], v[0:3], v[128:131]\n"
    "ds_read_b128 a[152:155], %[rd] offset:26624\n"
    "v_exp_f32 v48, v48\n"
    "v_mfma_f32_16x16x32_bf16 v[132:135], a[160:163], v[4:7], v[132:135]\n"
    "v_exp_f32 v49, v49\n"
    "v_cvt_pk_bf16_f32 v32, v32, v33\n"
    "v_mfma_f32_16x16x32_bf16 v[136:139], a[160:163], v[8:11], v[136:139]\n"
    "v_cvt_pk_bf16_f32 v33, v34, v35\n"
    "v_exp_f32 v50, v50\n"
    "v_mfma_f32_16x16x32_bf16 v[140:143], a[160:163], v[12:15], v[140:143]\n"
    "v_exp_f32 v51, v51\n"
    "v_mfma_f32_16x16x32_bf16 v[156:159], a[164:167], v[12:15], v[156:159]\n"
    "ds_read_b128 a[156:159], %[rd] offset:27648\n"
    "v_exp_f32 v52, v52\n"
    "v_mfma_f32_16x16x32_bf16 v[152:155], a[164:167], v[8:11], v[152:155]\n"
    "v_exp_f32 v53, v53\n"
    "v_cvt_pk_bf16_f32 v36, v36, v37\n"
    "v_mfma_f32_16x16x32_bf16 v[148:151], a[164:167], v[4:7], v[148:151]\n"
    "v_cvt_pk_bf16_f32 v37, v38, v39\n"
    "v_exp_f32 v54, v54\n"
    "v_mfma_f32_16x16x32_bf16 v[144:147], a[164:167], v[0:3], v[144:147]\n"
    "v_exp_f32 v55, v55\n"
    "v_cvt_pk_bf16_f32 v34, v48, v49\n"
    "v_mfma_f32_16x16x32_bf16 v[160:163], a[168:171], v[0:3], v[160:163]\n"
    "ds_read_b128 a[160:163], %[rd] offset:28672\n"
    "v_cvt_pk_bf16_f32 v35, v50, v51\n"
    "v_mfma_f32_16x16x32_bf16 v[164:167], a[168:171], v[4:7], v[164:167]\n"
    "v_exp_f32 v56, v56\n"
    "v_mfma_f32_16x16x32_bf16 v[168:171], a[168:171], v[8:11], v[168:171]\n"
    "v_exp_f32 v57, v57\n"
    "v_cvt_pk_bf16_f32 v40, v40, v41\n"
    "v_mfma_f32_16x16x32_bf16 v[172:175], a[168:171], v[12:15], v[172:175]\n"
    "v_cvt_pk_bf16_f32 v41, v42, v43\n"
    "v_exp_f32 v58, v58\n"
    "v_mfma_f32_16x16x32_bf16 v[188:191], a[172:175], v[12:15], v[188:191]\n"
    "ds_read_b128 a[164:167], %[rd] offset:29696\n"
    "v_exp_f32 v59, v59\n"
    "v_mfma_f32_16x16x32_bf16 v[184:187], a[172:175], v[8:11], v[184:187]\n"
    "v_cvt_pk_bf16_f32 v38, v52, v53\n"
    "v_cvt_pk_bf16_f32 v39, v54, v55\n"
    "v_mfma_f32_16x16x32_bf16 v[180:183], a[172:175], v[4:7], v[180:183]\n"
    "v_exp_f32 v60, v60\n"
    "v_mfma_f32_16x16x32_bf16 v[176:179], a[172:175], v[0:3], v[176:179]\n"
    "v_exp_f32 v61, v61\n"
    "v_cvt_pk_bf16_f32 v44, v44, v45\n"
    "v_mfma_f32_16x16x32_bf16 a[128:131], a[176:179], v[0:3], a[128:131]\n"
    "v_cvt_pk_bf16_f32 v45, v46, v47\n"
    "v_exp_f32 v62, v62\n"
    "v_mfma_f32_16x16x32_bf16 a[132:135], a[176:179], v[4:7], a[132:135]\n"
    "v_exp_f32 v63, v63\n"
    "v_cvt_pk_bf16_f32 v42, v56, v57\n"
    "v_mfma_f32_16x16x32_bf16 a[136:139], a[176:179], v[8:11], a[136:139]\n"
    "v_cvt_pk_bf16_f32 v43, v58, v59\n"
    "v_cvt_pk_bf16_f32 v46, v60, v61\n"
    "v_cvt_pk_bf16_f32 v47, v62, v63\n"
    "v_mfma_f32_16x16x32_bf16 a[140:143], a[176:179], v[12:15], a[140:143]\n"
    "s_waitcnt lgkmcnt(2)\n"
    "v_mfma_f32_16x16x32_bf16 v[64:67], a[144:147], v[32:35], v[64:67]\n"
    "ds_read_b128 a[168:171], %[rd] offset:30720\n"
    "v_mfma_f32_16x16x32_bf16 v[68:71], a[144:147], v[36:39], v[68:71]\n"
    "v_mfma_f32_16x16x32_bf16 v[72:75], a[144:147], v[40:43], v[72:75]\n"
    "v_mfma_f32_16x16x32_bf16 v[76:79], a[144:147], v[44:47], v[76:79]\n"
    "v_mfma_f32_16x16x32_bf16 v[92:95], a[148:151], v[44:47], v[92:95]\n"
    "ds_read_b128 a[172:175], %[rd] offset:31744\n"
    "v_mfma_f32_16x16x32_bf16 v[88:91], a[148:151], v[40:43], v[88:91]\n"
    "v_xor_b32 %[rd], 65536, %[rd]\n"
    "v_mfma_f32_16x16x32_bf16 v[84:87], a[148:151], v[36:39], v[84:87]\n"
    "v_mfma_f32_16x16x32_bf16 v[80:83], a[148:151], v[32:35], v[80:83]\n"
    "v_mfma_f32_16x16x32_bf16 v[96:99], a[152:155], v[32:35], v[96:99]\n"
    "v_mfma_f32_16x16x32_bf16 v[100:103], a[152:155], v[36:39], v[100:103]\n"
    "v_mfma_f32_16x16x32_bf16 v[104:107], a[152:155], v[40:43], v[104:107]\n"
    "v_mfma_f32_16x16x32_bf16 v[108:111], a[152:155], v[44:47], v[108:111]\n"
    "v_mfma_f32_16x16x32_bf16 v[124:127], a[156:159], v[44:47], v[124:127]\n"
    "v_mfma_f32_16x16x32_bf16 v[120:123], a[156:159], v[40:43], v[120:123]\n"
    "v_mfma_f32_16x16x32_bf16 v[116:119], a[156:159], v[36:39], v[116:119]\n"
    "v_mfma_f32_16x16x32_bf16 v[112:115], a[156:159], v[32:35], v[112:115]\n"
    "s_waitcnt lgkmcnt(0)\n"
    "s_setpc_b64 s[52:53]\n"
    ".p2align 6\n"
    "F1_%=:\n"
    "ds_read_b128 a[144:147], %[rd16] offset:16384\n"
    "ds_read_b128 a[148:151], %[rd16] offset:17408\n"
    "ds_read_b128 a[152:155], %[rd16] offset:18432\n"
    "ds_read_b128 a[156:159], %[rd16] offset:19456\n"
    "ds_read_b128 a[160:163], %[rd16] offset:20480\n"
    "ds_read_b128 a[164:167], %[rd16] offset:21504\n"
    "s_waitcnt lgkmcnt(2)\n"
    "v_mfma_f32_16x16x32_bf16 v[0:3], a[144:147], a[0:3], 0\n"
    "ds_read_b128 a[168:171], %[rd16] offset:22528\n"
    "v_mfma_f32_16x16x32_bf16 v[4:7], a[144:147], a[4:7], 0\n"
    "v_mfma_f32_16x16x32_bf16 v[8:11], a[144:147], a[8:11], 0\n"
    "v_mfma_f32_16x16x32_bf16 v[12:15], a[144:147], a[12:15], 0\n"
    "s_add_u32 m0, s40, 0\n"
    "v_mfma_f32_16x16x32_bf16 v[12:15], a[148:151], a[28:31], v[12:15]\n"
    "ds_read_b128 a[172:175], %[rd16] offset:23552\n"
    "buffer_load_dwordx4 %[vdma], %[srdk], %[so0] offen offset:0 lds\n"
    "v_mfma_f32_16x16x32_bf16 v[8:11], a[148:151], a[24:27], v[8:11]\n"
    "v_mfma_f32_16x16x32_bf16 v[4:7], a[148:151], a[20:23], v[4:7]\n"
    "v_mfma_f32_16x16x32_bf16 v[0:3], a[148:151], a[16:19], v[0:3]\n"
    "v_mfma_f32_16x16x32_bf16 v[0:3], a[152:155], a[32:35], v[0:3]\n"
    "ds_read_b128 a[144:147], %[rd] offset:0\n"
    "v_mfma_f32_16x16x32_bf16 v[4:7], a[152:155], a[36:39], v[4:7]\n"
    "v_mfma_f32_16x16x32_bf16 v[8:11], a[152:155], a[40:43], v[8:11]\n"
    "v_mfma_f32_16x16x32_bf16 v[12:15], a[152:155], a[44:47], v[12:15]\n"
    "v_mfma_f32_16x16x32_bf16 v[12:15], a[156:159], a[60:63], v[12:15]\n"
    "ds_read_b128 a[148:151], %[rd] offset:1024\n"
    "v_mfma_f32_16x16x32_bf16 v[8:11], a[156:159], a[56:59], v[8:11]\n"
    "v_mfma_f32_16x16x32_bf16 v[4:7], a[156:159], a[52:55], v[4:7]\n"
    "v_mfma_f32_16x16x32_bf16 v[0:3], a[156:159], a[48:51], v[0:3]\n"
    "s_waitcnt lgkmcnt(2)\n"
    "v_mfma_f32_16x16x32_bf16 v[16:19], a[160:163], a[0:3], 0\n"
    "ds_read_b128 a[152:155], %[rd] offset:2048\n"
    "buffer_load_dwordx4 %[vdma], %[srdk], %[so0] offen offset:1024 lds\n"
    "v_mfma_f32_16x16x32_bf16 v[20:23], a[160:163], a[4:7], 0\n"
    "v_mfma_f32_16x16x32_bf16 v[24:27], a[160:163], a[8:11], 0\n"
    "v_mfma_f32_16x16x32_bf16 v[28:31], a[160:163], a[12:15], 0\n"
    "v_mfma_f32_16x16x32_bf16 v[28:31], a[164:167], a[28:31], v[28:31]\n"
    "ds_read_b128 a[156:159], %[rd] offset:3072\n"
    "v_mfma_f32_16x16x32_bf16 v[24:27], a[164:167], a[24:27], v[24:27]\n"
    "v_mfma_f32_16x16x32_bf16 v[20:23], a[164:167], a[20:23], v[20:23]\n"
    "v_exp_f32 v0, v0\n"
    "v_exp_f32 v1, v1\n"
    "v_mfma_f32_16x16x32_bf16 v[16:19], a[164:167], a[16:19], v[16:19]\n"
    "v_exp_f32 v2, v2\n"
    "v_mfma_f32_16x16x32_bf16 v[16:19], a[168:171], a[32:35], v[16:19]\n"
    "ds_read_b128 a[160:163], %[rd] offset:4096\n"
    "buffer_load_dwordx4 %[vdma], %[srdk], %[so0] offen offset:2048 lds\n"
    "v_exp_f32 v3, v3\n"
    "v_mfma_f32_16x16x32_bf16 v[20:23], a[168:171], a[36:39], v[20:23]\n"
    "v_exp_f32 v4, v4\n"
    "v_mfma_f32_16x16x32_bf16 v[24:27], a[168:171], a[40:43], v[24:27]\n"
    "v_exp_f32 v5, v5\n"
    "v_mfma_f32_16x16x32_bf16 v[28:31], a[168:171], a[44:47], v[28:31]\n"
    "v_exp_f32 v6, v6\n"
    "v_mfma_f32_16x16x32_bf16 v[28:31], a[172:175], a[60:63], v[28:31]\n"
    "ds_read_b128 a[164:167], %[rd] offset:5120\n"
    "v_exp_f32 v7, v7\n"
    "v_mfma_f32_16x16x32_bf16 v[24:27], a[172:175], a[56:59], v[24:27]\n"
    "v_exp_f32 v8, v8\n"
    "v_mfma_f32_16x16x32_bf16 v[20:23], a[172:175], a[52:55], v[20:23]\n"
    "v_exp_f32 v9, v9\n"
    "v_exp_f32 v10, v10\n"
    "v_mfma_f32_16x16x32_bf16 v[16:19], a[172:175], a[48:51], v[16:19]\n"
    "s_waitcnt lgkmcnt(2)\n"
    "v_exp_f32 v11, v11\n"
    "v_mfma_f32_16x16x32_bf16 v[32:35], a[144:147], a[0:3], 0\n"
    "ds_read_b128 a[168:171], %[rd] offset:6144\n"
    "v_exp_f32 v12, v12\n"
    "v_mfma_f32_16x16x32_bf16 v[36:39], a[144:147], a[4:7], 0\n"
    "v_exp_f32 v13, v13\n"
    "v_mfma_f32_16x16x32_bf16 v[40:43], a[144:147], a[8:11], 0\n"
    "v_exp_f32 v14, v14\n"
    "v_mfma_f32_16x16x32_bf16 v[44:47], a[144:147], a[12:15], 0\n"
    "v_exp_f32 v15, v15\n"
    "v_mfma_f32_16x16x32_bf16 v[44:47], a[148:151], a[28:31], v[44:47]\n"
    "ds_read_b128 a[172:175], %[rd] offset:7168\n"
    "buffer_load_dwordx4 %[vdma], %[srdk], %[so0] offen offset:3072 lds\n"
    "v_mfma_f32_16x16x32_bf16 v[40:43], a[148:151], a[24:27], v[40:43]\n"
    "v_exp_f32 v16, v16\n"
    "v_mfma_f32_16x16x32_bf16 v[36:39], a[148:151], a[20:23], v[36:39]\n"
    "v_exp_f32 v17, v17\n"
    "v_cvt_pk_bf16_f32 v0, v0, v1\n"
    "v_mfma_f32_16x16x32_bf16 v[32:35], a[148:151], a[16:19], v[32:35]\n"
    "v_cvt_pk_bf16_f32 v1, v2, v3\n"
    "v_exp_f32 v18, v18\n"
    "v_mfma_f32_16x16x32_bf16 v[32:35], a[152:155], a[32:35], v[32:35]\n"
    "ds_read_b128 a[144:147], %[rd] offset:16384\n"
    "v_exp_f32 v19, v19\n"
    "v_mfma_f32_16x16x32_bf16 v[36:39], a[152:155], a[36:39], v[36:39]\n"
    "v_exp_f32 v20, v20\n"
    "v_mfma_f32_16x16x32_bf16 v[40:43], a[152:155], a[40:43], v[40:43]\n"
    "v_exp_f32 v21, v21\n"
    "v_mfma_f32_16x16x32_bf16 v[44:47], a[152:155], a[44:47], v[44:47]\n"
    "s_add_u32 m0, s40, 16384\n"
    "v_cvt_pk_bf16_f32 v4, v4, v5\n"
    "v_mfma_f32_16x16x32_bf16 v[44:47], a[156:159], a[60:63], v[44:47]\n"
    "ds_read_b128 a[148:151], %[rd] offset:17408\n"
    "buffer_load_dwordx4 %[vdma], %[srdv], %[so0] offen offset:0 lds\n"
    "v_mfma_f32_16x16x32_bf16 v[40:43], a[156:159], a[56:59], v[40:43]\n"
    "v_cvt_pk_bf16_f32 v5, v6, v7\n"
    "v_exp_f32 v22, v22\n"
    "v_mfma_f32_16x16x32_bf16 v[36:39], a[156:159], a[52:55], v[36:39]\n"
    "v_exp_f32 v23, v23\n"
    "v_mfma_f32_16x16x32_bf16 v[32:35], a[156:159], a[48:51], v[32:35]\n"
    "s_waitcnt lgkmcnt(2)\n"
    "v_cvt_pk_bf16_f32 v2, v16, v17\n"
    "v_mfma_f32_16x16x32_bf16 v[48:51], a[160:163], a[0:3], 0\n"
    "ds_read_b128 a[152:155], %[rd] offset:18432\n"
    "v_cvt_pk_bf16_f32 v3, v18, v19\n"
    "v_mfma_f32_16x16x32_bf16 v[52:55], a[160:163], a[4:7], 0\n"
    "v_exp_f32 v24, v24\n"
    "v_mfma_f32_16x16x32_bf16 v[56:59], a[160:163], a[8:11], 0\n"
    "v_exp_f32 v25, v25\n"
    "v_cvt_pk_bf16_f32 v8, v8, v9\n"
    "v_mfma_f32_16x16x32_bf16 v[60:63], a[160:163], a[12:15], 0\n"
    "v_cvt_pk_bf16_f32 v9, v10, v11\n"
    "v_exp_f32 v26, v26\n"
    "v_mfma_f32_16x16x32_bf16 v[60:63], a[164:167], a[28:31], v[60:63]\n"
    "ds_read_b128 a[156:159], %[rd] offset:19456\n"
    "v_mfma_f32_16x16x32_bf16 v[56:59], a[164:167], a[24:27], v[56:59]\n"
    "v_exp_f32 v27, v27\n"
    "v_mfma_f32_16x16x32_bf16 v[52:55], a[164:167], a[20:23], v[52:55]\n"
    "v_cvt_pk_bf16_f32 v6, v20, v21\n"
    "v_cvt_pk_bf16_f32 v7, v22, v23\n"
    "v_mfma_f32_16x16x32_bf16 v[48:51], a[164:167], a[16:19], v[48:51]\n"
    "v_exp_f32 v28, v28\n"
    "v_mfma_f32_16x16x32_bf16 v[48:51], a[168:171], a[32:35], v[48:51]\n"
    "ds_read_b128 a[160:163], %[rd] offset:20480\n"
    "buffer_load_dwordx4 %[vdma], %[srdv], %[so0] offen offset:1024 lds\n"
    "v_mfma_f32_16x16x32_bf16 v[52:55], a[168:171], a[36:39], v[52:55]\n"
    "v_exp_f32 v29, v29\n"
    "v_cvt_pk_bf16_f32 v12, v12, v13\n"
    "v_mfma_f32_16x16x32_bf16 v[56:59], a[168:171], a[40:43], v[56:59]\n"
    "v_cvt_pk_bf16_f32 v13, v14, v15\n"
    "v_exp_f32 v30, v30\n"
    "v_mfma_f32_16x16x32_bf16 v[60:63], a[168:171], a[44:47], v[60:63]\n"
    "v_exp_f32 v31, v31\n"
    "v_mfma_f32_16x16x32_bf16 v[60:63], a[172:175], a[60:63], v[60:63]\n"
    "ds_read_b128 a[164:167], %[rd] offset:21504\n"
    "v_mfma_f32_16x16x32_bf16 v[56:59], a[172:175], a[56:59], v[56:59]\n"
    "v_cvt_pk_bf16_f32 v10, v24, v25\n"
    "v_cvt_pk_bf16_f32 v11, v26, v27\n"
    "v_mfma_f32_16x16x32_bf16 v[52:55], a[172:175], a[52:55], v[52:55]\n"
    "v_cvt_pk_bf16_f32 v14, v28, v29\n"
    "v_cvt_pk_bf16_f32 v15, v30, v31\n"
    "v_mfma_f32_16x16x32_bf16 v[48:51], a[172:175], a[48:51], v[48:51]\n"
    "s_waitcnt lgkmcnt(2)\n"
    "v_mfma_f32_16x16x32_bf16 v[64:67], a[144:147], v[0:3], 0\n"
    "ds_read_b128 a[168:171], %[rd] offset:22528\n"
    "buffer_load_dwordx4 %[vdma], %[srdv], %[so0] offen offset:2048 lds\n"
    "v_mfma_f32_16x16x32_bf16 v[68:71], a[144:147], v[4:7], 0\n"
    "v_exp_f32 v32, v32\n"
    "v_mfma_f32_16x16x32_bf16 v[72:75], a[144:147], v[8:11], 0\n"
    "v_exp_f32 v33, v33\n"
    "v_mfma_f32_16x16x32_bf16 v[76:79], a[144:147], v[12:15], 0\n"
    "v_exp_f32 v34, v34\n"
    "v_mfma_f32_16x16x32_bf16 v[92:95], a[148:151], v[12:15], 0\n"
    "ds_read_b128 a[172:175], %[rd] offset:23552\n"
    "v_mfma_f32_16x16x32_bf16 v[88:91], a[148:151], v[8:11], 0\n"
    "v_exp_f32 v35, v35\n"
    "v_mfma_f32_16x16x32_bf16 v[84:87], a[148:151], v[4:7], 0\n"
    "v_mfma_f32_16x16x32_bf16 v[80:83], a[148:151], v[0:3], 0\n"
    "v_exp_f32 v36, v36\n"
    "v_mfma_f32_16x16x32_bf16 v[96:99], a[152:155], v[0:3], 0\n"
    "ds_read_b128 a[144:147], %[rd] offset:8192\n"
    "v_mfma_f32_16x16x32_bf16 v[100:103], a[152:155], v[4:7], 0\n"
    "v_exp_f32 v37, v37\n"
    "v_mfma_f32_16x16x32_bf16 v[104:107], a[152:155], v[8:11], 0\n"
    "v_exp_f32 v38, v38\n"
    "v_mfma_f32_16x16x32_bf16 v[108:111], a[152:155], v[12:15], 0\n"
    "v_mfma_f32_16x16x32_bf16 v[124:127], a[156:159], v[12:15], 0\n"
    "ds_read_b128 a[148:151], %[rd] offset:9216\n"
    "buffer_load_dwordx4 %[vdma], %[srdv], %[so0] offen offset:3072 lds\n"
    "v_mfma_f32_16x16x32_bf16 v[120:123], a[156:159], v[8:11], 0\n"
    "v_exp_f32 v39, v39\n"
    "v_mfma_f32_16x16x32_bf16 v[116:119], a[156:159], v[4:7], 0\n"
    "v_exp_f32 v40, v40\n"
    "v_mfma_f32_16x16x32_bf16 v[112:115], a[156:159], v[0:3], 0\n"
    "s_waitcnt lgkmcnt(2)\n"
    "v_mfma_f32_16x16x32_bf16 v[128:131], a[160:163], v[0:3], 0\n"
    "ds_read_b128 a[152:155], %[rd] offset:10240\n"
    "v_mfma_f32_16x16x32_bf16 v[132:135], a[160:163], v[4:7], 0\n"
    "v_exp_f32 v41, v41\n"
    "v_mfma_f32_16x16x32_bf16 v[136:139], a[160:163], v[8:11], 0\n"
    "v_exp_f32 v42, v42\n"
    "v_mfma_f32_16x16x32_bf16 v[140:143], a[160:163], v[12:15], 0\n"
    "v_exp_f32 v43, v43\n"
    "v_mfma_f32_16x16x32_bf16 v[156:159], a[164:167], v[12:15], 0\n"
    "ds_read_b128 a[156:159], %[rd] offset:11264\n"
    "v_mfma_f32_16x16x32_bf16 v[152:155], a[164:167], v[8:11], 0\n"
    "v_exp_f32 v44, v44\n"
    "v_mfma_f32_16x16x32_bf16 v[148:151], a[164:167], v[4:7], 0\n"
    "v_exp_f32 v45, v45\n"
    "v_mfma_f32_16x16x32_bf16 v[144:147], a[164:167], v[0:3], 0\n"
    "v_mfma_f32_16x16x32_bf16 v[160:163], a[168:171], v[0:3], 0\n"
    "ds_read_b128 a[160:163], %[rd] offset:12288\n"
    "v_mfma_f32_16x16x32_bf16 v[164:167], a[168:171], v[4:7], 0\n"
    "v_exp_f32 v46, v46\n"
    "v_mfma_f32_16x16x32_bf16 v[168:171], a[168:171], v[8:11], 0\n"
    "v_exp_f32 v47, v47\n"
    "v_mfma_f32_16x16x32_bf16 v[172:175], a[168:171], v[12:15], 0\n"
    "v_exp_f32 v48, v48\n"
    "v_mfma_f32_16x16x32_bf16 v[188:191], a[172:175], v[12:15], 0\n"
    "ds_read_b128 a[164:167], %[rd] offset:13312\n"
    "v_mfma_f32_16x16x32_bf16 v[184:187], a[172:175], v[8:11], 0\n"
    "v_exp_f32 v49, v49\n"
    "v_mfma_f32_16x16x32_bf16 v[180:183], a[172:175], v[4:7], 0\n"
    "v_mfma_f32_16x16x32_bf16 v[176:179], a[172:175], v[0:3], 0\n"
    "v_cvt_pk_bf16_f32 v32, v32, v33\n"
    "v_cvt_pk_bf16_f32 v33, v34, v35\n"
    "v_mfma_f32_16x16x32_bf16 a[128:131], a[176:179], v[0:3], 0\n"
    "v_exp_f32 v50, v50\n"
    "v_mfma_f32_16x16x32_bf16 a[132:135], a[176:179], v[4:7], 0\n"
    "v_exp_f32 v51, v51\n"
    "v_mfma_f32_16x16x32_bf16 a[136:139], a[176:179], v[8:11], 0\n"
    "v_mfma_f32_16x16x32_bf16 a[140:143], a[176:179], v[12:15], 0\n"
    "s_waitcnt lgkmcnt(2)\n"
    "v_exp_f32 v52, v52\n"
    "v_mfma_f32_16x16x32_bf16 v[0:3], a[144:147], a[0:3], v[192:195]\n"
    "ds_read_b128 a[168:171], %[rd] offset:14336\n"
    "v_mfma_f32_16x16x32_bf16 v[4:7], a[144:147], a[4:7], 0\n"
    "v_exp_f32 v53, v53\n"
    "v_mfma_f32_16x16x32_bf16 v[8:11], a[144:147], a[8:11], 0\n"
    "v_cvt_pk_bf16_f32 v36, v36, v37\n"
    "v_mfma_f32_16x16x32_bf16 v[12:15], a[144:147], a[12:15], 0\n"
    "v_cvt_pk_bf16_f32 v37, v38, v39\n"
    "v_mfma_f32_16x16x32_bf16 v[12:15], a[148:151], a[28:31], v[12:15]\n"
    "ds_read_b128 a[172:175], %[rd] offset:15360\n"
    "v_mfma_f32_16x16x32_bf16 v[8:11], a[148:151], a[24:27], v[8:11]\n"
    "v_exp_f32 v54, v54\n"
    "v_mfma_f32_16x16x32_bf16 v[4:7], a[148:151], a[20:23], v[4:7]\n"
    "v_exp_f32 v55, v55\n"
    "v_mfma_f32_16x16x32_bf16 v[0:3], a[148:151], a[16:19], v[0:3]\n"
    "v_cvt_pk_bf16_f32 v34, v48, v49\n"
    "v_mfma_f32_16x16x32_bf16 v[0:3], a[152:155], a[32:35], v[0:3]\n"
    "ds_read_b128 a[144:147], %[rd] offset:24576\n"
    "v_cvt_pk_bf16_f32 v35, v50, v51\n"
    "v_mfma_f32_16x16x32_bf16 v[4:7], a[152:155], a[36:39], v[4:7]\n"
    "v_exp_f32 v56, v56\n"
    "v_mfma_f32_16x16x32_bf16 v[8:11], a[152:155], a[40:43], v[8:11]\n"
    "v_mfma_f32_16x16x32_bf16 v[12:15], a[152:155], a[44:47], v[12:15]\n"
    "v_exp_f32 v57, v57\n"
    "v_mfma_f32_16x16x32_bf16 v[12:15], a[156:159], a[60:63], v[12:15]\n"
    "ds_read_b128 a[148:151], %[rd] offset:25600\n"
    "v_mfma_f32_16x16x32_bf16 v[8:11], a[156:159], a[56:59], v[8:11]\n"
    "v_cvt_pk_bf16_f32 v40, v40, v41\n"
    "v_cvt_pk_bf16_f32 v41, v42, v43\n"
    "v_mfma_f32_16x16x32_bf16 v[4:7], a[156:159], a[52:55], v[4:7]\n"
    "v_exp_f32 v58, v58\n"
    "v_mfma_f32_16x16x32_bf16 v[0:3], a[156:159], a[48:51], v[0:3]\n"
    "s_waitcnt lgkmcnt(2)\n"
    "v_exp_f32 v59, v59\n"
    "v_mfma_f32_16x16x32_bf16 v[16:19], a[160:163], a[0:3], v[196:199]\n"
    "ds_read_b128 a[152:155], %[rd] offset:26624\n"
    "v_mfma_f32_16x16x32_bf16 v[20:23], a[160:163], a[4:7], v[192:195]\n"
    "v_cvt_pk_bf16_f32 v38, v52, v53\n"
    "v_mfma_f32_16x16x32_bf16 v[24:27], a[160:163], a[8:11], 0\n"
    "v_cvt_pk_bf16_f32 v39, v54, v55\n"
    "v_mfma_f32_16x16x32_bf16 v[28:31], a[160:163], a[12:15], 0\n"
    "v_exp_f32 v60, v60\n"
    "v_mfma_f32_16x16x32_bf16 v[28:31], a[164:167], a[28:31], v[28:31]\n"
    "ds_read_b128 a[156:159], %[rd] offset:27648\n"
    "v_mfma_f32_16x16x32_bf16 v[24:27], a[164:167], a[24:27], v[24:27]\n"
    "v_exp_f32 v61, v61\n"
    "v_mfma_f32_16x16x32_bf16 v[20:23], a[164:167], a[20:23], v[20:23]\n"
    "v_cvt_pk_bf16_f32 v44, v44, v45\n"
    "v_mfma_f32_16x16x32_bf16 v[16:19], a[164:167], a[16:19], v[16:19]\n"
    "v_cvt_pk_bf16_f32 v45, v46, v47\n"
    "v_mfma_f32_16x16x32_bf16 v[16:19], a[168:171], a[32:35], v[16:19]\n"
    "ds_read_b128 a[160:163], %[rd] offset:28672\n"
    "v_mfma_f32_16x16x32_bf16 v[20:23], a[168:171], a[36:39], v[20:23]\n"
    "v_exp_f32 v62, v62\n"
    "v_mfma_f32_16x16x32_bf16 v[24:27], a[168:171], a[40:43], v[24:27]\n"
    "v_exp_f32 v63, v63\n"
    "v_mfma_f32_16x16x32_bf16 v[28:31], a[168:171], a[44:47], v[28:31]\n"
    "v_cvt_pk_bf16_f32 v42, v56, v57\n"
    "v_mfma_f32_16x16x32_bf16 v[28:31], a[172:175], a[60:63], v[28:31]\n"
    "ds_read_b128 a[164:167], %[rd] offset:29696\n"
    "v_cvt_pk_bf16_f32 v43, v58, v59\n"
    "v_mfma_f32_16x16x32_bf16 v[24:27], a[172:175], a[56:59], v[24:27]\n"
    "v_cvt_pk_bf16_f32 v46, v60, v61\n"
    "v_mfma_f32_16x16x32_bf16 v[20:23], a[172:175], a[52:55], v[20:23]\n"
    "v_cvt_pk_bf16_f32 v47, v62, v63\n"
    "v_mfma_f32_16x16x32_bf16 v[16:19], a[172:175], a[48:51], v[16:19]\n"
    "s_waitcnt lgkmcnt(2)\n"
    "v_mfma_f32_16x16x32_bf16 v[64:67], a[144:147], v[32:35], v[64:67]\n"
    "ds_read_b128 a[168:171], %[rd] offset:30720\n"
    "v_mfma_f32_16x16x32_bf16 v[68:71], a[144:147], v[36:39], v[68:71]\n"
    "v_mfma_f32_16x16x32_bf16 v[72:75], a[144:147], v[40:43], v[72:75]\n"
    "v_exp_f32 v0, v0\n"
    "v_mfma_f32_16x16x32_bf16 v[76:79], a[144:147], v[44:47], v[76:79]\n"
    "v_exp_f32 v1, v1\n"
    "v_mfma_f32_16x16x32_bf16 v[92:95], a[148:151], v[44:47], v[92:95]\n"
    "ds_read_b128 a[172:175], %[rd] offset:31744\n"
    "v_mfma_f32_16x16x32_bf16 v[88:91], a[148:151], v[40:43], v[88:91]\n"
    "v_xor_b32 %[rd], 65536, %[rd]\n"
    "v_mfma_f32_16x16x32_bf16 v[84:87], a[148:151], v[36:39], v[84:87]\n"
    "s_add_u32 %[so0], %[so0], 16384\n"
    "v_exp_f32 v2, v2\n"
    "v_mfma_f32_16x16x32_bf16 v[80:83], a[148:151], v[32:35], v[80:83]\n"
    "s_xor_b32 s40, s40, 65536\n"
    "v_exp_f32 v3, v3\n"
    "v_mfma_f32_16x16x32_bf16 v[96:99], a[152:155], v[32:35], v[96:99]\n"
    "v_exp_f32 v4, v4\n"
    "v_mfma_f32_16x16x32_bf16 v[100:103], a[152:155], v[36:39], v[100:103]\n"
    "v_exp_f32 v5, v5\n"
    "v_mfma_f32_16x16x32_bf16 v[104:107], a[152:155], v[40:43], v[104:107]\n"
    "v_exp_f32 v6, v6\n"
    "v_mfma_f32_16x16x32_bf16 v[108:111], a[152:155], v[44:47], v[108:111]\n"
    "v_exp_f32 v7, v7\n"
    "v_mfma_f32_16x16x32_bf16 v[124:127], a[156:159], v[44:47], v[124:127]\n"
    "v_exp_f32 v8, v8\n"
    "v_mfma_f32_16x16x32_bf16 v[120:123], a[156:159], v[40:43], v[120:123]\n"
    "v_exp_f32 v9, v9\n"
    "v_mfma_f32_16x16x32_bf16 v[116:119], a[156:159], v[36:39], v[116:119]\n"
    "v_exp_f32 v10, v10\n"
    "v_mfma_f32_16x16x32_bf16 v[112:115], a[156:159], v[32:35], v[112:115]\n"
    "v_exp_f32 v11, v11\n"
    "s_waitcnt vmcnt(0) lgkmcnt(0)\n"
    "s_barrier\n"
    "s_setpc_b64 s[52:53]\n"
    ".p2align 6\n"
    "F2_%=:\n"
    "ds_read_b128 a[144:147], %[rd16] offset:0\n"
    "ds_read_b128 a[148:151], %[rd16] offset:1024\n"
    "ds_read_b128 a[152:155], %[rd16] offset:2048\n"
    "ds_read_b128 a[156:159], %[rd16] offset:3072\n"
    "ds_read_b128 a[160:163], %[rd16] offset:4096\n"
    "ds_read_b128 a[164:167], %[rd16] offset:5120\n"
    "s_waitcnt lgkmcnt(2)\n"
    "v_mfma_f32_16x16x32_bf16 v[0:3], a[144:147], a[0:3], 0\n"
    "ds_read_b128 a[168:171], %[rd16] offset:6144\n"
    "v_mfma_f32_16x16x32_bf16 v[4:7], a[144:147], a[4:7], 0\n"
    "v_mfma_f32_16x16x32_bf16 v[8:11], a[144:147], a[8:11], 0\n"
    "v_mfma_f32_16x16x32_bf16 v[12:15], a[144:147], a[12:15], 0\n"
    "s_add_u32 m0, s40, 0\n"
    "v_mfma_f32_16x16x32_bf16 v[12:15], a[148:151], a[28:31], v[12:15]\n"
    "ds_read_b128 a[172:175], %[rd16] offset:7168\n"
    "buffer_load_dwordx4 %[vdma], %[srdk], %[so0] offen offset:0 lds\n"
    "v_mfma_f32_16x16x32_bf16 v[8:11], a[148:151], a[24:27], v[8:11]\n"
    "v_mfma_f32_16x16x32_bf16 v[4:7], a[148:151], a[20:23], v[4:7]\n"
    "v_mfma_f32_16x16x32_bf16 v[0:3], a[148:151], a[16:19], v[0:3]\n"
    "v_mfma_f32_16x16x32_bf16 v[0:3], a[152:155], a[32:35], v[0:3]\n"
    "ds_read_b128 a[144:147], %[rd] offset:0\n"
    "v_mfma_f32_16x16x32_bf16 v[4:7], a[152:155], a[36:39], v[4:7]\n"
    "buffer_load_dwordx4 %[vdma], %[srdk], %[so0] offen offset:1024 lds\n"
    "v_mfma_f32_16x16x32_bf16 v[8:11], a[152:155], a[40:43], v[8:11]\n"
    "v_mfma_f32_16x16x32_bf16 v[12:15], a[152:155], a[44:47], v[12:15]\n"
    "v_mfma_f32_16x16x32_bf16 v[12:15], a[156:159], a[60:63], v[12:15]\n"
    "ds_read_b128 a[148:151], %[rd] offset:1024\n"
    "v_mfma_f32_16x16x32_bf16 v[8:11], a[156:159], a[56:59], v[8:11]\n"
    "v_mfma_f32_16x16x32_bf16 v[4:7], a[156:159], a[52:55], v[4:7]\n"
    "v_mfma_f32_16x16x32_bf16 v[0:3], a[156:159], a[48:51], v[0:3]\n"
    "s_waitcnt lgkmcnt(2)\n"
    "v_mfma_f32_16x16x32_bf16 v[16:19], a[160:163], a[0:3], 0\n"
    "ds_read_b128 a[152:155], %[rd] offset:2048\n"
    "buffer_load_dwordx4 %[vdma], %[srdk], %[so0] offen offset:2048 lds\n"
    "v_mfma_f32_16x16x32_bf16 v[20:23], a[160:163], a[4:7], 0\n"
    "v_mfma_f32_16x16x32_bf16 v[24:27], a[160:163], a[8:11], 0\n"
    "v_mfma_f32_16x16x32_bf16 v[28:31], a[160:163], a[12:15], 0\n"
    "v_mfma_f32_16x16x32_bf16 v[28:31], a[164:167], a[28:31], v[28:31]\n"
    "ds_read_b128 a[156:159], %[rd] offset:3072\n"
    "buffer_load_dwordx4 %[vdma], %[srdk], %[so0] offen offset:3072 lds\n"
    "v_mfma_f32_16x16x32_bf16 v[24:27], a[164:167], a[24:27], v[24:27]\n"
    "v_mfma_f32_16x16x32_bf16 v[20:23], a[164:167], a[20:23], v[20:23]\n"
    "v_exp_f32 v0, v0\n"
    "v_exp_f32 v1, v1\n"
    "v_mfma_f32_16x16x32_bf16 v[16:19], a[164:167], a[16:19], v[16:19]\n"
    "s_add_u32 m0, s40, 16384\n"
    "v_exp_f32 v2, v2\n"
    "v_mfma_f32_16x16x32_bf16 v[16:19], a[168:171], a[32:35], v[16:19]\n"
    "ds_read_b128 a[160:163], %[rd] offset:4096\n"
    "buffer_load_dwordx4 %[vdma], %[srdv], %[so0] offen offset:0 lds\n"
    "v_exp_f32 v3, v3\n"
    "v_mfma_f32_16x16x32_bf16 v[20:23], a[168:171], a[36:39], v[20:23]\n"
    "v_exp_f32 v4, v4\n"
    "v_mfma_f32_16x16x32_bf16 v[24:27], a[168:171], a[40:43], v[24:27]\n"
    "v_exp_f32 v5, v5\n"
    "v_mfma_f32_16x16x32_bf16 v[28:31], a[168:171], a[44:47], v[28:31]\n"
    "v_exp_f32 v6, v6\n"
    "v_mfma_f32_16x16x32_bf16 v[28:31], a[172:175], a[60:63], v[28:31]\n"
    "ds_read_b128 a[164:167], %[rd] offset:5120\n"
    "v_exp_f32 v7, v7\n"
    "v_mfma_f32_16x16x32_bf16 v[24:27], a[172:175], a[56:59], v[24:27]\n"
    "buffer_load_dwordx4 %[vdma], %[srdv], %[so0] offen offset:1024 lds\n"
    "v_exp_f32 v8, v8\n"
    "v_mfma_f32_16x16x32_bf16 v[20:23], a[172:175], a[52:55], v[20:23]\n"
    "v_exp_f32 v9, v9\n"
    "v_exp_f32 v10, v10\n"
    "v_mfma_f32_16x16x32_bf16 v[16:19], a[172:175], a[48:51], v[16:19]\n"
    "s_waitcnt lgkmcnt(2)\n"
    "v_exp_f32 v11, v11\n"
    "v_mfma_f32_16x16x32_bf16 v[32:35], a[144:147], a[0:3], 0\n"
    "ds_read_b128 a[168:171], %[rd] offset:6144\n"
    "v_exp_f32 v12, v12\n"
    "v_mfma_f32_16x16x32_bf16 v[36:39], a[144:147], a[4:7], 0\n"
    "v_exp_f32 v13, v13\n"
    "v_mfma_f32_16x16x32_bf16 v[40:43], a[144:147], a[8:11], 0\n"
    "v_exp_f32 v14, v14\n"
    "v_mfma_f32_16x16x32_bf16 v[44:47], a[144:147], a[12:15], 0\n"
    "v_exp_f32 v15, v15\n"
    "v_mfma_f32_16x16x32_bf16 v[44:47], a[148:151], a[28:31], v[44:47]\n"
    "ds_read_b128 a[172:175], %[rd] offset:7168\n"
    "buffer_load_dwordx4 %[vdma], %[srdv], %[so0] offen offset:2048 lds\n"
    "v_mfma_f32_16x16x32_bf16 v[40:43], a[148:151], a[24:27], v[40:43]\n"
    "v_exp_f32 v16, v16\n"
    "v_mfma_f32_16x16x32_bf16 v[36:39], a[148:151], a[20:23], v[36:39]\n"
    "v_exp_f32 v17, v17\n"
    "v_cvt_pk_bf16_f32 v0, v0, v1\n"
    "v_mfma_f32_16x16x32_bf16 v[32:35], a[148:151], a[16:19], v[32:35]\n"
    "v_cvt_pk_bf16_f32 v1, v2, v3\n"
    "v_exp_f32 v18, v18\n"
    "v_mfma_f32_16x16x32_bf16 v[32:35], a[152:155], a[32:35], v[32:35]\n"
    "ds_read_b128 a[144:147], %[rd] offset:16384\n"
    "buffer_load_dwordx4 %[vdma], %[srdv], %[so0] offen offset:3072 lds\n"
    "v_mfma_f32_16x16x32_bf16 v[36:39], a[152:155], a[36:39], v[36:39]\n"
    "v_exp_f32 v19, v19\n"
    "v_mfma_f32_16x16x32_bf16 v[40:43], a[152:155], a[40:43], v[40:43]\n"
    "v_exp_f32 v20, v20\n"
    "v_mfma_f32_16x16x32_bf16 v[44:47], a[152:155], a[44:47], v[44:47]\n"
    "s_add_u32 m0, s40, 32768\n"
    "v_exp_f32 v21, v21\n"
    "v_cvt_pk_bf16_f32 v4, v4, v5\n"
    "v_mfma_f32_16x16x32_bf16 v[44:47], a[156:159], a[60:63], v[44:47]\n"
    "ds_read_b128 a[148:151], %[rd] offset:17408\n"
    "buffer_load_dwordx4 %[vdma], %[srdk], %[so1] offen offset:0 lds\n"
    "v_mfma_f32_16x16x32_bf16 v[40:43], a[156:159], a[56:59], v[40:43]\n"
    "v_cvt_pk_bf16_f32 v5, v6, v7\n"
    "v_exp_f32 v22, v22\n"
    "v_mfma_f32_16x16x32_bf16 v[36:39], a[156:159], a[52:55], v[36:39]\n"
    "v_exp_f32 v23, v23\n"
    "v_mfma_f32_16x16x32_bf16 v[32:35], a[156:159], a[48:51], v[32:35]\n"
    "s_waitcnt lgkmcnt(2)\n"
    "v_cvt_pk_bf16_f32 v2, v16, v17\n"
    "v_cvt_pk_bf16_f32 v3, v18, v19\n"
    "v_mfma_f32_16x16x32_bf16 v[48:51], a[160:163], a[0:3], 0\n"
    "ds_read_b128 a[152:155], %[rd] offset:18432\n"
    "v_exp_f32 v24, v24\n"
    "v_mfma_f32_16x16x32_bf16 v[52:55], a[160:163], a[4:7], 0\n"
    "buffer_load_dwordx4 %[vdma], %[srdk], %[so1] offen offset:1024 lds\n"
    "v_exp_f32 v25, v25\n"
    "v_mfma_f32_16x16x32_bf16 v[56:59], a[160:163], a[8:11], 0\n"
    "v_cvt_pk_bf16_f32 v8, v8, v9\n"
    "v_mfma_f32_16x16x32_bf16 v[60:63], a[160:163], a[12:15], 0\n"
    "v_cvt_pk_bf16_f32 v9, v10, v11\n"
    "v_exp_f32 v26, v26\n"
    "v_mfma_f32_16x16x32_bf16 v[60:63], a[164:167], a[28:31], v[60:63]\n"
    "ds_read_b128 a[156:159], %[rd] offset:19456\n"
    "v_mfma_f32_16x16x32_bf16 v[56:59], a[164:167], a[24:27], v[56:59]\n"
    "v_exp_f32 v27, v27\n"
    "v_mfma_f32_16x16x32_bf16 v[52:55], a[164:167], a[20:23], v[52:55]\n"
    "v_cvt_pk_bf16_f32 v6, v20, v21\n"
    "v_cvt_pk_bf16_f32 v7, v22, v23\n"
    "v_mfma_f32_16x16x32_bf16 v[48:51], a[164:167], a[16:19], v[48:51]\n"
    "v_exp_f32 v28, v28\n"
    "v_mfma_f32_16x16x32_bf16 v[48:51], a[168:171], a[32:35], v[48:51]\n"
    "ds_read_b128 a[160:163], %[rd] offset:20480\n"
    "buffer_load_dwordx4 %[vdma], %[srdk], %[so1] offen offset:2048 lds\n"
    "v_mfma_f32_16x16x32_bf16 v[52:55], a[168:171], a[36:39], v[52:55]\n"
    "v_exp_f32 v29, v29\n"
    "v_cvt_pk_bf16_f32 v12, v12, v13\n"
    "v_mfma_f32_16x16x32_bf16 v[56:59], a[168:171], a[40:43], v[56:59]\n"
    "v_cvt_pk_bf16_f32 v13, v14, v15\n"
    "v_exp_f32 v30, v30\n"
    "v_mfma_f32_16x16x32_bf16 v[60:63], a[168:171], a[44:47], v[60:63]\n"
    "v_exp_f32 v31, v31\n"
    "v_mfma_f32_16x16x32_bf16 v[60:63], a[172:175], a[60:63], v[60:63]\n"
    "ds_read_b128 a[164:167], %[rd] offset:21504\n"
    "buffer_load_dwordx4 %[vdma], %[srdk], %[so1] offen offset:3072 lds\n"
    "v_mfma_f32_16x16x32_bf16 v[56:59], a[172:175], a[56:59], v[56:59]\n"
    "v_cvt_pk_bf16_f32 v10, v24, v25\n"
    "v_cvt_pk_bf16_f32 v11, v26, v27\n"
    "v_mfma_f32_16x16x32_bf16 v[52:55], a[172:175], a[52:55], v[52:55]\n"
    "s_add_u32 m0, s40, 49152\n"
    "v_cvt_pk_bf16_f32 v14, v28, v29\n"
    "v_cvt_pk_bf16_f32 v15, v30, v31\n"
    "v_mfma_f32_16x16x32_bf16 v[48:51], a[172:175], a[48:51], v[48:51]\n"
    "s_waitcnt lgkmcnt(2)\n"
    "v_mfma_f32_16x16x32_bf16 v[64:67], a[144:147], v[0:3], 0\n"
    "ds_read_b128 a[168:171], %[rd] offset:22528\n"
    "buffer_load_dwordx4 %[vdma], %[srdv], %[so1] offen offset:0 lds\n"
    "v_mfma_f32_16x16x32_bf16 v[68:71], a[144:147], v[4:7], 0\n"
    "v_exp_f32 v32, v32\n"
    "v_mfma_f32_16x16x32_bf16 v[72:75], a[144:147], v[8:11], 0\n"
    "v_exp_f32 v33, v33\n"
    "v_mfma_f32_16x16x32_bf16 v[76:79], a[144:147], v[12:15], 0\n"
    "v_exp_f32 v34, v34\n"
    "v_mfma_f32_16x16x32_bf16 v[92:95], a[148:151], v[12:15], 0\n"
    "ds_read_b128 a[172:175], %[rd] offset:23552\n"
    "v_mfma_f32_16x16x32_bf16 v[88:91], a[148:151], v[8:11], 0\n"
    "buffer_load_dwordx4 %[vdma], %[srdv], %[so1] offen offset:1024 lds\n"
    "v_mfma_f32_16x16x32_bf16 v[84:87], a[148:151], v[4:7], 0\n"
    "v_exp_f32 v35, v35\n"
    "v_mfma_f32_16x16x32_bf16 v[80:83], a[148:151], v[0:3], 0\n"
    "v_exp_f32 v36, v36\n"
    "v_mfma_f32_16x16x32_bf16 v[96:99], a[152:155], v[0:3], 0\n"
    "ds_read_b128 a[144:147], %[rd] offset:8192\n"
    "v_mfma_f32_16x16x32_bf16 v[100:103], a[152:155], v[4:7], 0\n"
    "v_exp_f32 v37, v37\n"
    "v_mfma_f32_16x16x32_bf16 v[104:107], a[152:155], v[8:11], 0\n"
    "v_mfma_f32_16x16x32_bf16 v[108:111], a[152:155], v[12:15], 0\n"
    "v_exp_f32 v38, v38\n"
    "v_mfma_f32_16x16x32_bf16 v[124:127], a[156:159], v[12:15], 0\n"
    "ds_read_b128 a[148:151], %[rd] offset:9216\n"
    "buffer_load_dwordx4 %[vdma], %[srdv], %[so1] offen offset:2048 lds\n"
    "v_mfma_f32_16x16x32_bf16 v[120:123], a[156:159], v[8:11], 0\n"
    "v_exp_f32 v39, v39\n"
    "v_mfma_f32_16x16x32_bf16 v[116:119], a[156:159], v[4:7], 0\n"
    "v_exp_f32 v40, v40\n"
    "v_mfma_f32_16x16x32_bf16 v[112:115], a[156:159], v[0:3], 0\n"
    "s_waitcnt lgkmcnt(2)\n"
    "v_mfma_f32_16x16x32_bf16 v[128:131], a[160:163], v[0:3], 0\n"
    "ds_read_b128 a[152:155], %[rd] offset:10240\n"
    "buffer_load_dwordx4 %[vdma], %[srdv], %[so1] offen offset:3072 lds\n"
    "v_mfma_f32_16x16x32_bf16 v[132:135], a[160:163], v[4:7], 0\n"
    "v_exp_f32 v41, v41\n"
    "v_mfma_f32_16x16x32_bf16 v[136:139], a[160:163], v[8:11], 0\n"
    "v_exp_f32 v42, v42\n"
    "v_mfma_f32_16x16x32_bf16 v[140:143], a[160:163], v[12:15], 0\n"
    "v_mfma_f32_16x16x32_bf16 v[156:159], a[164:167], v[12:15], 0\n"
    "ds_read_b128 a[156:159], %[rd] offset:11264\n"
    "v_mfma_f32_16x16x32_bf16 v[152:155], a[164:167], v[8:11], 0\n"
    "v_exp_f32 v43, v43\n"
    "v_mfma_f32_16x16x32_bf16 v[148:151], a[164:167], v[4:7], 0\n"
    "v_exp_f32 v44, v44\n"
    "v_mfma_f32_16x16x32_bf16 v[144:147], a[164:167], v[0:3], 0\n"
    "v_exp_f32 v45, v45\n"
    "v_mfma_f32_16x16x32_bf16 v[160:163], a[168:171], v[0:3], 0\n"
    "ds_read_b128 a[160:163], %[rd] offset:12288\n"
    "v_mfma_f32_16x16x32_bf16 v[164:167], a[168:171], v[4:7], 0\n"
    "v_exp_f32 v46, v46\n"
    "v_mfma_f32_16x16x32_bf16 v[168:171], a[168:171], v[8:11], 0\n"
    "v_exp_f32 v47, v47\n"
    "v_mfma_f32_16x16x32_bf16 v[172:175], a[168:171], v[12:15], 0\n"
    "v_mfma_f32_16x16x32_bf16 v[188:191], a[172:175], v[12:15], 0\n"
    "ds_read_b128 a[164:167], %[rd] offset:13312\n"
    "v_mfma_f32_16x16x32_bf16 v[184:187], a[172:175], v[8:11], 0\n"
    "v_exp_f32 v48, v48\n"
    "v_mfma_f32_16x16x32_bf16 v[180:183], a[172:175], v[4:7], 0\n"
    "v_exp_f32 v49, v49\n"
    "v_mfma_f32_16x16x32_bf16 v[176:179], a[172:175], v[0:3], 0\n"
    "v_cvt_pk_bf16_f32 v32, v32, v33\n"
    "v_mfma_f32_16x16x32_bf16 a[128:131], a[176:179], v[0:3], 0\n"
    "v_cvt_pk_bf16_f32 v33, v34, v35\n"
    "v_mfma_f32_16x16x32_bf16 a[132:135], a[176:179], v[4:7], 0\n"
    "v_exp_f32 v50, v50\n"
    "v_mfma_f32_16x16x32_bf16 a[136:139], a[176:179], v[8:11], 0\n"
    "v_exp_f32 v51, v51\n"
    "v_mfma_f32_16x16x32_bf16 a[140:143], a[176:179], v[12:15], 0\n"
    "s_waitcnt lgkmcnt(2)\n"
    "v_exp_f32 v52, v52\n"
    "v_mfma_f32_16x16x32_bf16 v[0:3], a[144:147], a[0:3], v[192:195]\n"
    "ds_read_b128 a[168:171], %[rd] offset:14336\n"
    "v_mfma_f32_16x16x32_bf16 v[4:7], a[144:147], a[4:7], 0\n"
    "v_exp_f32 v53, v53\n"
    "v_mfma_f32_16x16x32_bf16 v[8:11], a[144:147], a[8:11], 0\n"
    "v_mfma_f32_16x16x32_bf16 v[12:15], a[144:147], a[12:15], 0\n"
    "v_cvt_pk_bf16_f32 v36, v36, v37\n"
    "v_cvt_pk_bf16_f32 v37, v38, v39\n"
    "v_mfma_f32_16x16x32_bf16 v[12:15], a[148:151], a[28:31], v[12:15]\n"
    "ds_read_b128 a[172:175], %[rd] offset:15360\n"
    "v_mfma_f32_16x16x32_bf16 v[8:11], a[148:151], a[24:27], v[8:11]\n"
    "v_exp_f32 v54, v54\n"
    "v_mfma_f32_16x16x32_bf16 v[4:7], a[148:151], a[20:23], v[4:7]\n"
    "v_exp_f32 v55, v55\n"
    "v_mfma_f32_16x16x32_bf16 v[0:3], a[148:151], a[16:19], v[0:3]\n"
    "v_mfma_f32_16x16x32_bf16 v[0:3], a[152:155], a[32:35], v[0:3]\n"
    "ds_read_b128 a[144:147], %[rd] offset:24576\n"
    "v_cvt_pk_bf16_f32 v34, v48, v49\n"
    "v_mfma_f32_16x16x32_bf16 v[4:7], a[152:155], a[36:39], v[4:7]\n"
    "v_cvt_pk_bf16_f32 v35, v50, v51\n"
    "v_mfma_f32_16x16x32_bf16 v[8:11], a[152:155], a[40:43], v[8:11]\n"
    "v_exp_f32 v56, v56\n"
    "v_mfma_f32_16x16x32_bf16 v[12:15], a[152:155], a[44:47], v[12:15]\n"
    "v_exp_f32 v57, v57\n"
    "v_mfma_f32_16x16x32_bf16 v[12:15], a[156:159], a[60:63], v[12:15]\n"
    "ds_read_b128 a[148:151], %[rd] offset:25600\n"
    "v_mfma_f32_16x16x32_bf16 v[8:11], a[156:159], a[56:59], v[8:11]\n"
    "v_cvt_pk_bf16_f32 v40, v40, v41\n"
    "v_mfma_f32_16x16x32_bf16 v[4:7], a[156:159], a[52:55], v[4:7]\n"
    "v_cvt_pk_bf16_f32 v41, v42, v43\n"
    "v_mfma_f32_16x16x32_bf16 v[0:3], a[156:159], a[48:51], v[0:3]\n"
    "s_waitcnt lgkmcnt(2)\n"
    "v_exp_f32 v58, v58\n"
    "v_mfma_f32_16x16x32_bf16 v[16:19], a[160:163], a[0:3], v[196:199]\n"
    "ds_read_b128 a[152:155], %[rd] offset:26624\n"
    "v_mfma_f32_16x16x32_bf16 v[20:23], a[160:163], a[4:7], v[192:195]\n"
    "v_exp_f32 v59, v59\n"
    "v_mfma_f32_16x16x32_bf16 v[24:27], a[160:163], a[8:11], 0\n"
    "v_cvt_pk_bf16_f32 v38, v52, v53\n"
    "v_cvt_pk_bf16_f32 v39, v54, v55\n"
    "v_mfma_f32_16x16x32_bf16 v[28:31], a[160:163], a[12:15], 0\n"
    "v_exp_f32 v60, v60\n"
    "v_mfma_f32_16x16x32_bf16 v[28:31], a[164:167], a[28:31], v[28:31]\n"
    "ds_read_b128 a[156:159], %[rd] offset:27648\n"
    "v_mfma_f32_16x16x32_bf16 v[24:27], a[164:167], a[24:27], v[24:27]\n"
    "v_exp_f32 v61, v61\n"
    "v_mfma_f32_16x16x32_bf16 v[20:23], a[164:167], a[20:23], v[20:23]\n"
    "v_cvt_pk_bf16_f32 v44, v44, v45\n"
    "v_mfma_f32_16x16x32_bf16 v[16:19], a[164:167], a[16:19], v[16:19]\n"
    "v_cvt_pk_bf16_f32 v45, v46, v47\n"
    "v_mfma_f32_16x16x32_bf16 v[16:19], a[168:171], a[32:35], v[16:19]\n"
    "ds_read_b128 a[160:163], %[rd] offset:28672\n"
    "v_mfma_f32_16x16x32_bf16 v[20:23], a[168:171], a[36:39], v[20:23]\n"
    "v_exp_f32 v62, v62\n"
    "v_mfma_f32_16x16x32_bf16 v[24:27], a[168:171], a[40:43], v[24:27]\n"
    "v_exp_f32 v63, v63\n"
    "v_mfma_f32_16x16x32_bf16 v[28:31], a[168:171], a[44:47], v[28:31]\n"
    "v_cvt_pk_bf16_f32 v42, v56, v57\n"
    "v_mfma_f32_16x16x32_bf16 v[28:31], a[172:175], a[60:63], v[28:31]\n"
    "ds_read_b128 a[164:167], %[rd] offset:29696\n"
    "v_cvt_pk_bf16_f32 v43, v58, v59\n"
    "v_mfma_f32_16x16x32_bf16 v[24:27], a[172:175], a[56:59], v[24:27]\n"
    "v_cvt_pk_bf16_f32 v46, v60, v61\n"
    "v_mfma_f32_16x16x32_bf16 v[20:23], a[172:175], a[52:55], v[20:23]\n"
    "v_cvt_pk_bf16_f32 v47, v62, v63\n"
    "v_mfma_f32_16x16x32_bf16 v[16:19], a[172:175], a[48:51], v[16:19]\n"
    "s_waitcnt lgkmcnt(2)\n"
    "v_mfma_f32_16x16x32_bf16 v[64:67], a[144:147], v[32:35], v[64:67]\n"
    "ds_read_b128 a[168:171], %[rd] offset:30720\n"
    "v_mfma_f32_16x16x32_bf16 v[68:71], a[144:147], v[36:39], v[68:71]\n"
    "v_mfma_f32_16x16x32_bf16 v[72:75], a[144:147], v[40:43], v[72:75]\n"
    "v_exp_f32 v0, v0\n"
    "v_mfma_f32_16x16x32_bf16 v[76:79], a[144:147], v[44:47], v[76:79]\n"
    "v_exp_f32 v1, v1\n"
    "v_mfma_f32_16x16x32_bf16 v[92:95], a[148:151], v[44:47], v[92:95]\n"
    "ds_read_b128 a[172:175], %[rd] offset:31744\n"
    "v_mfma_f32_16x16x32_bf16 v[88:91], a[148:151], v[40:43], v[88:91]\n"
    "v_xor_b32 %[rd], 65536, %[rd]\n"
    "v_mfma_f32_16x16x32_bf16 v[84:87], a[148:151], v[36:39], v[84:87]\n"
    "s_add_u32 %[so0], %[so0], 16384\n"
    "v_exp_f32 v2, v2\n"
    "v_mfma_f32_16x16x32_bf16 v[80:83], a[148:151], v[32:35], v[80:83]\n"
    "s_xor_b32 s40, s40, 65536\n"
    "v_exp_f32 v3, v3\n"
    "v_mfma_f32_16x16x32_bf16 v[96:99], a[152:155], v[32:35], v[96:99]\n"
    "s_add_u32 %[so1], %[so1], 16384\n"
    "v_exp_f32 v4, v4\n"
    "v_mfma_f32_16x16x32_bf16 v[100:103], a[152:155], v[36:39], v[100:103]\n"
    "v_exp_f32 v5, v5\n"
    "v_mfma_f32_16x16x32_bf16 v[104:107], a[152:155], v[40:43], v[104:107]\n"
    "v_exp_f32 v6, v6\n"
    "v_mfma_f32_16x16x32_bf16 v[108:111], a[152:155], v[44:47], v[108:111]\n"
    "v_exp_f32 v7, v7\n"
    "v_mfma_f32_16x16x32_bf16 v[124:127], a[156:159], v[44:47], v[124:127]\n"
    "v_exp_f32 v8, v8\n"
    "v_mfma_f32_16x16x32_bf16 v[120:123], a[156:159], v[40:43], v[120:123]\n"
    "v_exp_f32 v9, v9\n"
    "v_mfma_f32_16x16x32_bf16 v[116:119], a[156:159], v[36:39], v[116:119]\n"
    "v_exp_f32 v10, v10\n"
    "v_mfma_f32_16x16x32_bf16 v[112:115], a[156:159], v[32:35], v[112:115]\n"
    "v_exp_f32 v11, v11\n"
    "s_waitcnt vmcnt(0) lgkmcnt(0)\n"
    "s_barrier\n"
    "s_setpc_b64 s[52:53]\n"
    ".p2align 6\n"
    "SA_%=:\n"
    "ds_read_b128 a[144:147], %[rd16] offset:16384\n"
    "ds_read_b128 a[148:151], %[rd16] offset:17408\n"
    "ds_read_b128 a[152:155], %[rd16] offset:18432\n"
    "ds_read_b128 a[156:159], %[rd16] offset:19456\n"
    "ds_read_b128 a[160:163], %[rd16] offset:20480\n"
    "ds_read_b128 a[164:167], %[rd16] offset:21504\n"
    "ds_read_b128 a[168:171], %[rd16] offset:22528\n"
    "ds_read_b128 a[172:175], %[rd16] offset:23552\n"
    "v_mov_b32 v32, 0\n"
    "v_mov_b32 v33, 0\n"
    "v_mov_b32 v34, 0\n"
    "v_mov_b32 v35, 0\n"
    "v_mov_b32 v36, 0\n"
    "v_mov_b32 v37, 0\n"
    "v_mov_b32 v38, 0\n"
    "v_mov_b32 v39, 0\n"
    "v_mov_b32 v40, 0\n"
    "v_mov_b32 v41, 0\n"
    "v_mov_b32 v42, 0\n"
    "v_mov_b32 v43, 0\n"
    "v_mov_b32 v44, 0\n"
    "v_mov_b32 v45, 0\n"
    "v_mov_b32 v46, 0\n"
    "v_mov_b32 v47, 0\n"
    "v_accvgpr_write_b32 a128, 0\n"
    "v_accvgpr_write_b32 a129, 0\n"
    "v_accvgpr_write_b32 a130, 0\n"
    "v_accvgpr_write_b32 a131, 0\n"
    "v_accvgpr_write_b32 a132, 0\n"
    "v_accvgpr_write_b32 a133, 0\n"
    "v_accvgpr_write_b32 a134, 0\n"
    "v_accvgpr_write_b32 a135, 0\n"
    "v_accvgpr_write_b32 a136, 0\n"
    "v_accvgpr_write_b32 a137, 0\n"
    "v_accvgpr_write_b32 a138, 0\n"
    "v_accvgpr_write_b32 a139, 0\n"
    "v_accvgpr_write_b32 a140, 0\n"
    "v_accvgpr_write_b32 a141, 0\n"
    "v_accvgpr_write_b32 a142, 0\n"
    "v_accvgpr_write_b32 a143, 0\n"
    "v_mov_b32 v64, 0\n"
    "v_mov_b32 v65, 0\n"
    "v_mov_b32 v66, 0\n"
    "v_mov_b32 v67, 0\n"
    "v_mov_b32 v68, 0\n"
    "v_mov_b32 v69, 0\n"
    "v_mov_b32 v70, 0\n"
    "v_mov_b32 v71, 0\n"
    "v_mov_b32 v72, 0\n"
    "v_mov_b32 v73, 0\n"
    "v_mov_b32 v74, 0\n"
    "v_mov_b32 v75, 0\n"
    "v_mov_b32 v76, 0\n"
    "v_mov_b32 v77, 0\n"
    "v_mov_b32 v78, 0\n"
    "v_mov_b32 v79, 0\n"
    "v_mov_b32 v80, 0\n"
    "v_mov_b32 v81, 0\n"
    "v_mov_b32 v82, 0\n"
    "v_mov_b32 v83, 0\n"
    "v_mov_b32 v84, 0\n"
    "v_mov_b32 v85, 0\n"
    "v_mov_b32 v86, 0\n"
    "v_mov_b32 v87, 0\n"
    "v_mov_b32 v88, 0\n"
    "v_mov_b32 v89, 0\n"
    "v_mov_b32 v90, 0\n"
    "v_mov_b32 v91, 0\n"
    "v_mov_b32 v92, 0\n"
    "v_mov_b32 v93, 0\n"
    "v_mov_b32 v94, 0\n"
    "v_mov_b32 v95, 0\n"
    "v_mov_b32 v96, 0\n"
    "v_mov_b32 v97, 0\n"
    "v_mov_b32 v98, 0\n"
    "v_mov_b32 v99, 0\n"
    "v_mov_b32 v100, 0\n"
    "v_mov_b32 v101, 0\n"
    "v_mov_b32 v102, 0\n"
    "v_mov_b32 v103, 0\n"
    "v_mov_b32 v104, 0\n"
    "v_mov_b32 v105, 0\n"
    "v_mov_b32 v106, 0\n"
    "v_mov_b32 v107, 0\n"
    "v_mov_b32 v108, 0\n"
    "v_mov_b32 v109, 0\n"
    "v_mov_b32 v110, 0\n"
    "v_mov_b32 v111, 0\n"
    "v_mov_b32 v112, 0\n"
    "v_mov_b32 v113, 0\n"
    "v_mov_b32 v114, 0\n"
    "v_mov_b32 v115, 0\n"
    "v_mov_b32 v116, 0\n"
    "v_mov_b32 v117, 0\n"
    "v_mov_b32 v118, 0\n"
    "v_mov_b32 v119, 0\n"
    "v_mov_b32 v120, 0\n"
    "v_mov_b32 v121, 0\n"
    "v_mov_b32 v122, 0\n"
    "v_mov_b32 v123, 0\n"
    "v_mov_b32 v124, 0\n"
    "v_mov_b32 v125, 0\n"
    "v_mov_b32 v126, 0\n"
    "v_mov_b32 v127, 0\n"
    "s_waitcnt lgkmcnt(0)\n"
    "v_mfma_f32_16x16x32_bf16 v[0:3], a[144:147], a[0:3], v[192:195]\n"
    "v_mov_b32 v128, 0\n"
    "v_mov_b32 v129, 0\n"
    "v_mfma_f32_16x16x32_bf16 v[4:7], a[144:147], a[4:7], 0\n"
    "v_mov_b32 v130, 0\n"
    "v_mov_b32 v131, 0\n"
    "v_mfma_f32_16x16x32_bf16 v[8:11], a[144:147], a[8:11], 0\n"
    "v_mov_b32 v132, 0\n"
    "v_mov_b32 v133, 0\n"
    "v_mfma_f32_16x16x32_bf16 v[12:15], a[144:147], a[12:15], 0\n"
    "v_mov_b32 v134, 0\n"
    "v_mov_b32 v135, 0\n"
    "v_mfma_f32_16x16x32_bf16 v[12:15], a[148:151], a[28:31], v[12:15]\n"
    "v_mov_b32 v136, 0\n"
    "v_mov_b32 v137, 0\n"
    "v_mfma_f32_16x16x32_bf16 v[8:11], a[148:151], a[24:27], v[8:11]\n"
    "v_mov_b32 v138, 0\n"
    "v_mov_b32 v139, 0\n"
    "v_mfma_f32_16x16x32_bf16 v[4:7], a[148:151], a[20:23], v[4:7]\n"
    "v_mov_b32 v140, 0\n"
    "v_mov_b32 v141, 0\n"
    "v_mfma_f32_16x16x32_bf16 v[0:3], a[148:151], a[16:19], v[0:3]\n"
    "v_mov_b32 v142, 0\n"
    "v_mov_b32 v143, 0\n"
    "v_mfma_f32_16x16x32_bf16 v[0:3], a[152:155], a[32:35], v[0:3]\n"
    "v_mov_b32 v144, 0\n"
    "v_mov_b32 v145, 0\n"
    "v_mfma_f32_16x16x32_bf16 v[4:7], a[152:155], a[36:39], v[4:7]\n"
    "v_mov_b32 v146, 0\n"
    "v_mov_b32 v147, 0\n"
    "v_mfma_f32_16x16x32_bf16 v[8:11], a[152:155], a[40:43], v[8:11]\n"
    "v_mov_b32 v148, 0\n"
    "v_mov_b32 v149, 0\n"
    "v_mfma_f32_16x16x32_bf16 v[12:15], a[152:155], a[44:47], v[12:15]\n"
    "v_mov_b32 v150, 0\n"
    "v_mov_b32 v151, 0\n"
    "v_mfma_f32_16x16x32_bf16 v[12:15], a[156:159], a[60:63], v[12:15]\n"
    "v_mov_b32 v152, 0\n"
    "v_mov_b32 v153, 0\n"
    "v_mfma_f32_16x16x32_bf16 v[8:11], a[156:159], a[56:59], v[8:11]\n"
    "v_mov_b32 v154, 0\n"
    "v_mov_b32 v155, 0\n"
    "v_mfma_f32_16x16x32_bf16 v[4:7], a[156:159], a[52:55], v[4:7]\n"
    "v_mov_b32 v156, 0\n"
    "v_mov_b32 v157, 0\n"
    "v_mfma_f32_16x16x32_bf16 v[0:3], a[156:159], a[48:51], v[0:3]\n"
    "v_mov_b32 v158, 0\n"
    "v_mov_b32 v159, 0\n"
    "v_mfma_f32_16x16x32_bf16 v[16:19], a[160:163], a[0:3], v[196:199]\n"
    "v_mov_b32 v160, 0\n"
    "v_mov_b32 v161, 0\n"
    "v_mfma_f32_16x16x32_bf16 v[20:23], a[160:163], a[4:7], v[192:195]\n"
    "v_mov_b32 v162, 0\n"
    "v_mov_b32 v163, 0\n"
    "v_mfma_f32_16x16x32_bf16 v[24:27], a[160:163], a[8:11], 0\n"
    "v_mov_b32 v164, 0\n"
    "v_mov_b32 v165, 0\n"
    "v_mfma_f32_16x16x32_bf16 v[28:31], a[160:163], a[12:15], 0\n"
    "v_mov_b32 v166, 0\n"
    "v_mov_b32 v167, 0\n"
    "v_mfma_f32_16x16x32_bf16 v[28:31], a[164:167], a[28:31], v[28:31]\n"
    "v_mov_b32 v168, 0\n"
    "v_mov_b32 v169, 0\n"
    "v_mfma_f32_16x16x32_bf16 v[24:27], a[164:167], a[24:27], v[24:27]\n"
    "v_mov_b32 v170, 0\n"
    "v_mov_b32 v171, 0\n"
    "v_mfma_f32_16x16x32_bf16 v[20:23], a[164:167], a[20:23], v[20:23]\n"
    "v_mov_b32 v172, 0\n"
    "v_mov_b32 v173, 0\n"
    "v_mfma_f32_16x16x32_bf16 v[16:19], a[164:167], a[16:19], v[16:19]\n"
    "v_mov_b32 v174, 0\n"
    "v_mov_b32 v175, 0\n"
    "v_mfma_f32_16x16x32_bf16 v[16:19], a[168:171], a[32:35], v[16:19]\n"
    "v_mov_b32 v176, 0\n"
    "v_mov_b32 v177, 0\n"
    "v_mfma_f32_16x16x32_bf16 v[20:23], a[168:171], a[36:39], v[20:23]\n"
    "v_mov_b32 v178, 0\n"
    "v_mov_b32 v179, 0\n"
    "v_mfma_f32_16x16x32_bf16 v[24:27], a[168:171], a[40:43], v[24:27]\n"
    "v_mov_b32 v180, 0\n"
    "v_mov_b32 v181, 0\n"
    "v_mfma_f32_16x16x32_bf16 v[28:31], a[168:171], a[44:47], v[28:31]\n"
    "v_mov_b32 v182, 0\n"
    "v_mov_b32 v183, 0\n"
    "v_mfma_f32_16x16x32_bf16 v[28:31], a[172:175], a[60:63], v[28:31]\n"
    "v_mov_b32 v184, 0\n"
    "v_mov_b32 v185, 0\n"
    "v_mfma_f32_16x16x32_bf16 v[24:27], a[172:175], a[56:59], v[24:27]\n"
    "v_mov_b32 v186, 0\n"
    "v_mov_b32 v187, 0\n"
    "v_mfma_f32_16x16x32_bf16 v[20:23], a[172:175], a[52:55], v[20:23]\n"
    "v_mov_b32 v188, 0\n"
    "v_mov_b32 v189, 0\n"
    "v_mfma_f32_16x16x32_bf16 v[16:19], a[172:175], a[48:51], v[16:19]\n"
    "v_mov_b32 v190, 0\n"
    "v_mov_b32 v191, 0\n"
    "v_exp_f32 v0, v0\n"
    "v_exp_f32 v1, v1\n"
    "v_exp_f32 v2, v2\n"
    "v_exp_f32 v3, v3\n"
    "v_exp_f32 v4, v4\n"
    "v_exp_f32 v5, v5\n"
    "v_exp_f32 v6, v6\n"
    "v_exp_f32 v7, v7\n"
    "v_exp_f32 v8, v8\n"
    "v_exp_f32 v9, v9\n"
    "v_exp_f32 v10, v10\n"
    "v_exp_f32 v11, v11\n"
    "s_setpc_b64 s[52:53]\n"
    ".p2align 6\n"
    "SB_%=:\n"
    "ds_read_b128 a[144:147], %[rd16] offset:0\n"
    "ds_read_b128 a[148:151], %[rd16] offset:1024\n"
    "ds_read_b128 a[152:155], %[rd16] offset:2048\n"
    "ds_read_b128 a[156:159], %[rd16] offset:3072\n"
    "ds_read_b128 a[160:163], %[rd16] offset:4096\n"
    "ds_read_b128 a[164:167], %[rd16] offset:5120\n"
    "ds_read_b128 a[168:171], %[rd16] offset:6144\n"
    "ds_read_b128 a[172:175], %[rd16] offset:7168\n"
    "v_mov_b32 v32, 0\n"
    "v_mov_b32 v33, 0\n"
    "v_mov_b32 v34, 0\n"
    "v_mov_b32 v35, 0\n"
    "v_mov_b32 v36, 0\n"
    "v_mov_b32 v37, 0\n"
    "v_mov_b32 v38, 0\n"
    "v_mov_b32 v39, 0\n"
    "v_mov_b32 v40, 0\n"
    "v_mov_b32 v41, 0\n"
    "v_mov_b32 v42, 0\n"
    "v_mov_b32 v43, 0\n"
    "v_mov_b32 v44, 0\n"
    "v_mov_b32 v45, 0\n"
    "v_mov_b32 v46, 0\n"
    "v_mov_b32 v47, 0\n"
    "v_accvgpr_write_b32 a128, 0\n"
    "v_accvgpr_write_b32 a129, 0\n"
    "v_accvgpr_write_b32 a130, 0\n"
    "v_accvgpr_write_b32 a131, 0\n"
    "v_accvgpr_write_b32 a132, 0\n"
    "v_accvgpr_write_b32 a133, 0\n"
    "v_accvgpr_write_b32 a134, 0\n"
    "v_accvgpr_write_b32 a135, 0\n"
    "v_accvgpr_write_b32 a136, 0\n"
    "v_accvgpr_write_b32 a137, 0\n"
    "v_accvgpr_write_b32 a138, 0\n"
    "v_accvgpr_write_b32 a139, 0\n"
    "v_accvgpr_write_b32 a140, 0\n"
    "v_accvgpr_write_b32 a141, 0\n"
    "v_accvgpr_write_b32 a142, 0\n"
    "v_accvgpr_write_b32 a143, 0\n"
    "v_mov_b32 v64, 0\n"
    "v_mov_b32 v65, 0\n"
    "v_mov_b32 v66, 0\n"
    "v_mov_b32 v67, 0\n"
    "v_mov_b32 v68, 0\n"
    "v_mov_b32 v69, 0\n"
    "v_mov_b32 v70, 0\n"
    "v_mov_b32 v71, 0\n"
    "v_mov_b32 v72, 0\n"
    "v_mov_b32 v73, 0\n"
    "v_mov_b32 v74, 0\n"
    "v_mov_b32 v75, 0\n"
    "v_mov_b32 v76, 0\n"
    "v_mov_b32 v77, 0\n"
    "v_mov_b32 v78, 0\n"
    "v_mov_b32 v79, 0\n"
    "v_mov_b32 v80, 0\n"
    "v_mov_b32 v81, 0\n"
    "v_mov_b32 v82, 0\n"
    "v_mov_b32 v83, 0\n"
    "v_mov_b32 v84, 0\n"
    "v_mov_b32 v85, 0\n"
    "v_mov_b32 v86, 0\n"
    "v_mov_b32 v87, 0\n"
    "v_mov_b32 v88, 0\n"
    "v_mov_b32 v89, 0\n"
    "v_mov_b32 v90, 0\n"
    "v_mov_b32 v91, 0\n"
    "v_mov_b32 v92, 0\n"
    "v_mov_b32 v93, 0\n"
    "v_mov_b32 v94, 0\n"
    "v_mov_b32 v95, 0\n"
    "v_mov_b32 v96, 0\n"
    "v_mov_b32 v97, 0\n"
    "v_mov_b32 v98, 0\n"
    "v_mov_b32 v99, 0\n"
    "v_mov_b32 v100, 0\n"
    "v_mov_b32 v101, 0\n"
    "v_mov_b32 v102, 0\n"
    "v_mov_b32 v103, 0\n"
    "v_mov_b32 v104, 0\n"
    "v_mov_b32 v105, 0\n"
    "v_mov_b32 v106, 0\n"
    "v_mov_b32 v107, 0\n"
    "v_mov_b32 v108, 0\n"
    "v_mov_b32 v109, 0\n"
    "v_mov_b32 v110, 0\n"
    "v_mov_b32 v111, 0\n"
    "v_mov_b32 v112, 0\n"
    "v_mov_b32 v113, 0\n"
    "v_mov_b32 v114, 0\n"
    "v_mov_b32 v115, 0\n"
    "v_mov_b32 v116, 0\n"
    "v_mov_b32 v117, 0\n"
    "v_mov_b32 v118, 0\n"
    "v_mov_b32 v119, 0\n"
    "v_mov_b32 v120, 0\n"
    "v_mov_b32 v121, 0\n"
    "v_mov_b32 v122, 0\n"
    "v_mov_b32 v123, 0\n"
    "v_mov_b32 v124, 0\n"
    "v_mov_b32 v125, 0\n"
    "v_mov_b32 v126, 0\n"
    "v_mov_b32 v127, 0\n"
    "s_waitcnt lgkmcnt(0)\n"
    "v_mfma_f32_16x16x32_bf16 v[0:3], a[144:147], a[0:3], v[192:195]\n"
    "v_mov_b32 v128, 0\n"
    "v_mov_b32 v129, 0\n"
    "v_mfma_f32_16x16x32_bf16 v[4:7], a[144:147], a[4:7], 0\n"
    "v_mov_b32 v130, 0\n"
    "v_mov_b32 v131, 0\n"
    "v_mfma_f32_16x16x32_bf16 v[8:11], a[144:147], a[8:11], 0\n"
    "v_mov_b32 v132, 0\n"
    "v_mov_b32 v133, 0\n"
    "v_mfma_f32_16x16x32_bf16 v[12:15], a[144:147], a[12:15], 0\n"
    "v_mov_b32 v134, 0\n"
    "v_mov_b32 v135, 0\n"
    "v_mfma_f32_16x16x32_bf16 v[12:15], a[148:151], a[28:31], v[12:15]\n"
    "v_mov_b32 v136, 0\n"
    "v_mov_b32 v137, 0\n"
    "v_mfma_f32_16x16x32_bf16 v[8:11], a[148:151], a[24:27], v[8:11]\n"
    "v_mov_b32 v138, 0\n"
    "v_mov_b32 v139, 0\n"
    "v_mfma_f32_16x16x32_bf16 v[4:7], a[148:151], a[20:23], v[4:7]\n"
    "v_mov_b32 v140, 0\n"
    "v_mov_b32 v141, 0\n"
    "v_mfma_f32_16x16x32_bf16 v[0:3], a[148:151], a[16:19], v[0:3]\n"
    "v_mov_b32 v142, 0\n"
    "v_mov_b32 v143, 0\n"
    "v_mfma_f32_16x16x32_bf16 v[0:3], a[152:155], a[32:35], v[0:3]\n"
    "v_mov_b32 v144, 0\n"
    "v_mov_b32 v145, 0\n"
    "v_mfma_f32_16x16x32_bf16 v[4:7], a[152:155], a[36:39], v[4:7]\n"
    "v_mov_b32 v146, 0\n"
    "v_mov_b32 v147, 0\n"
    "v_mfma_f32_16x16x32_bf16 v[8:11], a[152:155], a[40:43], v[8:11]\n"
    "v_mov_b32 v148, 0\n"
    "v_mov_b32 v149, 0\n"
    "v_mfma_f32_16x16x32_bf16 v[12:15], a[152:155], a[44:47], v[12:15]\n"
    "v_mov_b32 v150, 0\n"
    "v_mov_b32 v151, 0\n"
    "v_mfma_f32_16x16x32_bf16 v[12:15], a[156:159], a[60:63], v[12:15]\n"
    "v_mov_b32 v152, 0\n"
    "v_mov_b32 v153, 0\n"
    "v_mfma_f32_16x16x32_bf16 v[8:11], a[156:159], a[56:59], v[8:11]\n"
    "v_mov_b32 v154, 0\n"
    "v_mov_b32 v155, 0\n"
    "v_mfma_f32_16x16x32_bf16 v[4:7], a[156:159], a[52:55], v[4:7]\n"
    "v_mov_b32 v156, 0\n"
    "v_mov_b32 v157, 0\n"
    "v_mfma_f32_16x16x32_bf16 v[0:3], a[156:159], a[48:51], v[0:3]\n"
    "v_mov_b32 v158, 0\n"
    "v_mov_b32 v159, 0\n"
    "v_mfma_f32_16x16x32_bf16 v[16:19], a[160:163], a[0:3], v[196:199]\n"
    "v_mov_b32 v160, 0\n"
    "v_mov_b32 v161, 0\n"
    "v_mfma_f32_16x16x32_bf16 v[20:23], a[160:163], a[4:7], v[192:195]\n"
    "v_mov_b32 v162, 0\n"
    "v_mov_b32 v163, 0\n"
    "v_mfma_f32_16x16x32_bf16 v[24:27], a[160:163], a[8:11], 0\n"
    "v_mov_b32 v164, 0\n"
    "v_mov_b32 v165, 0\n"
    "v_mfma_f32_16x16x32_bf16 v[28:31], a[160:163], a[12:15], 0\n"
    "v_mov_b32 v166, 0\n"
    "v_mov_b32 v167, 0\n"
    "v_mfma_f32_16x16x32_bf16 v[28:31], a[164:167], a[28:31], v[28:31]\n"
    "v_mov_b32 v168, 0\n"
    "v_mov_b32 v169, 0\n"
    "v_mfma_f32_16x16x32_bf16 v[24:27], a[164:167], a[24:27], v[24:27]\n"
    "v_mov_b32 v170, 0\n"
    "v_mov_b32 v171, 0\n"
    "v_mfma_f32_16x16x32_bf16 v[20:23], a[164:167], a[20:23], v[20:23]\n"
    "v_mov_b32 v172, 0\n"
    "v_mov_b32 v173, 0\n"
    "v_mfma_f32_16x16x32_bf16 v[16:19], a[164:167], a[16:19], v[16:19]\n"
    "v_mov_b32 v174, 0\n"
    "v_mov_b32 v175, 0\n"
    "v_mfma_f32_16x16x32_bf16 v[16:19], a[168:171], a[32:35], v[16:19]\n"
    "v_mov_b32 v176, 0\n"
    "v_mov_b32 v177, 0\n"
    "v_mfma_f32_16x16x32_bf16 v[20:23], a[168:171], a[36:39], v[20:23]\n"
    "v_mov_b32 v178, 0\n"
    "v_mov_b32 v179, 0\n"
    "v_mfma_f32_16x16x32_bf16 v[24:27], a[168:171], a[40:43], v[24:27]\n"
    "v_mov_b32 v180, 0\n"
    "v_mov_b32 v181, 0\n"
    "v_mfma_f32_16x16x32_bf16 v[28:31], a[168:171], a[44:47], v[28:31]\n"
    "v_mov_b32 v182, 0\n"
    "v_mov_b32 v183, 0\n"
    "v_mfma_f32_16x16x32_bf16 v[28:31], a[172:175], a[60:63], v[28:31]\n"
    "v_mov_b32 v184, 0\n"
    "v_mov_b32 v185, 0\n"
    "v_mfma_f32_16x16x32_bf16 v[24:27], a[172:175], a[56:59], v[24:27]\n"
    "v_mov_b32 v186, 0\n"
    "v_mov_b32 v187, 0\n"
    "v_mfma_f32_16x16x32_bf16 v[20:23], a[172:175], a[52:55], v[20:23]\n"
    "v_mov_b32 v188, 0\n"
    "v_mov_b32 v189, 0\n"
    "v_mfma_f32_16x16x32_bf16 v[16:19], a[172:175], a[48:51], v[16:19]\n"
    "v_mov_b32 v190, 0\n"
    "v_mov_b32 v191, 0\n"
    "v_exp_f32 v0, v0\n"
    "v_exp_f32 v1, v1\n"
    "v_exp_f32 v2, v2\n"
    "v_exp_f32 v3, v3\n"
    "v_exp_f32 v4, v4\n"
    "v_exp_f32 v5, v5\n"
    "v_exp_f32 v6, v6\n"
    "v_exp_f32 v7, v7\n"
    "v_exp_f32 v8, v8\n"
    "v_exp_f32 v9, v9\n"
    "v_exp_f32 v10, v10\n"
    "v_exp_f32 v11, v11\n"
    "s_setpc_b64 s[52:53]\n"
    ".p2align 6\n"
    "ES_%=:\n"
    "v_mfma_f32_16x16x32_bf16 v[136:139], a[160:163], v[40:43], v[136:139]\n"
    "v_accvgpr_mov_b32 a0, a64\n"
    "v_accvgpr_mov_b32 a1, a65\n"
    "v_mfma_f32_16x16x32_bf16 v[140:143], a[160:163], v[44:47], v[140:143]\n"
    "v_accvgpr_mov_b32 a2, a66\n"
    "v_accvgpr_mov_b32 a3, a67\n"
    "v_mfma_f32_16x16x32_bf16 v[156:159], a[164:167], v[44:47], v[156:159]\n"
    "v_accvgpr_mov_b32 a4, a68\n"
    "v_accvgpr_mov_b32 a5, a69\n"
    "v_mfma_f32_16x16x32_bf16 v[152:155], a[164:167], v[40:43], v[152:155]\n"
    "v_accvgpr_mov_b32 a6, a70\n"
    "v_accvgpr_mov_b32 a7, a71\n"
    "v_mfma_f32_16x16x32_bf16 v[168:171], a[168:171], v[40:43], v[168:171]\n"
    "v_accvgpr_mov_b32 a8, a72\n"
    "v_accvgpr_mov_b32 a9, a73\n"
    "v_mfma_f32_16x16x32_bf16 v[172:175], a[168:171], v[44:47], v[172:175]\n"
    "v_accvgpr_mov_b32 a10, a74\n"
    "v_accvgpr_mov_b32 a11, a75\n"
    "v_mfma_f32_16x16x32_bf16 v[188:191], a[172:175], v[44:47], v[188:191]\n"
    "v_accvgpr_mov_b32 a12, a76\n"
    "v_accvgpr_mov_b32 a13, a77\n"
    "v_mfma_f32_16x16x32_bf16 v[184:187], a[172:175], v[40:43], v[184:187]\n"
    "v_accvgpr_mov_b32 a14, a78\n"
    "v_accvgpr_mov_b32 a15, a79\n"
    "v_mfma_f32_16x16x32_bf16 a[136:139], a[176:179], v[40:43], a[136:139]\n"
    "v_accvgpr_mov_b32 a16, a80\n"
    "v_accvgpr_mov_b32 a17, a81\n"
    "v_mfma_f32_16x16x32_bf16 a[140:143], a[176:179], v[44:47], a[140:143]\n"
    "v_accvgpr_mov_b32 a18, a82\n"
    "v_accvgpr_mov_b32 a19, a83\n"
    "v_accvgpr_mov_b32 a20, a84\n"
    "v_accvgpr_mov_b32 a21, a85\n"
    "v_accvgpr_mov_b32 a22, a86\n"
    "v_accvgpr_mov_b32 a23, a87\n"
    "v_accvgpr_mov_b32 a24, a88\n"
    "v_accvgpr_mov_b32 a25, a89\n"
    "v_accvgpr_mov_b32 a26, a90\n"
    "v_accvgpr_mov_b32 a27, a91\n"
    "v_accvgpr_mov_b32 a28, a92\n"
    "v_accvgpr_mov_b32 a29, a93\n"
    "v_accvgpr_mov_b32 a30, a94\n"
    "v_accvgpr_mov_b32 a31, a95\n"
    "v_accvgpr_mov_b32 a32, a96\n"
    "v_accvgpr_mov_b32 a33, a97\n"
    "v_accvgpr_mov_b32 a34, a98\n"
    "v_accvgpr_mov_b32 a35, a99\n"
    "v_accvgpr_mov_b32 a36, a100\n"
    "v_accvgpr_mov_b32 a37, a101\n"
    "v_accvgpr_mov_b32 a38, a102\n"
    "v_accvgpr_mov_b32 a39, a103\n"
    "v_accvgpr_mov_b32 a40, a104\n"
    "v_accvgpr_mov_b32 a41, a105\n"
    "v_accvgpr_mov_b32 a42, a106\n"
    "v_accvgpr_mov_b32 a43, a107\n"
    "v_accvgpr_mov_b32 a44, a108\n"
    "v_accvgpr_mov_b32 a45, a109\n"
    "v_accvgpr_mov_b32 a46, a110\n"
    "v_accvgpr_mov_b32 a47, a111\n"
    "v_accvgpr_mov_b32 a48, a112\n"
    "v_accvgpr_mov_b32 a49, a113\n"
    "v_accvgpr_mov_b32 a50, a114\n"
    "v_accvgpr_mov_b32 a51, a115\n"
    "v_accvgpr_mov_b32 a52, a116\n"
    "v_accvgpr_mov_b32 a53, a117\n"
    "v_accvgpr_mov_b32 a54, a118\n"
    "v_accvgpr_mov_b32 a55, a119\n"
    "v_accvgpr_mov_b32 a56, a120\n"
    "v_accvgpr_mov_b32 a57, a121\n"
    "v_accvgpr_mov_b32 a58, a122\n"
    "v_accvgpr_mov_b32 a59, a123\n"
    "v_accvgpr_mov_b32 a60, a124\n"
    "v_accvgpr_mov_b32 a61, a125\n"
    "v_accvgpr_mov_b32 a62, a126\n"
    "v_accvgpr_mov_b32 a63, a127\n"
    "v_add_u32 %[rd], 32768, %[rd]\n"
    "s_mov_b32 s47, 0x2b800000\n"
    "v_mov_b32 v210, 0x53800000\n"
    "v_mov_b32 v211, 0\n"
    "v_accvgpr_read_b32 v200, a128\n"
    "v_accvgpr_read_b32 v202, a132\n"
    "v_accvgpr_read_b32 v204, a136\n"
    "v_accvgpr_read_b32 v206, a140\n"
    "v_med3_f32 v208, v200, s47, v210\n"
    "v_cmp_neq_f32 vcc, v208, v200\n"
    "v_rcp_f32 v200, v200\n"
    "s_nop 0\n"
    "v_cndmask_b32 v208, 0, 1, vcc\n"
    "v_or_b32 v211, v211, v208\n"
    "v_med3_f32 v208, v202, s47, v210\n"
    "v_cmp_neq_f32 vcc, v208, v202\n"
    "v_rcp_f32 v202, v202\n"
    "s_nop 0\n"
    "v_cndmask_b32 v208, 0, 1, vcc\n"
    "v_or_b32 v211, v211, v208\n"
    "v_med3_f32 v208, v204, s47, v210\n"
    "v_cmp_neq_f32 vcc, v208, v204\n"
    "v_rcp_f32 v204, v204\n"
    "s_nop 0\n"
    "v_cndmask_b32 v208, 0, 1, vcc\n"
    "v_or_b32 v211, v211, v208\n"
    "v_med3_f32 v208, v206, s47, v210\n"
    "v_cmp_neq_f32 vcc, v208, v206\n"
    "v_rcp_f32 v206, v206\n"
    "s_nop 0\n"
    "v_cndmask_b32 v208, 0, 1, vcc\n"
    "v_or_b32 v211, v211, v208\n"
    "s_add_u32 s44, %[soo], 0\n"
    "v_pk_mul_f32 v[64:65], v[64:65], v[200:201] op_sel_hi:[1,0]\n"
    "v_pk_mul_f32 v[66:67], v[66:67], v[200:201] op_sel_hi:[1,0]\n"
    "v_pk_mul_f32 v[80:81], v[80:81], v[200:201] op_sel_hi:[1,0]\n"
    "v_pk_mul_f32 v[82:83], v[82:83], v[200:201] op_sel_hi:[1,0]\n"
    "v_cvt_pk_bf16_f32 v64, v64, v65\n"
    "v_cvt_pk_bf16_f32 v65, v66, v67\n"
    "v_cvt_pk_bf16_f32 v66, v80, v81\n"
    "v_cvt_pk_bf16_f32 v67, v82, v83\n"
    "buffer_store_dwordx4 v[64:67], %[vo], %[srdo], s44 offen offset:0 sc1\n"
    "v_pk_mul_f32 v[96:97], v[96:97], v[200:201] op_sel_hi:[1,0]\n"
    "v_pk_mul_f32 v[98:99], v[98:99], v[200:201] op_sel_hi:[1,0]\n"
    "v_pk_mul_f32 v[112:113], v[112:113], v[200:201] op_sel_hi:[1,0]\n"
    "v_pk_mul_f32 v[114:115], v[114:115], v[200:201] op_sel_hi:[1,0]\n"
    "v_cvt_pk_bf16_f32 v96, v96, v97\n"
    "v_cvt_pk_bf16_f32 v97, v98, v99\n"
    "v_cvt_pk_bf16_f32 v98, v112, v113\n"
    "v_cvt_pk_bf16_f32 v99, v114, v115\n"
    "buffer_store_dwordx4 v[96:99], %[vo], %[srdo], s44 offen offset:1024 sc1\n"
    "v_pk_mul_f32 v[128:129], v[128:129], v[200:201] op_sel_hi:[1,0]\n"
    "v_pk_mul_f32 v[130:131], v[130:131], v[200:201] op_sel_hi:[1,0]\n"
    "v_pk_mul_f32 v[144:145], v[144:145], v[200:201] op_sel_hi:[1,0]\n"
    "v_pk_mul_f32 v[146:147], v[146:147], v[200:201] op_sel_hi:[1,0]\n"
    "v_cvt_pk_bf16_f32 v128, v128, v129\n"
    "v_cvt_pk_bf16_f32 v129, v130, v131\n"
    "v_cvt_pk_bf16_f32 v130, v144, v145\n"
    "v_cvt_pk_bf16_f32 v131, v146, v147\n"
    "buffer_store_dwordx4 v[128:131], %[vo], %[srdo], s44 offen offset:2048 sc1\n"
    "v_pk_mul_f32 v[160:161], v[160:161], v[200:201] op_sel_hi:[1,0]\n"
    "v_pk_mul_f32 v[162:163], v[162:163], v[200:201] op_sel_hi:[1,0]\n"
    "v_pk_mul_f32 v[176:177], v[176:177], v[200:201] op_sel_hi:[1,0]\n"
    "v_pk_mul_f32 v[178:179], v[178:179], v[200:201] op_sel_hi:[1,0]\n"
    "v_cvt_pk_bf16_f32 v160, v160, v161\n"
    "v_cvt_pk_bf16_f32 v161, v162, v163\n"
    "v_cvt_pk_bf16_f32 v162, v176, v177\n"
    "v_cvt_pk_bf16_f32 v163, v178, v179\n"
    "buffer_store_dwordx4 v[160:163], %[vo], %[srdo], s44 offen offset:3072 sc1\n"
    "s_add_u32 s44, %[soo], 131072\n"
    "v_pk_mul_f32 v[68:69], v[68:69], v[202:203] op_sel_hi:[1,0]\n"
    "v_pk_mul_f32 v[70:71], v[70:71], v[202:203] op_sel_hi:[1,0]\n"
    "v_pk_mul_f32 v[84:85], v[84:85], v[202:203] op_sel_hi:[1,0]\n"
    "v_pk_mul_f32 v[86:87], v[86:87], v[202:203] op_sel_hi:[1,0]\n"
    "v_cvt_pk_bf16_f32 v68, v68, v69\n"
    "v_cvt_pk_bf16_f32 v69, v70, v71\n"
    "v_cvt_pk_bf16_f32 v70, v84, v85\n"
    "v_cvt_pk_bf16_f32 v71, v86, v87\n"
    "buffer_store_dwordx4 v[68:71], %[vo], %[srdo], s44 offen offset:0 sc1\n"
    "v_pk_mul_f32 v[100:101], v[100:101], v[202:203] op_sel_hi:[1,0]\n"
    "v_pk_mul_f32 v[102:103], v[102:103], v[202:203] op_sel_hi:[1,0]\n"
    "v_pk_mul_f32 v[116:117], v[116:117], v[202:203] op_sel_hi:[1,0]\n"
    "v_pk_mul_f32 v[118:119], v[118:119], v[202:203] op_sel_hi:[1,0]\n"
    "v_cvt_pk_bf16_f32 v100, v100, v101\n"
    "v_cvt_pk_bf16_f32 v101, v102, v103\n"
    "v_cvt_pk_bf16_f32 v102, v116, v117\n"
    "v_cvt_pk_bf16_f32 v103, v118, v119\n"
    "buffer_store_dwordx4 v[100:103], %[vo], %[srdo], s44 offen offset:1024 sc1\n"
    "v_pk_mul_f32 v[132:133], v[132:133], v[202:203] op_sel_hi:[1,0]\n"
    "v_pk_mul_f32 v[134:135], v[134:135], v[202:203] op_sel_hi:[1,0]\n"
    "v_pk_mul_f32 v[148:149], v[148:149], v[202:203] op_sel_hi:[1,0]\n"
    "v_pk_mul_f32 v[150:151], v[150:151], v[202:203] op_sel_hi:[1,0]\n"
    "v_cvt_pk_bf16_f32 v132, v132, v133\n"
    "v_cvt_pk_bf16_f32 v133, v134, v135\n"
    "v_cvt_pk_bf16_f32 v134, v148, v149\n"
    "v_cvt_pk_bf16_f32 v135, v150, v151\n"
    "buffer_store_dwordx4 v[132:135], %[vo], %[srdo], s44 offen offset:2048 sc1\n"
    "v_pk_mul_f32 v[164:165], v[164:165], v[202:203] op_sel_hi:[1,0]\n"
    "v_pk_mul_f32 v[166:167], v[166:167], v[202:203] op_sel_hi:[1,0]\n"
    "v_pk_mul_f32 v[180:181], v[180:181], v[202:203] op_sel_hi:[1,0]\n"
    "v_pk_mul_f32 v[182:183], v[182:183], v[202:203] op_sel_hi:[1,0]\n"
    "v_cvt_pk_bf16_f32 v164, v164, v165\n"
    "v_cvt_pk_bf16_f32 v165, v166, v167\n"
    "v_cvt_pk_bf16_f32 v166, v180, v181\n"
    "v_cvt_pk_bf16_f32 v167, v182, v183\n"
    "buffer_store_dwordx4 v[164:167], %[vo], %[srdo], s44 offen offset:3072 sc1\n"
    "s_add_u32 s44, %[soo], 262144\n"
    "v_pk_mul_f32 v[72:73], v[72:73], v[204:205] op_sel_hi:[1,0]\n"
    "v_pk_mul_f32 v[74:75], v[74:75], v[204:205] op_sel_hi:[1,0]\n"
    "v_pk_mul_f32 v[88:89], v[88:89], v[204:205] op_sel_hi:[1,0]\n"
    "v_pk_mul_f32 v[90:91], v[90:91], v[204:205] op_sel_hi:[1,0]\n"
    "v_cvt_pk_bf16_f32 v72, v72, v73\n"
    "v_cvt_pk_bf16_f32 v73, v74, v75\n"
    "v_cvt_pk_bf16_f32 v74, v88, v89\n"
    "v_cvt_pk_bf16_f32 v75, v90, v91\n"
    "buffer_store_dwordx4 v[72:75], %[vo], %[srdo], s44 offen offset:0 sc1\n"
    "v_pk_mul_f32 v[104:105], v[104:105], v[204:205] op_sel_hi:[1,0]\n"
    "v_pk_mul_f32 v[106:107], v[106:107], v[204:205] op_sel_hi:[1,0]\n"
    "v_pk_mul_f32 v[120:121], v[120:121], v[204:205] op_sel_hi:[1,0]\n"
    "v_pk_mul_f32 v[122:123], v[122:123], v[204:205] op_sel_hi:[1,0]\n"
    "v_cvt_pk_bf16_f32 v104, v104, v105\n"
    "v_cvt_pk_bf16_f32 v105, v106, v107\n"
    "v_cvt_pk_bf16_f32 v106, v120, v121\n"
    "v_cvt_pk_bf16_f32 v107, v122, v123\n"
    "buffer_store_dwordx4 v[104:107], %[vo], %[srdo], s44 offen offset:1024 sc1\n"
    "v_pk_mul_f32 v[136:137], v[136:137], v[204:205] op_sel_hi:[1,0]\n"
    "v_pk_mul_f32 v[138:139], v[138:139], v[204:205] op_sel_hi:[1,0]\n"
    "v_pk_mul_f32 v[152:153], v[152:153], v[204:205] op_sel_hi:[1,0]\n"
    "v_pk_mul_f32 v[154:155], v[154:155], v[204:205] op_sel_hi:[1,0]\n"
    "v_cvt_pk_bf16_f32 v136, v136, v137\n"
    "v_cvt_pk_bf16_f32 v137, v138, v139\n"
    "v_cvt_pk_bf16_f32 v138, v152, v153\n"
    "v_cvt_pk_bf16_f32 v139, v154, v155\n"
    "buffer_store_dwordx4 v[136:139], %[vo], %[srdo], s44 offen offset:2048 sc1\n"
    "v_pk_mul_f32 v[168:169], v[168:169], v[204:205] op_sel_hi:[1,0]\n"
    "v_pk_mul_f32 v[170:171], v[170:171], v[204:205] op_sel_hi:[1,0]\n"
    "v_pk_mul_f32 v[184:185], v[184:185], v[204:205] op_sel_hi:[1,0]\n"
    "v_pk_mul_f32 v[186:187], v[186:187], v[204:205] op_sel_hi:[1,0]\n"
    "v_cvt_pk_bf16_f32 v168, v168, v169\n"
    "v_cvt_pk_bf16_f32 v169, v170, v171\n"
    "v_cvt_pk_bf16_f32 v170, v184, v185\n"
    "v_cvt_pk_bf16_f32 v171, v186, v187\n"
    "buffer_store_dwordx4 v[168:171], %[vo], %[srdo], s44 offen offset:3072 sc1\n"
    "s_add_u32 s44, %[soo], 393216\n"
    "v_pk_mul_f32 v[76:77], v[76:77], v[206:207] op_sel_hi:[1,0]\n"
    "v_pk_mul_f32 v[78:79], v[78:79], v[206:207] op_sel_hi:[1,0]\n"
    "v_pk_mul_f32 v[92:93], v[92:93], v[206:207] op_sel_hi:[1,0]\n"
    "v_pk_mul_f32 v[94:95], v[94:95], v[206:207] op_sel_hi:[1,0]\n"
    "v_cvt_pk_bf16_f32 v76, v76, v77\n"
    "v_cvt_pk_bf16_f32 v77, v78, v79\n"
    "v_cvt_pk_bf16_f32 v78, v92, v93\n"
    "v_cvt_pk_bf16_f32 v79, v94, v95\n"
    "buffer_store_dwordx4 v[76:79], %[vo], %[srdo], s44 offen offset:0 sc1\n"
    "v_pk_mul_f32 v[108:109], v[108:109], v[206:207] op_sel_hi:[1,0]\n"
    "v_pk_mul_f32 v[110:111], v[110:111], v[206:207] op_sel_hi:[1,0]\n"
    "v_pk_mul_f32 v[124:125], v[124:125], v[206:207] op_sel_hi:[1,0]\n"
    "v_pk_mul_f32 v[126:127], v[126:127], v[206:207] op_sel_hi:[1,0]\n"
    "v_cvt_pk_bf16_f32 v108, v108, v109\n"
    "v_cvt_pk_bf16_f32 v109, v110, v111\n"
    "v_cvt_pk_bf16_f32 v110, v124, v125\n"
    "v_cvt_pk_bf16_f32 v111, v126, v127\n"
    "buffer_store_dwordx4 v[108:111], %[vo], %[srdo], s44 offen offset:1024 sc1\n"
    "v_pk_mul_f32 v[140:141], v[140:141], v[206:207] op_sel_hi:[1,0]\n"
    "v_pk_mul_f32 v[142:143], v[142:143], v[206:207] op_sel_hi:[1,0]\n"
    "v_pk_mul_f32 v[156:157], v[156:157], v[206:207] op_sel_hi:[1,0]\n"
    "v_pk_mul_f32 v[158:159], v[158:159], v[206:207] op_sel_hi:[1,0]\n"
    "v_cvt_pk_bf16_f32 v140, v140, v141\n"
    "v_cvt_pk_bf16_f32 v141, v142, v143\n"
    "v_cvt_pk_bf16_f32 v142, v156, v157\n"
    "v_cvt_pk_bf16_f32 v143, v158, v159\n"
    "buffer_store_dwordx4 v[140:143], %[vo], %[srdo], s44 offen offset:2048 sc1\n"
    "v_pk_mul_f32 v[172:173], v[172:173], v[206:207] op_sel_hi:[1,0]\n"
    "v_pk_mul_f32 v[174:175], v[174:175], v[206:207] op_sel_hi:[1,0]\n"
    "v_pk_mul_f32 v[188:189], v[188:189], v[206:207] op_sel_hi:[1,0]\n"
    "v_pk_mul_f32 v[190:191], v[190:191], v[206:207] op_sel_hi:[1,0]\n"
    "v_cvt_pk_bf16_f32 v172, v172, v173\n"
    "v_cvt_pk_bf16_f32 v173, v174, v175\n"
    "v_cvt_pk_bf16_f32 v174, v188, v189\n"
    "v_cvt_pk_bf16_f32 v175, v190, v191\n"
    "buffer_store_dwordx4 v[172:175], %[vo], %[srdo], s44 offen offset:3072 sc1\n"
    "v_mov_b32 v208, %[lb]\n"
    "v_add_u32 v208, 160256, v208\n"
    "ds_or_b32 v208, v211\n"
    "s_mov_b32 %[soo], %[soo2]\n"
    "s_setpc_b64 s[52:53]\n"
    "END_%=:\n"
    "s_waitcnt vmcnt(0) lgkmcnt(0)\n"
    "s_barrier\n"
    : [sox]"+&s"(sox), [sow]"+&s"(sow), [wrap]"+&s"(wrap), [wrapr]"+&s"(wrapr), [rdx]"+&v"(rdx), [rdc]"+&v"(rdc), [rdw0]"+&v"(rdw[0]), [rdw1]"+&v"(rdw[1]), [rdw2]"+&v"(rdw[2]), [rdw3]"+&v"(rdw[3]), [rd]"+&v"(rd), [so0]"+&s"(so0), [so1]"+&s"(so1), [soo]"+&s"(soo), [vex]"+&v"(vex), [spf]"+&s"(spf)
    : [vx0]"v"(vx[0]), [vx1]"v"(vx[1]), [vx2]"v"(vx[2]), [vx3]"v"(vx[3]), [vw0]"v"(vw[0]), [vw1]"v"(vw[1]), [vw2]"v"(vw[2]), [vw3]"v"(vw[3]), [srdx]"s"(srdx), [srdw]"s"(srdw), [m0x]"s"(m0x), [m0w]"s"(m0w), [srdm]"s"(srdm), [vmod]"v"(vmod), [vcw]"v"(vcw), [srdk]"s"(srdk), [srdv]"s"(srdv), [srdk0]"s"(srdk0), [srdo]"s"(srdo), [vdma]"v"(vdma), [vkx]"v"(vkx), [rd16]"v"(rd16), [vo]"v"(vo), [vgc]"v"(vgc), [vqex]"v"(vqex), [m0kv]"s"(m0kv), [m0kx]"s"(m0kx), [soo2]"s"(soo2), [W]"s"(W), [wid]"s"(wid), [lb]"s"(lb), [sex]"s"(sex), [vpf]"v"(vpf), [srdp]"s"(srdp)
    : "memory", "scc", "vcc", "s40","s41","s42","s43","s44","s45","s46","s47","s48","s49","s50","s51","s52","s53","s54","s55","s56","s57", "v0","v1","v2","v3","v4","v5","v6","v7","v8","v9","v10","v11","v12","v13","v14","v15","v16","v17","v18","v19","v20","v21","v22","v23","v24","v25","v26","v27","v28","v29","v30","v31","v32","v33","v34","v35","v36","v37","v38","v39","v40","v41","v42","v43","v44","v45","v46","v47","v48","v49","v50","v51","v52","v53","v54","v55","v56","v57","v58","v59","v60","v61","v62","v63","v64","v65","v66","v67","v68","v69","v70","v71","v72","v73","v74","v75","v76","v77","v78","v79","v80","v81","v82","v83","v84","v85","v86","v87","v88","v89","v90","v91","v92","v93","v94","v95","v96","v97","v98","v99","v100","v101","v102","v103","v104","v105","v106","v107","v108","v109","v110","v111","v112","v113","v114","v115","v116","v117","v118","v119","v120","v121","v122","v123","v124","v125","v126","v127","v128","v129","v130","v131","v132","v133","v134","v135","v136","v137","v138","v139","v140","v141","v142","v143","v144","v145","v146","v147","v148","v149","v150","v151","v152","v153","v154","v155","v156","v157","v158","v159","v160","v161","v162","v163","v164","v165","v166","v167","v168","v169","v170","v171","v172","v173","v174","v175","v176","v177","v178","v179","v180","v181","v182","v183","v184","v185","v186","v187","v188","v189","v190","v191","v192","v193","v194","v195","v196","v197","v198","v199","v200","v201","v202","v203","v204","v205","v206","v207","v208","v209","v210","v211","v212","v213","v214","v215", "a0","a1","a2","a3","a4","a5","a6","a7","a8","a9","a10","a11","a12","a13","a14","a15","a16","a17","a18","a19","a20","a21","a22","a23","a24","a25","a26","a27","a28","a29","a30","a31","a32","a33","a34","a35","a36","a37","a38","a39","a40","a41","a42","a43","a44","a45","a46","a47","a48","a49","a50","a51","a52","a53","a54","a55","a56","a57","a58","a59","a60","a61","a62","a63","a64","a65","a66","a67","a68","a69","a70","a71","a72","a73","a74","a75","a76","a77","a78","a79","a80","a81","a82","a83","a84","a85","a86","a87","a88","a89","a90","a91","a92","a93","a94","a95","a96","a97","a98","a99","a100","a101","a102","a103","a104","a105","a106","a107","a108","a109","a110","a111","a112","a113","a114","a115","a116","a117","a118","a119","a120","a121","a122","a123","a124","a125","a126","a127","a128","a129","a130","a131","a132","a133","a134","a135","a136","a137","a138","a139","a140","a141","a142","a143","a144","a145","a146","a147","a148","a149","a150","a151","a152","a153","a154","a155","a156","a157","a158","a159","a160","a161","a162","a163","a164","a165","a166","a167","a168","a169","a170","a171","a172","a173","a174","a175","a176","a177","a178","a179","a180","a181","a182","a183","a184","a185","a186","a187","a188","a189","a190","a191","a192","a193","a194","a195","a196","a197","a198","a199","a200","a201","a202","a203","a204","a205","a206","a207","a208","a209","a210","a211","a212","a213","a214","a215","a216","a217","a218","a219","a220","a221","a222","a223","a224","a225","a226","a227","a228","a229","a230","a231","a232","a233","a234","a235","a236","a237","a238","a239","a240","a241","a242","a243","a244","a245","a246","a247","a248","a249","a250","a251","a252","a253","a254","a255");
  if (__builtin_amdgcn_readfirstlane(*(volatile __attribute__((address_space(3))) u32*)(uintptr_t)(lb + 160256)) != 0) {
    __syncthreads();
    attn_true_max(xb, wq, kb, vb, cb, lds, h, i, wid, __builtin_amdgcn_mbcnt_hi(~0u, __builtin_amdgcn_mbcnt_lo(~0u, 0u)));
  }
  STAMP_END(1);
}

extern "C" void kernel_launch(void* const* d_in, const int* in_sizes, int n_in, void* d_out, int out_size, void* d_ws, size_t ws_size, hipStream_t stream) {
  const u16 *x = (const u16*)d_in[0], *ckv = (const u16*)d_in[1], *wq = (const u16*)d_in[2], *wuk = (const u16*)d_in[3], *wuv = (const u16*)d_in[4], *wo = (const u16*)d_in[5];
  const u16 *w1 = (const u16*)d_in[7], *b1 = (const u16*)d_in[8], *w2 = (const u16*)d_in[9], *b2 = (const u16*)d_in[10], *c = (const u16*)d_in[11], *wm = (const u16*)d_in[12];
  char* ws = (char*)d_ws; float* modp = (float*)(ws + WS_MODP);
  k_kv<<<256, 256, 0, stream>>>((const char*)ckv, (const char*)wuk, (const char*)wuv, ws + WS_KB, x, c, wm, modp, ws + WS_XB, (const char*)wq);
  k_attn<<<256, 256, 0, stream>>>(ws + WS_XB, modp, (const char*)wq, ws + WS_KB, ws + WS_VB, ws + WS_CB, (const char*)wo, (const char*)w1, (const char*)w2);
  k_tail<<<256, 256, 0, stream>>>(ws, x, (const char*)wo, (const char*)w1, (const char*)b1, (const char*)w2, b2, (u16*)d_out);
}
```
